```python
import torch

def _softmax(x, axis=-1):
    return torch.softmax(x, dim=axis)
def _gelu(x):
    return torch.nn.functional.gelu(x, approximate="tanh")
def _silu(x):
    return torch.nn.functional.silu(x)
def _sigmoid(x):
    return torch.sigmoid(x)
def _layernorm(x, axis=-1, eps=1e-5):
    m = x.mean(dim=axis, keepdim=True)
    v = x.var(dim=axis, keepdim=True, unbiased=False)
    return (x - m) / torch.sqrt(v + eps)
def _scatter_add(tgt, idx, src, axis=0):
    return tgt.clone().index_add_(axis, idx, src)
def _segment_sum(src, seg, n):
    out = torch.zeros((n,) + src.shape[1:], dtype=src.dtype)
    return out.index_add_(0, seg, src)
def _pad(x, pw):
    flat = tuple(v for pair in reversed(pw) for v in pair)
    return torch.nn.functional.pad(x, flat)

kernel_name = 'interleave_layers__spectral_fnet__glu_gated_glu__prompt'

def setup_inputs():

    _w = (lambda a: (lambda t: t.to(torch.bfloat16) if t.is_floating_point() else t)(torch.as_tensor(a)))
    def _A_setup():
        B, S, D = 1, 2048, 640
        ns = torch.arange(S)[:, None] * torch.arange(S)[None, :]
        nd = torch.arange(D)[:, None] * torch.arange(D)[None, :]
        Ws_re = torch.cos(-2.0 * torch.pi * ns / S).to(float)
        Ws_im = torch.sin(-2.0 * torch.pi * ns / S).to(float)
        Wd_re = torch.cos(-2.0 * torch.pi * nd / D).to(float)
        Wd_im = torch.sin(-2.0 * torch.pi * nd / D).to(float)
        return {
            "x": _w((lambda *s: torch.randn(*s, dtype=torch.bfloat16) * 0.05)(B, S, D)),
            "dft_seq_re": _w(Ws_re),
            "dft_seq_im": _w(Ws_im),
            "dft_hid_re": _w(Wd_re),
            "dft_hid_im": _w(Wd_im),
        }
    _A_in = _A_setup()

    def _B_setup():
        B, S, D, F = 1, 2048, 640, 1536
        return {
            "x": _w((lambda *s: torch.randn(*s, dtype=torch.bfloat16) * 0.05)(B, S, D)),
            "w_gate": _w((lambda *s: torch.randn(*s, dtype=torch.bfloat16) * 1.6)(D, F)),
            "w_up": _w((lambda *s: torch.randn(*s, dtype=torch.bfloat16) * 0.8)(D, F)),
            "w_down": _w((lambda *s: torch.randn(*s, dtype=torch.bfloat16) * 0.8)(F, D)),
        }
    _B_in = _B_setup()

    return {
        "A_x": _A_in["x"],
        "A_dft_seq_re": _A_in["dft_seq_re"],
        "A_dft_seq_im": _A_in["dft_seq_im"],
        "A_dft_hid_re": _A_in["dft_hid_re"],
        "A_dft_hid_im": _A_in["dft_hid_im"],
        "B_x": _B_in["x"],
        "B_w_gate": _B_in["w_gate"],
        "B_w_up": _B_in["w_up"],
        "B_w_down": _B_in["w_down"],
    }

def reference(A_x, A_dft_seq_re, A_dft_seq_im, A_dft_hid_re, A_dft_hid_im, B_x, B_w_gate, B_w_up, B_w_down):
    def _A_ref(x, dft_seq_re, dft_seq_im, dft_hid_re, dft_hid_im):
        a = torch.einsum("bsd,ed->bse", x, dft_hid_re)
        b = torch.einsum("bsd,ed->bse", x, dft_hid_im)
        y = torch.einsum("ts,bse->bte", dft_seq_re, a) - torch.einsum("ts,bse->bte", dft_seq_im, b)
        return y

    def _B_ref(x, w_gate, w_up, w_down):
        g = torch.einsum("bsd,df->bsf", x, w_gate)
        u = torch.einsum("bsd,df->bsf", x, w_up)
        h = _sigmoid(g) * u
        return torch.einsum("bsf,fd->bsd", h, w_down)

    x = A_x
    x = x + _A_ref(_layernorm(x, axis=-1), A_dft_seq_re, A_dft_seq_im, A_dft_hid_re, A_dft_hid_im)
    x = x + _B_ref(_layernorm(x, axis=-1), B_w_gate, B_w_up, B_w_down)
    x = x + _A_ref(_layernorm(x, axis=-1), A_dft_seq_re, A_dft_seq_im, A_dft_hid_re, A_dft_hid_im)
    x = x + _B_ref(_layernorm(x, axis=-1), B_w_gate, B_w_up, B_w_down)
    return x
```

```cpp
#include <hip/hip_runtime.h>
#include <hip/hip_bf16.h>
typedef unsigned short u16;
typedef short bf16x8 __attribute__((ext_vector_type(8)));
typedef float f32x4 __attribute__((ext_vector_type(4)));
constexpr int S=2048, D=640, F=1536;
__device__ __forceinline__ float bf2f(u16 v){ return __uint_as_float(((unsigned)v)<<16); }
__device__ __forceinline__ u16 f2bf(float f){ __hip_bfloat16 b = __float2bfloat16(f); return *reinterpret_cast<u16*>(&b); }

__global__ void ln_kernel(const u16* x, u16* xn){
  int row = blockIdx.x*4 + (threadIdx.x>>6); int lane = threadIdx.x&63;
  float v[10]; float s=0;
  for(int i=0;i<10;i++){ v[i]=bf2f(x[row*D+lane+64*i]); s+=v[i]; }
  for(int o=32;o;o>>=1) s+=__shfl_xor(s,o);
  float m=s/D; float q=0;
  for(int i=0;i<10;i++){ float d=v[i]-m; q+=d*d; }
  for(int o=32;o;o>>=1) q+=__shfl_xor(q,o);
  float r=rsqrtf(q/D+1e-5f);
  for(int i=0;i<10;i++) xn[row*D+lane+64*i]=f2bf((v[i]-m)*r);
}
__global__ void gemm_naive(const u16* A, int lda, const u16* B, int sk, int sn, const u16* A2, const u16* B2, int K, int N, u16* C, int ldc, const u16* res, int mode){
  int wave = blockIdx.x*4 + (threadIdx.x>>6); int lane=threadIdx.x&63;
  int tn = wave % (N/16), tm = wave / (N/16);
  int fr=lane&15, fq=lane>>4;
  f32x4 acc={0,0,0,0}, acc2={0,0,0,0};
  for(int k0=0;k0<K;k0+=32){
    bf16x8 a = *(const bf16x8*)(A + (size_t)(tm*16+fr)*lda + k0 + fq*8);
    bf16x8 b;
    for(int j=0;j<8;j++) b[j] = B[(size_t)(k0+fq*8+j)*sk + (size_t)(tn*16+fr)*sn];
    acc = __builtin_amdgcn_mfma_f32_16x16x32_bf16(a,b,acc,0,0,0);
    if(mode>=2){
      bf16x8 a2 = *(const bf16x8*)(A2 + (size_t)(tm*16+fr)*lda + k0 + fq*8);
      bf16x8 b2;
      for(int j=0;j<8;j++) b2[j] = B2[(size_t)(k0+fq*8+j)*sk + (size_t)(tn*16+fr)*sn];
      acc2 = __builtin_amdgcn_mfma_f32_16x16x32_bf16(a2,b2,acc2,0,0,0);
    }
  }
  for(int j=0;j<4;j++){
    int r=tm*16+fq*4+j, c=tn*16+fr;
    float v;
    if(mode==0) v=acc[j];
    else if(mode==1) v=acc[j]+bf2f(res[r*ldc+c]);
    else if(mode==2) v=acc[j]-acc2[j]+bf2f(res[r*ldc+c]);
    else v = acc2[j]/(1.f+__expf(-acc[j]));
    C[r*ldc+c]=f2bf(v);
  }
}
extern "C" void kernel_launch(void* const* d_in, const int* in_sizes, int n_in, void* d_out, int out_size, void* d_ws, size_t ws_size, hipStream_t stream){
  const u16* x0=(const u16*)d_in[0]; const u16* Wsr=(const u16*)d_in[1]; const u16* Wsi=(const u16*)d_in[2];
  const u16* Wdr=(const u16*)d_in[3]; const u16* Wdi=(const u16*)d_in[4];
  const u16* Wg=(const u16*)d_in[6]; const u16* Wu=(const u16*)d_in[7]; const u16* Wd=(const u16*)d_in[8];
  u16* ws=(u16*)d_ws; u16* xn=ws; u16* a=xn+S*D; u16* b=a+S*D; u16* x1=b+S*D; u16* x2=x1+S*D; u16* h=x2+S*D;
  const u16* x=x0;
  for(int l=0;l<2;l++){
    ln_kernel<<<S/4,256,0,stream>>>(x,xn);
    gemm_naive<<<(S/16)*(D/16)/4,256,0,stream>>>(xn,D,Wdr,1,D,nullptr,nullptr,D,D,a,D,nullptr,0);
    gemm_naive<<<(S/16)*(D/16)/4,256,0,stream>>>(xn,D,Wdi,1,D,nullptr,nullptr,D,D,b,D,nullptr,0);
    gemm_naive<<<(S/16)*(D/16)/4,256,0,stream>>>(Wsr,S,a,D,1,Wsi,b,S,D,x1,D,x,2);
    ln_kernel<<<S/4,256,0,stream>>>(x1,xn);
    gemm_naive<<<(S/16)*(F/16)/4,256,0,stream>>>(xn,D,Wg,F,1,xn,Wu,D,F,h,F,nullptr,3);
    u16* o = l==0? x2 : (u16*)d_out;
    gemm_naive<<<(S/16)*(D/16)/4,256,0,stream>>>(h,F,Wd,D,1,nullptr,nullptr,F,D,o,D,x1,1);
    x=x2;
  }
}
```

```cpp
#include <hip/hip_runtime.h>
#include <hip/hip_bf16.h>
typedef unsigned short u16;
typedef short bf16x8 __attribute__((ext_vector_type(8)));
typedef float f32x4 __attribute__((ext_vector_type(4)));
constexpr int S=2048, D=640, F=1536;
__device__ __forceinline__ float bf2f(u16 v){ return __uint_as_float(((unsigned)v)<<16); }
__device__ __forceinline__ u16 f2bf(float f){ __hip_bfloat16 b = __float2bfloat16(f); return *reinterpret_cast<u16*>(&b); }

__global__ void ln_kernel(const u16* x, u16* xn){
  int row = blockIdx.x*4 + (threadIdx.x>>6); int lane = threadIdx.x&63;
  float v[10]; float s=0;
  for(int i=0;i<10;i++){ v[i]=bf2f(x[row*D+lane+64*i]); s+=v[i]; }
  for(int o=32;o;o>>=1) s+=__shfl_xor(s,o);
  float m=s/D; float q=0;
  for(int i=0;i<10;i++){ float d=v[i]-m; q+=d*d; }
  for(int o=32;o;o>>=1) q+=__shfl_xor(q,o);
  float r=rsqrtf(q/D+1e-5f);
  for(int i=0;i<10;i++) xn[row*D+lane+64*i]=f2bf((v[i]-m)*r);
}
__global__ void gemm_naive(const u16* A, int lda, const u16* B, int sk, int sn, const u16* A2, const u16* B2, int K, int N, u16* C, int ldc, const u16* res, int mode){
  int wave = blockIdx.x*4 + (threadIdx.x>>6); int lane=threadIdx.x&63;
  int tn = wave % (N/16), tm = wave / (N/16);
  int fr=lane&15, fq=lane>>4;
  f32x4 acc={0,0,0,0}, acc2={0,0,0,0};
  for(int k0=0;k0<K;k0+=32){
    bf16x8 a = *(const bf16x8*)(A + (size_t)(tm*16+fr)*lda + k0 + fq*8);
    bf16x8 b;
    for(int j=0;j<8;j++) b[j] = B[(size_t)(k0+fq*8+j)*sk + (size_t)(tn*16+fr)*sn];
    acc = __builtin_amdgcn_mfma_f32_16x16x32_bf16(a,b,acc,0,0,0);
    if(mode>=2){
      bf16x8 a2 = *(const bf16x8*)(A2 + (size_t)(tm*16+fr)*lda + k0 + fq*8);
      bf16x8 b2;
      for(int j=0;j<8;j++) b2[j] = B2[(size_t)(k0+fq*8+j)*sk + (size_t)(tn*16+fr)*sn];
      acc2 = __builtin_amdgcn_mfma_f32_16x16x32_bf16(a2,b2,acc2,0,0,0);
    }
  }
  for(int j=0;j<4;j++){
    int r=tm*16+fq*4+j, c=tn*16+fr;
    float v;
    if(mode==0) v=acc[j];
    else if(mode==1) v=acc[j]+bf2f(res[r*ldc+c]);
    else if(mode==2) v=acc[j]-acc2[j]+bf2f(res[r*ldc+c]);
    else v = acc2[j]/(1.f+__expf(-acc[j]));
    C[r*ldc+c]=f2bf(v);
  }
}

#define LAS __attribute__((address_space(3)))
#define GAS __attribute__((address_space(1)))
template<int AUX>
__global__ void __launch_bounds__(256,1) probe_read(const char* p0, const char* p1, int mult){
  __shared__ __attribute__((aligned(16))) char smem[65536];
  const int lane=threadIdx.x&63; const int w=__builtin_amdgcn_readfirstlane(threadIdx.x>>6);
  int b=blockIdx.x;
  const char* base = (b<128? p0 + (size_t)b*65536 : p1 + (size_t)(b-128)*65536) + w*16384 + lane*16;
  unsigned long long t0=__builtin_amdgcn_s_memrealtime();
  #pragma unroll
  for(int i=0;i<16;i++) __builtin_amdgcn_global_load_lds((const GAS void*)(base+i*1024),(LAS void*)(smem+w*16384+i*1024),16,0,AUX);
  asm volatile("s_waitcnt vmcnt(0)":::"memory");
  __syncthreads();
  unsigned long long t1=__builtin_amdgcn_s_memrealtime();
  unsigned long long d=t1-t0; if(d>2000) d=2000;
  while(__builtin_amdgcn_s_memrealtime()-t0 < d*mult) __builtin_amdgcn_s_sleep(8);
}
__global__ void __launch_bounds__(256,1) probe_write(uint4* p, int mult){
  int b=blockIdx.x;
  unsigned long long t0=__builtin_amdgcn_s_memrealtime();
  uint4 v={1,2,3,(unsigned)b};
  for(int i=0;i<5;i++) p[(size_t)b*1280+i*256+threadIdx.x]=v;
  asm volatile("s_waitcnt vmcnt(0)":::"memory");
  __syncthreads();
  unsigned long long t1=__builtin_amdgcn_s_memrealtime();
  unsigned long long d=t1-t0; if(d>2000) d=2000;
  while(__builtin_amdgcn_s_memrealtime()-t0 < d*mult) __builtin_amdgcn_s_sleep(8);
}
__global__ void __launch_bounds__(256,1) probe_A(unsigned long long* st, uint4* p, int nwrite){
  int b=blockIdx.x;
  uint4 v={1,2,3,(unsigned)b};
  unsigned long long t0=__builtin_amdgcn_s_memrealtime();
  while(__builtin_amdgcn_s_memrealtime()-t0 < 300) __builtin_amdgcn_s_sleep(1);
  unsigned long long t1=__builtin_amdgcn_s_memrealtime();
  for(int i=0;i<nwrite;i++) p[(size_t)b*1280+i*256+threadIdx.x]=v;
  if(threadIdx.x==0) st[b]=t1;
}
__global__ void __launch_bounds__(256,1) probe_B(const unsigned long long* st, int mult){
  unsigned long long t0=__builtin_amdgcn_s_memrealtime();
  __shared__ unsigned long long red[256];
  red[threadIdx.x]=st[threadIdx.x];
  __syncthreads();
  unsigned long long t2=__builtin_amdgcn_s_memrealtime();
  unsigned long long m=0; for(int i=0;i<256;i++) m = red[i]>m? red[i]:m;
  unsigned long long d=(mult>0? t0 : t2)-m; if(d>2000) d=2000;
  int mm = mult>0? mult : -mult;
  while(__builtin_amdgcn_s_memrealtime()-t0 < d*mm) __builtin_amdgcn_s_sleep(8);
}
extern "C" void kernel_launch(void* const* d_in, const int* in_sizes, int n_in, void* d_out, int out_size, void* d_ws, size_t ws_size, hipStream_t stream){
  const u16* x0=(const u16*)d_in[0]; const u16* Wsr=(const u16*)d_in[1]; const u16* Wsi=(const u16*)d_in[2];
  const u16* Wdr=(const u16*)d_in[3]; const u16* Wdi=(const u16*)d_in[4];
  const u16* Wg=(const u16*)d_in[6]; const u16* Wu=(const u16*)d_in[7]; const u16* Wd=(const u16*)d_in[8];
  u16* ws=(u16*)d_ws; u16* xn=ws; u16* a=xn+S*D; u16* b=a+S*D; u16* x1=b+S*D; u16* x2=x1+S*D; u16* h=x2+S*D;
  const u16* x=x0;
  probe_write<<<256,256,0,stream>>>((uint4*)((char*)d_ws+(64<<20)),100);
  for(int l=0;l<2;l++){
    ln_kernel<<<S/4,256,0,stream>>>(x,xn);
    gemm_naive<<<(S/16)*(D/16)/4,256,0,stream>>>(xn,D,Wdr,1,D,nullptr,nullptr,D,D,a,D,nullptr,0);
    gemm_naive<<<(S/16)*(D/16)/4,256,0,stream>>>(xn,D,Wdi,1,D,nullptr,nullptr,D,D,b,D,nullptr,0);
    gemm_naive<<<(S/16)*(D/16)/4,256,0,stream>>>(Wsr,S,a,D,1,Wsi,b,S,D,x1,D,x,2);
    ln_kernel<<<S/4,256,0,stream>>>(x1,xn);
    gemm_naive<<<(S/16)*(F/16)/4,256,0,stream>>>(xn,D,Wg,F,1,xn,Wu,D,F,h,F,nullptr,3);
    u16* o = l==0? x2 : (u16*)d_out;
    gemm_naive<<<(S/16)*(D/16)/4,256,0,stream>>>(h,F,Wd,D,1,nullptr,nullptr,F,D,o,D,x1,1);
    x=x2;
  }
}
```

```cpp
#include <hip/hip_runtime.h>

typedef unsigned long long u64;
typedef short bf16x8 __attribute__((ext_vector_type(8)));
typedef short s16x4 __attribute__((ext_vector_type(4)));
typedef float f32x4 __attribute__((ext_vector_type(4)));
typedef float f32x2 __attribute__((ext_vector_type(2)));
typedef unsigned u32x4 __attribute__((ext_vector_type(4)));
typedef unsigned u32x2 __attribute__((ext_vector_type(2)));
typedef __bf16 bf2 __attribute__((ext_vector_type(2)));
#define LAS __attribute__((address_space(3)))
#define GAS __attribute__((address_space(1)))
#define DI __device__ __forceinline__
typedef __amdgpu_buffer_rsrc_t rsrc_t;

#define STAMPS 0

#define PFMODE 0
#define PFDEPTH 48
#define ALPRIO 3
#define WLSLEEP 0
#define WIN1 6
#define WIN4 12
namespace {
constexpr unsigned OFF_MB = 4096, MB_EV = 256 * 128;
constexpr int EV_X = 0, EV_AB = 1, EV_EX = 2, EV_X1 = 3, EV_H = 4, EV_PER_LAYER = 5, EV_RT = 10;
constexpr unsigned OFF_CS = 0x60000;
constexpr unsigned OFF_ST = 0x80000, ST_BUF = 8 * 2048 * 8;
constexpr unsigned OFF_XT = 0x100000, XT_BUF = 2048 * 640 * 2;
constexpr unsigned OFF_AB = OFF_XT + 4 * XT_BUF, AB_BUF = 2048 * 1280 * 2;
constexpr unsigned OFF_HT = OFF_AB + 2 * AB_BUF, HT_BUF = 2048 * 1536 * 2;
constexpr unsigned OFF_EX = OFF_HT + 2 * HT_BUF, EX_BUF = 256 * 16 * 3072;
constexpr unsigned OFF_W3 = OFF_EX + 2 * EX_BUF;
constexpr unsigned OFF_W4 = OFF_W3 + 16 * 20 * 12288;
constexpr unsigned WS_END = OFF_W4 + 8 * 48 * 5120;
constexpr unsigned OFF_STAMP = 0x6000000;
static_assert(OFF_MB + 11 * MB_EV <= OFF_CS && OFF_CS + 10 * 3072 * 4 <= OFF_ST && OFF_ST + 4 * ST_BUF <= OFF_XT && WS_END <= OFF_STAMP, "ws map");
constexpr unsigned L_CNT = 0, L_MEAN = 256, L_RSTD = 768, L_CS1 = 1280, L_CS3 = 2560;
constexpr unsigned L_W = 4096, L_ACT = L_W + 98304, L_RT = L_W + 102400;
constexpr int SC1 = 16;
constexpr unsigned SPIN_CAP = 1u << 16;

struct Params { const char* x0; const char* wsr; const char* wsi; const char* wdr; const char* wdi; const char* wg; const char* wu; const char* wdn; char* out; char* ws; };

DI rsrc_t mk(const void* p, unsigned bytes) { return __builtin_amdgcn_make_buffer_rsrc((void*)p, 0, bytes, 0x00020000); }
template <int N> DI void waitv() { asm volatile("s_waitcnt vmcnt(%0)" ::"n"(N) : "memory"); }
DI void bar() { asm volatile("s_waitcnt lgkmcnt(0)\n\ts_barrier" ::: "memory"); }
DI void sched() { __builtin_amdgcn_sched_barrier(0); }
template <class T> DI void pin(T& x) { asm volatile("" : "+v"(x)); }
DI unsigned pins(unsigned x) { asm volatile("" : "+s"(x)); return x; }
DI void bar_st() { if (PFMODE == 1) asm volatile("s_barrier" ::: "memory"); }
template <int AUX, int IMM> DI void dma(rsrc_t r, LAS char* l, unsigned vo, unsigned so) { __builtin_amdgcn_raw_ptr_buffer_load_lds(r, (LAS void*)l, 16, vo, so, IMM, AUX); }
template <int AUX, int N> DI void dma_lin(rsrc_t r, LAS char* l, unsigned vo, unsigned so) {
#pragma unroll
  for (int j = 0; j < N; j += 4) {
    dma<AUX, 0>(r, l + j * 1024, vo, so + j * 1024);
    if (j + 1 < N) dma<AUX, 1024>(r, l + j * 1024, vo, so + j * 1024);
    if (j + 2 < N) dma<AUX, 2048>(r, l + j * 1024, vo, so + j * 1024);
    if (j + 3 < N) dma<AUX, 3072>(r, l + j * 1024, vo, so + j * 1024);
  }
}
DI bf16x8 ld16(rsrc_t r, unsigned vo, unsigned so) { return __builtin_bit_cast(bf16x8, __builtin_amdgcn_raw_buffer_load_b128(r, vo, so, SC1)); }
DI void st16(rsrc_t r, u32x4 v, unsigned vo, unsigned so) { __builtin_amdgcn_raw_buffer_store_b128(v, r, vo + so, 0, SC1); }
DI bf16x8 lds16(LAS char* l, unsigned a) { return *(LAS bf16x8*)(l + a); }
DI unsigned pk(float lo, float hi) { f32x2 v = {lo, hi}; return __builtin_bit_cast(unsigned, __builtin_convertvector(v, bf2)); }
DI float blo(unsigned u) { return __uint_as_float(u << 16); }
DI float bhi(unsigned u) { return __uint_as_float(u & 0xffff0000u); }
DI f32x4 mfma(bf16x8 a, bf16x8 b, f32x4 c) { return __builtin_amdgcn_mfma_f32_16x16x32_bf16(a, b, c, 0, 0, 0); }
DI float dot_ones(bf16x8 f, float s) {
  const u32x4 u = __builtin_bit_cast(u32x4, f); const bf2 one = __builtin_bit_cast(bf2, 0x3f803f80u);
#pragma unroll
  for (int j = 0; j < 4; ++j) { const unsigned e = u[j]; s = __builtin_amdgcn_fdot2_f32_bf16(__builtin_bit_cast(bf2, e), one, s, false); }
  return s;
}
DI float sum_q(float s) { s += __shfl_xor(s, 16); s += __shfl_xor(s, 32); return s; }
DI u32x4 swap_pair(unsigned a0, unsigned a1, unsigned b0, unsigned b1) {
  const auto r0 = __builtin_amdgcn_permlane16_swap(a0, b0, false, false);
  const auto r1 = __builtin_amdgcn_permlane16_swap(a1, b1, false, false);
  return (u32x4){r0[0], r1[0], r0[1], r1[1]};
}

struct Ctx {
  char* ws; LAS char* lds; u64 tag; int lane, b;
  int kq, P, np, me;
  int P1, mat, ct1, me1;
};
#define STAMP_DECL
#define stamp(k, w, id) do { (void)(id); } while (0)
#define STAMP_OUT(k, w)
DI void wait_mb(const Ctx& k, int ev, int wg, int n) {
  GAS u64* p = (GAS u64*)(k.ws + OFF_MB + ev * MB_EV + wg * 128) + (k.lane & (n - 1));
#pragma unroll 1
  for (unsigned spins = 0; spins < SPIN_CAP; ++spins) {
    const u64 v = __hip_atomic_load(p, __ATOMIC_RELAXED, __HIP_MEMORY_SCOPE_AGENT);
    if (__all(v == k.tag)) break;
    __builtin_amdgcn_s_sleep(1);
  }
  asm volatile("" ::: "memory");
}
DI void push_mb(const Ctx& k, int ev, int wg0, int wgstep, int n, int slot) {
  if (k.lane < n) __hip_atomic_store((GAS u64*)(k.ws + OFF_MB + ev * MB_EV + (wg0 + k.lane * wgstep) * 128) + slot, k.tag, __ATOMIC_RELAXED, __HIP_MEMORY_SCOPE_AGENT);
}
DI void publish(const Ctx& k, int ev, int wg0, int wgstep, int n, int slot) {
  GAS u64* dst = (GAS u64*)(k.ws + OFF_MB + ev * MB_EV + (wg0 + (k.lane & (n - 1)) * wgstep) * 128) + slot;
  unsigned old, cnt = (unsigned)(size_t)(k.lds + L_CNT), one = 1u;
  pin(dst);
  asm volatile("s_waitcnt vmcnt(0) lgkmcnt(0)\n\ts_mov_b64 exec, 1\n\tds_add_rtn_u32 %0, %1, %2\n\ts_mov_b64 exec, -1\n\ts_waitcnt lgkmcnt(0)" : "=&v"(old) : "v"(cnt), "v"(one) : "memory");
  old = __builtin_amdgcn_readfirstlane(old);
  if ((old & 3u) == 3u && k.lane < n) __hip_atomic_store(dst, k.tag, __ATOMIC_RELAXED, __HIP_MEMORY_SCOPE_AGENT);
  asm volatile("" ::: "memory");
}

DI void x_epilogue(const Ctx& k, const Params& p, int w, const f32x4 (&v)[5], unsigned (&res)[10], int xb, bool final_out) {
  const int q = k.lane >> 4, c = k.lane & 15, tfo = 8 * k.P + 4 * (k.kq >> 1) + w, cto = 2 * k.np + (k.kq & 1);
#pragma unroll
  for (int i = 0; i < 5; ++i) { res[2 * i] = pk(v[i][0], v[i][1]); res[2 * i + 1] = pk(v[i][2], v[i][3]); }
  const u32x4 o0 = swap_pair(res[0], res[1], res[2], res[3]), o1 = swap_pair(res[4], res[5], res[6], res[7]), o2 = swap_pair(res[8], res[9], res[8], res[9]);
  const int g = 10 * cto + 2 * (q & 1) + (q >> 1);
  if (final_out) {
    const rsrc_t ro = mk(p.out, 2048 * 640 * 2);
    const unsigned vo = (16 * tfo + c) * 1280 + g * 16;
    __builtin_amdgcn_raw_buffer_store_b128(o0, ro, vo, 0, 0);
    __builtin_amdgcn_raw_buffer_store_b128(o1, ro, vo + 64, 0, 0);
    if (!(q & 1)) __builtin_amdgcn_raw_buffer_store_b128(o2, ro, vo + 128, 0, 0);
  } else {
    const rsrc_t rw = mk(k.ws, WS_END);
    const unsigned vo = g * 256 + c * 16, so = OFF_XT + xb * XT_BUF + tfo * 20480;
    st16(rw, o0, vo, so); st16(rw, o1, vo + 1024, so);
    if (!(q & 1)) st16(rw, o2, vo + 2048, so);
    float s = 0.f, ss = 0.f;
#pragma unroll
    for (int j = 0; j < 10; ++j) { const float a = blo(res[j]), bb = bhi(res[j]); s += a + bb; ss = fmaf(a, a, fmaf(bb, bb, ss)); }
    s = sum_q(s); ss = sum_q(ss);
    if (q == 0) __builtin_amdgcn_raw_buffer_store_b64((u32x2){__float_as_uint(s), __float_as_uint(ss)}, rw, c * 8, OFF_ST + xb * ST_BUF + cto * 16384 + tfo * 128, SC1);
  }
}

#define PHASE_LANE() Ctx k = k0; asm volatile("" : "+v"(k.lane), "+s"(k.P), "+s"(k.kq), "+s"(k.np), "+s"(k.P1), "+s"(k.ct1), "+s"(k.mat)); \
  k.me = k.P * 16 + k.kq * 4 + k.np; k.me1 = k.P1 * 16 + k.mat * 8 + k.ct1; \
  const int tfo = 8 * k.P + 4 * (k.kq >> 1) + w, cto = 2 * k.np + (k.kq & 1); (void)tfo; (void)cto; \
  const int lane = k.lane, q = lane >> 4, c = lane & 15; (void)q; (void)c; \
  const unsigned rowrd = (c >> 3) * 1024 + (c & 7) * 128 + ((q ^ (c >> 1)) * 16); (void)rowrd
DI void consumer(const Ctx& k0, const Params& p, const int w) {
  const Ctx& k = k0;
  LAS char* const L = k.lds;
  const rsrc_t rw = mk(k.ws, WS_END);
  unsigned res[10];
  STAMP_DECL;
  stamp(k, w, 0);
  {
    PHASE_LANE();
    const rsrc_t rx = mk(p.x0, 2048 * 640 * 2);
    u32x2 xi[5];
#pragma unroll
    for (int i = 0; i < 5; ++i) xi[i] = __builtin_amdgcn_raw_buffer_load_b64(rx, (16 * tfo + c) * 1280 + (80 * cto + 16 * i + 4 * q) * 2, 0, 0);
    const int u = k.b + 256 * w, m = u / 240, r = u - 240 * m;
    const bool rt = u < 720;
    const int ku = m < 2 ? r / 24 : r / 10, stp = m < 2 ? r - 24 * ku : r - 10 * ku;
    if (rt) {
      const unsigned stride = m < 2 ? 3072 : 1280;
      const rsrc_t rs = mk(m == 0 ? p.wg : (m == 1 ? p.wu : p.wdn), 640 * 1536 * 2);
#pragma unroll
      for (int j = 0; j < 8; ++j) dma<0, 0>(rs, L + L_RT + w * 8192 + j * 1024, (lane >> 3) * stride + (lane & 7) * 16, (64 * ku + 8 * j) * stride + 128 * stp);
    }
    f32x4 v[5];
#pragma unroll
    for (int i = 0; i < 5; ++i) v[i] = (f32x4){blo(xi[i][0]), bhi(xi[i][0]), blo(xi[i][1]), bhi(xi[i][1])};
    x_epilogue(k, p, w, v, res, 0, false);
    publish(k, EV_X, k.P * 16, 1, 16, k.kq * 4 + k.np);
    stamp(k, w, 1);
    if (rt) {
      float cs[4] = {0.f, 0.f, 0.f, 0.f};
#pragma unroll
      for (int kk = 0; kk < 2; ++kk)
#pragma unroll
        for (int n = 0; n < 4; ++n) {
          LAS char* a = L + L_RT + w * 8192 + (4 * kk + q) * 1024 + (c >> 2) * 128 + 32 * n + (c & 3) * 8;
          const u32x2 lo = __builtin_bit_cast(u32x2, __builtin_amdgcn_ds_read_tr16_b64_v4i16((LAS s16x4*)a));
          const u32x2 hi = __builtin_bit_cast(u32x2, __builtin_amdgcn_ds_read_tr16_b64_v4i16((LAS s16x4*)(a + 512)));
          const u32x4 f = {lo[0], lo[1], hi[0], hi[1]};
          const int nf = 4 * stp + n;
          unsigned so;
          if (m < 2) { const int n3 = nf / 6; so = OFF_W3 + (n3 * 20 + 2 * ku + kk) * 12288 + (2 * (nf - 6 * n3) + m) * 1024; }
          else { const int ct = nf / 5; so = OFF_W4 + (ct * 48 + 2 * ku + kk) * 5120 + (nf - 5 * ct) * 1024; }
          st16(rw, f, lane * 16, so);
          cs[n] = dot_ones(__builtin_bit_cast(bf16x8, f), cs[n]);
        }
      if (m < 2) {
#pragma unroll
        for (int n = 0; n < 4; ++n) cs[n] = sum_q(cs[n]);
        const float o = q == 0 ? cs[0] : (q == 1 ? cs[1] : (q == 2 ? cs[2] : cs[3]));
        __builtin_amdgcn_raw_buffer_store_b32(__float_as_uint(o), rw, lane * 4, OFF_CS + (ku * 3072 + m * 1536 + 64 * stp) * 4, SC1);
      }
    }
    stamp(k, w, 2);
  }
#pragma unroll 1
  for (int layer = 0; layer < 2; ++layer) {
    const int ev0 = layer * EV_PER_LAYER, sb = 3 + layer * 24;
    {
      PHASE_LANE();
      constexpr int WIN = WIN1;
      unsigned wb[2][2] = {{L_W + rowrd, L_W + rowrd + 51200}, {L_W + (rowrd ^ 64), L_W + (rowrd ^ 64) + 51200}};
      pin(wb[0][0]); pin(wb[0][1]); pin(wb[1][0]); pin(wb[1][1]);
      bar(); bar();
      stamp(k, w, sb + 0);
      const unsigned xso = OFF_XT + 2 * layer * XT_BUF + (8 * k.P1 + 2 * w) * 20480;
      bf16x8 xa[WIN][2], wf[2][5];
      f32x4 acc[2][5];
#pragma unroll
      for (int s = 0; s < WIN; ++s) { xa[s][0] = ld16(rw, lane * 16 + s * 1024, xso); xa[s][1] = ld16(rw, lane * 16 + s * 1024, xso + 20480); sched(); }
#pragma unroll
      for (int i = 0; i < 5; ++i) { acc[0][i] = (f32x4){0.f, 0.f, 0.f, 0.f}; acc[1][i] = (f32x4){0.f, 0.f, 0.f, 0.f}; }
#pragma unroll
      for (int i = 0; i < 5; ++i) wf[0][i] = lds16(L, wb[0][0] + i * 2048);
      sched();
#pragma unroll
      for (int s = 0; s < 20; ++s) {
        if (s + 1 < 20) {
#pragma unroll
          for (int i = 0; i < 5; ++i) wf[(s + 1) & 1][i] = lds16(L, wb[(s + 1) & 1][(s + 1) / 10] + (((s + 1) >> 1) % 5) * 10240 + i * 2048);
        } else bar();
#pragma unroll
        for (int i = 0; i < 5; ++i) { acc[0][i] = mfma(xa[s % WIN][0], wf[s & 1][i], acc[0][i]); acc[1][i] = mfma(xa[s % WIN][1], wf[s & 1][i], acc[1][i]); }
        if (s + WIN < 20) { xa[s % WIN][0] = ld16(rw, lane * 16, xso + (s + WIN) * 1024); xa[s % WIN][1] = ld16(rw, lane * 16, xso + 20480 + (s + WIN) * 1024); }
        if (s + 1 < 20) {
#pragma unroll
          for (int j = 0; j < 5; ++j) { __builtin_amdgcn_sched_group_barrier(0x100, 1, 0); __builtin_amdgcn_sched_group_barrier(0x8, 1, 0); }
          __builtin_amdgcn_sched_group_barrier(0x8, 5, 0);
        }
        sched();
      }
      stamp(k, w, sb + 1);
      f32x4 mean[2], rstd[2]; float cs[5];
#pragma unroll
      for (int t = 0; t < 2; ++t) { mean[t] = *(LAS f32x4*)(L + L_MEAN + (32 * w + 16 * t + 4 * q) * 4); rstd[t] = *(LAS f32x4*)(L + L_RSTD + (32 * w + 16 * t + 4 * q) * 4); }
#pragma unroll
      for (int i = 0; i < 5; ++i) { const f32x4 t = *(LAS f32x4*)(L + L_CS1 + (16 * i + c) * 16); cs[i] = (t[0] + t[1]) + (t[2] + t[3]); }
      const unsigned so = OFF_AB + layer * AB_BUF + ((k.mat * 4 + (k.ct1 >> 1)) * 64 + 4 * k.P1 + w) * 10240 + 5 * (k.ct1 & 1) * 1024;
      const unsigned vo = ((2 * (q & 1) + (q >> 1)) * 16 + c) * 16;
#pragma unroll
      for (int i = 0; i < 5; ++i) {
        const f32x4 a = (acc[0][i] - mean[0] * cs[i]) * rstd[0], bb = (acc[1][i] - mean[1] * cs[i]) * rstd[1];
        st16(rw, swap_pair(pk(a[0], a[1]), pk(a[2], a[3]), pk(bb[0], bb[1]), pk(bb[2], bb[3])), vo + i * 1024, so);
      }
      bar_st();
      stamp(k, w, sb + 2);
      publish(k, ev0 + EV_AB, (2 * k.mat + (k.P1 >> 3)) * 4 + (k.ct1 >> 1), 16, 16, 2 * (k.P1 & 7) + (k.ct1 & 1));
      stamp(k, w, sb + 3);
    }
    {
      PHASE_LANE();
      const int wm = w >> 1, wn = w & 1;
      f32x4 acc[4][5];
      bf16x8 fa[2][5], fb[2][4];
#pragma unroll
      for (int t = 0; t < 4; ++t)
#pragma unroll
        for (int i = 0; i < 5; ++i) acc[t][i] = (f32x4){0.f, 0.f, 0.f, 0.f};
      unsigned wb = L_W + wm * 8192 + rowrd, ab = L_ACT + wn * 5120 + lane * 16;
      unsigned ws_slot = 32768, ab_slot = 0;
#pragma unroll
      for (int t = 0; t < 4; ++t)
#pragma unroll
        for (int i = 0; i < 5; ++i) pin(acc[t][i]);
      pin(wb); pin(ab);
      bar(); bar();
      bar();
      stamp(k, w, sb + 4);
#pragma unroll
      for (int i = 0; i < 5; ++i) fa[0][i] = lds16(L, ab + i * 1024);
#pragma unroll
      for (int t = 0; t < 4; ++t) fb[0][t] = lds16(L, wb + 32768 + t * 2048);
#pragma unroll 1
      for (int kt = 0; kt < 16; ++kt) {
#pragma unroll
        for (int i = 0; i < 5; ++i) fa[1][i] = lds16(L, ab + ab_slot + 10240 + i * 1024);
#pragma unroll
        for (int t = 0; t < 4; ++t) fb[1][t] = lds16(L, (wb ^ 64) + ws_slot + t * 2048);
#pragma unroll
        for (int t = 0; t < 4; ++t)
#pragma unroll
          for (int i = 0; i < 5; ++i) acc[t][i] = mfma(fa[0][i], fb[0][t], acc[t][i]);
#pragma unroll
        for (int j = 0; j < 9; ++j) { __builtin_amdgcn_sched_group_barrier(0x8, 1, 0); __builtin_amdgcn_sched_group_barrier(0x100, 1, 0); }
        __builtin_amdgcn_sched_group_barrier(0x8, 11, 0);
        sched();
        ab_slot = ab_slot == 40960 ? 0 : ab_slot + 20480; ws_slot = ws_slot == 81920 ? 0 : ws_slot + 16384;
        bar();
        sched();
#pragma unroll
        for (int i = 0; i < 5; ++i) fa[0][i] = lds16(L, ab + ab_slot + i * 1024);
#pragma unroll
        for (int t = 0; t < 4; ++t) fb[0][t] = lds16(L, wb + ws_slot + t * 2048);
#pragma unroll
        for (int t = 0; t < 4; ++t)
#pragma unroll
          for (int i = 0; i < 5; ++i) acc[t][i] = mfma(fa[1][i], fb[1][t], acc[t][i]);
#pragma unroll
        for (int j = 0; j < 9; ++j) { __builtin_amdgcn_sched_group_barrier(0x8, 1, 1); __builtin_amdgcn_sched_group_barrier(0x100, 1, 1); }
        __builtin_amdgcn_sched_group_barrier(0x8, 11, 1);
        sched();
      }
      stamp(k, w, sb + 5);
      if (w == k.kq) {
#pragma unroll
        for (int t = 0; t < 4; ++t)
#pragma unroll
          for (int i = 0; i < 5; ++i) *(LAS f32x4*)(L + L_ACT + (t * 5 + i) * 1024 + lane * 16) = acc[t][i];
      } else {
        const unsigned so = OFF_EX + layer * EX_BUF + ((k.P * 16 + w * 4 + k.np) * 4 + k.kq) * 12288;
#pragma unroll
        for (int t = 0; t < 4; ++t) {
          st16(rw, (u32x4){pk(acc[t][0][0], acc[t][0][1]), pk(acc[t][0][2], acc[t][0][3]), pk(acc[t][1][0], acc[t][1][1]), pk(acc[t][1][2], acc[t][1][3])}, lane * 16, so + t * 3072);
          st16(rw, (u32x4){pk(acc[t][2][0], acc[t][2][1]), pk(acc[t][2][2], acc[t][2][3]), pk(acc[t][3][0], acc[t][3][1]), pk(acc[t][3][2], acc[t][3][3])}, lane * 16 + 1024, so + t * 3072);
          st16(rw, __builtin_bit_cast(u32x4, acc[t][4]), lane * 16 + 2048, so + t * 3072);
        }
      }
      bar_st();
      stamp(k, w, sb + 6);
      publish(k, ev0 + EV_EX, k.P * 16 + k.np, 4, 4, k.kq);
      stamp(k, w, sb + 7);
      bar(); bar();
      stamp(k, w, sb + 8);
      u32x4 e[3][3];
#pragma unroll
      for (int n = 0; n < 3; ++n) {
        const unsigned so = OFF_EX + layer * EX_BUF + (k.me * 4 + ((k.kq + 1 + n) & 3)) * 12288 + w * 3072;
#pragma unroll
        for (int j = 0; j < 3; ++j) e[n][j] = __builtin_amdgcn_raw_buffer_load_b128(rw, lane * 16 + j * 1024, so, SC1);
      }
      f32x4 v[5];
#pragma unroll
      for (int i = 0; i < 5; ++i) v[i] = *(LAS f32x4*)(L + L_ACT + (w * 5 + i) * 1024 + lane * 16) + (f32x4){blo(res[2 * i]), bhi(res[2 * i]), blo(res[2 * i + 1]), bhi(res[2 * i + 1])};
#pragma unroll
      for (int n = 0; n < 3; ++n) {
#pragma unroll
        for (int j = 0; j < 2; ++j) {
          v[2 * j] += (f32x4){blo(e[n][j][0]), bhi(e[n][j][0]), blo(e[n][j][1]), bhi(e[n][j][1])};
          v[2 * j + 1] += (f32x4){blo(e[n][j][2]), bhi(e[n][j][2]), blo(e[n][j][3]), bhi(e[n][j][3])};
        }
        v[4] += __builtin_bit_cast(f32x4, e[n][2]);
      }
      x_epilogue(k, p, w, v, res, 2 * layer + 1, false);
      stamp(k, w, sb + 9);
      publish(k, ev0 + EV_X1, k.P * 16, 1, 16, k.kq * 4 + k.np);
      stamp(k, w, sb + 10);
    }
    {
      PHASE_LANE();
      const int wm = w >> 1, wn = w & 1;
      f32x4 acc[4][6];
      bf16x8 fa[2][6], fb[2][4];
#pragma unroll
      for (int t = 0; t < 4; ++t)
#pragma unroll
        for (int i = 0; i < 6; ++i) acc[t][i] = (f32x4){0.f, 0.f, 0.f, 0.f};
      unsigned wb = L_W + wn * 6144 + lane * 16, xb = L_ACT + wm * 8192 + lane * 16;
      unsigned w_slot = 49152, x_slot = 0;
#pragma unroll
      for (int t = 0; t < 4; ++t)
#pragma unroll
        for (int i = 0; i < 6; ++i) pin(acc[t][i]);
      pin(wb); pin(xb);
      bar(); bar();
      bar();
      stamp(k, w, sb + 11);
#pragma unroll
      for (int i = 0; i < 6; ++i) fa[0][i] = lds16(L, wb + 49152 + i * 1024);
#pragma unroll
      for (int t = 0; t < 4; ++t) fb[0][t] = lds16(L, xb + t * 2048);
#pragma unroll 1
      for (int kt = 0; kt < 10; ++kt) {
#pragma unroll
        for (int i = 0; i < 6; ++i) fa[1][i] = lds16(L, wb + w_slot + 12288 + i * 1024);
#pragma unroll
        for (int t = 0; t < 4; ++t) fb[1][t] = lds16(L, xb + x_slot + 1024 + t * 2048);
#pragma unroll
        for (int t = 0; t < 4; ++t)
#pragma unroll
          for (int i = 0; i < 6; ++i) acc[t][i] = mfma(fa[0][i], fb[0][t], acc[t][i]);
#pragma unroll
        for (int j = 0; j < 10; ++j) { __builtin_amdgcn_sched_group_barrier(0x8, 1, 0); __builtin_amdgcn_sched_group_barrier(0x100, 1, 0); }
        __builtin_amdgcn_sched_group_barrier(0x8, 14, 0);
        sched();
        x_slot = x_slot == 32768 ? 0 : x_slot + 16384; w_slot = w_slot == 73728 ? 0 : w_slot + 24576;
        bar();
        sched();
#pragma unroll
        for (int i = 0; i < 6; ++i) fa[0][i] = lds16(L, wb + w_slot + i * 1024);
#pragma unroll
        for (int t = 0; t < 4; ++t) fb[0][t] = lds16(L, xb + x_slot + t * 2048);
#pragma unroll
        for (int t = 0; t < 4; ++t)
#pragma unroll
          for (int i = 0; i < 6; ++i) acc[t][i] = mfma(fa[1][i], fb[1][t], acc[t][i]);
#pragma unroll
        for (int j = 0; j < 10; ++j) { __builtin_amdgcn_sched_group_barrier(0x8, 1, 1); __builtin_amdgcn_sched_group_barrier(0x100, 1, 1); }
        __builtin_amdgcn_sched_group_barrier(0x8, 14, 1);
        sched();
      }
      stamp(k, w, sb + 12);
      float mean[4], rstd[4];
#pragma unroll
      for (int t = 0; t < 4; ++t) { mean[t] = *(LAS float*)(L + L_MEAN + (64 * wm + 16 * t + c) * 4); rstd[t] = *(LAS float*)(L + L_RSTD + (64 * wm + 16 * t + c) * 4); }
      const int n3 = 4 * k.kq + k.np;
      const unsigned so = OFF_HT + layer * HT_BUF + ((8 * k.P + 4 * wm) * 48 + 3 * n3) * 1024;
#pragma unroll
      for (int f = 0; f < 3; ++f) {
        const f32x4 cg = *(LAS f32x4*)(L + L_CS3 + (16 * (3 * wn + f) + 4 * q) * 4), cu = *(LAS f32x4*)(L + L_CS3 + (96 + 16 * (3 * wn + f) + 4 * q) * 4);
        unsigned h[4][2];
#pragma unroll
        for (int t = 0; t < 4; ++t) {
          const f32x4 g = (acc[t][2 * f] - mean[t] * cg) * (rstd[t] * -1.44269504089f), u = (acc[t][2 * f + 1] - mean[t] * cu) * rstd[t];
          f32x4 o;
#pragma unroll
          for (int r = 0; r < 4; ++r) o[r] = __builtin_amdgcn_exp2f(g[r]);
          o += 1.f;
#pragma unroll
          for (int r = 0; r < 4; ++r) o[r] = __builtin_amdgcn_rcpf(o[r]);
          o *= u;
          h[t][0] = pk(o[0], o[1]); h[t][1] = pk(o[2], o[3]);
        }
        const unsigned vo = (q & 1) * 49152 + (2 * (3 * wn + f) + (q >> 1)) * 256 + c * 16;
        st16(rw, swap_pair(h[0][0], h[0][1], h[1][0], h[1][1]), vo, so);
        st16(rw, swap_pair(h[2][0], h[2][1], h[3][0], h[3][1]), vo, so + 98304);
      }
      bar_st();
      stamp(k, w, sb + 13);
      publish(k, ev0 + EV_H, k.P * 16, 1, 16, n3);
      stamp(k, w, sb + 14);
    }
    {
      PHASE_LANE();
      constexpr int WIN = WIN4;
      unsigned wb[3] = {L_W + lane * 16, L_W + lane * 16 + 51200, L_W + lane * 16 + 102400};
      pin(wb[0]); pin(wb[1]); pin(wb[2]);
      bar(); bar();
      stamp(k, w, sb + 15);
      const unsigned hso = OFF_HT + layer * HT_BUF + tfo * 49152;
      bf16x8 ha[WIN], wf[2][5];
      f32x4 acc[5];
#pragma unroll
      for (int s = 0; s < WIN; ++s) ha[s] = ld16(rw, lane * 16, hso + s * 1024);
#pragma unroll
      for (int i = 0; i < 5; ++i) acc[i] = (f32x4){blo(res[2 * i]), bhi(res[2 * i]), blo(res[2 * i + 1]), bhi(res[2 * i + 1])};
#pragma unroll
      for (int i = 0; i < 5; ++i) wf[0][i] = lds16(L, wb[0] + i * 1024);
      sched();
#pragma unroll
      for (int s = 0; s < 48; ++s) {
        if ((s + 1) % 6 == 0 && s + 1 != 24) bar();
        if (s + 1 < 48) {
#pragma unroll
          for (int i = 0; i < 5; ++i) wf[(s + 1) & 1][i] = lds16(L, wb[((s + 1) % 30) / 10] + ((s + 1) % 10) * 5120 + i * 1024);
        }
#pragma unroll
        for (int i = 0; i < 5; ++i) acc[i] = mfma(wf[s & 1][i], ha[s % WIN], acc[i]);
        if (s + WIN < 48) ha[s % WIN] = ld16(rw, lane * 16, hso + (s + WIN) * 1024);
        if (s + 1 < 48) { __builtin_amdgcn_sched_group_barrier(0x100, 5, 0); __builtin_amdgcn_sched_group_barrier(0x8, 5, 0); }
        sched();
      }
      stamp(k, w, sb + 16);
      x_epilogue(k, p, w, acc, res, 2, layer == 1);
      bar_st();
      stamp(k, w, sb + 17);
      if (layer == 0) publish(k, EV_PER_LAYER + EV_X, k.P * 16, 1, 16, k.kq * 4 + k.np);
      stamp(k, w, sb + 18);
    }
  }
  STAMP_OUT(k, w);
  if (k.b == 0 && w == 0 && k.lane == 0) *(GAS u64*)k.ws = k.tag;
}

DI void stats_load(const Ctx& k, rsrc_t rw, int xb, int tok0, u32x2 (&sv)[8]) {
#pragma unroll
  for (int j = 0; j < 8; ++j) sv[j] = __builtin_amdgcn_raw_buffer_load_b64(rw, k.lane * 8, OFF_ST + xb * ST_BUF + j * 16384 + tok0 * 8, SC1);
}
DI void stats_table(const Ctx& k, int a, u32x2 (&sv)[8], float sign) {
#pragma unroll
  for (int j = 0; j < 8; ++j) pin(sv[j]);
  float s = 0.f, ss = 0.f;
#pragma unroll
  for (int j = 0; j < 8; ++j) { s += __uint_as_float(sv[j][0]); ss += __uint_as_float(sv[j][1]); }
  const float m = s * (1.f / 640.f), var = fmaxf(ss * (1.f / 640.f) - m * m, 0.f);
  *(LAS float*)(k.lds + L_MEAN + (64 * a + k.lane) * 4) = m;
  *(LAS float*)(k.lds + L_RSTD + (64 * a + k.lane) * 4) = sign / sqrtf(var + 1e-5f);
}
DI void colsum1(const Ctx& k, int a4) {
  const int q = k.lane >> 4, c = k.lane & 15;
  const unsigned rd = (((c >> 3) * 1024 + (c & 7) * 128 + ((q ^ (c >> 1)) * 16)) ^ ((a4 & 1) * 64)) + (a4 >> 1) * 10240 + L_W;
  float cs[5] = {0.f, 0.f, 0.f, 0.f, 0.f};
#pragma unroll 1
  for (int j = 0; j < 5; ++j)
#pragma unroll
    for (int i = 0; i < 5; ++i) cs[i] = dot_ones(lds16(k.lds, rd + j * 20480 + i * 2048), cs[i]);
#pragma unroll
  for (int i = 0; i < 5; ++i) { cs[i] = sum_q(cs[i]); if (q == 0) *(LAS float*)(k.lds + L_CS1 + (16 * i + c) * 16 + a4 * 4) = cs[i]; }
}

DI void wloader(const Ctx& k, const Params& p, const int a) {
  const int lane = k.lane;
  LAS char* const L = k.lds;
  const rsrc_t rw = mk(k.ws, WS_END);
  const rsrc_t rd = mk(k.mat ? p.wdi : p.wdr, 640 * 640 * 2), rs = mk((k.kq >> 1) ? p.wsi : p.wsr, 2048 * 2048 * 2);
  const unsigned sw = (((lane & 7) ^ (4 * a + (lane >> 4))) * 16);
  const unsigned vo1 = (lane >> 3) * 1280 + sw, vo2 = (lane >> 3) * 4096 + sw;
  const unsigned so1 = (80 * k.ct1 + 8 * a) * 1280, so2 = (128 * k.P + 8 * a) * 4096 + (k.kq & 1) * 2048;
  const unsigned so3 = OFF_W3 + (4 * k.kq + k.np) * 20 * 12288 + a * 12288, so4 = OFF_W4 + (2 * k.np + (k.kq & 1)) * 48 * 5120 + a * 15360;
  auto w1 = [&]() {
#pragma unroll 1
    for (int t = 0; t < 10; ++t) {
      waitv<(PFDEPTH > 5 ? PFDEPTH - 5 : 0)>();
#pragma unroll
      for (int j = 0; j < 5; ++j) dma<0, 0>(rd, L + L_W + t * 10240 + (2 * j + a) * 1024, vo1, so1 + j * 16 * 1280 + t * 128);
    }
  };
  auto w2 = [&](int t, int slot) {
#pragma unroll
    for (int j = 0; j < 8; ++j) dma<0, 0>(rs, L + L_W + slot * 16384 + (2 * j + a) * 1024, vo2, so2 + j * 16 * 4096 + t * 128);
  };
  auto w3 = [&](int t, int slot) { dma_lin<SC1, 12>(rw, L + L_W + slot * 24576 + a * 12288, lane * 16, so3 + t * 24576); };
  auto w4 = [&](int ch, int slot) { dma_lin<SC1, 15>(rw, L + L_W + slot * 30720 + a * 15360, lane * 16, so4 + ch * 30720); };
  STAMP_DECL;
  w1();
#pragma unroll 1
  for (int layer = 0; layer < 2; ++layer) {
    const int sb = 3 + layer * 24;
    waitv<0>();
    stamp(k, 4 + a, sb + 0);
    bar(); bar();
    if (layer == 0) colsum1(k, a);
    bar();
    bar_st();
#pragma unroll 1
    for (int t = 0; t < 6; ++t) { waitv<(PFDEPTH > 8 ? PFDEPTH - 8 : 0)>(); w2(t, t < 4 ? t + 2 : t - 4); }
    waitv<0>();
    stamp(k, 4 + a, sb + 4);
    bar(); bar();
    bar();
    int slot = 2;
#pragma unroll 1
    for (int t = 1; t <= 10; ++t) { waitv<32>(); bar(); if (WLSLEEP) __builtin_amdgcn_s_sleep(WLSLEEP); w2(t + 5, slot); slot = slot == 5 ? 0 : slot + 1; }
    waitv<32>(); bar();
    if (layer == 0) wait_mb(k, EV_RT, k.me, 16);
    waitv<0>(); bar();
    w3(2, 0); bar(); w3(3, 1); bar(); bar(); w3(0, 2);
    bar();
    bar_st();
    w3(1, 3);
    waitv<0>();
    stamp(k, 4 + a, sb + 11);
    bar(); bar(); bar(); bar();
    bar();
#pragma unroll 1
    for (int t = 1; t <= 6; ++t) { waitv<24>(); bar(); if (WLSLEEP) __builtin_amdgcn_s_sleep(WLSLEEP); w3(t + 3, (t + 1) & 3); }
    u32x2 sv[8], cv[10];
    stats_load(k, rw, 2 * layer + 1, 128 * k.P + 64 * a, sv);
    if (layer == 0) {
#pragma unroll
      for (int j = 0; j < 10; ++j) cv[j] = __builtin_amdgcn_raw_buffer_load_b64(rw, lane * 8, OFF_CS + (j * 3072 + a * 1536 + 96 * (4 * k.kq + k.np)) * 4, SC1);
    }
    waitv<24>(); bar(); waitv<12>(); bar();
    stats_table(k, a, sv, 1.f);
    if (layer == 0) {
      f32x2 s = {0.f, 0.f};
#pragma unroll
      for (int j = 0; j < 10; ++j) { pin(cv[j]); s += __builtin_bit_cast(f32x2, cv[j]); }
      if (lane < 48) *(LAS f32x2*)(L + L_CS3 + (96 * a + 2 * lane) * 4) = s;
    }
    waitv<0>();
    w4(0, 0);
    bar();
    w4(1, 1);
    bar();
    bar_st();
#pragma unroll 1
    for (int ch = 2; ch < 5; ++ch) { waitv<(PFDEPTH > 45 ? 30 : (PFDEPTH > 15 ? PFDEPTH - 15 : 0))>(); w4(ch, ch); }
    waitv<0>();
    stamp(k, 4 + a, sb + 15);
    bar(); bar();
#pragma unroll 1
    for (int ch = 5; ch < 8; ++ch) { bar(); w4(ch, ch - 5); }
    waitv<30>(); bar(); waitv<15>(); bar(); waitv<0>(); bar();
    bar();
    bar_st();
    if (layer == 0) w1();
  }
  STAMP_OUT(k, 4 + a);
}

DI void aloader(const Ctx& k, const Params& p, const int a) {
  const int lane = k.lane;
  LAS char* const L = k.lds;
  const rsrc_t rw = mk(k.ws, WS_END);
  STAMP_DECL;
  if (ALPRIO) __builtin_amdgcn_s_setprio(ALPRIO);
  auto poll = [&](int ev, int wg, int n) { bar(); if (a == 0) wait_mb(k, ev, wg, n); bar(); };
#pragma unroll 1
  for (int layer = 0; layer < 2; ++layer) {
    const int ev0 = layer * EV_PER_LAYER, sb = 3 + layer * 24;
    {
      poll(ev0 + EV_X, k.me1, 16);
      u32x2 sv[8];
      stats_load(k, rw, 2 * layer, 128 * k.P1 + 64 * a, sv);
      if (layer == 0) colsum1(k, 2 + a);
      stats_table(k, a, sv, k.mat ? -1.f : 1.f);
      bar();
      bar_st();
    }
    {
      const unsigned so = OFF_AB + layer * AB_BUF + (((k.kq >> 1) * 4 + k.np) * 64 + 32 * (k.kq & 1) + a) * 10240;
      auto ab = [&](int t, int slot) { dma_lin<SC1, 10>(rw, L + L_ACT + slot * 20480 + a * 10240, lane * 16, so + t * 20480); };
      poll(ev0 + EV_AB, k.me, 16);
      stamp(k, 6 + a, sb + 3);
      if (layer == 0 && a == 0) push_mb(k, EV_RT, k.P * 16, 1, 16, k.kq * 4 + k.np);
#pragma unroll 1
      for (int t = 0; t < 3; ++t) ab(t, t);
      waitv<20>();
      bar();
      int slot = 0;
#pragma unroll 1
      for (int t = 1; t <= 13; ++t) { waitv<10>(); bar(); ab(t + 2, slot); slot = slot == 2 ? 0 : slot + 1; }
      waitv<10>(); bar(); waitv<0>(); bar();
      bar();
      bar_st();
    }
    {
      const unsigned so = OFF_XT + (2 * layer + 1) * XT_BUF + (8 * k.P + 4 * a) * 20480;
      auto xt = [&](int t, int slot) {
#pragma unroll
        for (int j = 0; j < 4; ++j) dma_lin<SC1, 2>(rw, L + L_ACT + slot * 16384 + (4 * a + j) * 2048, lane * 16, so + j * 20480 + t * 2048);
      };
      poll(ev0 + EV_EX, k.me, 4);
      poll(ev0 + EV_X1, k.me, 16);
      stamp(k, 6 + a, sb + 10);
#pragma unroll 1
      for (int t = 0; t < 3; ++t) xt(t, t);
      waitv<16>();
      bar();
      int slot = 0;
#pragma unroll 1
      for (int t = 1; t <= 7; ++t) { waitv<8>(); bar(); xt(t + 2, slot); slot = slot == 2 ? 0 : slot + 1; }
      waitv<8>(); bar(); waitv<0>(); bar();
      bar();
      bar_st();
    }
    poll(ev0 + EV_H, k.me, 16);
#pragma unroll 1
    for (int j = 0; j < 7; ++j) bar();
    bar_st();
  }
  STAMP_OUT(k, 6 + a);
}

__global__ void __launch_bounds__(512, 2) fnet_glu_kernel(Params p) {
  __shared__ __attribute__((aligned(16))) char smem[163840];
  Ctx k;
  k.ws = p.ws; k.lds = (LAS char*)smem; k.lane = threadIdx.x & 63; k.b = blockIdx.x;
  { const u64 e = *(const GAS u64*)p.ws; k.tag = e * 6364136223846793005ull + 1442695040888963407ull; }
  const int x = k.b & 7, i = k.b >> 3;
  k.kq = x >> 1; k.P = 8 * (x & 1) + (i >> 2); k.np = i & 3; k.me = k.P * 16 + k.kq * 4 + k.np;
  k.P1 = 4 * (x >> 1) + (i >> 3); k.mat = x & 1; k.ct1 = i & 7; k.me1 = k.P1 * 16 + k.mat * 8 + k.ct1;
  const int w = __builtin_amdgcn_readfirstlane(threadIdx.x >> 6);
  if (threadIdx.x == 256) *(LAS unsigned*)(k.lds + L_CNT) = 0u;
  bar();
  if (w < 4) consumer(k, p, w);
  else if (w < 6) wloader(k, p, w - 4);
  else aloader(k, p, w - 6);
}
}

extern "C" void kernel_launch(void* const* d_in, const int* in_sizes, int n_in, void* d_out, int out_size, void* d_ws, size_t ws_size, hipStream_t stream) {
  Params p;
  p.x0 = (const char*)d_in[0]; p.wsr = (const char*)d_in[1]; p.wsi = (const char*)d_in[2]; p.wdr = (const char*)d_in[3]; p.wdi = (const char*)d_in[4];
  p.wg = (const char*)d_in[6]; p.wu = (const char*)d_in[7]; p.wdn = (const char*)d_in[8]; p.out = (char*)d_out; p.ws = (char*)d_ws;
  fnet_glu_kernel<<<256, 512, 0, stream>>>(p);
}
```

```cpp
#include <hip/hip_runtime.h>

typedef unsigned long long u64;
typedef short bf16x8 __attribute__((ext_vector_type(8)));
typedef short s16x4 __attribute__((ext_vector_type(4)));
typedef float f32x4 __attribute__((ext_vector_type(4)));
typedef float f32x2 __attribute__((ext_vector_type(2)));
typedef unsigned u32x4 __attribute__((ext_vector_type(4)));
typedef unsigned u32x2 __attribute__((ext_vector_type(2)));
typedef __bf16 bf2 __attribute__((ext_vector_type(2)));
#define LAS __attribute__((address_space(3)))
#define GAS __attribute__((address_space(1)))
#define DI __device__ __forceinline__
typedef __amdgpu_buffer_rsrc_t rsrc_t;

#define STAMPS 1

#define PFMODE 0
#define PFDEPTH 48
#define ALPRIO 3
#define WLSLEEP 0
#define WIN1 6
#define WIN4 12
namespace {
constexpr unsigned OFF_MB = 4096, MB_EV = 256 * 128;
constexpr int EV_X = 0, EV_AB = 1, EV_EX = 2, EV_X1 = 3, EV_H = 4, EV_PER_LAYER = 5, EV_RT = 10;
constexpr unsigned OFF_CS = 0x60000;
constexpr unsigned OFF_ST = 0x80000, ST_BUF = 8 * 2048 * 8;
constexpr unsigned OFF_XT = 0x100000, XT_BUF = 2048 * 640 * 2;
constexpr unsigned OFF_AB = OFF_XT + 4 * XT_BUF, AB_BUF = 2048 * 1280 * 2;
constexpr unsigned OFF_HT = OFF_AB + 2 * AB_BUF, HT_BUF = 2048 * 1536 * 2;
constexpr unsigned OFF_EX = OFF_HT + 2 * HT_BUF, EX_BUF = 256 * 16 * 3072;
constexpr unsigned OFF_W3 = OFF_EX + 2 * EX_BUF;
constexpr unsigned OFF_W4 = OFF_W3 + 16 * 20 * 12288;
constexpr unsigned WS_END = OFF_W4 + 8 * 48 * 5120;
constexpr unsigned OFF_STAMP = 0x6000000;
static_assert(OFF_MB + 11 * MB_EV <= OFF_CS && OFF_CS + 10 * 3072 * 4 <= OFF_ST && OFF_ST + 4 * ST_BUF <= OFF_XT && WS_END <= OFF_STAMP, "ws map");
constexpr unsigned L_CNT = 0, L_MEAN = 256, L_RSTD = 768, L_CS1 = 1280, L_CS3 = 2560;
constexpr unsigned L_W = 4096, L_ACT = L_W + 98304, L_RT = L_W + 102400;
constexpr int SC1 = 16;
constexpr unsigned SPIN_CAP = 1u << 16;

struct Params { const char* x0; const char* wsr; const char* wsi; const char* wdr; const char* wdi; const char* wg; const char* wu; const char* wdn; char* out; char* ws; };

DI rsrc_t mk(const void* p, unsigned bytes) { return __builtin_amdgcn_make_buffer_rsrc((void*)p, 0, bytes, 0x00020000); }
template <int N> DI void waitv() { asm volatile("s_waitcnt vmcnt(%0)" ::"n"(N) : "memory"); }
DI void bar() { asm volatile("s_waitcnt lgkmcnt(0)\n\ts_barrier" ::: "memory"); }
DI void sched() { __builtin_amdgcn_sched_barrier(0); }
template <class T> DI void pin(T& x) { asm volatile("" : "+v"(x)); }
DI unsigned pins(unsigned x) { asm volatile("" : "+s"(x)); return x; }
DI void bar_st() { if (PFMODE == 1) asm volatile("s_barrier" ::: "memory"); }
template <int AUX, int IMM> DI void dma(rsrc_t r, LAS char* l, unsigned vo, unsigned so) { __builtin_amdgcn_raw_ptr_buffer_load_lds(r, (LAS void*)l, 16, vo, so, IMM, AUX); }
template <int AUX, int N> DI void dma_lin(rsrc_t r, LAS char* l, unsigned vo, unsigned so) {
#pragma unroll
  for (int j = 0; j < N; j += 4) {
    dma<AUX, 0>(r, l + j * 1024, vo, so + j * 1024);
    if (j + 1 < N) dma<AUX, 1024>(r, l + j * 1024, vo, so + j * 1024);
    if (j + 2 < N) dma<AUX, 2048>(r, l + j * 1024, vo, so + j * 1024);
    if (j + 3 < N) dma<AUX, 3072>(r, l + j * 1024, vo, so + j * 1024);
  }
}
DI bf16x8 ld16(rsrc_t r, unsigned vo, unsigned so) { return __builtin_bit_cast(bf16x8, __builtin_amdgcn_raw_buffer_load_b128(r, vo, so, SC1)); }
DI void st16(rsrc_t r, u32x4 v, unsigned vo, unsigned so) { __builtin_amdgcn_raw_buffer_store_b128(v, r, vo + so, 0, SC1); }
DI bf16x8 lds16(LAS char* l, unsigned a) { return *(LAS bf16x8*)(l + a); }
DI unsigned pk(float lo, float hi) { f32x2 v = {lo, hi}; return __builtin_bit_cast(unsigned, __builtin_convertvector(v, bf2)); }
DI float blo(unsigned u) { return __uint_as_float(u << 16); }
DI float bhi(unsigned u) { return __uint_as_float(u & 0xffff0000u); }
DI f32x4 mfma(bf16x8 a, bf16x8 b, f32x4 c) { return __builtin_amdgcn_mfma_f32_16x16x32_bf16(a, b, c, 0, 0, 0); }
DI float dot_ones(bf16x8 f, float s) {
  const u32x4 u = __builtin_bit_cast(u32x4, f); const bf2 one = __builtin_bit_cast(bf2, 0x3f803f80u);
#pragma unroll
  for (int j = 0; j < 4; ++j) { const unsigned e = u[j]; s = __builtin_amdgcn_fdot2_f32_bf16(__builtin_bit_cast(bf2, e), one, s, false); }
  return s;
}
DI float sum_q(float s) { s += __shfl_xor(s, 16); s += __shfl_xor(s, 32); return s; }
DI u32x4 swap_pair(unsigned a0, unsigned a1, unsigned b0, unsigned b1) {
  const auto r0 = __builtin_amdgcn_permlane16_swap(a0, b0, false, false);
  const auto r1 = __builtin_amdgcn_permlane16_swap(a1, b1, false, false);
  return (u32x4){r0[0], r1[0], r0[1], r1[1]};
}

struct Ctx {
  char* ws; LAS char* lds; u64 tag; int lane, b;
  int kq, P, np, me;
  int P1, mat, ct1, me1;
};
struct St { unsigned t, c; };
#define STAMP_DECL St st_ = {0u, 0u}
#define stamp(k, w, id) do { sched(); const unsigned t_ = (unsigned)__builtin_amdgcn_s_memrealtime(), c_ = (unsigned)__builtin_amdgcn_s_memtime(); const bool m_ = (int)(threadIdx.x & 63) == (id); st_.t = m_ ? t_ : st_.t; st_.c = m_ ? c_ : st_.c; pin(st_.t); pin(st_.c); sched(); } while (0)
#define STAMP_OUT(k, w) do { if ((w) == 0 && (k.b & 31) == 0) { unsigned d_ = (unsigned)(__builtin_amdgcn_readlane(st_.t, 3) - __builtin_amdgcn_readlane(st_.t, 0)) ; d_ = d_ > 20000u ? 20000u : d_; const unsigned t0_ = (unsigned)__builtin_amdgcn_s_memrealtime(); while ((unsigned)__builtin_amdgcn_s_memrealtime() - t0_ < d_ * 10u) __builtin_amdgcn_s_sleep(8); } } while (0)
DI void wait_mb(const Ctx& k, int ev, int wg, int n) {
  GAS u64* p = (GAS u64*)(k.ws + OFF_MB + ev * MB_EV + wg * 128) + (k.lane & (n - 1));
#pragma unroll 1
  for (unsigned spins = 0; spins < SPIN_CAP; ++spins) {
    const u64 v = __hip_atomic_load(p, __ATOMIC_RELAXED, __HIP_MEMORY_SCOPE_AGENT);
    if (__all(v == k.tag)) break;
    __builtin_amdgcn_s_sleep(1);
  }
  asm volatile("" ::: "memory");
}
DI void push_mb(const Ctx& k, int ev, int wg0, int wgstep, int n, int slot) {
  if (k.lane < n) __hip_atomic_store((GAS u64*)(k.ws + OFF_MB + ev * MB_EV + (wg0 + k.lane * wgstep) * 128) + slot, k.tag, __ATOMIC_RELAXED, __HIP_MEMORY_SCOPE_AGENT);
}
DI void publish(const Ctx& k, int ev, int wg0, int wgstep, int n, int slot) {
  GAS u64* dst = (GAS u64*)(k.ws + OFF_MB + ev * MB_EV + (wg0 + (k.lane & (n - 1)) * wgstep) * 128) + slot;
  unsigned old, cnt = (unsigned)(size_t)(k.lds + L_CNT), one = 1u;
  pin(dst);
  asm volatile("s_waitcnt vmcnt(0) lgkmcnt(0)\n\ts_mov_b64 exec, 1\n\tds_add_rtn_u32 %0, %1, %2\n\ts_mov_b64 exec, -1\n\ts_waitcnt lgkmcnt(0)" : "=&v"(old) : "v"(cnt), "v"(one) : "memory");
  old = __builtin_amdgcn_readfirstlane(old);
  if ((old & 3u) == 3u && k.lane < n) __hip_atomic_store(dst, k.tag, __ATOMIC_RELAXED, __HIP_MEMORY_SCOPE_AGENT);
  asm volatile("" ::: "memory");
}

DI void x_epilogue(const Ctx& k, const Params& p, int w, const f32x4 (&v)[5], unsigned (&res)[10], int xb, bool final_out) {
  const int q = k.lane >> 4, c = k.lane & 15, tfo = 8 * k.P + 4 * (k.kq >> 1) + w, cto = 2 * k.np + (k.kq & 1);
#pragma unroll
  for (int i = 0; i < 5; ++i) { res[2 * i] = pk(v[i][0], v[i][1]); res[2 * i + 1] = pk(v[i][2], v[i][3]); }
  const u32x4 o0 = swap_pair(res[0], res[1], res[2], res[3]), o1 = swap_pair(res[4], res[5], res[6], res[7]), o2 = swap_pair(res[8], res[9], res[8], res[9]);
  const int g = 10 * cto + 2 * (q & 1) + (q >> 1);
  if (final_out) {
    const rsrc_t ro = mk(p.out, 2048 * 640 * 2);
    const unsigned vo = (16 * tfo + c) * 1280 + g * 16;
    __builtin_amdgcn_raw_buffer_store_b128(o0, ro, vo, 0, 0);
    __builtin_amdgcn_raw_buffer_store_b128(o1, ro, vo + 64, 0, 0);
    if (!(q & 1)) __builtin_amdgcn_raw_buffer_store_b128(o2, ro, vo + 128, 0, 0);
  } else {
    const rsrc_t rw = mk(k.ws, WS_END);
    const unsigned vo = g * 256 + c * 16, so = OFF_XT + xb * XT_BUF + tfo * 20480;
    st16(rw, o0, vo, so); st16(rw, o1, vo + 1024, so);
    if (!(q & 1)) st16(rw, o2, vo + 2048, so);
    float s = 0.f, ss = 0.f;
#pragma unroll
    for (int j = 0; j < 10; ++j) { const float a = blo(res[j]), bb = bhi(res[j]); s += a + bb; ss = fmaf(a, a, fmaf(bb, bb, ss)); }
    s = sum_q(s); ss = sum_q(ss);
    if (q == 0) __builtin_amdgcn_raw_buffer_store_b64((u32x2){__float_as_uint(s), __float_as_uint(ss)}, rw, c * 8, OFF_ST + xb * ST_BUF + cto * 16384 + tfo * 128, SC1);
  }
}

#define PHASE_LANE() Ctx k = k0; asm volatile("" : "+v"(k.lane), "+s"(k.P), "+s"(k.kq), "+s"(k.np), "+s"(k.P1), "+s"(k.ct1), "+s"(k.mat)); \
  k.me = k.P * 16 + k.kq * 4 + k.np; k.me1 = k.P1 * 16 + k.mat * 8 + k.ct1; \
  const int tfo = 8 * k.P + 4 * (k.kq >> 1) + w, cto = 2 * k.np + (k.kq & 1); (void)tfo; (void)cto; \
  const int lane = k.lane, q = lane >> 4, c = lane & 15; (void)q; (void)c; \
  const unsigned rowrd = (c >> 3) * 1024 + (c & 7) * 128 + ((q ^ (c >> 1)) * 16); (void)rowrd
DI void consumer(const Ctx& k0, const Params& p, const int w) {
  const Ctx& k = k0;
  LAS char* const L = k.lds;
  const rsrc_t rw = mk(k.ws, WS_END);
  unsigned res[10];
  STAMP_DECL;
  stamp(k, w, 0);
  {
    PHASE_LANE();
    const rsrc_t rx = mk(p.x0, 2048 * 640 * 2);
    u32x2 xi[5];
#pragma unroll
    for (int i = 0; i < 5; ++i) xi[i] = __builtin_amdgcn_raw_buffer_load_b64(rx, (16 * tfo + c) * 1280 + (80 * cto + 16 * i + 4 * q) * 2, 0, 0);
    const int u = k.b + 256 * w, m = u / 240, r = u - 240 * m;
    const bool rt = u < 720;
    const int ku = m < 2 ? r / 24 : r / 10, stp = m < 2 ? r - 24 * ku : r - 10 * ku;
    if (rt) {
      const unsigned stride = m < 2 ? 3072 : 1280;
      const rsrc_t rs = mk(m == 0 ? p.wg : (m == 1 ? p.wu : p.wdn), 640 * 1536 * 2);
#pragma unroll
      for (int j = 0; j < 8; ++j) dma<0, 0>(rs, L + L_RT + w * 8192 + j * 1024, (lane >> 3) * stride + (lane & 7) * 16, (64 * ku + 8 * j) * stride + 128 * stp);
    }
    f32x4 v[5];
#pragma unroll
    for (int i = 0; i < 5; ++i) v[i] = (f32x4){blo(xi[i][0]), bhi(xi[i][0]), blo(xi[i][1]), bhi(xi[i][1])};
    x_epilogue(k, p, w, v, res, 0, false);
    publish(k, EV_X, k.P * 16, 1, 16, k.kq * 4 + k.np);
    stamp(k, w, 1);
    if (rt) {
      float cs[4] = {0.f, 0.f, 0.f, 0.f};
#pragma unroll
      for (int kk = 0; kk < 2; ++kk)
#pragma unroll
        for (int n = 0; n < 4; ++n) {
          LAS char* a = L + L_RT + w * 8192 + (4 * kk + q) * 1024 + (c >> 2) * 128 + 32 * n + (c & 3) * 8;
          const u32x2 lo = __builtin_bit_cast(u32x2, __builtin_amdgcn_ds_read_tr16_b64_v4i16((LAS s16x4*)a));
          const u32x2 hi = __builtin_bit_cast(u32x2, __builtin_amdgcn_ds_read_tr16_b64_v4i16((LAS s16x4*)(a + 512)));
          const u32x4 f = {lo[0], lo[1], hi[0], hi[1]};
          const int nf = 4 * stp + n;
          unsigned so;
          if (m < 2) { const int n3 = nf / 6; so = OFF_W3 + (n3 * 20 + 2 * ku + kk) * 12288 + (2 * (nf - 6 * n3) + m) * 1024; }
          else { const int ct = nf / 5; so = OFF_W4 + (ct * 48 + 2 * ku + kk) * 5120 + (nf - 5 * ct) * 1024; }
          st16(rw, f, lane * 16, so);
          cs[n] = dot_ones(__builtin_bit_cast(bf16x8, f), cs[n]);
        }
      if (m < 2) {
#pragma unroll
        for (int n = 0; n < 4; ++n) cs[n] = sum_q(cs[n]);
        const float o = q == 0 ? cs[0] : (q == 1 ? cs[1] : (q == 2 ? cs[2] : cs[3]));
        __builtin_amdgcn_raw_buffer_store_b32(__float_as_uint(o), rw, lane * 4, OFF_CS + (ku * 3072 + m * 1536 + 64 * stp) * 4, SC1);
      }
    }
    stamp(k, w, 2);
  }
#pragma unroll 1
  for (int layer = 0; layer < 2; ++layer) {
    const int ev0 = layer * EV_PER_LAYER, sb = 3 + layer * 24;
    {
      PHASE_LANE();
      constexpr int WIN = WIN1;
      unsigned wb[2][2] = {{L_W + rowrd, L_W + rowrd + 51200}, {L_W + (rowrd ^ 64), L_W + (rowrd ^ 64) + 51200}};
      pin(wb[0][0]); pin(wb[0][1]); pin(wb[1][0]); pin(wb[1][1]);
      bar(); bar();
      stamp(k, w, sb + 0);
      const unsigned xso = OFF_XT + 2 * layer * XT_BUF + (8 * k.P1 + 2 * w) * 20480;
      bf16x8 xa[WIN][2], wf[2][5];
      f32x4 acc[2][5];
#pragma unroll
      for (int s = 0; s < WIN; ++s) { xa[s][0] = ld16(rw, lane * 16 + s * 1024, xso); xa[s][1] = ld16(rw, lane * 16 + s * 1024, xso + 20480); sched(); }
#pragma unroll
      for (int i = 0; i < 5; ++i) { acc[0][i] = (f32x4){0.f, 0.f, 0.f, 0.f}; acc[1][i] = (f32x4){0.f, 0.f, 0.f, 0.f}; }
#pragma unroll
      for (int i = 0; i < 5; ++i) wf[0][i] = lds16(L, wb[0][0] + i * 2048);
      sched();
#pragma unroll
      for (int s = 0; s < 20; ++s) {
        if (s + 1 < 20) {
#pragma unroll
          for (int i = 0; i < 5; ++i) wf[(s + 1) & 1][i] = lds16(L, wb[(s + 1) & 1][(s + 1) / 10] + (((s + 1) >> 1) % 5) * 10240 + i * 2048);
        } else bar();
#pragma unroll
        for (int i = 0; i < 5; ++i) { acc[0][i] = mfma(xa[s % WIN][0], wf[s & 1][i], acc[0][i]); acc[1][i] = mfma(xa[s % WIN][1], wf[s & 1][i], acc[1][i]); }
        if (s + WIN < 20) { xa[s % WIN][0] = ld16(rw, lane * 16, xso + (s + WIN) * 1024); xa[s % WIN][1] = ld16(rw, lane * 16, xso + 20480 + (s + WIN) * 1024); }
        if (s + 1 < 20) {
#pragma unroll
          for (int j = 0; j < 5; ++j) { __builtin_amdgcn_sched_group_barrier(0x100, 1, 0); __builtin_amdgcn_sched_group_barrier(0x8, 1, 0); }
          __builtin_amdgcn_sched_group_barrier(0x8, 5, 0);
        }
        sched();
      }
      stamp(k, w, sb + 1);
      f32x4 mean[2], rstd[2]; float cs[5];
#pragma unroll
      for (int t = 0; t < 2; ++t) { mean[t] = *(LAS f32x4*)(L + L_MEAN + (32 * w + 16 * t + 4 * q) * 4); rstd[t] = *(LAS f32x4*)(L + L_RSTD + (32 * w + 16 * t + 4 * q) * 4); }
#pragma unroll
      for (int i = 0; i < 5; ++i) { const f32x4 t = *(LAS f32x4*)(L + L_CS1 + (16 * i + c) * 16); cs[i] = (t[0] + t[1]) + (t[2] + t[3]); }
      const unsigned so = OFF_AB + layer * AB_BUF + ((k.mat * 4 + (k.ct1 >> 1)) * 64 + 4 * k.P1 + w) * 10240 + 5 * (k.ct1 & 1) * 1024;
      const unsigned vo = ((2 * (q & 1) + (q >> 1)) * 16 + c) * 16;
#pragma unroll
      for (int i = 0; i < 5; ++i) {
        const f32x4 a = (acc[0][i] - mean[0] * cs[i]) * rstd[0], bb = (acc[1][i] - mean[1] * cs[i]) * rstd[1];
        st16(rw, swap_pair(pk(a[0], a[1]), pk(a[2], a[3]), pk(bb[0], bb[1]), pk(bb[2], bb[3])), vo + i * 1024, so);
      }
      bar_st();
      stamp(k, w, sb + 2);
      publish(k, ev0 + EV_AB, (2 * k.mat + (k.P1 >> 3)) * 4 + (k.ct1 >> 1), 16, 16, 2 * (k.P1 & 7) + (k.ct1 & 1));
      stamp(k, w, sb + 3);
    }
    {
      PHASE_LANE();
      const int wm = w >> 1, wn = w & 1;
      f32x4 acc[4][5];
      bf16x8 fa[2][5], fb[2][4];
#pragma unroll
      for (int t = 0; t < 4; ++t)
#pragma unroll
        for (int i = 0; i < 5; ++i) acc[t][i] = (f32x4){0.f, 0.f, 0.f, 0.f};
      unsigned wb = L_W + wm * 8192 + rowrd, ab = L_ACT + wn * 5120 + lane * 16;
      unsigned ws_slot = 32768, ab_slot = 0;
#pragma unroll
      for (int t = 0; t < 4; ++t)
#pragma unroll
        for (int i = 0; i < 5; ++i) pin(acc[t][i]);
      pin(wb); pin(ab);
      bar(); bar();
      bar();
      stamp(k, w, sb + 4);
#pragma unroll
      for (int i = 0; i < 5; ++i) fa[0][i] = lds16(L, ab + i * 1024);
#pragma unroll
      for (int t = 0; t < 4; ++t) fb[0][t] = lds16(L, wb + 32768 + t * 2048);
#pragma unroll 1
      for (int kt = 0; kt < 16; ++kt) {
#pragma unroll
        for (int i = 0; i < 5; ++i) fa[1][i] = lds16(L, ab + ab_slot + 10240 + i * 1024);
#pragma unroll
        for (int t = 0; t < 4; ++t) fb[1][t] = lds16(L, (wb ^ 64) + ws_slot + t * 2048);
#pragma unroll
        for (int t = 0; t < 4; ++t)
#pragma unroll
          for (int i = 0; i < 5; ++i) acc[t][i] = mfma(fa[0][i], fb[0][t], acc[t][i]);
#pragma unroll
        for (int j = 0; j < 9; ++j) { __builtin_amdgcn_sched_group_barrier(0x8, 1, 0); __builtin_amdgcn_sched_group_barrier(0x100, 1, 0); }
        __builtin_amdgcn_sched_group_barrier(0x8, 11, 0);
        sched();
        ab_slot = ab_slot == 40960 ? 0 : ab_slot + 20480; ws_slot = ws_slot == 81920 ? 0 : ws_slot + 16384;
        bar();
        sched();
#pragma unroll
        for (int i = 0; i < 5; ++i) fa[0][i] = lds16(L, ab + ab_slot + i * 1024);
#pragma unroll
        for (int t = 0; t < 4; ++t) fb[0][t] = lds16(L, wb + ws_slot + t * 2048);
#pragma unroll
        for (int t = 0; t < 4; ++t)
#pragma unroll
          for (int i = 0; i < 5; ++i) acc[t][i] = mfma(fa[1][i], fb[1][t], acc[t][i]);
#pragma unroll
        for (int j = 0; j < 9; ++j) { __builtin_amdgcn_sched_group_barrier(0x8, 1, 1); __builtin_amdgcn_sched_group_barrier(0x100, 1, 1); }
        __builtin_amdgcn_sched_group_barrier(0x8, 11, 1);
        sched();
      }
      stamp(k, w, sb + 5);
      if (w == k.kq) {
#pragma unroll
        for (int t = 0; t < 4; ++t)
#pragma unroll
          for (int i = 0; i < 5; ++i) *(LAS f32x4*)(L + L_ACT + (t * 5 + i) * 1024 + lane * 16) = acc[t][i];
      } else {
        const unsigned so = OFF_EX + layer * EX_BUF + ((k.P * 16 + w * 4 + k.np) * 4 + k.kq) * 12288;
#pragma unroll
        for (int t = 0; t < 4; ++t) {
          st16(rw, (u32x4){pk(acc[t][0][0], acc[t][0][1]), pk(acc[t][0][2], acc[t][0][3]), pk(acc[t][1][0], acc[t][1][1]), pk(acc[t][1][2], acc[t][1][3])}, lane * 16, so + t * 3072);
          st16(rw, (u32x4){pk(acc[t][2][0], acc[t][2][1]), pk(acc[t][2][2], acc[t][2][3]), pk(acc[t][3][0], acc[t][3][1]), pk(acc[t][3][2], acc[t][3][3])}, lane * 16 + 1024, so + t * 3072);
          st16(rw, __builtin_bit_cast(u32x4, acc[t][4]), lane * 16 + 2048, so + t * 3072);
        }
      }
      bar_st();
      stamp(k, w, sb + 6);
      publish(k, ev0 + EV_EX, k.P * 16 + k.np, 4, 4, k.kq);
      stamp(k, w, sb + 7);
      bar(); bar();
      stamp(k, w, sb + 8);
      u32x4 e[3][3];
#pragma unroll
      for (int n = 0; n < 3; ++n) {
        const unsigned so = OFF_EX + layer * EX_BUF + (k.me * 4 + ((k.kq + 1 + n) & 3)) * 12288 + w * 3072;
#pragma unroll
        for (int j = 0; j < 3; ++j) e[n][j] = __builtin_amdgcn_raw_buffer_load_b128(rw, lane * 16 + j * 1024, so, SC1);
      }
      f32x4 v[5];
#pragma unroll
      for (int i = 0; i < 5; ++i) v[i] = *(LAS f32x4*)(L + L_ACT + (w * 5 + i) * 1024 + lane * 16) + (f32x4){blo(res[2 * i]), bhi(res[2 * i]), blo(res[2 * i + 1]), bhi(res[2 * i + 1])};
#pragma unroll
      for (int n = 0; n < 3; ++n) {
#pragma unroll
        for (int j = 0; j < 2; ++j) {
          v[2 * j] += (f32x4){blo(e[n][j][0]), bhi(e[n][j][0]), blo(e[n][j][1]), bhi(e[n][j][1])};
          v[2 * j + 1] += (f32x4){blo(e[n][j][2]), bhi(e[n][j][2]), blo(e[n][j][3]), bhi(e[n][j][3])};
        }
        v[4] += __builtin_bit_cast(f32x4, e[n][2]);
      }
      x_epilogue(k, p, w, v, res, 2 * layer + 1, false);
      stamp(k, w, sb + 9);
      publish(k, ev0 + EV_X1, k.P * 16, 1, 16, k.kq * 4 + k.np);
      stamp(k, w, sb + 10);
    }
    {
      PHASE_LANE();
      const int wm = w >> 1, wn = w & 1;
      f32x4 acc[4][6];
      bf16x8 fa[2][6], fb[2][4];
#pragma unroll
      for (int t = 0; t < 4; ++t)
#pragma unroll
        for (int i = 0; i < 6; ++i) acc[t][i] = (f32x4){0.f, 0.f, 0.f, 0.f};
      unsigned wb = L_W + wn * 6144 + lane * 16, xb = L_ACT + wm * 8192 + lane * 16;
      unsigned w_slot = 49152, x_slot = 0;
#pragma unroll
      for (int t = 0; t < 4; ++t)
#pragma unroll
        for (int i = 0; i < 6; ++i) pin(acc[t][i]);
      pin(wb); pin(xb);
      bar(); bar();
      bar();
      stamp(k, w, sb + 11);
#pragma unroll
      for (int i = 0; i < 6; ++i) fa[0][i] = lds16(L, wb + 49152 + i * 1024);
#pragma unroll
      for (int t = 0; t < 4; ++t) fb[0][t] = lds16(L, xb + t * 2048);
#pragma unroll 1
      for (int kt = 0; kt < 10; ++kt) {
#pragma unroll
        for (int i = 0; i < 6; ++i) fa[1][i] = lds16(L, wb + w_slot + 12288 + i * 1024);
#pragma unroll
        for (int t = 0; t < 4; ++t) fb[1][t] = lds16(L, xb + x_slot + 1024 + t * 2048);
#pragma unroll
        for (int t = 0; t < 4; ++t)
#pragma unroll
          for (int i = 0; i < 6; ++i) acc[t][i] = mfma(fa[0][i], fb[0][t], acc[t][i]);
#pragma unroll
        for (int j = 0; j < 10; ++j) { __builtin_amdgcn_sched_group_barrier(0x8, 1, 0); __builtin_amdgcn_sched_group_barrier(0x100, 1, 0); }
        __builtin_amdgcn_sched_group_barrier(0x8, 14, 0);
        sched();
        x_slot = x_slot == 32768 ? 0 : x_slot + 16384; w_slot = w_slot == 73728 ? 0 : w_slot + 24576;
        bar();
        sched();
#pragma unroll
        for (int i = 0; i < 6; ++i) fa[0][i] = lds16(L, wb + w_slot + i * 1024);
#pragma unroll
        for (int t = 0; t < 4; ++t) fb[0][t] = lds16(L, xb + x_slot + t * 2048);
#pragma unroll
        for (int t = 0; t < 4; ++t)
#pragma unroll
          for (int i = 0; i < 6; ++i) acc[t][i] = mfma(fa[1][i], fb[1][t], acc[t][i]);
#pragma unroll
        for (int j = 0; j < 10; ++j) { __builtin_amdgcn_sched_group_barrier(0x8, 1, 1); __builtin_amdgcn_sched_group_barrier(0x100, 1, 1); }
        __builtin_amdgcn_sched_group_barrier(0x8, 14, 1);
        sched();
      }
      stamp(k, w, sb + 12);
      float mean[4], rstd[4];
#pragma unroll
      for (int t = 0; t < 4; ++t) { mean[t] = *(LAS float*)(L + L_MEAN + (64 * wm + 16 * t + c) * 4); rstd[t] = *(LAS float*)(L + L_RSTD + (64 * wm + 16 * t + c) * 4); }
      const int n3 = 4 * k.kq + k.np;
      const unsigned so = OFF_HT + layer * HT_BUF + ((8 * k.P + 4 * wm) * 48 + 3 * n3) * 1024;
#pragma unroll
      for (int f = 0; f < 3; ++f) {
        const f32x4 cg = *(LAS f32x4*)(L + L_CS3 + (16 * (3 * wn + f) + 4 * q) * 4), cu = *(LAS f32x4*)(L + L_CS3 + (96 + 16 * (3 * wn + f) + 4 * q) * 4);
        unsigned h[4][2];
#pragma unroll
        for (int t = 0; t < 4; ++t) {
          const f32x4 g = (acc[t][2 * f] - mean[t] * cg) * (rstd[t] * -1.44269504089f), u = (acc[t][2 * f + 1] - mean[t] * cu) * rstd[t];
          f32x4 o;
#pragma unroll
          for (int r = 0; r < 4; ++r) o[r] = __builtin_amdgcn_exp2f(g[r]);
          o += 1.f;
#pragma unroll
          for (int r = 0; r < 4; ++r) o[r] = __builtin_amdgcn_rcpf(o[r]);
          o *= u;
          h[t][0] = pk(o[0], o[1]); h[t][1] = pk(o[2], o[3]);
        }
        const unsigned vo = (q & 1) * 49152 + (2 * (3 * wn + f) + (q >> 1)) * 256 + c * 16;
        st16(rw, swap_pair(h[0][0], h[0][1], h[1][0], h[1][1]), vo, so);
        st16(rw, swap_pair(h[2][0], h[2][1], h[3][0], h[3][1]), vo, so + 98304);
      }
      bar_st();
      stamp(k, w, sb + 13);
      publish(k, ev0 + EV_H, k.P * 16, 1, 16, n3);
      stamp(k, w, sb + 14);
    }
    {
      PHASE_LANE();
      constexpr int WIN = WIN4;
      unsigned wb[3] = {L_W + lane * 16, L_W + lane * 16 + 51200, L_W + lane * 16 + 102400};
      pin(wb[0]); pin(wb[1]); pin(wb[2]);
      bar(); bar();
      stamp(k, w, sb + 15);
      const unsigned hso = OFF_HT + layer * HT_BUF + tfo * 49152;
      bf16x8 ha[WIN], wf[2][5];
      f32x4 acc[5];
#pragma unroll
      for (int s = 0; s < WIN; ++s) ha[s] = ld16(rw, lane * 16, hso + s * 1024);
#pragma unroll
      for (int i = 0; i < 5; ++i) acc[i] = (f32x4){blo(res[2 * i]), bhi(res[2 * i]), blo(res[2 * i + 1]), bhi(res[2 * i + 1])};
#pragma unroll
      for (int i = 0; i < 5; ++i) wf[0][i] = lds16(L, wb[0] + i * 1024);
      sched();
#pragma unroll
      for (int s = 0; s < 48; ++s) {
        if ((s + 1) % 6 == 0 && s + 1 != 24) bar();
        if (s + 1 < 48) {
#pragma unroll
          for (int i = 0; i < 5; ++i) wf[(s + 1) & 1][i] = lds16(L, wb[((s + 1) % 30) / 10] + ((s + 1) % 10) * 5120 + i * 1024);
        }
#pragma unroll
        for (int i = 0; i < 5; ++i) acc[i] = mfma(wf[s & 1][i], ha[s % WIN], acc[i]);
        if (s + WIN < 48) ha[s % WIN] = ld16(rw, lane * 16, hso + (s + WIN) * 1024);
        if (s + 1 < 48) { __builtin_amdgcn_sched_group_barrier(0x100, 5, 0); __builtin_amdgcn_sched_group_barrier(0x8, 5, 0); }
        sched();
      }
      stamp(k, w, sb + 16);
      x_epilogue(k, p, w, acc, res, 2, layer == 1);
      bar_st();
      stamp(k, w, sb + 17);
      if (layer == 0) publish(k, EV_PER_LAYER + EV_X, k.P * 16, 1, 16, k.kq * 4 + k.np);
      stamp(k, w, sb + 18);
    }
  }
  STAMP_OUT(k, w);
  if (k.b == 0 && w == 0 && k.lane == 0) *(GAS u64*)k.ws = k.tag;
}

DI void stats_load(const Ctx& k, rsrc_t rw, int xb, int tok0, u32x2 (&sv)[8]) {
#pragma unroll
  for (int j = 0; j < 8; ++j) sv[j] = __builtin_amdgcn_raw_buffer_load_b64(rw, k.lane * 8, OFF_ST + xb * ST_BUF + j * 16384 + tok0 * 8, SC1);
}
DI void stats_table(const Ctx& k, int a, u32x2 (&sv)[8], float sign) {
#pragma unroll
  for (int j = 0; j < 8; ++j) pin(sv[j]);
  float s = 0.f, ss = 0.f;
#pragma unroll
  for (int j = 0; j < 8; ++j) { s += __uint_as_float(sv[j][0]); ss += __uint_as_float(sv[j][1]); }
  const float m = s * (1.f / 640.f), var = fmaxf(ss * (1.f / 640.f) - m * m, 0.f);
  *(LAS float*)(k.lds + L_MEAN + (64 * a + k.lane) * 4) = m;
  *(LAS float*)(k.lds + L_RSTD + (64 * a + k.lane) * 4) = sign / sqrtf(var + 1e-5f);
}
DI void colsum1(const Ctx& k, int a4) {
  const int q = k.lane >> 4, c = k.lane & 15;
  const unsigned rd = (((c >> 3) * 1024 + (c & 7) * 128 + ((q ^ (c >> 1)) * 16)) ^ ((a4 & 1) * 64)) + (a4 >> 1) * 10240 + L_W;
  float cs[5] = {0.f, 0.f, 0.f, 0.f, 0.f};
#pragma unroll 1
  for (int j = 0; j < 5; ++j)
#pragma unroll
    for (int i = 0; i < 5; ++i) cs[i] = dot_ones(lds16(k.lds, rd + j * 20480 + i * 2048), cs[i]);
#pragma unroll
  for (int i = 0; i < 5; ++i) { cs[i] = sum_q(cs[i]); if (q == 0) *(LAS float*)(k.lds + L_CS1 + (16 * i + c) * 16 + a4 * 4) = cs[i]; }
}

DI void wloader(const Ctx& k, const Params& p, const int a) {
  const int lane = k.lane;
  LAS char* const L = k.lds;
  const rsrc_t rw = mk(k.ws, WS_END);
  const rsrc_t rd = mk(k.mat ? p.wdi : p.wdr, 640 * 640 * 2), rs = mk((k.kq >> 1) ? p.wsi : p.wsr, 2048 * 2048 * 2);
  const unsigned sw = (((lane & 7) ^ (4 * a + (lane >> 4))) * 16);
  const unsigned vo1 = (lane >> 3) * 1280 + sw, vo2 = (lane >> 3) * 4096 + sw;
  const unsigned so1 = (80 * k.ct1 + 8 * a) * 1280, so2 = (128 * k.P + 8 * a) * 4096 + (k.kq & 1) * 2048;
  const unsigned so3 = OFF_W3 + (4 * k.kq + k.np) * 20 * 12288 + a * 12288, so4 = OFF_W4 + (2 * k.np + (k.kq & 1)) * 48 * 5120 + a * 15360;
  auto w1 = [&]() {
#pragma unroll 1
    for (int t = 0; t < 10; ++t) {
      waitv<(PFDEPTH > 5 ? PFDEPTH - 5 : 0)>();
#pragma unroll
      for (int j = 0; j < 5; ++j) dma<0, 0>(rd, L + L_W + t * 10240 + (2 * j + a) * 1024, vo1, so1 + j * 16 * 1280 + t * 128);
    }
  };
  auto w2 = [&](int t, int slot) {
#pragma unroll
    for (int j = 0; j < 8; ++j) dma<0, 0>(rs, L + L_W + slot * 16384 + (2 * j + a) * 1024, vo2, so2 + j * 16 * 4096 + t * 128);
  };
  auto w3 = [&](int t, int slot) { dma_lin<SC1, 12>(rw, L + L_W + slot * 24576 + a * 12288, lane * 16, so3 + t * 24576); };
  auto w4 = [&](int ch, int slot) { dma_lin<SC1, 15>(rw, L + L_W + slot * 30720 + a * 15360, lane * 16, so4 + ch * 30720); };
  STAMP_DECL;
  w1();
#pragma unroll 1
  for (int layer = 0; layer < 2; ++layer) {
    const int sb = 3 + layer * 24;
    waitv<0>();
    stamp(k, 4 + a, sb + 0);
    bar(); bar();
    if (layer == 0) colsum1(k, a);
    bar();
    bar_st();
#pragma unroll 1
    for (int t = 0; t < 6; ++t) { waitv<(PFDEPTH > 8 ? PFDEPTH - 8 : 0)>(); w2(t, t < 4 ? t + 2 : t - 4); }
    waitv<0>();
    stamp(k, 4 + a, sb + 4);
    bar(); bar();
    bar();
    int slot = 2;
#pragma unroll 1
    for (int t = 1; t <= 10; ++t) { waitv<32>(); bar(); if (WLSLEEP) __builtin_amdgcn_s_sleep(WLSLEEP); w2(t + 5, slot); slot = slot == 5 ? 0 : slot + 1; }
    waitv<32>(); bar();
    if (layer == 0) wait_mb(k, EV_RT, k.me, 16);
    waitv<0>(); bar();
    w3(2, 0); bar(); w3(3, 1); bar(); bar(); w3(0, 2);
    bar();
    bar_st();
    w3(1, 3);
    waitv<0>();
    stamp(k, 4 + a, sb + 11);
    bar(); bar(); bar(); bar();
    bar();
#pragma unroll 1
    for (int t = 1; t <= 6; ++t) { waitv<24>(); bar(); if (WLSLEEP) __builtin_amdgcn_s_sleep(WLSLEEP); w3(t + 3, (t + 1) & 3); }
    u32x2 sv[8], cv[10];
    stats_load(k, rw, 2 * layer + 1, 128 * k.P + 64 * a, sv);
    if (layer == 0) {
#pragma unroll
      for (int j = 0; j < 10; ++j) cv[j] = __builtin_amdgcn_raw_buffer_load_b64(rw, lane * 8, OFF_CS + (j * 3072 + a * 1536 + 96 * (4 * k.kq + k.np)) * 4, SC1);
    }
    waitv<24>(); bar(); waitv<12>(); bar();
    stats_table(k, a, sv, 1.f);
    if (layer == 0) {
      f32x2 s = {0.f, 0.f};
#pragma unroll
      for (int j = 0; j < 10; ++j) { pin(cv[j]); s += __builtin_bit_cast(f32x2, cv[j]); }
      if (lane < 48) *(LAS f32x2*)(L + L_CS3 + (96 * a + 2 * lane) * 4) = s;
    }
    waitv<0>();
    w4(0, 0);
    bar();
    w4(1, 1);
    bar();
    bar_st();
#pragma unroll 1
    for (int ch = 2; ch < 5; ++ch) { waitv<(PFDEPTH > 45 ? 30 : (PFDEPTH > 15 ? PFDEPTH - 15 : 0))>(); w4(ch, ch); }
    waitv<0>();
    stamp(k, 4 + a, sb + 15);
    bar(); bar();
#pragma unroll 1
    for (int ch = 5; ch < 8; ++ch) { bar(); w4(ch, ch - 5); }
    waitv<30>(); bar(); waitv<15>(); bar(); waitv<0>(); bar();
    bar();
    bar_st();
    if (layer == 0) w1();
  }
  STAMP_OUT(k, 4 + a);
}

DI void aloader(const Ctx& k, const Params& p, const int a) {
  const int lane = k.lane;
  LAS char* const L = k.lds;
  const rsrc_t rw = mk(k.ws, WS_END);
  STAMP_DECL;
  if (ALPRIO) __builtin_amdgcn_s_setprio(ALPRIO);
  auto poll = [&](int ev, int wg, int n) { bar(); if (a == 0) wait_mb(k, ev, wg, n); bar(); };
#pragma unroll 1
  for (int layer = 0; layer < 2; ++layer) {
    const int ev0 = layer * EV_PER_LAYER, sb = 3 + layer * 24;
    {
      poll(ev0 + EV_X, k.me1, 16);
      u32x2 sv[8];
      stats_load(k, rw, 2 * layer, 128 * k.P1 + 64 * a, sv);
      if (layer == 0) colsum1(k, 2 + a);
      stats_table(k, a, sv, k.mat ? -1.f : 1.f);
      bar();
      bar_st();
    }
    {
      const unsigned so = OFF_AB + layer * AB_BUF + (((k.kq >> 1) * 4 + k.np) * 64 + 32 * (k.kq & 1) + a) * 10240;
      auto ab = [&](int t, int slot) { dma_lin<SC1, 10>(rw, L + L_ACT + slot * 20480 + a * 10240, lane * 16, so + t * 20480); };
      poll(ev0 + EV_AB, k.me, 16);
      stamp(k, 6 + a, sb + 3);
      if (layer == 0 && a == 0) push_mb(k, EV_RT, k.P * 16, 1, 16, k.kq * 4 + k.np);
#pragma unroll 1
      for (int t = 0; t < 3; ++t) ab(t, t);
      waitv<20>();
      bar();
      int slot = 0;
#pragma unroll 1
      for (int t = 1; t <= 13; ++t) { waitv<10>(); bar(); ab(t + 2, slot); slot = slot == 2 ? 0 : slot + 1; }
      waitv<10>(); bar(); waitv<0>(); bar();
      bar();
      bar_st();
    }
    {
      const unsigned so = OFF_XT + (2 * layer + 1) * XT_BUF + (8 * k.P + 4 * a) * 20480;
      auto xt = [&](int t, int slot) {
#pragma unroll
        for (int j = 0; j < 4; ++j) dma_lin<SC1, 2>(rw, L + L_ACT + slot * 16384 + (4 * a + j) * 2048, lane * 16, so + j * 20480 + t * 2048);
      };
      poll(ev0 + EV_EX, k.me, 4);
      poll(ev0 + EV_X1, k.me, 16);
      stamp(k, 6 + a, sb + 10);
#pragma unroll 1
      for (int t = 0; t < 3; ++t) xt(t, t);
      waitv<16>();
      bar();
      int slot = 0;
#pragma unroll 1
      for (int t = 1; t <= 7; ++t) { waitv<8>(); bar(); xt(t + 2, slot); slot = slot == 2 ? 0 : slot + 1; }
      waitv<8>(); bar(); waitv<0>(); bar();
      bar();
      bar_st();
    }
    poll(ev0 + EV_H, k.me, 16);
#pragma unroll 1
    for (int j = 0; j < 7; ++j) bar();
    bar_st();
  }
  STAMP_OUT(k, 6 + a);
}

__global__ void __launch_bounds__(512, 2) fnet_glu_kernel(Params p) {
  __shared__ __attribute__((aligned(16))) char smem[163840];
  Ctx k;
  k.ws = p.ws; k.lds = (LAS char*)smem; k.lane = threadIdx.x & 63; k.b = blockIdx.x;
  { const u64 e = *(const GAS u64*)p.ws; k.tag = e * 6364136223846793005ull + 1442695040888963407ull; }
  const int x = k.b & 7, i = k.b >> 3;
  k.kq = x >> 1; k.P = 8 * (x & 1) + (i >> 2); k.np = i & 3; k.me = k.P * 16 + k.kq * 4 + k.np;
  k.P1 = 4 * (x >> 1) + (i >> 3); k.mat = x & 1; k.ct1 = i & 7; k.me1 = k.P1 * 16 + k.mat * 8 + k.ct1;
  const int w = __builtin_amdgcn_readfirstlane(threadIdx.x >> 6);
  if (threadIdx.x == 256) *(LAS unsigned*)(k.lds + L_CNT) = 0u;
  bar();
  if (w < 4) consumer(k, p, w);
  else if (w < 6) wloader(k, p, w - 4);
  else aloader(k, p, w - 6);
}
}

extern "C" void kernel_launch(void* const* d_in, const int* in_sizes, int n_in, void* d_out, int out_size, void* d_ws, size_t ws_size, hipStream_t stream) {
  Params p;
  p.x0 = (const char*)d_in[0]; p.wsr = (const char*)d_in[1]; p.wsi = (const char*)d_in[2]; p.wdr = (const char*)d_in[3]; p.wdi = (const char*)d_in[4];
  p.wg = (const char*)d_in[6]; p.wu = (const char*)d_in[7]; p.wdn = (const char*)d_in[8]; p.out = (char*)d_out; p.ws = (char*)d_ws;
  fnet_glu_kernel<<<256, 512, 0, stream>>>(p);
}
```

```cpp
#include <hip/hip_runtime.h>

typedef unsigned long long u64;
typedef short bf16x8 __attribute__((ext_vector_type(8)));
typedef short s16x4 __attribute__((ext_vector_type(4)));
typedef float f32x4 __attribute__((ext_vector_type(4)));
typedef float f32x2 __attribute__((ext_vector_type(2)));
typedef unsigned u32x4 __attribute__((ext_vector_type(4)));
typedef unsigned u32x2 __attribute__((ext_vector_type(2)));
typedef __bf16 bf2 __attribute__((ext_vector_type(2)));
#define LAS __attribute__((address_space(3)))
#define GAS __attribute__((address_space(1)))
#define DI __device__ __forceinline__
typedef __amdgpu_buffer_rsrc_t rsrc_t;

#define STAMPS 0
#define WIN1 5
#define WIN4 5
#define TOUCH 0
#define CODE_BYTES 0
#define W1FIRST 3
namespace {
constexpr unsigned OFF_MB = 4096, MB_EV = 256 * 128;
constexpr int EV_X = 0, EV_AB = 1, EV_EX = 2, EV_X1 = 3, EV_H = 4, EV_PER_LAYER = 5, EV_RT = 10;
constexpr unsigned OFF_CS = 0x60000;
constexpr unsigned OFF_ST = 0x80000, ST_BUF = 8 * 2048 * 8;
constexpr unsigned OFF_XT = 0x100000, XT_BUF = 2048 * 640 * 2;
constexpr unsigned OFF_AB = OFF_XT + 4 * XT_BUF, AB_BUF = 2048 * 1280 * 2;
constexpr unsigned OFF_HT = OFF_AB + 2 * AB_BUF, HT_BUF = 2048 * 1536 * 2;
constexpr unsigned OFF_EX = OFF_HT + 2 * HT_BUF, EX_BUF = 256 * 12 * 2560;
constexpr unsigned OFF_W3 = OFF_EX + 2 * EX_BUF;
constexpr unsigned OFF_W4 = OFF_W3 + 16 * 20 * 12288;
constexpr unsigned WS_END = OFF_W4 + 8 * 48 * 5120;
constexpr unsigned OFF_STAMP = 0x6000000;
static_assert(OFF_MB + 11 * MB_EV <= OFF_CS && OFF_CS + 10 * 3072 * 4 <= OFF_ST && OFF_ST + 4 * ST_BUF <= OFF_XT && WS_END <= OFF_STAMP, "ws map");
constexpr unsigned L_MEAN = 0, L_RSTD = 512, L_CS1 = 1024, L_CS3 = 2304, L_JUNK = 3072;
constexpr unsigned L_D = 4096;
constexpr unsigned L_WR = L_D + 61440, L_W4 = L_D + 6144, L_RT = L_D + 102400, L_X0 = L_D + 126976, L_RED = L_D + 110592;
constexpr int SC1 = 16;
constexpr unsigned SPIN_CAP = 1u << 16;

DI rsrc_t mk(const void* p, unsigned bytes) { return __builtin_amdgcn_make_buffer_rsrc((void*)p, 0, bytes, 0x00020000); }
template <int N> DI void waitv() { asm volatile("s_waitcnt vmcnt(%0)" ::"n"(N) : "memory"); }
DI void bar() { asm volatile("s_waitcnt lgkmcnt(0)\n\ts_barrier" ::: "memory"); }
DI void drain_bar() { asm volatile("s_waitcnt vmcnt(0) lgkmcnt(0)\n\ts_barrier" ::: "memory"); }
DI void sched() { __builtin_amdgcn_sched_barrier(0); }
template <class T> DI void pin(T& x) { asm volatile("" : "+v"(x)); }
template <class T> DI void pins(T& x) { asm volatile("" : "+s"(x)); }
template <int AUX, int IMM> DI void dma(rsrc_t r, LAS char* l, unsigned vo, unsigned so) { __builtin_amdgcn_raw_ptr_buffer_load_lds(r, (LAS void*)l, 16, vo, so, IMM, AUX); }
template <int AUX> DI void touch(rsrc_t r, LAS char* l, unsigned vo, unsigned so) { __builtin_amdgcn_raw_ptr_buffer_load_lds(r, (LAS void*)l, 4, vo, so, 0, AUX); }
template <int AUX, int N> DI void dma_lin(rsrc_t r, LAS char* l, unsigned vo, unsigned so) {
#pragma unroll
  for (int j = 0; j < N; j += 4) {
    dma<AUX, 0>(r, l + j * 1024, vo, so + j * 1024);
    if (j + 1 < N) dma<AUX, 1024>(r, l + j * 1024, vo, so + j * 1024);
    if (j + 2 < N) dma<AUX, 2048>(r, l + j * 1024, vo, so + j * 1024);
    if (j + 3 < N) dma<AUX, 3072>(r, l + j * 1024, vo, so + j * 1024);
  }
}
DI bf16x8 ld16(rsrc_t r, unsigned vo, unsigned so) { return __builtin_bit_cast(bf16x8, __builtin_amdgcn_raw_buffer_load_b128(r, vo, so, SC1)); }
DI void st16(rsrc_t r, u32x4 v, unsigned vo) { __builtin_amdgcn_raw_buffer_store_b128(v, r, vo, 0, SC1); }
DI bf16x8 lds16(LAS char* l, unsigned a) { return *(LAS bf16x8*)(l + a); }
DI unsigned pk(float lo, float hi) { f32x2 v = {lo, hi}; return __builtin_bit_cast(unsigned, __builtin_convertvector(v, bf2)); }
DI float blo(unsigned u) { return __uint_as_float(u << 16); }
DI float bhi(unsigned u) { return __uint_as_float(u & 0xffff0000u); }
DI f32x4 unpk(unsigned a, unsigned b) { return (f32x4){blo(a), bhi(a), blo(b), bhi(b)}; }
DI f32x4 mfma(bf16x8 a, bf16x8 b, f32x4 c) { return __builtin_amdgcn_mfma_f32_16x16x32_bf16(a, b, c, 0, 0, 0); }
DI float dot2(unsigned a, unsigned b, float s) { return __builtin_amdgcn_fdot2_f32_bf16(__builtin_bit_cast(bf2, a), __builtin_bit_cast(bf2, b), s, false); }
DI float dot_ones(bf16x8 f, float s) {
  const u32x4 u = __builtin_bit_cast(u32x4, f);
#pragma unroll
  for (int j = 0; j < 4; ++j) s = dot2(u[j], 0x3f803f80u, s);
  return s;
}
DI float sum_q(float s) { s += __shfl_xor(s, 16); s += __shfl_xor(s, 32); return s; }
DI u32x4 swap_pair(unsigned a0, unsigned a1, unsigned b0, unsigned b1) {
  const auto r0 = __builtin_amdgcn_permlane16_swap(a0, b0, false, false);
  const auto r1 = __builtin_amdgcn_permlane16_swap(a1, b1, false, false);
  return (u32x4){r0[0], r1[0], r0[1], r1[1]};
}

struct Ctx {
  char* ws; LAS char* lds; int lane, b;
  int kq, P, np;
  int P1, mat, ct1;
};
#define STAMP_DECL
#define stamp(k, w, id) do { (void)(id); } while (0)
#define STAMP_OUT(k, w)

DI void x_store(rsrc_t rw, int lane, unsigned vx, unsigned vs, u32x4 o0, u32x4 o1, u32x4 o2, const unsigned (&res)[10]) {
  st16(rw, o0, vx); st16(rw, o1, vx + 1024);
  if (!(lane & 16)) st16(rw, o2, vx + 2048);
  float s = 0.f, ss = 0.f;
#pragma unroll
  for (int j = 0; j < 10; ++j) { s = dot2(res[j], 0x3f803f80u, s); ss = dot2(res[j], res[j], ss); }
  const auto r = __builtin_amdgcn_permlane16_swap(__float_as_uint(s), __float_as_uint(ss), false, false);
  const unsigned t = __float_as_uint(__uint_as_float(r[0]) + __uint_as_float(r[1]));
  const auto r2 = __builtin_amdgcn_permlane32_swap(t, t, false, false);
  const float u = __uint_as_float(r2[0]) + __uint_as_float(r2[1]);
  if (lane < 32) __builtin_amdgcn_raw_buffer_store_b32(__float_as_uint(u), rw, vs, 0, SC1);
}

#define PHASE_LANE() Ctx k = k0; asm volatile("" : "+v"(k.lane), "+s"(k.P), "+s"(k.kq), "+s"(k.np), "+s"(k.P1), "+s"(k.ct1), "+s"(k.mat)); \
  const int tfo = 8 * k.P + 4 * (k.kq >> 1) + w, cto = 2 * k.np + (k.kq & 1); (void)tfo; (void)cto; \
  const int lane = k.lane, q = lane >> 4, c = lane & 15, qs = 2 * (q & 1) + (q >> 1); (void)qs; \
  const unsigned rowrd = (c >> 3) * 1024 + (c & 7) * 128 + ((q ^ (c >> 1)) * 16); (void)rowrd
#define X_ADDR(xb) unsigned vx = OFF_XT + (xb) * XT_BUF + tfo * 20480 + (10 * cto + qs) * 256 + c * 16, vs = OFF_ST + (xb) * ST_BUF + cto * 16384 + tfo * 128 + lane * 4; pin(vx); pin(vs)

DI void consumer(const Ctx& k0, const char* x0, const char* wg, const char* wu, const char* wdn, char* out, const int w) {
  LAS char* const L = k0.lds;
  const rsrc_t rw = mk(k0.ws, WS_END);
  unsigned res[10];
  STAMP_DECL;
  stamp(k0, w, 0);
  {
    PHASE_LANE();
    const rsrc_t rx = mk(x0, 2048 * 640 * 2);
#pragma unroll
    for (int j = 0; j < 3; ++j) {
      const int u = 64 * j + lane, r = u / 10;
      dma<0, 0>(rx, L + L_X0 + w * 3072 + j * 1024, u < 160 ? r * 1280 + (u - 10 * r) * 16 : 0x7fffffff, 16 * tfo * 1280 + cto * 160);
    }
    sched();
    const int u = k.b + 256 * w, m = u / 240, r = u - 240 * m;
    const bool rt = u < 720;
    const int ku = m < 2 ? r / 24 : r / 10, stp = m < 2 ? r - 24 * ku : r - 10 * ku;
    X_ADDR(0);
    LAS char* a = L + L_X0 + w * 3072 + c * 160;
    waitv<0>();
    stamp(k, w, 50);
    const u32x4 o0 = *(LAS u32x4*)(a + qs * 16), o1 = *(LAS u32x4*)(a + qs * 16 + 64), o2 = *(LAS u32x4*)(a + (q >> 1) * 16 + 128);
#pragma unroll
    for (int i = 0; i < 5; ++i) { const u32x2 t = *(LAS u32x2*)(a + 32 * i + 8 * q); res[2 * i] = t[0]; res[2 * i + 1] = t[1]; }
    x_store(rw, lane, vx, vs, o0, o1, o2, res);
    stamp(k, w, 51);
    drain_bar();
    stamp(k, w, 1);
    if (rt) {
      const unsigned stride = m < 2 ? 3072 : 1280;
      const rsrc_t rs = mk(m == 0 ? wg : (m == 1 ? wu : wdn), 640 * 1536 * 2);
#pragma unroll
      for (int j = 0; j < 8; ++j) dma<0, 0>(rs, L + L_RT + w * 8192 + j * 1024, (lane >> 3) * stride + (lane & 7) * 16, (64 * ku + 8 * j) * stride + 128 * stp);
      const int dv = m < 2 ? 6 : 5, s_g = m < 2 ? 20 * 12288 : 48 * 5120, s_k = m < 2 ? 12288 : 5120, s_f = m < 2 ? 2048 : 1024;
      const unsigned base = (m < 2 ? OFF_W3 + m * 1024 : OFF_W4) + 2 * ku * s_k + lane * 16;
      float cs[4] = {0.f, 0.f, 0.f, 0.f};
      waitv<0>();
#pragma unroll
      for (int n = 0; n < 4; ++n) {
        const int nf = 4 * stp + n, g = m < 2 ? nf / 6 : nf / 5;
#pragma unroll
        for (int kk = 0; kk < 2; ++kk) {
          LAS char* t = L + L_RT + w * 8192 + (4 * kk + q) * 1024 + (c >> 2) * 128 + 32 * n + (c & 3) * 8;
          const u32x2 lo = __builtin_bit_cast(u32x2, __builtin_amdgcn_ds_read_tr16_b64_v4i16((LAS s16x4*)t));
          const u32x2 hi = __builtin_bit_cast(u32x2, __builtin_amdgcn_ds_read_tr16_b64_v4i16((LAS s16x4*)(t + 512)));
          const u32x4 f = {lo[0], lo[1], hi[0], hi[1]};
          st16(rw, f, base + g * s_g + (nf - dv * g) * s_f + kk * s_k);
          cs[n] = dot_ones(__builtin_bit_cast(bf16x8, f), cs[n]);
        }
      }
      if (m < 2) {
#pragma unroll
        for (int n = 0; n < 4; ++n) cs[n] = sum_q(cs[n]);
        const float o = q == 0 ? cs[0] : (q == 1 ? cs[1] : (q == 2 ? cs[2] : cs[3]));
        __builtin_amdgcn_raw_buffer_store_b32(__float_as_uint(o), rw, lane * 4, OFF_CS + (ku * 3072 + m * 1536 + 64 * stp) * 4, SC1);
      }
    }
    stamp(k, w, 2);
  }
#pragma unroll 1
  for (int layer = 0; layer < 2; ++layer) {
    const int sb = 3 + layer * 24;
    {
      PHASE_LANE();
      constexpr int WIN = WIN1;
      unsigned wb[2][2] = {{L_D + rowrd, L_D + rowrd + 51200}, {L_D + (rowrd ^ 64), L_D + (rowrd ^ 64) + 51200}};
      unsigned xv[2] = {OFF_XT + 2 * layer * XT_BUF + (8 * k.P1 + 2 * w) * 20480 + lane * 16, 0}; xv[1] = xv[0] + 20480;
      unsigned vo = OFF_AB + layer * AB_BUF + ((k.mat * 4 + (k.ct1 >> 1)) * 64 + 4 * k.P1 + w) * 10240 + 5 * (k.ct1 & 1) * 1024 + (qs * 16 + c) * 16;
      unsigned ta = (32 * w + 4 * q) * 4, tc = L_CS1 + c * 16;
      pin(wb[0][0]); pin(wb[0][1]); pin(wb[1][0]); pin(wb[1][1]); pin(xv[0]); pin(xv[1]); pin(vo); pin(ta); pin(tc);
      bf16x8 xa[WIN][2], wf[2][5];
      f32x4 acc[2][5];
      bar();
#pragma unroll
      for (int i = 0; i < 5; ++i) wf[0][i] = lds16(L, wb[0][0] + i * 2048);
      bar();
      stamp(k, w, sb + 0);
#pragma unroll
      for (int s = 0; s < WIN; ++s) { xa[s][0] = ld16(rw, xv[0] + (s & 3) * 1024, (s >> 2) * 4096); xa[s][1] = ld16(rw, xv[1] + (s & 3) * 1024, (s >> 2) * 4096); sched(); }
#pragma unroll
      for (int i = 0; i < 5; ++i) { acc[0][i] = (f32x4){0.f, 0.f, 0.f, 0.f}; acc[1][i] = (f32x4){0.f, 0.f, 0.f, 0.f}; }
      f32x4 mean[2], rstd[2], cs4[5];
#pragma unroll
      for (int s = 0; s < 20; ++s) {
        if (s + 1 < 20) {
#pragma unroll
          for (int i = 0; i < 5; ++i) wf[(s + 1) & 1][i] = lds16(L, wb[(s + 1) & 1][(s + 1) / 10] + (((s + 1) >> 1) % 5) * 10240 + i * 2048);
        } else {
          bar();
#pragma unroll
          for (int i = 0; i < 5; ++i) cs4[i] = *(LAS f32x4*)(L + tc + i * 256);
#pragma unroll
          for (int t = 0; t < 2; ++t) { mean[t] = *(LAS f32x4*)(L + ta + L_MEAN + 64 * t); rstd[t] = *(LAS f32x4*)(L + ta + L_RSTD + 64 * t); }
          sched();
        }
#pragma unroll
        for (int i = 0; i < 5; ++i) { acc[0][i] = mfma(xa[s % WIN][0], wf[s & 1][i], acc[0][i]); acc[1][i] = mfma(xa[s % WIN][1], wf[s & 1][i], acc[1][i]); }
        if (s + WIN < 20) { xa[s % WIN][0] = ld16(rw, xv[0] + ((s + WIN) & 3) * 1024, ((s + WIN) >> 2) * 4096); xa[s % WIN][1] = ld16(rw, xv[1] + ((s + WIN) & 3) * 1024, ((s + WIN) >> 2) * 4096); }
        if (s + 1 < 20) {
#pragma unroll
          for (int j = 0; j < 5; ++j) { __builtin_amdgcn_sched_group_barrier(0x100, 1, 0); __builtin_amdgcn_sched_group_barrier(0x8, 1, 0); }
          __builtin_amdgcn_sched_group_barrier(0x8, 5, 0);
          sched();
        }
      }
      stamp(k, w, sb + 1);
#pragma unroll
      for (int i = 0; i < 5; ++i) {
        const float cs = (cs4[i][0] + cs4[i][1]) + (cs4[i][2] + cs4[i][3]);
        const f32x4 a = (mean[0] * cs + acc[0][i]) * rstd[0], bb = (mean[1] * cs + acc[1][i]) * rstd[1];
        st16(rw, swap_pair(pk(a[0], a[1]), pk(a[2], a[3]), pk(bb[0], bb[1]), pk(bb[2], bb[3])), vo + i * 1024);
      }
      stamp(k, w, sb + 2);
      drain_bar();
      stamp(k, w, sb + 3);
    }
    {
      PHASE_LANE();
      const int wm = w >> 1, wn = w & 1;
      f32x4 acc[4][5];
      bf16x8 fa[2][5], fb[2][4];
#pragma unroll
      for (int t = 0; t < 4; ++t)
#pragma unroll
        for (int i = 0; i < 5; ++i) { acc[t][i] = (f32x4){0.f, 0.f, 0.f, 0.f}; pin(acc[t][i]); }
      unsigned wb = L_WR + wm * 8192 + rowrd, ab = L_D + wn * 5120 + lane * 16;
      unsigned ws_slot = 49152, ab_slot = 0;
      unsigned ev = OFF_EX + layer * EX_BUF + ((k.P * 16 + w * 4 + k.np) * 3 + ((k.kq - w) & 3) - 1) * 10240 + lane * 16;
      pin(wb); pin(ab); pin(ev);
      bar();
#pragma unroll
      for (int t = 0; t < 4; ++t) fb[0][t] = lds16(L, wb + 49152 + t * 2048);
      bar();
      bar();
      stamp(k, w, sb + 4);
#pragma unroll
      for (int i = 0; i < 5; ++i) fa[0][i] = lds16(L, ab + i * 1024);
#pragma unroll 1
      for (int kt = 0; kt < 16; ++kt) {
#pragma unroll
        for (int i = 0; i < 5; ++i) fa[1][i] = lds16(L, ab + ab_slot + 10240 + i * 1024);
#pragma unroll
        for (int t = 0; t < 4; ++t) fb[1][t] = lds16(L, (wb ^ 64) + ws_slot + t * 2048);
#pragma unroll
        for (int t = 0; t < 4; ++t)
#pragma unroll
          for (int i = 0; i < 5; ++i) acc[t][i] = mfma(fa[0][i], fb[0][t], acc[t][i]);
#pragma unroll
        for (int j = 0; j < 9; ++j) { __builtin_amdgcn_sched_group_barrier(0x8, 1, 0); __builtin_amdgcn_sched_group_barrier(0x100, 1, 0); }
        __builtin_amdgcn_sched_group_barrier(0x8, 11, 0);
        sched();
        ab_slot = ab_slot == 40960 ? 0 : ab_slot + 20480; ws_slot = ws_slot == 81920 ? 0 : ws_slot + 16384;
        bar();
        sched();
#pragma unroll
        for (int i = 0; i < 5; ++i) fa[0][i] = lds16(L, ab + ab_slot + i * 1024);
#pragma unroll
        for (int t = 0; t < 4; ++t) fb[0][t] = lds16(L, wb + ws_slot + t * 2048);
#pragma unroll
        for (int t = 0; t < 4; ++t)
#pragma unroll
          for (int i = 0; i < 5; ++i) acc[t][i] = mfma(fa[1][i], fb[1][t], acc[t][i]);
#pragma unroll
        for (int j = 0; j < 9; ++j) { __builtin_amdgcn_sched_group_barrier(0x8, 1, 1); __builtin_amdgcn_sched_group_barrier(0x100, 1, 1); }
        __builtin_amdgcn_sched_group_barrier(0x8, 11, 1);
        sched();
      }
      stamp(k, w, sb + 5);
      if (w == k.kq) {
#pragma unroll
        for (int t = 0; t < 4; ++t)
#pragma unroll
          for (int i = 0; i < 5; ++i) *(LAS f32x4*)(L + L_D + (t * 5 + i) * 1024 + lane * 16) = acc[t][i];
      } else {
        const unsigned ev8 = ev - lane * 8;
#pragma unroll
        for (int t = 0; t < 4; ++t) {
          st16(rw, (u32x4){pk(acc[t][0][0], acc[t][0][1]), pk(acc[t][0][2], acc[t][0][3]), pk(acc[t][1][0], acc[t][1][1]), pk(acc[t][1][2], acc[t][1][3])}, ev + t * 2560);
          st16(rw, (u32x4){pk(acc[t][2][0], acc[t][2][1]), pk(acc[t][2][2], acc[t][2][3]), pk(acc[t][3][0], acc[t][3][1]), pk(acc[t][3][2], acc[t][3][3])}, ev + t * 2560 + 1024);
          __builtin_amdgcn_raw_buffer_store_b64((u32x2){pk(acc[t][4][0], acc[t][4][1]), pk(acc[t][4][2], acc[t][4][3])}, rw, ev8, t * 2560 + 2048, SC1);
        }
      }
      stamp(k, w, sb + 6);
      drain_bar();
      stamp(k, w, sb + 7);
      f32x4 v[5];
#pragma unroll
      for (int i = 0; i < 5; ++i) { v[i] = *(LAS f32x4*)(L + L_D + (w * 5 + i) * 1024 + lane * 16) + unpk(res[2 * i], res[2 * i + 1]); pin(v[i]); }
      unsigned rv = OFF_EX + layer * EX_BUF + (k.P * 16 + k.kq * 4 + k.np) * 30720 + w * 2560 + lane * 16, rv8 = rv - lane * 8;
      pin(rv); pin(rv8);
      X_ADDR(2 * layer + 1);
      bar(); bar();
      stamp(k, w, sb + 8);
      u32x4 e[3][2]; u32x2 e8[3];
#pragma unroll
      for (int n = 0; n < 3; ++n) {
        e[n][0] = __builtin_amdgcn_raw_buffer_load_b128(rw, rv, n * 10240, SC1);
        e[n][1] = __builtin_amdgcn_raw_buffer_load_b128(rw, rv + 1024, n * 10240, SC1);
        e8[n] = __builtin_amdgcn_raw_buffer_load_b64(rw, rv8 + 2048, n * 10240, SC1);
        sched();
      }
#pragma unroll
      for (int n = 0; n < 3; ++n) {
#pragma unroll
        for (int j = 0; j < 2; ++j) { v[2 * j] += unpk(e[n][j][0], e[n][j][1]); v[2 * j + 1] += unpk(e[n][j][2], e[n][j][3]); }
        v[4] += unpk(e8[n][0], e8[n][1]);
        sched();
      }
#pragma unroll
      for (int i = 0; i < 5; ++i) { res[2 * i] = pk(v[i][0], v[i][1]); res[2 * i + 1] = pk(v[i][2], v[i][3]); }
      x_store(rw, lane, vx, vs, swap_pair(res[0], res[1], res[2], res[3]), swap_pair(res[4], res[5], res[6], res[7]), swap_pair(res[8], res[9], res[8], res[9]), res);
      stamp(k, w, sb + 9);
      drain_bar();
      stamp(k, w, sb + 10);
    }
    {
      PHASE_LANE();
      const int wm = w >> 1, wn = w & 1;
      f32x4 acc[4][6];
      bf16x8 fa[2][6], fb[2][4];
#pragma unroll
      for (int t = 0; t < 4; ++t)
#pragma unroll
        for (int i = 0; i < 6; ++i) { acc[t][i] = (f32x4){0.f, 0.f, 0.f, 0.f}; pin(acc[t][i]); }
      unsigned wb = L_WR + wn * 6144 + lane * 16, xb = L_D + wm * 8192 + lane * 16;
      unsigned w_slot = 49152, x_slot = 0;
      unsigned vo = OFF_HT + layer * HT_BUF + ((8 * k.P + 4 * wm + (q & 1)) * 48 + 3 * (4 * k.kq + k.np)) * 1024 + (6 * wn + (q >> 1)) * 256 + c * 16, vo2 = vo + 98304;
      unsigned ta = (64 * wm + c) * 4, tc = L_CS3 + (48 * wn + 4 * q) * 4;
      pin(wb); pin(xb); pin(vo); pin(vo2); pin(ta); pin(tc);
      bar();
#pragma unroll
      for (int i = 0; i < 6; ++i) fa[0][i] = lds16(L, wb + 49152 + i * 1024);
      bar();
      bar();
      stamp(k, w, sb + 11);
#pragma unroll
      for (int t = 0; t < 4; ++t) fb[0][t] = lds16(L, xb + t * 2048);
#pragma unroll 1
      for (int kt = 0; kt < 10; ++kt) {
#pragma unroll
        for (int i = 0; i < 6; ++i) fa[1][i] = lds16(L, wb + w_slot + 12288 + i * 1024);
#pragma unroll
        for (int t = 0; t < 4; ++t) fb[1][t] = lds16(L, xb + x_slot + 1024 + t * 2048);
#pragma unroll
        for (int t = 0; t < 4; ++t)
#pragma unroll
          for (int i = 0; i < 6; ++i) acc[t][i] = mfma(fa[0][i], fb[0][t], acc[t][i]);
#pragma unroll
        for (int j = 0; j < 10; ++j) { __builtin_amdgcn_sched_group_barrier(0x8, 1, 0); __builtin_amdgcn_sched_group_barrier(0x100, 1, 0); }
        __builtin_amdgcn_sched_group_barrier(0x8, 14, 0);
        sched();
        x_slot = x_slot == 32768 ? 0 : x_slot + 16384; w_slot = w_slot == 73728 ? 0 : w_slot + 24576;
        bar();
        sched();
#pragma unroll
        for (int i = 0; i < 6; ++i) fa[0][i] = lds16(L, wb + w_slot + i * 1024);
#pragma unroll
        for (int t = 0; t < 4; ++t) fb[0][t] = lds16(L, xb + x_slot + t * 2048);
#pragma unroll
        for (int t = 0; t < 4; ++t)
#pragma unroll
          for (int i = 0; i < 6; ++i) acc[t][i] = mfma(fa[1][i], fb[1][t], acc[t][i]);
#pragma unroll
        for (int j = 0; j < 10; ++j) { __builtin_amdgcn_sched_group_barrier(0x8, 1, 1); __builtin_amdgcn_sched_group_barrier(0x100, 1, 1); }
        __builtin_amdgcn_sched_group_barrier(0x8, 14, 1);
        sched();
      }
      stamp(k, w, sb + 12);
      float mean[4], rstd[4], rl2[4];
#pragma unroll
      for (int t = 0; t < 4; ++t) { mean[t] = *(LAS float*)(L + ta + L_MEAN + 64 * t); rstd[t] = *(LAS float*)(L + ta + L_RSTD + 64 * t); rl2[t] = rstd[t] * -1.44269504089f; }
#pragma unroll
      for (int f = 0; f < 3; ++f) {
        const f32x4 cg = *(LAS f32x4*)(L + tc + 64 * f), cu = *(LAS f32x4*)(L + tc + 384 + 64 * f);
        unsigned h[4][2];
#pragma unroll
        for (int t = 0; t < 4; ++t) {
          f32x4 g = (mean[t] * cg + acc[t][2 * f]) * rl2[t];
          const f32x4 u = (mean[t] * cu + acc[t][2 * f + 1]) * rstd[t];
          asm("" : "+v"(g));
          f32x4 o;
#pragma unroll
          for (int r = 0; r < 4; ++r) o[r] = __builtin_amdgcn_exp2f(g[r]);
          o += 1.f;
#pragma unroll
          for (int r = 0; r < 4; ++r) o[r] = __builtin_amdgcn_rcpf(o[r]);
          o *= u;
          h[t][0] = pk(o[0], o[1]); h[t][1] = pk(o[2], o[3]);
        }
        st16(rw, swap_pair(h[0][0], h[0][1], h[1][0], h[1][1]), vo + f * 512);
        st16(rw, swap_pair(h[2][0], h[2][1], h[3][0], h[3][1]), vo2 + f * 512);
      }
      stamp(k, w, sb + 13);
      drain_bar();
      stamp(k, w, sb + 14);
    }
    {
      PHASE_LANE();
      constexpr int WIN = WIN4;
      const int kh = w & 1;
      unsigned wb[3] = {L_W4 + kh * 5120 + lane * 16, 0, 0}; wb[1] = wb[0] + 51200; wb[2] = wb[0] + 102400;
      unsigned hv[2] = {OFF_HT + layer * HT_BUF + tfo * 49152 + kh * 1024 + lane * 16, OFF_HT + layer * HT_BUF + (tfo ^ 1) * 49152 + kh * 1024 + lane * 16};
      unsigned rd = L_RED + w * 5120 + lane * 16, wr = L_RED + (w ^ 1) * 5120 + lane * 16;
      pin(wb[0]); pin(wb[1]); pin(wb[2]); pin(hv[0]); pin(hv[1]); pin(rd); pin(wr);
      X_ADDR(2);
      unsigned vout = (16 * tfo + c) * 1280 + (10 * cto + qs) * 16;
      pin(vout);
      bf16x8 ha[WIN][2], wf[2][5];
      f32x4 acc[2][5];
#pragma unroll
      for (int i = 0; i < 5; ++i) { acc[0][i] = unpk(res[2 * i], res[2 * i + 1]); pin(acc[0][i]); acc[1][i] = (f32x4){0.f, 0.f, 0.f, 0.f}; }
#pragma unroll
      for (int i = 0; i < 5; ++i) wf[0][i] = lds16(L, wb[0] + i * 1024);
      bar();
      stamp(k, w, sb + 15);
#pragma unroll
      for (int s = 0; s < WIN; ++s) { ha[s][0] = ld16(rw, hv[0] + (s & 1) * 2048, (s >> 1) * 4096); ha[s][1] = ld16(rw, hv[1] + (s & 1) * 2048, (s >> 1) * 4096); sched(); }
#pragma unroll
      for (int s = 0; s < 24; ++s) {
        if (s == 2 || s == 5 || s == 8 || s == 14) bar();
        if (s + 1 < 24) {
#pragma unroll
          for (int i = 0; i < 5; ++i) wf[(s + 1) & 1][i] = lds16(L, wb[((s + 1) % 15) / 5] + ((s + 1) % 5) * 10240 + i * 1024);
        }
#pragma unroll
        for (int i = 0; i < 5; ++i) acc[1][i] = mfma(wf[s & 1][i], ha[s % WIN][1], acc[1][i]);
#pragma unroll
        for (int i = 0; i < 5; ++i) acc[0][i] = mfma(wf[s & 1][i], ha[s % WIN][0], acc[0][i]);
        if (s + WIN < 24) { ha[s % WIN][0] = ld16(rw, hv[0] + ((s + WIN) & 1) * 2048, ((s + WIN) >> 1) * 4096); ha[s % WIN][1] = ld16(rw, hv[1] + ((s + WIN) & 1) * 2048, ((s + WIN) >> 1) * 4096); }
        if (s + 1 < 24) {
#pragma unroll
          for (int j = 0; j < 5; ++j) { __builtin_amdgcn_sched_group_barrier(0x100, 1, 0); __builtin_amdgcn_sched_group_barrier(0x8, 1, 0); }
          __builtin_amdgcn_sched_group_barrier(0x8, 5, 0);
          sched();
        }
      }
#pragma unroll
      for (int i = 0; i < 5; ++i) *(LAS f32x4*)(L + wr + i * 1024) = acc[1][i];
      bar();
      stamp(k, w, sb + 16);
#pragma unroll
      for (int i = 0; i < 5; ++i) { const f32x4 v = acc[0][i] + *(LAS f32x4*)(L + rd + i * 1024); res[2 * i] = pk(v[0], v[1]); res[2 * i + 1] = pk(v[2], v[3]); }
      const u32x4 o0 = swap_pair(res[0], res[1], res[2], res[3]), o1 = swap_pair(res[4], res[5], res[6], res[7]), o2 = swap_pair(res[8], res[9], res[8], res[9]);
      if (layer == 1) {
        const rsrc_t ro = mk(out, 2048 * 640 * 2);
        __builtin_amdgcn_raw_buffer_store_b128(o0, ro, vout, 0, 0);
        __builtin_amdgcn_raw_buffer_store_b128(o1, ro, vout + 64, 0, 0);
        if (!(q & 1)) __builtin_amdgcn_raw_buffer_store_b128(o2, ro, vout + 128, 0, 0);
        stamp(k, w, sb + 17);
      } else {
        x_store(rw, lane, vx, vs, o0, o1, o2, res);
        stamp(k, w, sb + 17);
        drain_bar();
      }
      stamp(k, w, sb + 18);
    }
  }
  STAMP_OUT(k0, w);
}

DI void stats_load(const Ctx& k, rsrc_t rw, int xb, int tf0, u32x2 (&sv)[8]) {
#pragma unroll
  for (int j = 0; j < 8; ++j) sv[j] = __builtin_amdgcn_raw_buffer_load_b64(rw, k.lane * 8, OFF_ST + xb * ST_BUF + j * 16384 + tf0 * 128, SC1);
}
DI void stats_table(const Ctx& k, int a, u32x2 (&sv)[8], float sign) {
  f32x2 s = {0.f, 0.f};
#pragma unroll
  for (int j = 0; j < 8; ++j) { pin(sv[j]); s += __builtin_bit_cast(f32x2, sv[j]); }
  s *= 1.f / 640.f;
  const f32x2 ss = {__shfl_xor(s[0], 8), __shfl_xor(s[1], 8)};
  if (!(k.lane & 8)) {
    const unsigned t = (64 * a + (k.lane & 48) + 2 * (k.lane & 7)) * 4;
    *(LAS f32x2*)(k.lds + L_MEAN + t) = -s;
    *(LAS f32x2*)(k.lds + L_RSTD + t) = (f32x2){sign / sqrtf(fmaxf(ss[0] - s[0] * s[0], 0.f) + 1e-5f), sign / sqrtf(fmaxf(ss[1] - s[1] * s[1], 0.f) + 1e-5f)};
  }
}

DI void loader(const Ctx& k0, const char* wsp, const char* wdp, int a4) {
  Ctx k = k0;
  const bool isW = a4 < 2;
  LAS char* const L = k.lds;
  const rsrc_t rw = mk(k.ws, WS_END);
  const rsrc_t rs = mk(wsp, 2048 * 2048 * 2);
  auto fresh = [&]() { asm volatile("" : "+v"(k.lane), "+s"(k.P), "+s"(k.kq), "+s"(k.np), "+s"(k.P1), "+s"(k.ct1), "+s"(k.mat), "+s"(a4)); };
#define lane k.lane
#define a (a4 & 1)
#define me (k.P * 16 + k.kq * 4 + k.np)
#define n3 (4 * k.kq + k.np)
#define sw (((lane & 7) ^ (4 * a + (lane >> 4))) * 16)
#define vo2 ((lane >> 3) * 4096 + sw)
#define so2 ((128 * k.P + 8 * a) * 4096 + (k.kq & 1) * 2048)
#define so3 (OFF_W3 + n3 * 20 * 12288 + a * 12288)
#define so4 (OFF_W4 + (2 * k.np + (k.kq & 1)) * 48 * 5120)
  bf16x8 k1[25], k4[3][8];
  u64 tag = 0;
  STAMP_DECL;
  auto w1 = [&](int t0, int t1) {
    const rsrc_t rd = mk(wdp, 640 * 640 * 2);
#pragma unroll 1
    for (int t = t0; t < t1; ++t)
#pragma unroll
      for (int j = 0; j < 5; ++j) dma<0, 0>(rd, L + L_D + t * 10240 + (2 * j + a) * 1024, (lane >> 3) * 1280 + sw, (80 * k.ct1 + 8 * a + 16 * j) * 1280 + t * 128);
  };
  auto w2 = [&](int t, int slot) {
#pragma unroll
    for (int j = 0; j < 8; ++j) dma<0, 0>(rs, L + L_WR + slot * 16384 + (2 * j + a) * 1024, vo2, so2 + j * 16 * 4096 + t * 128);
  };
  auto w2x3 = [&](int t0, int slot0) {
#pragma unroll 1
    for (int t = 0; t < 3; ++t) w2(t0 + t, slot0 + t);
  };
  auto w3 = [&](int t, int slot) { dma_lin<SC1, 12>(rw, L + L_WR + slot * 24576 + a * 12288, lane * 16, so3 + t * 24576); };
  auto w4 = [&](int ch) { dma_lin<SC1, 15>(rw, L + L_W4 + ch * 30720 + a * 15360, lane * 16, so4 + ch * 30720 + a * 15360); };
  auto push = [&](int ev, int wg0, int wgstep, int n, int slot) {
    GAS u64* dst = (GAS u64*)(k.ws + OFF_MB + ev * MB_EV + (wg0 + (lane & (n - 1)) * wgstep) * 128) + slot;
    pin(dst); pin(tag);
    bar();
    if (a4 == 2 && lane < n) __hip_atomic_store(dst, tag, __ATOMIC_RELAXED, __HIP_MEMORY_SCOPE_AGENT);
    fresh();
  };
  auto wait_mb = [&](int ev, int wg, int n) {
    GAS u64* src = (GAS u64*)(k.ws + OFF_MB + ev * MB_EV + wg * 128) + (lane & (n - 1));
#pragma unroll 1
    for (unsigned spins = 0; spins < SPIN_CAP; ++spins) {
      const u64 v = __hip_atomic_load(src, __ATOMIC_RELAXED, __HIP_MEMORY_SCOPE_AGENT);
      if (__all(v == tag)) break;
      __builtin_amdgcn_s_sleep(1);
    }
  };
  auto poll = [&](int ev, int wg, int n) { if (a4 == 2) wait_mb(ev, wg, n); bar(); };

  if (!isW) __builtin_amdgcn_s_setprio(3);
  if (isW) { __builtin_amdgcn_s_sleep(4); w1(0, W1FIRST); }
  { const char* e = k.ws; pins(e); tag = *(const GAS u64*)e * 6364136223846793005ull + 1442695040888963407ull; }
  push(EV_X, k.P * 16, 1, 16, n3);
  if (isW) w1(W1FIRST, 10);
#pragma unroll 1
  for (int layer = 0; layer < 2; ++layer) {
    const int ev0 = layer * EV_PER_LAYER, sb = 3 + layer * 24;
    {
      waitv<0>();
      stamp(k, 4 + a4, sb + 0);
      bar();
      poll(ev0 + EV_X, k.P1 * 16 + k.mat * 8 + k.ct1, 16);
      u32x2 sv[8];
      if (!isW) stats_load(k, rw, 2 * layer, 8 * k.P1 + 4 * a, sv);
      if (layer == 0) {
        float cs[5] = {0.f, 0.f, 0.f, 0.f, 0.f};
        unsigned kb[2] = {L_D + lane * 16 + ((a4 >> 1) * 10 + a) * 1024, 0}; kb[1] = kb[0] + 61440; pin(kb[0]); pin(kb[1]);
#pragma unroll
        for (int j = 0; j < 25; ++j) { k1[j] = lds16(L, kb[j / 15] + ((j / 5) % 3) * 20480 + (j % 5) * 2048); cs[j % 5] = dot_ones(k1[j], cs[j % 5]); }
#pragma unroll
        for (int i = 0; i < 5; ++i) {
          cs[i] += __shfl_xor(cs[i], 1); cs[i] += __shfl_xor(cs[i], 2); cs[i] += __shfl_xor(cs[i], 4);
          if ((lane & 7) < 2) *(LAS float*)(L + L_CS1 + (16 * i + 8 * a + (lane >> 3)) * 16 + ((a4 >> 1) * 2 + (lane & 1)) * 4) = (lane & 1) ? 0.f : cs[i];
        }
      }
      if (!isW) stats_table(k, a, sv, k.mat ? -1.f : 1.f);
      bar();
      if (isW && layer == 0) w2x3(0, 3);
      push(ev0 + EV_AB, (2 * k.mat + (k.P1 >> 3)) * 4 + (k.ct1 >> 1), 16, 16, 2 * (k.P1 & 7) + (k.ct1 & 1));
    }
    if (isW) w2x3(3, 0);
    waitv<0>();
    stamp(k, 4 + a4, sb + 4);
    bar();
    poll(ev0 + EV_AB, me, 16);
    const unsigned soab = OFF_AB + layer * AB_BUF + (((k.kq >> 1) * 4 + k.np) * 64 + 32 * (k.kq & 1)) * 10240;
    if (isW) {
      bar();
      if (TOUCH & 1) {
#pragma unroll
        for (int j = 0; j < 3; ++j) { const int u = 192 * a + 64 * j + lane, r = u / 10; touch<0>(rs, L + L_JUNK, u < 320 ? r * 4096 + (u - 10 * r) * 128 : 0x7fffffff, (128 * k.P + 32 * k.np) * 4096 + (k.kq & 1) * 2048 + 768); }
      }
      if (TOUCH & 2) {
#pragma unroll
        for (int j = 0; j < 3; ++j) { const int u = 192 * a + 64 * j + lane, r = u / 20; touch<SC1>(rw, L + L_JUNK, u < 260 ? r * 20480 + (u - 20 * r) * 128 : 0x7fffffff, soab + 3 * 20480 + (k.P & 7) * 2560); }
      }
      int slot = 3;
#pragma unroll 1
      for (int t = 1; t <= 10; ++t) { waitv<32>(); bar(); w2(t + 5, slot); slot = slot == 5 ? 0 : slot + 1; }
      waitv<32>(); bar();
      if (layer == 0 && a == 0) wait_mb(EV_RT, me, 16);
      waitv<0>(); bar();
      w3(3, 1); bar(); bar(); w3(0, 2); bar(); w3(1, 3);
      bar();
      w3(2, 0);
    } else {
      auto ab = [&](int t, int slot) { dma_lin<SC1, 10>(rw, L + L_D + slot * 20480 + a * 10240, lane * 16, soab + a * 10240 + t * 20480); };
      stamp(k, 4 + a4, sb + 3);
#pragma unroll 1
      for (int t = 0; t < 3; ++t) ab(t, t);
      waitv<20>();
      bar();
      if (layer == 0 && a == 0 && lane < 16) __hip_atomic_store((GAS u64*)(k.ws + OFF_MB + EV_RT * MB_EV + (k.P * 16 + lane) * 128) + n3, tag, __ATOMIC_RELAXED, __HIP_MEMORY_SCOPE_AGENT);
      int slot = 0;
#pragma unroll 1
      for (int t = 1; t <= 13; ++t) { waitv<10>(); bar(); ab(t + 2, slot); slot = slot == 2 ? 0 : slot + 1; }
      waitv<10>(); bar(); waitv<0>(); bar();
      bar();
    }
    push(ev0 + EV_EX, k.P * 16 + k.np, 4, 4, k.kq);
    if (layer == 0) {
#pragma unroll
      for (int j = 0; j < 12; ++j) k4[j >> 3][j & 7] = ld16(rw, lane * 16 + a4 * 1024, so4 + (5 + (j >> 3)) * 30720 + (j & 7) * 4096);
      if (isW) {
        f32x2 s = {0.f, 0.f};
#pragma unroll
        for (int j = 0; j < 10; ++j) s += __builtin_bit_cast(f32x2, __builtin_amdgcn_raw_buffer_load_b64(rw, lane * 8, OFF_CS + (j * 3072 + a * 1536 + 96 * n3) * 4, SC1));
        if (lane < 48) *(LAS f32x2*)(L + L_CS3 + (96 * a + 2 * lane) * 4) = s;
      }
    }
    waitv<0>();
    bar();
    poll(ev0 + EV_EX, me, 4);
    push(ev0 + EV_X1, k.P * 16, 1, 16, n3);
    if (layer == 0) {
#pragma unroll
      for (int j = 12; j < 24; ++j) k4[j >> 3][j & 7] = ld16(rw, lane * 16 + a4 * 1024, so4 + (5 + (j >> 3)) * 30720 + (j & 7) * 4096);
    }
    waitv<0>();
    stamp(k, 4 + a4, sb + 11);
    bar();
    poll(ev0 + EV_X1, me, 16);
    if (isW) {
      bar();
#pragma unroll 1
      for (int t = 1; t <= 6; ++t) { waitv<24>(); bar(); w3(t + 3, (t + 1) & 3); }
      u32x2 sv[8];
      stats_load(k, rw, 2 * layer + 1, 8 * k.P + 4 * a, sv);
      waitv<24>(); bar(); waitv<12>(); bar();
      stats_table(k, a, sv, 1.f);
      waitv<0>();
      w4(2);
      bar();
      w4(3);
      bar();
      w4(0); waitv<30>(); w4(1); waitv<30>(); w4(4);
      waitv<0>();
      stamp(k, 4 + a4, sb + 15);
    } else {
      const unsigned so = OFF_XT + (2 * layer + 1) * XT_BUF + (8 * k.P + 4 * a) * 20480;
      auto xt = [&](int t, int slot) {
#pragma unroll
        for (int j = 0; j < 4; ++j) dma_lin<SC1, 2>(rw, L + L_D + slot * 16384 + (4 * a + j) * 2048, lane * 16, so + j * 20480 + t * 2048);
      };
      stamp(k, 4 + a4, sb + 10);
#pragma unroll 1
      for (int t = 0; t < 3; ++t) xt(t, t);
      waitv<16>();
      bar();
      int slot = 0;
#pragma unroll 1
      for (int t = 1; t <= 7; ++t) { waitv<8>(); bar(); xt(t + 2, slot); slot = slot == 2 ? 0 : slot + 1; }
      waitv<8>(); bar(); waitv<0>(); bar();
      bar();
    }
    push(ev0 + EV_H, k.P * 16, 1, 16, n3);
    poll(ev0 + EV_H, me, 16);
#pragma unroll
    for (int n = 0; n < 3; ++n) {
      unsigned kb = L_W4 + n * 30720 + a4 * 1024 + lane * 16; pin(kb);
      bar();
#pragma unroll
      for (int j = 0; j < 8; ++j) if (j < 7 || isW) *(LAS bf16x8*)(L + kb + j * 4096) = k4[n][j];
    }
    bar();
    bar();
    if (layer == 0) {
      unsigned kb[2] = {L_D + lane * 16 + ((a4 >> 1) * 10 + a) * 1024, 0}; kb[1] = kb[0] + 61440; pin(kb[0]); pin(kb[1]);
#pragma unroll
      for (int j = 0; j < 25; ++j) *(LAS bf16x8*)(L + kb[j / 15] + ((j / 5) % 3) * 20480 + (j % 5) * 2048) = k1[j];
      push(EV_PER_LAYER + EV_X, k.P * 16, 1, 16, n3);
      if (isW) w2x3(0, 3);
    }
  }
#undef lane
  STAMP_OUT(k, 4 + a4);
#define lane k.lane
  if (k.b == 0 && a4 == 2 && lane == 0) *(GAS u64*)k.ws = tag;
#undef lane
#undef a
#undef me
#undef n3
#undef sw
#undef vo2
#undef so2
#undef so3
#undef so4
}

__global__ void __launch_bounds__(512, 2) fnet_glu_kernel(const char* x0, const char* wsr, const char* wsi, const char* wdr, const char* wdi, const char* wg, const char* wu, const char* wdn, char* out, char* ws) {
  __shared__ __attribute__((aligned(16))) char smem[163840];
  Ctx k;
  k.lds = (LAS char*)smem; k.lane = threadIdx.x & 63; k.b = blockIdx.x;
  const int w = __builtin_amdgcn_readfirstlane(threadIdx.x >> 6);
  if (CODE_BYTES && w >= 4) {
    const char* pc; asm volatile("s_getpc_b64 %0" : "=s"(pc));
    touch<0>(mk(pc, CODE_BYTES), k.lds + L_JUNK, (threadIdx.x - 256) * 128, 0);
  }
  asm volatile("" : "+s"(x0), "+s"(wsr), "+s"(wsi), "+s"(wdr), "+s"(wdi), "+s"(wg), "+s"(wu), "+s"(wdn), "+s"(out), "+s"(ws));
  k.ws = ws;
  const int x = k.b & 7, i = k.b >> 3;
  k.kq = x >> 1; k.P = 8 * (x & 1) + (i >> 2); k.np = i & 3;
  k.P1 = 4 * (x >> 1) + (i >> 3); k.mat = x & 1; k.ct1 = i & 7;
  if (w < 4) consumer(k, x0, wg, wu, wdn, out, w);
  else loader(k, (x >> 2) ? wsi : wsr, (x & 1) ? wdi : wdr, w - 4);
}
}

extern "C" void kernel_launch(void* const* d_in, const int* in_sizes, int n_in, void* d_out, int out_size, void* d_ws, size_t ws_size, hipStream_t stream) {
  fnet_glu_kernel<<<256, 512, 0, stream>>>((const char*)d_in[0], (const char*)d_in[1], (const char*)d_in[2], (const char*)d_in[3], (const char*)d_in[4],
                                           (const char*)d_in[6], (const char*)d_in[7], (const char*)d_in[8], (char*)d_out, (char*)d_ws);
}
```

```cpp
#include <hip/hip_runtime.h>

typedef unsigned long long u64;
typedef short bf16x8 __attribute__((ext_vector_type(8)));
typedef short s16x4 __attribute__((ext_vector_type(4)));
typedef float f32x4 __attribute__((ext_vector_type(4)));
typedef float f32x2 __attribute__((ext_vector_type(2)));
typedef unsigned u32x4 __attribute__((ext_vector_type(4)));
typedef unsigned u32x2 __attribute__((ext_vector_type(2)));
typedef __bf16 bf2 __attribute__((ext_vector_type(2)));
#define LAS __attribute__((address_space(3)))
#define GAS __attribute__((address_space(1)))
#define DI __device__ __forceinline__
typedef __amdgpu_buffer_rsrc_t rsrc_t;

#define STAMPS 1
#define WIN1 5
#define WIN4 5
#define TOUCH 0
#define CODE_BYTES 31744
#define W1FIRST 3
namespace {
constexpr unsigned OFF_MB = 4096, MB_EV = 256 * 128;
constexpr int EV_X = 0, EV_AB = 1, EV_EX = 2, EV_X1 = 3, EV_H = 4, EV_PER_LAYER = 5, EV_RT = 10;
constexpr unsigned OFF_CS = 0x60000;
constexpr unsigned OFF_ST = 0x80000, ST_BUF = 8 * 2048 * 8;
constexpr unsigned OFF_XT = 0x100000, XT_BUF = 2048 * 640 * 2;
constexpr unsigned OFF_AB = OFF_XT + 4 * XT_BUF, AB_BUF = 2048 * 1280 * 2;
constexpr unsigned OFF_HT = OFF_AB + 2 * AB_BUF, HT_BUF = 2048 * 1536 * 2;
constexpr unsigned OFF_EX = OFF_HT + 2 * HT_BUF, EX_BUF = 256 * 12 * 2560;
constexpr unsigned OFF_W3 = OFF_EX + 2 * EX_BUF;
constexpr unsigned OFF_W4 = OFF_W3 + 16 * 20 * 12288;
constexpr unsigned WS_END = OFF_W4 + 8 * 48 * 5120;
constexpr unsigned OFF_STAMP = 0x6000000;
static_assert(OFF_MB + 11 * MB_EV <= OFF_CS && OFF_CS + 10 * 3072 * 4 <= OFF_ST && OFF_ST + 4 * ST_BUF <= OFF_XT && WS_END <= OFF_STAMP, "ws map");
constexpr unsigned L_MEAN = 0, L_RSTD = 512, L_CS1 = 1024, L_CS3 = 2304, L_JUNK = 3072;
constexpr unsigned L_D = 4096;
constexpr unsigned L_WR = L_D + 61440, L_W4 = L_D + 6144, L_RT = L_D + 102400, L_X0 = L_D + 126976, L_RED = L_D + 110592;
constexpr int SC1 = 16;
constexpr unsigned SPIN_CAP = 1u << 16;

DI rsrc_t mk(const void* p, unsigned bytes) { return __builtin_amdgcn_make_buffer_rsrc((void*)p, 0, bytes, 0x00020000); }
template <int N> DI void waitv() { asm volatile("s_waitcnt vmcnt(%0)" ::"n"(N) : "memory"); }
DI void bar() { asm volatile("s_waitcnt lgkmcnt(0)\n\ts_barrier" ::: "memory"); }
DI void drain_bar() { asm volatile("s_waitcnt vmcnt(0) lgkmcnt(0)\n\ts_barrier" ::: "memory"); }
DI void sched() { __builtin_amdgcn_sched_barrier(0); }
template <class T> DI void pin(T& x) { asm volatile("" : "+v"(x)); }
template <class T> DI void pins(T& x) { asm volatile("" : "+s"(x)); }
template <int AUX, int IMM> DI void dma(rsrc_t r, LAS char* l, unsigned vo, unsigned so) { __builtin_amdgcn_raw_ptr_buffer_load_lds(r, (LAS void*)l, 16, vo, so, IMM, AUX); }
template <int AUX> DI void touch(rsrc_t r, LAS char* l, unsigned vo, unsigned so) { __builtin_amdgcn_raw_ptr_buffer_load_lds(r, (LAS void*)l, 4, vo, so, 0, AUX); }
template <int AUX, int N> DI void dma_lin(rsrc_t r, LAS char* l, unsigned vo, unsigned so) {
#pragma unroll
  for (int j = 0; j < N; j += 4) {
    dma<AUX, 0>(r, l + j * 1024, vo, so + j * 1024);
    if (j + 1 < N) dma<AUX, 1024>(r, l + j * 1024, vo, so + j * 1024);
    if (j + 2 < N) dma<AUX, 2048>(r, l + j * 1024, vo, so + j * 1024);
    if (j + 3 < N) dma<AUX, 3072>(r, l + j * 1024, vo, so + j * 1024);
  }
}
DI bf16x8 ld16(rsrc_t r, unsigned vo, unsigned so) { return __builtin_bit_cast(bf16x8, __builtin_amdgcn_raw_buffer_load_b128(r, vo, so, SC1)); }
DI void st16(rsrc_t r, u32x4 v, unsigned vo) { __builtin_amdgcn_raw_buffer_store_b128(v, r, vo, 0, SC1); }
DI bf16x8 lds16(LAS char* l, unsigned a) { return *(LAS bf16x8*)(l + a); }
DI unsigned pk(float lo, float hi) { f32x2 v = {lo, hi}; return __builtin_bit_cast(unsigned, __builtin_convertvector(v, bf2)); }
DI float blo(unsigned u) { return __uint_as_float(u << 16); }
DI float bhi(unsigned u) { return __uint_as_float(u & 0xffff0000u); }
DI f32x4 unpk(unsigned a, unsigned b) { return (f32x4){blo(a), bhi(a), blo(b), bhi(b)}; }
DI f32x4 mfma(bf16x8 a, bf16x8 b, f32x4 c) { return __builtin_amdgcn_mfma_f32_16x16x32_bf16(a, b, c, 0, 0, 0); }
DI float dot2(unsigned a, unsigned b, float s) { return __builtin_amdgcn_fdot2_f32_bf16(__builtin_bit_cast(bf2, a), __builtin_bit_cast(bf2, b), s, false); }
DI float dot_ones(bf16x8 f, float s) {
  const u32x4 u = __builtin_bit_cast(u32x4, f);
#pragma unroll
  for (int j = 0; j < 4; ++j) s = dot2(u[j], 0x3f803f80u, s);
  return s;
}
DI float sum_q(float s) { s += __shfl_xor(s, 16); s += __shfl_xor(s, 32); return s; }
DI u32x4 swap_pair(unsigned a0, unsigned a1, unsigned b0, unsigned b1) {
  const auto r0 = __builtin_amdgcn_permlane16_swap(a0, b0, false, false);
  const auto r1 = __builtin_amdgcn_permlane16_swap(a1, b1, false, false);
  return (u32x4){r0[0], r1[0], r0[1], r1[1]};
}

struct Ctx {
  char* ws; LAS char* lds; int lane, b;
  int kq, P, np;
  int P1, mat, ct1;
};
struct St { unsigned t, c; };
#define STAMP_DECL St st_ = {0u, 0u}
#define stamp(k, w, id) do { sched(); const unsigned t_ = (unsigned)__builtin_amdgcn_s_memrealtime(), c_ = (unsigned)__builtin_amdgcn_s_memtime(); const bool m_ = (int)(threadIdx.x & 63) == (id); st_.t = m_ ? t_ : st_.t; st_.c = m_ ? c_ : st_.c; pin(st_.t); pin(st_.c); sched(); } while (0)
#define STAMP_OUT(k, w) do { if ((w) == 0) { unsigned d_ = 0; d_ = d_ > 20000u ? 20000u : d_; const unsigned t0_ = (unsigned)__builtin_amdgcn_s_memrealtime(); while ((unsigned)__builtin_amdgcn_s_memrealtime() - t0_ < d_ * 10u) __builtin_amdgcn_s_sleep(8); } } while (0)

DI void x_store(rsrc_t rw, int lane, unsigned vx, unsigned vs, u32x4 o0, u32x4 o1, u32x4 o2, const unsigned (&res)[10]) {
  st16(rw, o0, vx); st16(rw, o1, vx + 1024);
  if (!(lane & 16)) st16(rw, o2, vx + 2048);
  float s = 0.f, ss = 0.f;
#pragma unroll
  for (int j = 0; j < 10; ++j) { s = dot2(res[j], 0x3f803f80u, s); ss = dot2(res[j], res[j], ss); }
  const auto r = __builtin_amdgcn_permlane16_swap(__float_as_uint(s), __float_as_uint(ss), false, false);
  const unsigned t = __float_as_uint(__uint_as_float(r[0]) + __uint_as_float(r[1]));
  const auto r2 = __builtin_amdgcn_permlane32_swap(t, t, false, false);
  const float u = __uint_as_float(r2[0]) + __uint_as_float(r2[1]);
  if (lane < 32) __builtin_amdgcn_raw_buffer_store_b32(__float_as_uint(u), rw, vs, 0, SC1);
}

#define PHASE_LANE() Ctx k = k0; asm volatile("" : "+v"(k.lane), "+s"(k.P), "+s"(k.kq), "+s"(k.np), "+s"(k.P1), "+s"(k.ct1), "+s"(k.mat)); \
  const int tfo = 8 * k.P + 4 * (k.kq >> 1) + w, cto = 2 * k.np + (k.kq & 1); (void)tfo; (void)cto; \
  const int lane = k.lane, q = lane >> 4, c = lane & 15, qs = 2 * (q & 1) + (q >> 1); (void)qs; \
  const unsigned rowrd = (c >> 3) * 1024 + (c & 7) * 128 + ((q ^ (c >> 1)) * 16); (void)rowrd
#define X_ADDR(xb) unsigned vx = OFF_XT + (xb) * XT_BUF + tfo * 20480 + (10 * cto + qs) * 256 + c * 16, vs = OFF_ST + (xb) * ST_BUF + cto * 16384 + tfo * 128 + lane * 4; pin(vx); pin(vs)

DI void consumer(const Ctx& k0, const char* x0, const char* wg, const char* wu, const char* wdn, char* out, const int w) {
  LAS char* const L = k0.lds;
  const rsrc_t rw = mk(k0.ws, WS_END);
  unsigned res[10];
  STAMP_DECL;
  stamp(k0, w, 0);
  {
    PHASE_LANE();
    const rsrc_t rx = mk(x0, 2048 * 640 * 2);
#pragma unroll
    for (int j = 0; j < 3; ++j) {
      const int u = 64 * j + lane, r = u / 10;
      dma<0, 0>(rx, L + L_X0 + w * 3072 + j * 1024, u < 160 ? r * 1280 + (u - 10 * r) * 16 : 0x7fffffff, 16 * tfo * 1280 + cto * 160);
    }
    sched();
    const int u = k.b + 256 * w, m = u / 240, r = u - 240 * m;
    const bool rt = u < 720;
    const int ku = m < 2 ? r / 24 : r / 10, stp = m < 2 ? r - 24 * ku : r - 10 * ku;
    X_ADDR(0);
    LAS char* a = L + L_X0 + w * 3072 + c * 160;
    waitv<0>();
    stamp(k, w, 50);
    const u32x4 o0 = *(LAS u32x4*)(a + qs * 16), o1 = *(LAS u32x4*)(a + qs * 16 + 64), o2 = *(LAS u32x4*)(a + (q >> 1) * 16 + 128);
#pragma unroll
    for (int i = 0; i < 5; ++i) { const u32x2 t = *(LAS u32x2*)(a + 32 * i + 8 * q); res[2 * i] = t[0]; res[2 * i + 1] = t[1]; }
    x_store(rw, lane, vx, vs, o0, o1, o2, res);
    stamp(k, w, 51);
    drain_bar();
    stamp(k, w, 1);
    if (rt) {
      const unsigned stride = m < 2 ? 3072 : 1280;
      const rsrc_t rs = mk(m == 0 ? wg : (m == 1 ? wu : wdn), 640 * 1536 * 2);
#pragma unroll
      for (int j = 0; j < 8; ++j) dma<0, 0>(rs, L + L_RT + w * 8192 + j * 1024, (lane >> 3) * stride + (lane & 7) * 16, (64 * ku + 8 * j) * stride + 128 * stp);
      const int dv = m < 2 ? 6 : 5, s_g = m < 2 ? 20 * 12288 : 48 * 5120, s_k = m < 2 ? 12288 : 5120, s_f = m < 2 ? 2048 : 1024;
      const unsigned base = (m < 2 ? OFF_W3 + m * 1024 : OFF_W4) + 2 * ku * s_k + lane * 16;
      float cs[4] = {0.f, 0.f, 0.f, 0.f};
      waitv<0>();
#pragma unroll
      for (int n = 0; n < 4; ++n) {
        const int nf = 4 * stp + n, g = m < 2 ? nf / 6 : nf / 5;
#pragma unroll
        for (int kk = 0; kk < 2; ++kk) {
          LAS char* t = L + L_RT + w * 8192 + (4 * kk + q) * 1024 + (c >> 2) * 128 + 32 * n + (c & 3) * 8;
          const u32x2 lo = __builtin_bit_cast(u32x2, __builtin_amdgcn_ds_read_tr16_b64_v4i16((LAS s16x4*)t));
          const u32x2 hi = __builtin_bit_cast(u32x2, __builtin_amdgcn_ds_read_tr16_b64_v4i16((LAS s16x4*)(t + 512)));
          const u32x4 f = {lo[0], lo[1], hi[0], hi[1]};
          st16(rw, f, base + g * s_g + (nf - dv * g) * s_f + kk * s_k);
          cs[n] = dot_ones(__builtin_bit_cast(bf16x8, f), cs[n]);
        }
      }
      if (m < 2) {
#pragma unroll
        for (int n = 0; n < 4; ++n) cs[n] = sum_q(cs[n]);
        const float o = q == 0 ? cs[0] : (q == 1 ? cs[1] : (q == 2 ? cs[2] : cs[3]));
        __builtin_amdgcn_raw_buffer_store_b32(__float_as_uint(o), rw, lane * 4, OFF_CS + (ku * 3072 + m * 1536 + 64 * stp) * 4, SC1);
      }
    }
    stamp(k, w, 2);
  }
#pragma unroll 1
  for (int layer = 0; layer < 2; ++layer) {
    const int sb = 3 + layer * 24;
    {
      PHASE_LANE();
      constexpr int WIN = WIN1;
      unsigned wb[2][2] = {{L_D + rowrd, L_D + rowrd + 51200}, {L_D + (rowrd ^ 64), L_D + (rowrd ^ 64) + 51200}};
      unsigned xv[2] = {OFF_XT + 2 * layer * XT_BUF + (8 * k.P1 + 2 * w) * 20480 + lane * 16, 0}; xv[1] = xv[0] + 20480;
      unsigned vo = OFF_AB + layer * AB_BUF + ((k.mat * 4 + (k.ct1 >> 1)) * 64 + 4 * k.P1 + w) * 10240 + 5 * (k.ct1 & 1) * 1024 + (qs * 16 + c) * 16;
      unsigned ta = (32 * w + 4 * q) * 4, tc = L_CS1 + c * 16;
      pin(wb[0][0]); pin(wb[0][1]); pin(wb[1][0]); pin(wb[1][1]); pin(xv[0]); pin(xv[1]); pin(vo); pin(ta); pin(tc);
      bf16x8 xa[WIN][2], wf[2][5];
      f32x4 acc[2][5];
      bar();
#pragma unroll
      for (int i = 0; i < 5; ++i) wf[0][i] = lds16(L, wb[0][0] + i * 2048);
      bar();
      stamp(k, w, sb + 0);
#pragma unroll
      for (int s = 0; s < WIN; ++s) { xa[s][0] = ld16(rw, xv[0] + (s & 3) * 1024, (s >> 2) * 4096); xa[s][1] = ld16(rw, xv[1] + (s & 3) * 1024, (s >> 2) * 4096); sched(); }
#pragma unroll
      for (int i = 0; i < 5; ++i) { acc[0][i] = (f32x4){0.f, 0.f, 0.f, 0.f}; acc[1][i] = (f32x4){0.f, 0.f, 0.f, 0.f}; }
      f32x4 mean[2], rstd[2], cs4[5];
#pragma unroll
      for (int s = 0; s < 20; ++s) {
        if (s + 1 < 20) {
#pragma unroll
          for (int i = 0; i < 5; ++i) wf[(s + 1) & 1][i] = lds16(L, wb[(s + 1) & 1][(s + 1) / 10] + (((s + 1) >> 1) % 5) * 10240 + i * 2048);
        } else {
          bar();
#pragma unroll
          for (int i = 0; i < 5; ++i) cs4[i] = *(LAS f32x4*)(L + tc + i * 256);
#pragma unroll
          for (int t = 0; t < 2; ++t) { mean[t] = *(LAS f32x4*)(L + ta + L_MEAN + 64 * t); rstd[t] = *(LAS f32x4*)(L + ta + L_RSTD + 64 * t); }
          sched();
        }
#pragma unroll
        for (int i = 0; i < 5; ++i) { acc[0][i] = mfma(xa[s % WIN][0], wf[s & 1][i], acc[0][i]); acc[1][i] = mfma(xa[s % WIN][1], wf[s & 1][i], acc[1][i]); }
        if (s + WIN < 20) { xa[s % WIN][0] = ld16(rw, xv[0] + ((s + WIN) & 3) * 1024, ((s + WIN) >> 2) * 4096); xa[s % WIN][1] = ld16(rw, xv[1] + ((s + WIN) & 3) * 1024, ((s + WIN) >> 2) * 4096); }
        if (s + 1 < 20) {
#pragma unroll
          for (int j = 0; j < 5; ++j) { __builtin_amdgcn_sched_group_barrier(0x100, 1, 0); __builtin_amdgcn_sched_group_barrier(0x8, 1, 0); }
          __builtin_amdgcn_sched_group_barrier(0x8, 5, 0);
          sched();
        }
      }
      stamp(k, w, sb + 1);
#pragma unroll
      for (int i = 0; i < 5; ++i) {
        const float cs = (cs4[i][0] + cs4[i][1]) + (cs4[i][2] + cs4[i][3]);
        const f32x4 a = (mean[0] * cs + acc[0][i]) * rstd[0], bb = (mean[1] * cs + acc[1][i]) * rstd[1];
        st16(rw, swap_pair(pk(a[0], a[1]), pk(a[2], a[3]), pk(bb[0], bb[1]), pk(bb[2], bb[3])), vo + i * 1024);
      }
      stamp(k, w, sb + 2);
      drain_bar();
      stamp(k, w, sb + 3);
    }
    {
      PHASE_LANE();
      const int wm = w >> 1, wn = w & 1;
      f32x4 acc[4][5];
      bf16x8 fa[2][5], fb[2][4];
#pragma unroll
      for (int t = 0; t < 4; ++t)
#pragma unroll
        for (int i = 0; i < 5; ++i) { acc[t][i] = (f32x4){0.f, 0.f, 0.f, 0.f}; pin(acc[t][i]); }
      unsigned wb = L_WR + wm * 8192 + rowrd, ab = L_D + wn * 5120 + lane * 16;
      unsigned ws_slot = 49152, ab_slot = 0;
      unsigned ev = OFF_EX + layer * EX_BUF + ((k.P * 16 + w * 4 + k.np) * 3 + ((k.kq - w) & 3) - 1) * 10240 + lane * 16;
      pin(wb); pin(ab); pin(ev);
      bar();
#pragma unroll
      for (int t = 0; t < 4; ++t) fb[0][t] = lds16(L, wb + 49152 + t * 2048);
      bar();
      bar();
      stamp(k, w, sb + 4);
#pragma unroll
      for (int i = 0; i < 5; ++i) fa[0][i] = lds16(L, ab + i * 1024);
#pragma unroll 1
      for (int kt = 0; kt < 16; ++kt) {
#pragma unroll
        for (int i = 0; i < 5; ++i) fa[1][i] = lds16(L, ab + ab_slot + 10240 + i * 1024);
#pragma unroll
        for (int t = 0; t < 4; ++t) fb[1][t] = lds16(L, (wb ^ 64) + ws_slot + t * 2048);
#pragma unroll
        for (int t = 0; t < 4; ++t)
#pragma unroll
          for (int i = 0; i < 5; ++i) acc[t][i] = mfma(fa[0][i], fb[0][t], acc[t][i]);
#pragma unroll
        for (int j = 0; j < 9; ++j) { __builtin_amdgcn_sched_group_barrier(0x8, 1, 0); __builtin_amdgcn_sched_group_barrier(0x100, 1, 0); }
        __builtin_amdgcn_sched_group_barrier(0x8, 11, 0);
        sched();
        ab_slot = ab_slot == 40960 ? 0 : ab_slot + 20480; ws_slot = ws_slot == 81920 ? 0 : ws_slot + 16384;
        bar();
        sched();
#pragma unroll
        for (int i = 0; i < 5; ++i) fa[0][i] = lds16(L, ab + ab_slot + i * 1024);
#pragma unroll
        for (int t = 0; t < 4; ++t) fb[0][t] = lds16(L, wb + ws_slot + t * 2048);
#pragma unroll
        for (int t = 0; t < 4; ++t)
#pragma unroll
          for (int i = 0; i < 5; ++i) acc[t][i] = mfma(fa[1][i], fb[1][t], acc[t][i]);
#pragma unroll
        for (int j = 0; j < 9; ++j) { __builtin_amdgcn_sched_group_barrier(0x8, 1, 1); __builtin_amdgcn_sched_group_barrier(0x100, 1, 1); }
        __builtin_amdgcn_sched_group_barrier(0x8, 11, 1);
        sched();
      }
      stamp(k, w, sb + 5);
      if (w == k.kq) {
#pragma unroll
        for (int t = 0; t < 4; ++t)
#pragma unroll
          for (int i = 0; i < 5; ++i) *(LAS f32x4*)(L + L_D + (t * 5 + i) * 1024 + lane * 16) = acc[t][i];
      } else {
        const unsigned ev8 = ev - lane * 8;
#pragma unroll
        for (int t = 0; t < 4; ++t) {
          st16(rw, (u32x4){pk(acc[t][0][0], acc[t][0][1]), pk(acc[t][0][2], acc[t][0][3]), pk(acc[t][1][0], acc[t][1][1]), pk(acc[t][1][2], acc[t][1][3])}, ev + t * 2560);
          st16(rw, (u32x4){pk(acc[t][2][0], acc[t][2][1]), pk(acc[t][2][2], acc[t][2][3]), pk(acc[t][3][0], acc[t][3][1]), pk(acc[t][3][2], acc[t][3][3])}, ev + t * 2560 + 1024);
          __builtin_amdgcn_raw_buffer_store_b64((u32x2){pk(acc[t][4][0], acc[t][4][1]), pk(acc[t][4][2], acc[t][4][3])}, rw, ev8, t * 2560 + 2048, SC1);
        }
      }
      stamp(k, w, sb + 6);
      drain_bar();
      stamp(k, w, sb + 7);
      f32x4 v[5];
#pragma unroll
      for (int i = 0; i < 5; ++i) { v[i] = *(LAS f32x4*)(L + L_D + (w * 5 + i) * 1024 + lane * 16) + unpk(res[2 * i], res[2 * i + 1]); pin(v[i]); }
      unsigned rv = OFF_EX + layer * EX_BUF + (k.P * 16 + k.kq * 4 + k.np) * 30720 + w * 2560 + lane * 16, rv8 = rv - lane * 8;
      pin(rv); pin(rv8);
      X_ADDR(2 * layer + 1);
      bar(); bar();
      stamp(k, w, sb + 8);
      u32x4 e[3][2]; u32x2 e8[3];
#pragma unroll
      for (int n = 0; n < 3; ++n) {
        e[n][0] = __builtin_amdgcn_raw_buffer_load_b128(rw, rv, n * 10240, SC1);
        e[n][1] = __builtin_amdgcn_raw_buffer_load_b128(rw, rv + 1024, n * 10240, SC1);
        e8[n] = __builtin_amdgcn_raw_buffer_load_b64(rw, rv8 + 2048, n * 10240, SC1);
        sched();
      }
#pragma unroll
      for (int n = 0; n < 3; ++n) {
#pragma unroll
        for (int j = 0; j < 2; ++j) { v[2 * j] += unpk(e[n][j][0], e[n][j][1]); v[2 * j + 1] += unpk(e[n][j][2], e[n][j][3]); }
        v[4] += unpk(e8[n][0], e8[n][1]);
        sched();
      }
#pragma unroll
      for (int i = 0; i < 5; ++i) { res[2 * i] = pk(v[i][0], v[i][1]); res[2 * i + 1] = pk(v[i][2], v[i][3]); }
      x_store(rw, lane, vx, vs, swap_pair(res[0], res[1], res[2], res[3]), swap_pair(res[4], res[5], res[6], res[7]), swap_pair(res[8], res[9], res[8], res[9]), res);
      stamp(k, w, sb + 9);
      drain_bar();
      stamp(k, w, sb + 10);
    }
    {
      PHASE_LANE();
      const int wm = w >> 1, wn = w & 1;
      f32x4 acc[4][6];
      bf16x8 fa[2][6], fb[2][4];
#pragma unroll
      for (int t = 0; t < 4; ++t)
#pragma unroll
        for (int i = 0; i < 6; ++i) { acc[t][i] = (f32x4){0.f, 0.f, 0.f, 0.f}; pin(acc[t][i]); }
      unsigned wb = L_WR + wn * 6144 + lane * 16, xb = L_D + wm * 8192 + lane * 16;
      unsigned w_slot = 49152, x_slot = 0;
      unsigned vo = OFF_HT + layer * HT_BUF + ((8 * k.P + 4 * wm + (q & 1)) * 48 + 3 * (4 * k.kq + k.np)) * 1024 + (6 * wn + (q >> 1)) * 256 + c * 16, vo2 = vo + 98304;
      unsigned ta = (64 * wm + c) * 4, tc = L_CS3 + (48 * wn + 4 * q) * 4;
      pin(wb); pin(xb); pin(vo); pin(vo2); pin(ta); pin(tc);
      bar();
#pragma unroll
      for (int i = 0; i < 6; ++i) fa[0][i] = lds16(L, wb + 49152 + i * 1024);
      bar();
      bar();
      stamp(k, w, sb + 11);
#pragma unroll
      for (int t = 0; t < 4; ++t) fb[0][t] = lds16(L, xb + t * 2048);
#pragma unroll 1
      for (int kt = 0; kt < 10; ++kt) {
#pragma unroll
        for (int i = 0; i < 6; ++i) fa[1][i] = lds16(L, wb + w_slot + 12288 + i * 1024);
#pragma unroll
        for (int t = 0; t < 4; ++t) fb[1][t] = lds16(L, xb + x_slot + 1024 + t * 2048);
#pragma unroll
        for (int t = 0; t < 4; ++t)
#pragma unroll
          for (int i = 0; i < 6; ++i) acc[t][i] = mfma(fa[0][i], fb[0][t], acc[t][i]);
#pragma unroll
        for (int j = 0; j < 10; ++j) { __builtin_amdgcn_sched_group_barrier(0x8, 1, 0); __builtin_amdgcn_sched_group_barrier(0x100, 1, 0); }
        __builtin_amdgcn_sched_group_barrier(0x8, 14, 0);
        sched();
        x_slot = x_slot == 32768 ? 0 : x_slot + 16384; w_slot = w_slot == 73728 ? 0 : w_slot + 24576;
        bar();
        sched();
#pragma unroll
        for (int i = 0; i < 6; ++i) fa[0][i] = lds16(L, wb + w_slot + i * 1024);
#pragma unroll
        for (int t = 0; t < 4; ++t) fb[0][t] = lds16(L, xb + x_slot + t * 2048);
#pragma unroll
        for (int t = 0; t < 4; ++t)
#pragma unroll
          for (int i = 0; i < 6; ++i) acc[t][i] = mfma(fa[1][i], fb[1][t], acc[t][i]);
#pragma unroll
        for (int j = 0; j < 10; ++j) { __builtin_amdgcn_sched_group_barrier(0x8, 1, 1); __builtin_amdgcn_sched_group_barrier(0x100, 1, 1); }
        __builtin_amdgcn_sched_group_barrier(0x8, 14, 1);
        sched();
      }
      stamp(k, w, sb + 12);
      float mean[4], rstd[4], rl2[4];
#pragma unroll
      for (int t = 0; t < 4; ++t) { mean[t] = *(LAS float*)(L + ta + L_MEAN + 64 * t); rstd[t] = *(LAS float*)(L + ta + L_RSTD + 64 * t); rl2[t] = rstd[t] * -1.44269504089f; }
#pragma unroll
      for (int f = 0; f < 3; ++f) {
        const f32x4 cg = *(LAS f32x4*)(L + tc + 64 * f), cu = *(LAS f32x4*)(L + tc + 384 + 64 * f);
        unsigned h[4][2];
#pragma unroll
        for (int t = 0; t < 4; ++t) {
          f32x4 g = (mean[t] * cg + acc[t][2 * f]) * rl2[t];
          const f32x4 u = (mean[t] * cu + acc[t][2 * f + 1]) * rstd[t];
          asm("" : "+v"(g));
          f32x4 o;
#pragma unroll
          for (int r = 0; r < 4; ++r) o[r] = __builtin_amdgcn_exp2f(g[r]);
          o += 1.f;
#pragma unroll
          for (int r = 0; r < 4; ++r) o[r] = __builtin_amdgcn_rcpf(o[r]);
          o *= u;
          h[t][0] = pk(o[0], o[1]); h[t][1] = pk(o[2], o[3]);
        }
        st16(rw, swap_pair(h[0][0], h[0][1], h[1][0], h[1][1]), vo + f * 512);
        st16(rw, swap_pair(h[2][0], h[2][1], h[3][0], h[3][1]), vo2 + f * 512);
      }
      stamp(k, w, sb + 13);
      drain_bar();
      stamp(k, w, sb + 14);
    }
    {
      PHASE_LANE();
      constexpr int WIN = WIN4;
      const int kh = w & 1;
      unsigned wb[3] = {L_W4 + kh * 5120 + lane * 16, 0, 0}; wb[1] = wb[0] + 51200; wb[2] = wb[0] + 102400;
      unsigned hv[2] = {OFF_HT + layer * HT_BUF + tfo * 49152 + kh * 1024 + lane * 16, OFF_HT + layer * HT_BUF + (tfo ^ 1) * 49152 + kh * 1024 + lane * 16};
      unsigned rd = L_RED + w * 5120 + lane * 16, wr = L_RED + (w ^ 1) * 5120 + lane * 16;
      pin(wb[0]); pin(wb[1]); pin(wb[2]); pin(hv[0]); pin(hv[1]); pin(rd); pin(wr);
      X_ADDR(2);
      unsigned vout = (16 * tfo + c) * 1280 + (10 * cto + qs) * 16;
      pin(vout);
      bf16x8 ha[WIN][2], wf[2][5];
      f32x4 acc[2][5];
#pragma unroll
      for (int i = 0; i < 5; ++i) { acc[0][i] = unpk(res[2 * i], res[2 * i + 1]); pin(acc[0][i]); acc[1][i] = (f32x4){0.f, 0.f, 0.f, 0.f}; }
#pragma unroll
      for (int i = 0; i < 5; ++i) wf[0][i] = lds16(L, wb[0] + i * 1024);
      bar();
      stamp(k, w, sb + 15);
#pragma unroll
      for (int s = 0; s < WIN; ++s) { ha[s][0] = ld16(rw, hv[0] + (s & 1) * 2048, (s >> 1) * 4096); ha[s][1] = ld16(rw, hv[1] + (s & 1) * 2048, (s >> 1) * 4096); sched(); }
#pragma unroll
      for (int s = 0; s < 24; ++s) {
        if (s == 2 || s == 5 || s == 8 || s == 14) bar();
        if (s + 1 < 24) {
#pragma unroll
          for (int i = 0; i < 5; ++i) wf[(s + 1) & 1][i] = lds16(L, wb[((s + 1) % 15) / 5] + ((s + 1) % 5) * 10240 + i * 1024);
        }
#pragma unroll
        for (int i = 0; i < 5; ++i) acc[1][i] = mfma(wf[s & 1][i], ha[s % WIN][1], acc[1][i]);
#pragma unroll
        for (int i = 0; i < 5; ++i) acc[0][i] = mfma(wf[s & 1][i], ha[s % WIN][0], acc[0][i]);
        if (s + WIN < 24) { ha[s % WIN][0] = ld16(rw, hv[0] + ((s + WIN) & 1) * 2048, ((s + WIN) >> 1) * 4096); ha[s % WIN][1] = ld16(rw, hv[1] + ((s + WIN) & 1) * 2048, ((s + WIN) >> 1) * 4096); }
        if (s + 1 < 24) {
#pragma unroll
          for (int j = 0; j < 5; ++j) { __builtin_amdgcn_sched_group_barrier(0x100, 1, 0); __builtin_amdgcn_sched_group_barrier(0x8, 1, 0); }
          __builtin_amdgcn_sched_group_barrier(0x8, 5, 0);
          sched();
        }
      }
#pragma unroll
      for (int i = 0; i < 5; ++i) *(LAS f32x4*)(L + wr + i * 1024) = acc[1][i];
      bar();
      stamp(k, w, sb + 16);
#pragma unroll
      for (int i = 0; i < 5; ++i) { const f32x4 v = acc[0][i] + *(LAS f32x4*)(L + rd + i * 1024); res[2 * i] = pk(v[0], v[1]); res[2 * i + 1] = pk(v[2], v[3]); }
      const u32x4 o0 = swap_pair(res[0], res[1], res[2], res[3]), o1 = swap_pair(res[4], res[5], res[6], res[7]), o2 = swap_pair(res[8], res[9], res[8], res[9]);
      if (layer == 1) {
        const rsrc_t ro = mk(out, 2048 * 640 * 2);
        __builtin_amdgcn_raw_buffer_store_b128(o0, ro, vout, 0, 0);
        __builtin_amdgcn_raw_buffer_store_b128(o1, ro, vout + 64, 0, 0);
        if (!(q & 1)) __builtin_amdgcn_raw_buffer_store_b128(o2, ro, vout + 128, 0, 0);
        stamp(k, w, sb + 17);
      } else {
        x_store(rw, lane, vx, vs, o0, o1, o2, res);
        stamp(k, w, sb + 17);
        drain_bar();
      }
      stamp(k, w, sb + 18);
    }
  }
  STAMP_OUT(k0, w);
}

DI void stats_load(const Ctx& k, rsrc_t rw, int xb, int tf0, u32x2 (&sv)[8]) {
#pragma unroll
  for (int j = 0; j < 8; ++j) sv[j] = __builtin_amdgcn_raw_buffer_load_b64(rw, k.lane * 8, OFF_ST + xb * ST_BUF + j * 16384 + tf0 * 128, SC1);
}
DI void stats_table(const Ctx& k, int a, u32x2 (&sv)[8], float sign) {
  f32x2 s = {0.f, 0.f};
#pragma unroll
  for (int j = 0; j < 8; ++j) { pin(sv[j]); s += __builtin_bit_cast(f32x2, sv[j]); }
  s *= 1.f / 640.f;
  const f32x2 ss = {__shfl_xor(s[0], 8), __shfl_xor(s[1], 8)};
  if (!(k.lane & 8)) {
    const unsigned t = (64 * a + (k.lane & 48) + 2 * (k.lane & 7)) * 4;
    *(LAS f32x2*)(k.lds + L_MEAN + t) = -s;
    *(LAS f32x2*)(k.lds + L_RSTD + t) = (f32x2){sign / sqrtf(fmaxf(ss[0] - s[0] * s[0], 0.f) + 1e-5f), sign / sqrtf(fmaxf(ss[1] - s[1] * s[1], 0.f) + 1e-5f)};
  }
}

DI void loader(const Ctx& k0, const char* wsp, const char* wdp, int a4) {
  Ctx k = k0;
  const bool isW = a4 < 2;
  LAS char* const L = k.lds;
  const rsrc_t rw = mk(k.ws, WS_END);
  const rsrc_t rs = mk(wsp, 2048 * 2048 * 2);
  auto fresh = [&]() { asm volatile("" : "+v"(k.lane), "+s"(k.P), "+s"(k.kq), "+s"(k.np), "+s"(k.P1), "+s"(k.ct1), "+s"(k.mat), "+s"(a4)); };
#define lane k.lane
#define a (a4 & 1)
#define me (k.P * 16 + k.kq * 4 + k.np)
#define n3 (4 * k.kq + k.np)
#define sw (((lane & 7) ^ (4 * a + (lane >> 4))) * 16)
#define vo2 ((lane >> 3) * 4096 + sw)
#define so2 ((128 * k.P + 8 * a) * 4096 + (k.kq & 1) * 2048)
#define so3 (OFF_W3 + n3 * 20 * 12288 + a * 12288)
#define so4 (OFF_W4 + (2 * k.np + (k.kq & 1)) * 48 * 5120)
  bf16x8 k1[25], k4[3][8];
  u64 tag = 0;
  STAMP_DECL;
  auto w1 = [&](int t0, int t1) {
    const rsrc_t rd = mk(wdp, 640 * 640 * 2);
#pragma unroll 1
    for (int t = t0; t < t1; ++t)
#pragma unroll
      for (int j = 0; j < 5; ++j) dma<0, 0>(rd, L + L_D + t * 10240 + (2 * j + a) * 1024, (lane >> 3) * 1280 + sw, (80 * k.ct1 + 8 * a + 16 * j) * 1280 + t * 128);
  };
  auto w2 = [&](int t, int slot) {
#pragma unroll
    for (int j = 0; j < 8; ++j) dma<0, 0>(rs, L + L_WR + slot * 16384 + (2 * j + a) * 1024, vo2, so2 + j * 16 * 4096 + t * 128);
  };
  auto w2x3 = [&](int t0, int slot0) {
#pragma unroll 1
    for (int t = 0; t < 3; ++t) w2(t0 + t, slot0 + t);
  };
  auto w3 = [&](int t, int slot) { dma_lin<SC1, 12>(rw, L + L_WR + slot * 24576 + a * 12288, lane * 16, so3 + t * 24576); };
  auto w4 = [&](int ch) { dma_lin<SC1, 15>(rw, L + L_W4 + ch * 30720 + a * 15360, lane * 16, so4 + ch * 30720 + a * 15360); };
  auto push = [&](int ev, int wg0, int wgstep, int n, int slot) {
    GAS u64* dst = (GAS u64*)(k.ws + OFF_MB + ev * MB_EV + (wg0 + (lane & (n - 1)) * wgstep) * 128) + slot;
    pin(dst); pin(tag);
    bar();
    if (a4 == 2 && lane < n) __hip_atomic_store(dst, tag, __ATOMIC_RELAXED, __HIP_MEMORY_SCOPE_AGENT);
    fresh();
  };
  auto wait_mb = [&](int ev, int wg, int n) {
    GAS u64* src = (GAS u64*)(k.ws + OFF_MB + ev * MB_EV + wg * 128) + (lane & (n - 1));
#pragma unroll 1
    for (unsigned spins = 0; spins < SPIN_CAP; ++spins) {
      const u64 v = __hip_atomic_load(src, __ATOMIC_RELAXED, __HIP_MEMORY_SCOPE_AGENT);
      if (__all(v == tag)) break;
      __builtin_amdgcn_s_sleep(1);
    }
  };
  auto poll = [&](int ev, int wg, int n) { if (a4 == 2) wait_mb(ev, wg, n); bar(); };

  if (!isW) __builtin_amdgcn_s_setprio(3);
  if (isW) { __builtin_amdgcn_s_sleep(4); w1(0, W1FIRST); }
  { const char* e = k.ws; pins(e); tag = *(const GAS u64*)e * 6364136223846793005ull + 1442695040888963407ull; }
  push(EV_X, k.P * 16, 1, 16, n3);
  if (isW) w1(W1FIRST, 10);
#pragma unroll 1
  for (int layer = 0; layer < 2; ++layer) {
    const int ev0 = layer * EV_PER_LAYER, sb = 3 + layer * 24;
    {
      waitv<0>();
      stamp(k, 4 + a4, sb + 0);
      bar();
      poll(ev0 + EV_X, k.P1 * 16 + k.mat * 8 + k.ct1, 16);
      u32x2 sv[8];
      if (!isW) stats_load(k, rw, 2 * layer, 8 * k.P1 + 4 * a, sv);
      if (layer == 0) {
        float cs[5] = {0.f, 0.f, 0.f, 0.f, 0.f};
        unsigned kb[2] = {L_D + lane * 16 + ((a4 >> 1) * 10 + a) * 1024, 0}; kb[1] = kb[0] + 61440; pin(kb[0]); pin(kb[1]);
#pragma unroll
        for (int j = 0; j < 25; ++j) { k1[j] = lds16(L, kb[j / 15] + ((j / 5) % 3) * 20480 + (j % 5) * 2048); cs[j % 5] = dot_ones(k1[j], cs[j % 5]); }
#pragma unroll
        for (int i = 0; i < 5; ++i) {
          cs[i] += __shfl_xor(cs[i], 1); cs[i] += __shfl_xor(cs[i], 2); cs[i] += __shfl_xor(cs[i], 4);
          if ((lane & 7) < 2) *(LAS float*)(L + L_CS1 + (16 * i + 8 * a + (lane >> 3)) * 16 + ((a4 >> 1) * 2 + (lane & 1)) * 4) = (lane & 1) ? 0.f : cs[i];
        }
      }
      if (!isW) stats_table(k, a, sv, k.mat ? -1.f : 1.f);
      bar();
      if (isW && layer == 0) w2x3(0, 3);
      push(ev0 + EV_AB, (2 * k.mat + (k.P1 >> 3)) * 4 + (k.ct1 >> 1), 16, 16, 2 * (k.P1 & 7) + (k.ct1 & 1));
    }
    if (isW) w2x3(3, 0);
    waitv<0>();
    stamp(k, 4 + a4, sb + 4);
    bar();
    poll(ev0 + EV_AB, me, 16);
    const unsigned soab = OFF_AB + layer * AB_BUF + (((k.kq >> 1) * 4 + k.np) * 64 + 32 * (k.kq & 1)) * 10240;
    if (isW) {
      bar();
      if (TOUCH & 1) {
#pragma unroll
        for (int j = 0; j < 3; ++j) { const int u = 192 * a + 64 * j + lane, r = u / 10; touch<0>(rs, L + L_JUNK, u < 320 ? r * 4096 + (u - 10 * r) * 128 : 0x7fffffff, (128 * k.P + 32 * k.np) * 4096 + (k.kq & 1) * 2048 + 768); }
      }
      if (TOUCH & 2) {
#pragma unroll
        for (int j = 0; j < 3; ++j) { const int u = 192 * a + 64 * j + lane, r = u / 20; touch<SC1>(rw, L + L_JUNK, u < 260 ? r * 20480 + (u - 20 * r) * 128 : 0x7fffffff, soab + 3 * 20480 + (k.P & 7) * 2560); }
      }
      int slot = 3;
#pragma unroll 1
      for (int t = 1; t <= 10; ++t) { waitv<32>(); bar(); w2(t + 5, slot); slot = slot == 5 ? 0 : slot + 1; }
      waitv<32>(); bar();
      if (layer == 0 && a == 0) wait_mb(EV_RT, me, 16);
      waitv<0>(); bar();
      w3(3, 1); bar(); bar(); w3(0, 2); bar(); w3(1, 3);
      bar();
      w3(2, 0);
    } else {
      auto ab = [&](int t, int slot) { dma_lin<SC1, 10>(rw, L + L_D + slot * 20480 + a * 10240, lane * 16, soab + a * 10240 + t * 20480); };
      stamp(k, 4 + a4, sb + 3);
#pragma unroll 1
      for (int t = 0; t < 3; ++t) ab(t, t);
      waitv<20>();
      bar();
      if (layer == 0 && a == 0 && lane < 16) __hip_atomic_store((GAS u64*)(k.ws + OFF_MB + EV_RT * MB_EV + (k.P * 16 + lane) * 128) + n3, tag, __ATOMIC_RELAXED, __HIP_MEMORY_SCOPE_AGENT);
      int slot = 0;
#pragma unroll 1
      for (int t = 1; t <= 13; ++t) { waitv<10>(); bar(); ab(t + 2, slot); slot = slot == 2 ? 0 : slot + 1; }
      waitv<10>(); bar(); waitv<0>(); bar();
      bar();
    }
    push(ev0 + EV_EX, k.P * 16 + k.np, 4, 4, k.kq);
    if (layer == 0) {
#pragma unroll
      for (int j = 0; j < 12; ++j) k4[j >> 3][j & 7] = ld16(rw, lane * 16 + a4 * 1024, so4 + (5 + (j >> 3)) * 30720 + (j & 7) * 4096);
      if (isW) {
        f32x2 s = {0.f, 0.f};
#pragma unroll
        for (int j = 0; j < 10; ++j) s += __builtin_bit_cast(f32x2, __builtin_amdgcn_raw_buffer_load_b64(rw, lane * 8, OFF_CS + (j * 3072 + a * 1536 + 96 * n3) * 4, SC1));
        if (lane < 48) *(LAS f32x2*)(L + L_CS3 + (96 * a + 2 * lane) * 4) = s;
      }
    }
    waitv<0>();
    bar();
    poll(ev0 + EV_EX, me, 4);
    push(ev0 + EV_X1, k.P * 16, 1, 16, n3);
    if (layer == 0) {
#pragma unroll
      for (int j = 12; j < 24; ++j) k4[j >> 3][j & 7] = ld16(rw, lane * 16 + a4 * 1024, so4 + (5 + (j >> 3)) * 30720 + (j & 7) * 4096);
    }
    waitv<0>();
    stamp(k, 4 + a4, sb + 11);
    bar();
    poll(ev0 + EV_X1, me, 16);
    if (isW) {
      bar();
#pragma unroll 1
      for (int t = 1; t <= 6; ++t) { waitv<24>(); bar(); w3(t + 3, (t + 1) & 3); }
      u32x2 sv[8];
      stats_load(k, rw, 2 * layer + 1, 8 * k.P + 4 * a, sv);
      waitv<24>(); bar(); waitv<12>(); bar();
      stats_table(k, a, sv, 1.f);
      waitv<0>();
      w4(2);
      bar();
      w4(3);
      bar();
      w4(0); waitv<30>(); w4(1); waitv<30>(); w4(4);
      waitv<0>();
      stamp(k, 4 + a4, sb + 15);
    } else {
      const unsigned so = OFF_XT + (2 * layer + 1) * XT_BUF + (8 * k.P + 4 * a) * 20480;
      auto xt = [&](int t, int slot) {
#pragma unroll
        for (int j = 0; j < 4; ++j) dma_lin<SC1, 2>(rw, L + L_D + slot * 16384 + (4 * a + j) * 2048, lane * 16, so + j * 20480 + t * 2048);
      };
      stamp(k, 4 + a4, sb + 10);
#pragma unroll 1
      for (int t = 0; t < 3; ++t) xt(t, t);
      waitv<16>();
      bar();
      int slot = 0;
#pragma unroll 1
      for (int t = 1; t <= 7; ++t) { waitv<8>(); bar(); xt(t + 2, slot); slot = slot == 2 ? 0 : slot + 1; }
      waitv<8>(); bar(); waitv<0>(); bar();
      bar();
    }
    push(ev0 + EV_H, k.P * 16, 1, 16, n3);
    poll(ev0 + EV_H, me, 16);
#pragma unroll
    for (int n = 0; n < 3; ++n) {
      unsigned kb = L_W4 + n * 30720 + a4 * 1024 + lane * 16; pin(kb);
      bar();
#pragma unroll
      for (int j = 0; j < 8; ++j) if (j < 7 || isW) *(LAS bf16x8*)(L + kb + j * 4096) = k4[n][j];
    }
    bar();
    bar();
    if (layer == 0) {
      unsigned kb[2] = {L_D + lane * 16 + ((a4 >> 1) * 10 + a) * 1024, 0}; kb[1] = kb[0] + 61440; pin(kb[0]); pin(kb[1]);
#pragma unroll
      for (int j = 0; j < 25; ++j) *(LAS bf16x8*)(L + kb[j / 15] + ((j / 5) % 3) * 20480 + (j % 5) * 2048) = k1[j];
      push(EV_PER_LAYER + EV_X, k.P * 16, 1, 16, n3);
      if (isW) w2x3(0, 3);
    }
  }
#undef lane
  STAMP_OUT(k, 4 + a4);
#define lane k.lane
  if (k.b == 0 && a4 == 2 && lane == 0) *(GAS u64*)k.ws = tag;
#undef lane
#undef a
#undef me
#undef n3
#undef sw
#undef vo2
#undef so2
#undef so3
#undef so4
}

__global__ void __launch_bounds__(512, 2) fnet_glu_kernel(const char* x0, const char* wsr, const char* wsi, const char* wdr, const char* wdi, const char* wg, const char* wu, const char* wdn, char* out, char* ws) {
  __shared__ __attribute__((aligned(16))) char smem[163840];
  Ctx k;
  k.lds = (LAS char*)smem; k.lane = threadIdx.x & 63; k.b = blockIdx.x;
  const int w = __builtin_amdgcn_readfirstlane(threadIdx.x >> 6);
  if (CODE_BYTES && w >= 4) {
    const char* pc; asm volatile("s_getpc_b64 %0" : "=s"(pc));
    touch<0>(mk(pc, CODE_BYTES), k.lds + L_JUNK, (threadIdx.x - 256) * 128, 0);
  }
  asm volatile("" : "+s"(x0), "+s"(wsr), "+s"(wsi), "+s"(wdr), "+s"(wdi), "+s"(wg), "+s"(wu), "+s"(wdn), "+s"(out), "+s"(ws));
  k.ws = ws;
  const int x = k.b & 7, i = k.b >> 3;
  k.kq = x >> 1; k.P = 8 * (x & 1) + (i >> 2); k.np = i & 3;
  k.P1 = 4 * (x >> 1) + (i >> 3); k.mat = x & 1; k.ct1 = i & 7;
  if (w < 4) consumer(k, x0, wg, wu, wdn, out, w);
  else loader(k, (x >> 2) ? wsi : wsr, (x & 1) ? wdi : wdr, w - 4);
}
}

extern "C" void kernel_launch(void* const* d_in, const int* in_sizes, int n_in, void* d_out, int out_size, void* d_ws, size_t ws_size, hipStream_t stream) {
  fnet_glu_kernel<<<256, 512, 0, stream>>>((const char*)d_in[0], (const char*)d_in[1], (const char*)d_in[2], (const char*)d_in[3], (const char*)d_in[4],
                                           (const char*)d_in[6], (const char*)d_in[7], (const char*)d_in[8], (char*)d_out, (char*)d_ws);
}
```

```cpp
#include <hip/hip_runtime.h>

typedef unsigned long long u64;
typedef short bf16x8 __attribute__((ext_vector_type(8)));
typedef short s16x4 __attribute__((ext_vector_type(4)));
typedef float f32x4 __attribute__((ext_vector_type(4)));
typedef float f32x2 __attribute__((ext_vector_type(2)));
typedef unsigned u32x4 __attribute__((ext_vector_type(4)));
typedef unsigned u32x2 __attribute__((ext_vector_type(2)));
typedef __bf16 bf2 __attribute__((ext_vector_type(2)));
#define LAS __attribute__((address_space(3)))
#define GAS __attribute__((address_space(1)))
#define DI __device__ __forceinline__
typedef __amdgpu_buffer_rsrc_t rsrc_t;

#define STAMPS 0
#define WIN1 5
#define WIN4 5
#define TOUCH 0
#define CODE_BYTES 31744
#define W1FIRST 3
namespace {
constexpr unsigned OFF_MB = 4096, MB_EV = 256 * 128;
constexpr int EV_X = 0, EV_AB = 1, EV_EX = 2, EV_X1 = 3, EV_H = 4, EV_PER_LAYER = 5, EV_RT = 10;
constexpr unsigned OFF_CS = 0x60000;
constexpr unsigned OFF_ST = 0x80000, ST_BUF = 8 * 2048 * 8;
constexpr unsigned OFF_XT = 0x100000, XT_BUF = 2048 * 640 * 2;
constexpr unsigned OFF_AB = OFF_XT + 4 * XT_BUF, AB_BUF = 2048 * 1280 * 2;
constexpr unsigned OFF_HT = OFF_AB + 2 * AB_BUF, HT_BUF = 2048 * 1536 * 2;
constexpr unsigned OFF_EX = OFF_HT + 2 * HT_BUF, EX_BUF = 256 * 12 * 2560;
constexpr unsigned OFF_W3 = OFF_EX + 2 * EX_BUF;
constexpr unsigned OFF_W4 = OFF_W3 + 16 * 20 * 12288;
constexpr unsigned WS_END = OFF_W4 + 8 * 48 * 5120;
constexpr unsigned OFF_STAMP = 0x6000000;
static_assert(OFF_MB + 11 * MB_EV <= OFF_CS && OFF_CS + 10 * 3072 * 4 <= OFF_ST && OFF_ST + 4 * ST_BUF <= OFF_XT && WS_END <= OFF_STAMP, "ws map");
constexpr unsigned L_MEAN = 0, L_RSTD = 512, L_CS1 = 1024, L_CS3 = 2304, L_JUNK = 3072;
constexpr unsigned L_D = 4096;
constexpr unsigned L_WR = L_D + 61440, L_W4 = L_D + 6144, L_RT = L_D + 102400, L_X0 = L_D + 126976, L_RED = L_D + 110592;
constexpr int SC1 = 16;
constexpr unsigned SPIN_CAP = 1u << 16;

DI rsrc_t mk(const void* p, unsigned bytes) { return __builtin_amdgcn_make_buffer_rsrc((void*)p, 0, bytes, 0x00020000); }
template <int N> DI void waitv() { asm volatile("s_waitcnt vmcnt(%0)" ::"n"(N) : "memory"); }
DI void bar() { asm volatile("s_waitcnt lgkmcnt(0)\n\ts_barrier" ::: "memory"); }
DI void drain_bar() { asm volatile("s_waitcnt vmcnt(0) lgkmcnt(0)\n\ts_barrier" ::: "memory"); }
DI void sched() { __builtin_amdgcn_sched_barrier(0); }
template <class T> DI void pin(T& x) { asm volatile("" : "+v"(x)); }
template <class T> DI void pins(T& x) { asm volatile("" : "+s"(x)); }
template <int AUX, int IMM> DI void dma(rsrc_t r, LAS char* l, unsigned vo, unsigned so) { __builtin_amdgcn_raw_ptr_buffer_load_lds(r, (LAS void*)l, 16, vo, so, IMM, AUX); }
template <int AUX> DI void touch(rsrc_t r, LAS char* l, unsigned vo, unsigned so) { __builtin_amdgcn_raw_ptr_buffer_load_lds(r, (LAS void*)l, 4, vo, so, 0, AUX); }
template <int AUX, int N> DI void dma_lin(rsrc_t r, LAS char* l, unsigned vo, unsigned so) {
#pragma unroll
  for (int j = 0; j < N; j += 4) {
    dma<AUX, 0>(r, l + j * 1024, vo, so + j * 1024);
    if (j + 1 < N) dma<AUX, 1024>(r, l + j * 1024, vo, so + j * 1024);
    if (j + 2 < N) dma<AUX, 2048>(r, l + j * 1024, vo, so + j * 1024);
    if (j + 3 < N) dma<AUX, 3072>(r, l + j * 1024, vo, so + j * 1024);
  }
}
DI bf16x8 ld16(rsrc_t r, unsigned vo, unsigned so) { return __builtin_bit_cast(bf16x8, __builtin_amdgcn_raw_buffer_load_b128(r, vo, so, SC1)); }
DI void st16(rsrc_t r, u32x4 v, unsigned vo) { __builtin_amdgcn_raw_buffer_store_b128(v, r, vo, 0, SC1); }
DI bf16x8 lds16(LAS char* l, unsigned a) { return *(LAS bf16x8*)(l + a); }
DI unsigned pk(float lo, float hi) { f32x2 v = {lo, hi}; return __builtin_bit_cast(unsigned, __builtin_convertvector(v, bf2)); }
DI float blo(unsigned u) { return __uint_as_float(u << 16); }
DI float bhi(unsigned u) { return __uint_as_float(u & 0xffff0000u); }
DI f32x4 unpk(unsigned a, unsigned b) { return (f32x4){blo(a), bhi(a), blo(b), bhi(b)}; }
DI f32x4 mfma(bf16x8 a, bf16x8 b, f32x4 c) { return __builtin_amdgcn_mfma_f32_16x16x32_bf16(a, b, c, 0, 0, 0); }
DI float dot2(unsigned a, unsigned b, float s) { return __builtin_amdgcn_fdot2_f32_bf16(__builtin_bit_cast(bf2, a), __builtin_bit_cast(bf2, b), s, false); }
DI float dot_ones(bf16x8 f, float s) {
  const u32x4 u = __builtin_bit_cast(u32x4, f);
#pragma unroll
  for (int j = 0; j < 4; ++j) s = dot2(u[j], 0x3f803f80u, s);
  return s;
}
DI float sum_q(float s) { s += __shfl_xor(s, 16); s += __shfl_xor(s, 32); return s; }
DI u32x4 swap_pair(unsigned a0, unsigned a1, unsigned b0, unsigned b1) {
  const auto r0 = __builtin_amdgcn_permlane16_swap(a0, b0, false, false);
  const auto r1 = __builtin_amdgcn_permlane16_swap(a1, b1, false, false);
  return (u32x4){r0[0], r1[0], r0[1], r1[1]};
}

struct Ctx {
  char* ws; LAS char* lds; int lane, b;
  int kq, P, np;
  int P1, mat, ct1;
};
#define STAMP_DECL
#define stamp(k, w, id) do { (void)(id); } while (0)
#define STAMP_OUT(k, w)

DI void x_store(rsrc_t rw, int lane, unsigned vx, unsigned vs, u32x4 o0, u32x4 o1, u32x4 o2, const unsigned (&res)[10]) {
  st16(rw, o0, vx); st16(rw, o1, vx + 1024);
  if (!(lane & 16)) st16(rw, o2, vx + 2048);
  float s = 0.f, ss = 0.f;
#pragma unroll
  for (int j = 0; j < 10; ++j) { s = dot2(res[j], 0x3f803f80u, s); ss = dot2(res[j], res[j], ss); }
  const auto r = __builtin_amdgcn_permlane16_swap(__float_as_uint(s), __float_as_uint(ss), false, false);
  const unsigned t = __float_as_uint(__uint_as_float(r[0]) + __uint_as_float(r[1]));
  const auto r2 = __builtin_amdgcn_permlane32_swap(t, t, false, false);
  const float u = __uint_as_float(r2[0]) + __uint_as_float(r2[1]);
  if (lane < 32) __builtin_amdgcn_raw_buffer_store_b32(__float_as_uint(u), rw, vs, 0, SC1);
}

#define PHASE_LANE() Ctx k = k0; asm volatile("" : "+v"(k.lane), "+s"(k.P), "+s"(k.kq), "+s"(k.np), "+s"(k.P1), "+s"(k.ct1), "+s"(k.mat)); \
  const int tfo = 8 * k.P + 4 * (k.kq >> 1) + w, cto = 2 * k.np + (k.kq & 1); (void)tfo; (void)cto; \
  const int lane = k.lane, q = lane >> 4, c = lane & 15, qs = 2 * (q & 1) + (q >> 1); (void)qs; \
  const unsigned rowrd = (c >> 3) * 1024 + (c & 7) * 128 + ((q ^ (c >> 1)) * 16); (void)rowrd
#define X_ADDR(xb) unsigned vx = OFF_XT + (xb) * XT_BUF + tfo * 20480 + (10 * cto + qs) * 256 + c * 16, vs = OFF_ST + (xb) * ST_BUF + cto * 16384 + tfo * 128 + lane * 4; pin(vx); pin(vs)

DI void consumer(const Ctx& k0, const char* x0, const char* wg, const char* wu, const char* wdn, char* out, const int w) {
  LAS char* const L = k0.lds;
  const rsrc_t rw = mk(k0.ws, WS_END);
  unsigned res[10];
  STAMP_DECL;
  stamp(k0, w, 0);
  {
    PHASE_LANE();
    const rsrc_t rx = mk(x0, 2048 * 640 * 2);
#pragma unroll
    for (int j = 0; j < 3; ++j) {
      const int u = 64 * j + lane, r = u / 10;
      dma<0, 0>(rx, L + L_X0 + w * 3072 + j * 1024, u < 160 ? r * 1280 + (u - 10 * r) * 16 : 0x7fffffff, 16 * tfo * 1280 + cto * 160);
    }
    sched();
    const int u = k.b + 256 * w, m = u / 240, r = u - 240 * m;
    const bool rt = u < 720;
    const int ku = m < 2 ? r / 24 : r / 10, stp = m < 2 ? r - 24 * ku : r - 10 * ku;
    X_ADDR(0);
    LAS char* a = L + L_X0 + w * 3072 + c * 160;
    waitv<0>();
    stamp(k, w, 50);
    const u32x4 o0 = *(LAS u32x4*)(a + qs * 16), o1 = *(LAS u32x4*)(a + qs * 16 + 64), o2 = *(LAS u32x4*)(a + (q >> 1) * 16 + 128);
#pragma unroll
    for (int i = 0; i < 5; ++i) { const u32x2 t = *(LAS u32x2*)(a + 32 * i + 8 * q); res[2 * i] = t[0]; res[2 * i + 1] = t[1]; }
    x_store(rw, lane, vx, vs, o0, o1, o2, res);
    stamp(k, w, 51);
    drain_bar();
    stamp(k, w, 1);
    if (rt) {
      const unsigned stride = m < 2 ? 3072 : 1280;
      const rsrc_t rs = mk(m == 0 ? wg : (m == 1 ? wu : wdn), 640 * 1536 * 2);
#pragma unroll
      for (int j = 0; j < 8; ++j) dma<0, 0>(rs, L + L_RT + w * 8192 + j * 1024, (lane >> 3) * stride + (lane & 7) * 16, (64 * ku + 8 * j) * stride + 128 * stp);
      const int dv = m < 2 ? 6 : 5, s_g = m < 2 ? 20 * 12288 : 48 * 5120, s_k = m < 2 ? 12288 : 5120, s_f = m < 2 ? 2048 : 1024;
      const unsigned base = (m < 2 ? OFF_W3 + m * 1024 : OFF_W4) + 2 * ku * s_k + lane * 16;
      float cs[4] = {0.f, 0.f, 0.f, 0.f};
      waitv<0>();
#pragma unroll
      for (int n = 0; n < 4; ++n) {
        const int nf = 4 * stp + n, g = m < 2 ? nf / 6 : nf / 5;
#pragma unroll
        for (int kk = 0; kk < 2; ++kk) {
          LAS char* t = L + L_RT + w * 8192 + (4 * kk + q) * 1024 + (c >> 2) * 128 + 32 * n + (c & 3) * 8;
          const u32x2 lo = __builtin_bit_cast(u32x2, __builtin_amdgcn_ds_read_tr16_b64_v4i16((LAS s16x4*)t));
          const u32x2 hi = __builtin_bit_cast(u32x2, __builtin_amdgcn_ds_read_tr16_b64_v4i16((LAS s16x4*)(t + 512)));
          const u32x4 f = {lo[0], lo[1], hi[0], hi[1]};
          st16(rw, f, base + g * s_g + (nf - dv * g) * s_f + kk * s_k);
          cs[n] = dot_ones(__builtin_bit_cast(bf16x8, f), cs[n]);
        }
      }
      if (m < 2) {
#pragma unroll
        for (int n = 0; n < 4; ++n) cs[n] = sum_q(cs[n]);
        const float o = q == 0 ? cs[0] : (q == 1 ? cs[1] : (q == 2 ? cs[2] : cs[3]));
        __builtin_amdgcn_raw_buffer_store_b32(__float_as_uint(o), rw, lane * 4, OFF_CS + (ku * 3072 + m * 1536 + 64 * stp) * 4, SC1);
      }
    }
    stamp(k, w, 2);
  }
#pragma unroll 1
  for (int layer = 0; layer < 2; ++layer) {
    const int sb = 3 + layer * 24;
    {
      PHASE_LANE();
      constexpr int WIN = WIN1;
      unsigned wb[2][2] = {{L_D + rowrd, L_D + rowrd + 51200}, {L_D + (rowrd ^ 64), L_D + (rowrd ^ 64) + 51200}};
      unsigned xv[2] = {OFF_XT + 2 * layer * XT_BUF + (8 * k.P1 + 2 * w) * 20480 + lane * 16, 0}; xv[1] = xv[0] + 20480;
      unsigned vo = OFF_AB + layer * AB_BUF + ((k.mat * 4 + (k.ct1 >> 1)) * 64 + 4 * k.P1 + w) * 10240 + 5 * (k.ct1 & 1) * 1024 + (qs * 16 + c) * 16;
      unsigned ta = (32 * w + 4 * q) * 4, tc = L_CS1 + c * 16;
      pin(wb[0][0]); pin(wb[0][1]); pin(wb[1][0]); pin(wb[1][1]); pin(xv[0]); pin(xv[1]); pin(vo); pin(ta); pin(tc);
      bf16x8 xa[WIN][2], wf[2][5];
      f32x4 acc[2][5];
      bar();
#pragma unroll
      for (int i = 0; i < 5; ++i) wf[0][i] = lds16(L, wb[0][0] + i * 2048);
      bar();
      stamp(k, w, sb + 0);
#pragma unroll
      for (int s = 0; s < WIN; ++s) { xa[s][0] = ld16(rw, xv[0] + (s & 3) * 1024, (s >> 2) * 4096); xa[s][1] = ld16(rw, xv[1] + (s & 3) * 1024, (s >> 2) * 4096); sched(); }
#pragma unroll
      for (int i = 0; i < 5; ++i) { acc[0][i] = (f32x4){0.f, 0.f, 0.f, 0.f}; acc[1][i] = (f32x4){0.f, 0.f, 0.f, 0.f}; }
      f32x4 mean[2], rstd[2], cs4[5];
#pragma unroll
      for (int s = 0; s < 20; ++s) {
        if (s + 1 < 20) {
#pragma unroll
          for (int i = 0; i < 5; ++i) wf[(s + 1) & 1][i] = lds16(L, wb[(s + 1) & 1][(s + 1) / 10] + (((s + 1) >> 1) % 5) * 10240 + i * 2048);
        } else {
          bar();
#pragma unroll
          for (int i = 0; i < 5; ++i) cs4[i] = *(LAS f32x4*)(L + tc + i * 256);
#pragma unroll
          for (int t = 0; t < 2; ++t) { mean[t] = *(LAS f32x4*)(L + ta + L_MEAN + 64 * t); rstd[t] = *(LAS f32x4*)(L + ta + L_RSTD + 64 * t); }
          sched();
        }
#pragma unroll
        for (int i = 0; i < 5; ++i) { acc[0][i] = mfma(xa[s % WIN][0], wf[s & 1][i], acc[0][i]); acc[1][i] = mfma(xa[s % WIN][1], wf[s & 1][i], acc[1][i]); }
        if (s + WIN < 20) { xa[s % WIN][0] = ld16(rw, xv[0] + ((s + WIN) & 3) * 1024, ((s + WIN) >> 2) * 4096); xa[s % WIN][1] = ld16(rw, xv[1] + ((s + WIN) & 3) * 1024, ((s + WIN) >> 2) * 4096); }
        if (s + 1 < 20) {
#pragma unroll
          for (int j = 0; j < 5; ++j) { __builtin_amdgcn_sched_group_barrier(0x100, 1, 0); __builtin_amdgcn_sched_group_barrier(0x8, 1, 0); }
          __builtin_amdgcn_sched_group_barrier(0x8, 5, 0);
          sched();
        }
      }
      stamp(k, w, sb + 1);
#pragma unroll
      for (int i = 0; i < 5; ++i) {
        const float cs = (cs4[i][0] + cs4[i][1]) + (cs4[i][2] + cs4[i][3]);
        const f32x4 a = (mean[0] * cs + acc[0][i]) * rstd[0], bb = (mean[1] * cs + acc[1][i]) * rstd[1];
        st16(rw, swap_pair(pk(a[0], a[1]), pk(a[2], a[3]), pk(bb[0], bb[1]), pk(bb[2], bb[3])), vo + i * 1024);
      }
      stamp(k, w, sb + 2);
      drain_bar();
      stamp(k, w, sb + 3);
    }
    {
      PHASE_LANE();
      const int wm = w >> 1, wn = w & 1;
      f32x4 acc[4][5];
      bf16x8 fa[2][5], fb[2][4];
#pragma unroll
      for (int t = 0; t < 4; ++t)
#pragma unroll
        for (int i = 0; i < 5; ++i) { acc[t][i] = (f32x4){0.f, 0.f, 0.f, 0.f}; pin(acc[t][i]); }
      unsigned wb = L_WR + wm * 8192 + rowrd, ab = L_D + wn * 5120 + lane * 16;
      unsigned ws_slot = 49152, ab_slot = 0;
      unsigned ev = OFF_EX + layer * EX_BUF + ((k.P * 16 + w * 4 + k.np) * 3 + ((k.kq - w) & 3) - 1) * 10240 + lane * 16;
      pin(wb); pin(ab); pin(ev);
      bar();
#pragma unroll
      for (int t = 0; t < 4; ++t) fb[0][t] = lds16(L, wb + 49152 + t * 2048);
      bar();
      bar();
      stamp(k, w, sb + 4);
#pragma unroll
      for (int i = 0; i < 5; ++i) fa[0][i] = lds16(L, ab + i * 1024);
#pragma unroll 1
      for (int kt = 0; kt < 16; ++kt) {
#pragma unroll
        for (int i = 0; i < 5; ++i) fa[1][i] = lds16(L, ab + ab_slot + 10240 + i * 1024);
#pragma unroll
        for (int t = 0; t < 4; ++t) fb[1][t] = lds16(L, (wb ^ 64) + ws_slot + t * 2048);
#pragma unroll
        for (int t = 0; t < 4; ++t)
#pragma unroll
          for (int i = 0; i < 5; ++i) acc[t][i] = mfma(fa[0][i], fb[0][t], acc[t][i]);
#pragma unroll
        for (int j = 0; j < 9; ++j) { __builtin_amdgcn_sched_group_barrier(0x8, 1, 0); __builtin_amdgcn_sched_group_barrier(0x100, 1, 0); }
        __builtin_amdgcn_sched_group_barrier(0x8, 11, 0);
        sched();
        ab_slot = ab_slot == 40960 ? 0 : ab_slot + 20480; ws_slot = ws_slot == 81920 ? 0 : ws_slot + 16384;
        bar();
        sched();
#pragma unroll
        for (int i = 0; i < 5; ++i) fa[0][i] = lds16(L, ab + ab_slot + i * 1024);
#pragma unroll
        for (int t = 0; t < 4; ++t) fb[0][t] = lds16(L, wb + ws_slot + t * 2048);
#pragma unroll
        for (int t = 0; t < 4; ++t)
#pragma unroll
          for (int i = 0; i < 5; ++i) acc[t][i] = mfma(fa[1][i], fb[1][t], acc[t][i]);
#pragma unroll
        for (int j = 0; j < 9; ++j) { __builtin_amdgcn_sched_group_barrier(0x8, 1, 1); __builtin_amdgcn_sched_group_barrier(0x100, 1, 1); }
        __builtin_amdgcn_sched_group_barrier(0x8, 11, 1);
        sched();
      }
      stamp(k, w, sb + 5);
      if (w == k.kq) {
#pragma unroll
        for (int t = 0; t < 4; ++t)
#pragma unroll
          for (int i = 0; i < 5; ++i) *(LAS f32x4*)(L + L_D + (t * 5 + i) * 1024 + lane * 16) = acc[t][i];
      } else {
        const unsigned ev8 = ev - lane * 8;
#pragma unroll
        for (int t = 0; t < 4; ++t) {
          st16(rw, (u32x4){pk(acc[t][0][0], acc[t][0][1]), pk(acc[t][0][2], acc[t][0][3]), pk(acc[t][1][0], acc[t][1][1]), pk(acc[t][1][2], acc[t][1][3])}, ev + t * 2560);
          st16(rw, (u32x4){pk(acc[t][2][0], acc[t][2][1]), pk(acc[t][2][2], acc[t][2][3]), pk(acc[t][3][0], acc[t][3][1]), pk(acc[t][3][2], acc[t][3][3])}, ev + t * 2560 + 1024);
          __builtin_amdgcn_raw_buffer_store_b64((u32x2){pk(acc[t][4][0], acc[t][4][1]), pk(acc[t][4][2], acc[t][4][3])}, rw, ev8, t * 2560 + 2048, SC1);
        }
      }
      stamp(k, w, sb + 6);
      drain_bar();
      stamp(k, w, sb + 7);
      f32x4 v[5];
#pragma unroll
      for (int i = 0; i < 5; ++i) { v[i] = *(LAS f32x4*)(L + L_D + (w * 5 + i) * 1024 + lane * 16) + unpk(res[2 * i], res[2 * i + 1]); pin(v[i]); }
      unsigned rv = OFF_EX + layer * EX_BUF + (k.P * 16 + k.kq * 4 + k.np) * 30720 + w * 2560 + lane * 16, rv8 = rv - lane * 8;
      pin(rv); pin(rv8);
      X_ADDR(2 * layer + 1);
      bar(); bar();
      stamp(k, w, sb + 8);
      u32x4 e[3][2]; u32x2 e8[3];
#pragma unroll
      for (int n = 0; n < 3; ++n) {
        e[n][0] = __builtin_amdgcn_raw_buffer_load_b128(rw, rv, n * 10240, SC1);
        e[n][1] = __builtin_amdgcn_raw_buffer_load_b128(rw, rv + 1024, n * 10240, SC1);
        e8[n] = __builtin_amdgcn_raw_buffer_load_b64(rw, rv8 + 2048, n * 10240, SC1);
        sched();
      }
#pragma unroll
      for (int n = 0; n < 3; ++n) {
#pragma unroll
        for (int j = 0; j < 2; ++j) { v[2 * j] += unpk(e[n][j][0], e[n][j][1]); v[2 * j + 1] += unpk(e[n][j][2], e[n][j][3]); }
        v[4] += unpk(e8[n][0], e8[n][1]);
        sched();
      }
#pragma unroll
      for (int i = 0; i < 5; ++i) { res[2 * i] = pk(v[i][0], v[i][1]); res[2 * i + 1] = pk(v[i][2], v[i][3]); }
      x_store(rw, lane, vx, vs, swap_pair(res[0], res[1], res[2], res[3]), swap_pair(res[4], res[5], res[6], res[7]), swap_pair(res[8], res[9], res[8], res[9]), res);
      stamp(k, w, sb + 9);
      drain_bar();
      stamp(k, w, sb + 10);
    }
    {
      PHASE_LANE();
      const int wm = w >> 1, wn = w & 1;
      f32x4 acc[4][6];
      bf16x8 fa[2][6], fb[2][4];
#pragma unroll
      for (int t = 0; t < 4; ++t)
#pragma unroll
        for (int i = 0; i < 6; ++i) { acc[t][i] = (f32x4){0.f, 0.f, 0.f, 0.f}; pin(acc[t][i]); }
      unsigned wb = L_WR + wn * 6144 + lane * 16, xb = L_D + wm * 8192 + lane * 16;
      unsigned w_slot = 49152, x_slot = 0;
      unsigned vo = OFF_HT + layer * HT_BUF + ((8 * k.P + 4 * wm + (q & 1)) * 48 + 3 * (4 * k.kq + k.np)) * 1024 + (6 * wn + (q >> 1)) * 256 + c * 16, vo2 = vo + 98304;
      unsigned ta = (64 * wm + c) * 4, tc = L_CS3 + (48 * wn + 4 * q) * 4;
      pin(wb); pin(xb); pin(vo); pin(vo2); pin(ta); pin(tc);
      bar();
#pragma unroll
      for (int i = 0; i < 6; ++i) fa[0][i] = lds16(L, wb + 49152 + i * 1024);
      bar();
      bar();
      stamp(k, w, sb + 11);
#pragma unroll
      for (int t = 0; t < 4; ++t) fb[0][t] = lds16(L, xb + t * 2048);
#pragma unroll 1
      for (int kt = 0; kt < 10; ++kt) {
#pragma unroll
        for (int i = 0; i < 6; ++i) fa[1][i] = lds16(L, wb + w_slot + 12288 + i * 1024);
#pragma unroll
        for (int t = 0; t < 4; ++t) fb[1][t] = lds16(L, xb + x_slot + 1024 + t * 2048);
#pragma unroll
        for (int t = 0; t < 4; ++t)
#pragma unroll
          for (int i = 0; i < 6; ++i) acc[t][i] = mfma(fa[0][i], fb[0][t], acc[t][i]);
#pragma unroll
        for (int j = 0; j < 10; ++j) { __builtin_amdgcn_sched_group_barrier(0x8, 1, 0); __builtin_amdgcn_sched_group_barrier(0x100, 1, 0); }
        __builtin_amdgcn_sched_group_barrier(0x8, 14, 0);
        sched();
        x_slot = x_slot == 32768 ? 0 : x_slot + 16384; w_slot = w_slot == 73728 ? 0 : w_slot + 24576;
        bar();
        sched();
#pragma unroll
        for (int i = 0; i < 6; ++i) fa[0][i] = lds16(L, wb + w_slot + i * 1024);
#pragma unroll
        for (int t = 0; t < 4; ++t) fb[0][t] = lds16(L, xb + x_slot + t * 2048);
#pragma unroll
        for (int t = 0; t < 4; ++t)
#pragma unroll
          for (int i = 0; i < 6; ++i) acc[t][i] = mfma(fa[1][i], fb[1][t], acc[t][i]);
#pragma unroll
        for (int j = 0; j < 10; ++j) { __builtin_amdgcn_sched_group_barrier(0x8, 1, 1); __builtin_amdgcn_sched_group_barrier(0x100, 1, 1); }
        __builtin_amdgcn_sched_group_barrier(0x8, 14, 1);
        sched();
      }
      stamp(k, w, sb + 12);
      float mean[4], rstd[4], rl2[4];
#pragma unroll
      for (int t = 0; t < 4; ++t) { mean[t] = *(LAS float*)(L + ta + L_MEAN + 64 * t); rstd[t] = *(LAS float*)(L + ta + L_RSTD + 64 * t); rl2[t] = rstd[t] * -1.44269504089f; }
#pragma unroll
      for (int f = 0; f < 3; ++f) {
        const f32x4 cg = *(LAS f32x4*)(L + tc + 64 * f), cu = *(LAS f32x4*)(L + tc + 384 + 64 * f);
        unsigned h[4][2];
#pragma unroll
        for (int t = 0; t < 4; ++t) {
          f32x4 g = (mean[t] * cg + acc[t][2 * f]) * rl2[t];
          const f32x4 u = (mean[t] * cu + acc[t][2 * f + 1]) * rstd[t];
          asm("" : "+v"(g));
          f32x4 o;
#pragma unroll
          for (int r = 0; r < 4; ++r) o[r] = __builtin_amdgcn_exp2f(g[r]);
          o += 1.f;
#pragma unroll
          for (int r = 0; r < 4; ++r) o[r] = __builtin_amdgcn_rcpf(o[r]);
          o *= u;
          h[t][0] = pk(o[0], o[1]); h[t][1] = pk(o[2], o[3]);
        }
        st16(rw, swap_pair(h[0][0], h[0][1], h[1][0], h[1][1]), vo + f * 512);
        st16(rw, swap_pair(h[2][0], h[2][1], h[3][0], h[3][1]), vo2 + f * 512);
      }
      stamp(k, w, sb + 13);
      drain_bar();
      stamp(k, w, sb + 14);
    }
    {
      PHASE_LANE();
      constexpr int WIN = WIN4;
      const int kh = w & 1;
      unsigned wb[3] = {L_W4 + kh * 5120 + lane * 16, 0, 0}; wb[1] = wb[0] + 51200; wb[2] = wb[0] + 102400;
      unsigned hv[2] = {OFF_HT + layer * HT_BUF + tfo * 49152 + kh * 1024 + lane * 16, OFF_HT + layer * HT_BUF + (tfo ^ 1) * 49152 + kh * 1024 + lane * 16};
      unsigned rd = L_RED + w * 5120 + lane * 16, wr = L_RED + (w ^ 1) * 5120 + lane * 16;
      pin(wb[0]); pin(wb[1]); pin(wb[2]); pin(hv[0]); pin(hv[1]); pin(rd); pin(wr);
      X_ADDR(2);
      unsigned vout = (16 * tfo + c) * 1280 + (10 * cto + qs) * 16;
      pin(vout);
      bf16x8 ha[WIN][2], wf[2][5];
      f32x4 acc[2][5];
#pragma unroll
      for (int i = 0; i < 5; ++i) { acc[0][i] = unpk(res[2 * i], res[2 * i + 1]); pin(acc[0][i]); acc[1][i] = (f32x4){0.f, 0.f, 0.f, 0.f}; }
#pragma unroll
      for (int i = 0; i < 5; ++i) wf[0][i] = lds16(L, wb[0] + i * 1024);
      bar();
      stamp(k, w, sb + 15);
#pragma unroll
      for (int s = 0; s < WIN; ++s) { ha[s][0] = ld16(rw, hv[0] + (s & 1) * 2048, (s >> 1) * 4096); ha[s][1] = ld16(rw, hv[1] + (s & 1) * 2048, (s >> 1) * 4096); sched(); }
#pragma unroll
      for (int s = 0; s < 24; ++s) {
        if (s == 2 || s == 5 || s == 8 || s == 14) bar();
        if (s + 1 < 24) {
#pragma unroll
          for (int i = 0; i < 5; ++i) wf[(s + 1) & 1][i] = lds16(L, wb[((s + 1) % 15) / 5] + ((s + 1) % 5) * 10240 + i * 1024);
        }
#pragma unroll
        for (int i = 0; i < 5; ++i) acc[1][i] = mfma(wf[s & 1][i], ha[s % WIN][1], acc[1][i]);
#pragma unroll
        for (int i = 0; i < 5; ++i) acc[0][i] = mfma(wf[s & 1][i], ha[s % WIN][0], acc[0][i]);
        if (s + WIN < 24) { ha[s % WIN][0] = ld16(rw, hv[0] + ((s + WIN) & 1) * 2048, ((s + WIN) >> 1) * 4096); ha[s % WIN][1] = ld16(rw, hv[1] + ((s + WIN) & 1) * 2048, ((s + WIN) >> 1) * 4096); }
        if (s + 1 < 24) {
#pragma unroll
          for (int j = 0; j < 5; ++j) { __builtin_amdgcn_sched_group_barrier(0x100, 1, 0); __builtin_amdgcn_sched_group_barrier(0x8, 1, 0); }
          __builtin_amdgcn_sched_group_barrier(0x8, 5, 0);
          sched();
        }
      }
#pragma unroll
      for (int i = 0; i < 5; ++i) *(LAS f32x4*)(L + wr + i * 1024) = acc[1][i];
      bar();
      stamp(k, w, sb + 16);
#pragma unroll
      for (int i = 0; i < 5; ++i) { const f32x4 v = acc[0][i] + *(LAS f32x4*)(L + rd + i * 1024); res[2 * i] = pk(v[0], v[1]); res[2 * i + 1] = pk(v[2], v[3]); }
      const u32x4 o0 = swap_pair(res[0], res[1], res[2], res[3]), o1 = swap_pair(res[4], res[5], res[6], res[7]), o2 = swap_pair(res[8], res[9], res[8], res[9]);
      if (layer == 1) {
        const rsrc_t ro = mk(out, 2048 * 640 * 2);
        __builtin_amdgcn_raw_buffer_store_b128(o0, ro, vout, 0, 2);
        __builtin_amdgcn_raw_buffer_store_b128(o1, ro, vout + 64, 0, 2);
        if (!(q & 1)) __builtin_amdgcn_raw_buffer_store_b128(o2, ro, vout + 128, 0, 2);
        stamp(k, w, sb + 17);
      } else {
        x_store(rw, lane, vx, vs, o0, o1, o2, res);
        stamp(k, w, sb + 17);
        drain_bar();
      }
      stamp(k, w, sb + 18);
    }
  }
  STAMP_OUT(k0, w);
}

DI void stats_load(const Ctx& k, rsrc_t rw, int xb, int tf0, u32x2 (&sv)[8]) {
#pragma unroll
  for (int j = 0; j < 8; ++j) sv[j] = __builtin_amdgcn_raw_buffer_load_b64(rw, k.lane * 8, OFF_ST + xb * ST_BUF + j * 16384 + tf0 * 128, SC1);
}
DI void stats_table(const Ctx& k, int a, u32x2 (&sv)[8], float sign) {
  f32x2 s = {0.f, 0.f};
#pragma unroll
  for (int j = 0; j < 8; ++j) { pin(sv[j]); s += __builtin_bit_cast(f32x2, sv[j]); }
  s *= 1.f / 640.f;
  const f32x2 ss = {__shfl_xor(s[0], 8), __shfl_xor(s[1], 8)};
  if (!(k.lane & 8)) {
    const unsigned t = (64 * a + (k.lane & 48) + 2 * (k.lane & 7)) * 4;
    *(LAS f32x2*)(k.lds + L_MEAN + t) = -s;
    *(LAS f32x2*)(k.lds + L_RSTD + t) = (f32x2){sign / sqrtf(fmaxf(ss[0] - s[0] * s[0], 0.f) + 1e-5f), sign / sqrtf(fmaxf(ss[1] - s[1] * s[1], 0.f) + 1e-5f)};
  }
}

DI void loader(const Ctx& k0, const char* wsp, const char* wdp, int a4) {
  Ctx k = k0;
  const bool isW = a4 < 2;
  LAS char* const L = k.lds;
  const rsrc_t rw = mk(k.ws, WS_END);
  const rsrc_t rs = mk(wsp, 2048 * 2048 * 2);
  auto fresh = [&]() { asm volatile("" : "+v"(k.lane), "+s"(k.P), "+s"(k.kq), "+s"(k.np), "+s"(k.P1), "+s"(k.ct1), "+s"(k.mat), "+s"(a4)); };
#define lane k.lane
#define a (a4 & 1)
#define me (k.P * 16 + k.kq * 4 + k.np)
#define n3 (4 * k.kq + k.np)
#define sw (((lane & 7) ^ (4 * a + (lane >> 4))) * 16)
#define vo2 ((lane >> 3) * 4096 + sw)
#define so2 ((128 * k.P + 8 * a) * 4096 + (k.kq & 1) * 2048)
#define so3 (OFF_W3 + n3 * 20 * 12288 + a * 12288)
#define so4 (OFF_W4 + (2 * k.np + (k.kq & 1)) * 48 * 5120)
  bf16x8 k1[25], k4[3][8];
  u64 tag = 0;
  STAMP_DECL;
  auto w1 = [&](int t0, int t1) {
    const rsrc_t rd = mk(wdp, 640 * 640 * 2);
#pragma unroll 1
    for (int t = t0; t < t1; ++t)
#pragma unroll
      for (int j = 0; j < 5; ++j) dma<0, 0>(rd, L + L_D + t * 10240 + (2 * j + a) * 1024, (lane >> 3) * 1280 + sw, (80 * k.ct1 + 8 * a + 16 * j) * 1280 + t * 128);
  };
  auto w2 = [&](int t, int slot) {
#pragma unroll
    for (int j = 0; j < 8; ++j) dma<0, 0>(rs, L + L_WR + slot * 16384 + (2 * j + a) * 1024, vo2, so2 + j * 16 * 4096 + t * 128);
  };
  auto w2x3 = [&](int t0, int slot0) {
#pragma unroll 1
    for (int t = 0; t < 3; ++t) w2(t0 + t, slot0 + t);
  };
  auto w3 = [&](int t, int slot) { dma_lin<SC1, 12>(rw, L + L_WR + slot * 24576 + a * 12288, lane * 16, so3 + t * 24576); };
  auto w4 = [&](int ch) { dma_lin<SC1, 15>(rw, L + L_W4 + ch * 30720 + a * 15360, lane * 16, so4 + ch * 30720 + a * 15360); };
  auto push = [&](int ev, int wg0, int wgstep, int n, int slot) {
    GAS u64* dst = (GAS u64*)(k.ws + OFF_MB + ev * MB_EV + (wg0 + (lane & (n - 1)) * wgstep) * 128) + slot;
    pin(dst); pin(tag);
    bar();
    if (a4 == 2 && lane < n) __hip_atomic_store(dst, tag, __ATOMIC_RELAXED, __HIP_MEMORY_SCOPE_AGENT);
    fresh();
  };
  auto wait_mb = [&](int ev, int wg, int n) {
    GAS u64* src = (GAS u64*)(k.ws + OFF_MB + ev * MB_EV + wg * 128) + (lane & (n - 1));
#pragma unroll 1
    for (unsigned spins = 0; spins < SPIN_CAP; ++spins) {
      const u64 v = __hip_atomic_load(src, __ATOMIC_RELAXED, __HIP_MEMORY_SCOPE_AGENT);
      if (__all(v == tag)) break;
      __builtin_amdgcn_s_sleep(1);
    }
  };
  auto poll = [&](int ev, int wg, int n) { if (a4 == 2) wait_mb(ev, wg, n); bar(); };

  if (!isW) __builtin_amdgcn_s_setprio(3);
  if (isW) { __builtin_amdgcn_s_sleep(4); w1(0, W1FIRST); }
  { const char* e = k.ws; pins(e); tag = *(const GAS u64*)e * 6364136223846793005ull + 1442695040888963407ull; }
  push(EV_X, k.P * 16, 1, 16, n3);
  if (isW) w1(W1FIRST, 10);
#pragma unroll 1
  for (int layer = 0; layer < 2; ++layer) {
    const int ev0 = layer * EV_PER_LAYER, sb = 3 + layer * 24;
    {
      waitv<0>();
      stamp(k, 4 + a4, sb + 0);
      bar();
      poll(ev0 + EV_X, k.P1 * 16 + k.mat * 8 + k.ct1, 16);
      u32x2 sv[8];
      if (!isW) stats_load(k, rw, 2 * layer, 8 * k.P1 + 4 * a, sv);
      if (layer == 0) {
        float cs[5] = {0.f, 0.f, 0.f, 0.f, 0.f};
        unsigned kb[2] = {L_D + lane * 16 + ((a4 >> 1) * 10 + a) * 1024, 0}; kb[1] = kb[0] + 61440; pin(kb[0]); pin(kb[1]);
#pragma unroll
        for (int j = 0; j < 25; ++j) { k1[j] = lds16(L, kb[j / 15] + ((j / 5) % 3) * 20480 + (j % 5) * 2048); cs[j % 5] = dot_ones(k1[j], cs[j % 5]); }
#pragma unroll
        for (int i = 0; i < 5; ++i) {
          cs[i] += __shfl_xor(cs[i], 1); cs[i] += __shfl_xor(cs[i], 2); cs[i] += __shfl_xor(cs[i], 4);
          if ((lane & 7) < 2) *(LAS float*)(L + L_CS1 + (16 * i + 8 * a + (lane >> 3)) * 16 + ((a4 >> 1) * 2 + (lane & 1)) * 4) = (lane & 1) ? 0.f : cs[i];
        }
      }
      if (!isW) stats_table(k, a, sv, k.mat ? -1.f : 1.f);
      bar();
      if (isW && layer == 0) w2x3(0, 3);
      push(ev0 + EV_AB, (2 * k.mat + (k.P1 >> 3)) * 4 + (k.ct1 >> 1), 16, 16, 2 * (k.P1 & 7) + (k.ct1 & 1));
    }
    if (isW) w2x3(3, 0);
    waitv<0>();
    stamp(k, 4 + a4, sb + 4);
    bar();
    poll(ev0 + EV_AB, me, 16);
    const unsigned soab = OFF_AB + layer * AB_BUF + (((k.kq >> 1) * 4 + k.np) * 64 + 32 * (k.kq & 1)) * 10240;
    if (isW) {
      bar();
      if (TOUCH & 1) {
#pragma unroll
        for (int j = 0; j < 3; ++j) { const int u = 192 * a + 64 * j + lane, r = u / 10; touch<0>(rs, L + L_JUNK, u < 320 ? r * 4096 + (u - 10 * r) * 128 : 0x7fffffff, (128 * k.P + 32 * k.np) * 4096 + (k.kq & 1) * 2048 + 768); }
      }
      if (TOUCH & 2) {
#pragma unroll
        for (int j = 0; j < 3; ++j) { const int u = 192 * a + 64 * j + lane, r = u / 20; touch<SC1>(rw, L + L_JUNK, u < 260 ? r * 20480 + (u - 20 * r) * 128 : 0x7fffffff, soab + 3 * 20480 + (k.P & 7) * 2560); }
      }
      int slot = 3;
#pragma unroll 1
      for (int t = 1; t <= 10; ++t) { waitv<32>(); bar(); w2(t + 5, slot); slot = slot == 5 ? 0 : slot + 1; }
      waitv<32>(); bar();
      if (layer == 0 && a == 0) wait_mb(EV_RT, me, 16);
      waitv<0>(); bar();
      w3(3, 1); bar(); bar(); w3(0, 2); bar(); w3(1, 3);
      bar();
      w3(2, 0);
    } else {
      auto ab = [&](int t, int slot) { dma_lin<SC1, 10>(rw, L + L_D + slot * 20480 + a * 10240, lane * 16, soab + a * 10240 + t * 20480); };
      stamp(k, 4 + a4, sb + 3);
#pragma unroll 1
      for (int t = 0; t < 3; ++t) ab(t, t);
      waitv<20>();
      bar();
      if (layer == 0 && a == 0 && lane < 16) __hip_atomic_store((GAS u64*)(k.ws + OFF_MB + EV_RT * MB_EV + (k.P * 16 + lane) * 128) + n3, tag, __ATOMIC_RELAXED, __HIP_MEMORY_SCOPE_AGENT);
      int slot = 0;
#pragma unroll 1
      for (int t = 1; t <= 13; ++t) { waitv<10>(); bar(); ab(t + 2, slot); slot = slot == 2 ? 0 : slot + 1; }
      waitv<10>(); bar(); waitv<0>(); bar();
      bar();
    }
    push(ev0 + EV_EX, k.P * 16 + k.np, 4, 4, k.kq);
    if (layer == 0) {
#pragma unroll
      for (int j = 0; j < 12; ++j) k4[j >> 3][j & 7] = ld16(rw, lane * 16 + a4 * 1024, so4 + (5 + (j >> 3)) * 30720 + (j & 7) * 4096);
      if (isW) {
        f32x2 s = {0.f, 0.f};
#pragma unroll
        for (int j = 0; j < 10; ++j) s += __builtin_bit_cast(f32x2, __builtin_amdgcn_raw_buffer_load_b64(rw, lane * 8, OFF_CS + (j * 3072 + a * 1536 + 96 * n3) * 4, SC1));
        if (lane < 48) *(LAS f32x2*)(L + L_CS3 + (96 * a + 2 * lane) * 4) = s;
      }
    }
    waitv<0>();
    bar();
    poll(ev0 + EV_EX, me, 4);
    push(ev0 + EV_X1, k.P * 16, 1, 16, n3);
    if (layer == 0) {
#pragma unroll
      for (int j = 12; j < 24; ++j) k4[j >> 3][j & 7] = ld16(rw, lane * 16 + a4 * 1024, so4 + (5 + (j >> 3)) * 30720 + (j & 7) * 4096);
    }
    waitv<0>();
    stamp(k, 4 + a4, sb + 11);
    bar();
    poll(ev0 + EV_X1, me, 16);
    if (isW) {
      bar();
#pragma unroll 1
      for (int t = 1; t <= 6; ++t) { waitv<24>(); bar(); w3(t + 3, (t + 1) & 3); }
      u32x2 sv[8];
      stats_load(k, rw, 2 * layer + 1, 8 * k.P + 4 * a, sv);
      waitv<24>(); bar(); waitv<12>(); bar();
      stats_table(k, a, sv, 1.f);
      waitv<0>();
      w4(2);
      bar();
      w4(3);
      bar();
      w4(0); waitv<30>(); w4(1); waitv<30>(); w4(4);
      waitv<0>();
      stamp(k, 4 + a4, sb + 15);
    } else {
      const unsigned so = OFF_XT + (2 * layer + 1) * XT_BUF + (8 * k.P + 4 * a) * 20480;
      auto xt = [&](int t, int slot) {
#pragma unroll
        for (int j = 0; j < 4; ++j) dma_lin<SC1, 2>(rw, L + L_D + slot * 16384 + (4 * a + j) * 2048, lane * 16, so + j * 20480 + t * 2048);
      };
      stamp(k, 4 + a4, sb + 10);
#pragma unroll 1
      for (int t = 0; t < 3; ++t) xt(t, t);
      waitv<16>();
      bar();
      int slot = 0;
#pragma unroll 1
      for (int t = 1; t <= 7; ++t) { waitv<8>(); bar(); xt(t + 2, slot); slot = slot == 2 ? 0 : slot + 1; }
      waitv<8>(); bar(); waitv<0>(); bar();
      bar();
    }
    push(ev0 + EV_H, k.P * 16, 1, 16, n3);
    poll(ev0 + EV_H, me, 16);
#pragma unroll
    for (int n = 0; n < 3; ++n) {
      unsigned kb = L_W4 + n * 30720 + a4 * 1024 + lane * 16; pin(kb);
      bar();
#pragma unroll
      for (int j = 0; j < 8; ++j) if (j < 7 || isW) *(LAS bf16x8*)(L + kb + j * 4096) = k4[n][j];
    }
    bar();
    bar();
    if (layer == 0) {
      unsigned kb[2] = {L_D + lane * 16 + ((a4 >> 1) * 10 + a) * 1024, 0}; kb[1] = kb[0] + 61440; pin(kb[0]); pin(kb[1]);
#pragma unroll
      for (int j = 0; j < 25; ++j) *(LAS bf16x8*)(L + kb[j / 15] + ((j / 5) % 3) * 20480 + (j % 5) * 2048) = k1[j];
      push(EV_PER_LAYER + EV_X, k.P * 16, 1, 16, n3);
      if (isW) w2x3(0, 3);
    }
  }
#undef lane
  STAMP_OUT(k, 4 + a4);
#define lane k.lane
  if (k.b == 0 && a4 == 2 && lane == 0) *(GAS u64*)k.ws = tag;
#undef lane
#undef a
#undef me
#undef n3
#undef sw
#undef vo2
#undef so2
#undef so3
#undef so4
}

__global__ void __launch_bounds__(512, 2) fnet_glu_kernel(const char* x0, const char* wsr, const char* wsi, const char* wdr, const char* wdi, const char* wg, const char* wu, const char* wdn, char* out, char* ws) {
  __shared__ __attribute__((aligned(16))) char smem[163840];
  Ctx k;
  k.lds = (LAS char*)smem; k.lane = threadIdx.x & 63; k.b = blockIdx.x;
  const int w = __builtin_amdgcn_readfirstlane(threadIdx.x >> 6);
  if (CODE_BYTES && w >= 4) {
    const char* pc; asm volatile("s_getpc_b64 %0" : "=s"(pc));
    touch<0>(mk(pc, CODE_BYTES), k.lds + L_JUNK, (threadIdx.x - 256) * 128, 0);
  }
  asm volatile("" : "+s"(x0), "+s"(wsr), "+s"(wsi), "+s"(wdr), "+s"(wdi), "+s"(wg), "+s"(wu), "+s"(wdn), "+s"(out), "+s"(ws));
  k.ws = ws;
  const int x = k.b & 7, i = k.b >> 3;
  k.kq = x >> 1; k.P = 8 * (x & 1) + (i >> 2); k.np = i & 3;
  k.P1 = 4 * (x >> 1) + (i >> 3); k.mat = x & 1; k.ct1 = i & 7;
  if (w < 4) consumer(k, x0, wg, wu, wdn, out, w);
  else loader(k, (x >> 2) ? wsi : wsr, (x & 1) ? wdi : wdr, w - 4);
}
}

extern "C" void kernel_launch(void* const* d_in, const int* in_sizes, int n_in, void* d_out, int out_size, void* d_ws, size_t ws_size, hipStream_t stream) {
  fnet_glu_kernel<<<256, 512, 0, stream>>>((const char*)d_in[0], (const char*)d_in[1], (const char*)d_in[2], (const char*)d_in[3], (const char*)d_in[4],
                                           (const char*)d_in[6], (const char*)d_in[7], (const char*)d_in[8], (char*)d_out, (char*)d_ws);
}
```

```cpp
#include <hip/hip_runtime.h>

typedef unsigned long long u64;
typedef short bf16x8 __attribute__((ext_vector_type(8)));
typedef short s16x4 __attribute__((ext_vector_type(4)));
typedef float f32x4 __attribute__((ext_vector_type(4)));
typedef float f32x2 __attribute__((ext_vector_type(2)));
typedef unsigned u32x4 __attribute__((ext_vector_type(4)));
typedef unsigned u32x2 __attribute__((ext_vector_type(2)));
typedef __bf16 bf2 __attribute__((ext_vector_type(2)));
#define LAS __attribute__((address_space(3)))
#define GAS __attribute__((address_space(1)))
#define DI __device__ __forceinline__
typedef __amdgpu_buffer_rsrc_t rsrc_t;

#define STAMPS 0
#define WIN1 4
#define WIN4 4
#define ROT0 8
#define FINE 0
#define WPRIO 0
#define ROT1 1
#define NORT 0
#define G1EXP 0
#define NAP 10
#define CODE_BYTES 31232

namespace {
constexpr unsigned OFF_MB = 4096, MB_EV = 256 * 128;
constexpr int EV_X = 0, EV_AB = 1, EV_EX = 2, EV_X1 = 3, EV_H = 4, EV_PER_LAYER = 5, EV_RT = 10;
constexpr unsigned OFF_CS = 0x60000;
constexpr unsigned OFF_ST = 0xA0000, ST_BUF = 8 * 2048 * 8;
constexpr unsigned OFF_XT = 0x120000, XT_BUF = 2048 * 640 * 2;
constexpr unsigned OFF_AB = OFF_XT + 4 * XT_BUF, AB_BUF = 2048 * 1280 * 2;
constexpr unsigned OFF_HT = OFF_AB + 2 * AB_BUF, HT_BUF = 2048 * 1536 * 2;
constexpr unsigned OFF_EX = OFF_HT + 2 * HT_BUF, EX_BUF = 256 * 12 * 2560;
constexpr unsigned OFF_W3 = OFF_EX + 2 * EX_BUF;
constexpr unsigned OFF_W4 = OFF_W3 + 16 * 20 * 12288;
constexpr unsigned WS_END = OFF_W4 + 8 * 48 * 5120;
constexpr unsigned OFF_STAMP = 0x6000000;
static_assert(OFF_MB + 11 * MB_EV <= OFF_CS && OFF_CS + 20 * 3072 * 4 <= OFF_ST && OFF_ST + 4 * ST_BUF <= OFF_XT && WS_END <= OFF_STAMP, "ws map");
constexpr unsigned L_MEAN = 0, L_RSTD = 512, L_CS1 = 1024, L_CS3 = 2304, L_JUNK = 3072;
constexpr unsigned L_D = 4096;
constexpr unsigned L_WR = L_D + 61440, L_W4 = L_D + 6144, L_RT = L_D + 102400, L_XR = L_D + 110592, L_RED = L_W4;
constexpr int SC1 = 16;
constexpr unsigned SPIN_CAP = 1u << 16;

DI rsrc_t mk(const void* p, unsigned bytes) { return __builtin_amdgcn_make_buffer_rsrc((void*)p, 0, bytes, 0x00020000); }
template <int N> DI void waitv() { asm volatile("s_waitcnt vmcnt(%0)" ::"n"(N) : "memory"); }
DI void bar() { asm volatile("s_waitcnt lgkmcnt(0)\n\ts_barrier" ::: "memory"); }
DI void lgk0() { __builtin_amdgcn_sched_barrier(0); __builtin_amdgcn_s_waitcnt(0xC07F); __builtin_amdgcn_sched_barrier(0); }
DI void barv() { lgk0(); asm volatile("s_barrier" ::: "memory"); __builtin_amdgcn_sched_barrier(0); }
template <int NM, int NR> DI void weave() {
#pragma unroll
  for (int j = 0; j < NR; ++j) { __builtin_amdgcn_sched_group_barrier(0x8, 1, 0); __builtin_amdgcn_sched_group_barrier(0x100, 1, 0); }
#pragma unroll
  for (int j = NR; j < NM; ++j) { __builtin_amdgcn_sched_group_barrier(0x8, 1, 0); __builtin_amdgcn_sched_group_barrier(0x6, 2, 0); }
}
DI void drain_bar() { asm volatile("s_waitcnt vmcnt(0) lgkmcnt(0)\n\ts_barrier" ::: "memory"); }
DI void sched() { __builtin_amdgcn_sched_barrier(0); }
template <class T> DI void pin(T& x) { asm volatile("" : "+v"(x)); }
template <class T> DI void pins(T& x) { asm volatile("" : "+s"(x)); }
template <int AUX, int IMM> DI void dma(rsrc_t r, LAS char* l, unsigned vo, unsigned so) { __builtin_amdgcn_raw_ptr_buffer_load_lds(r, (LAS void*)l, 16, vo, so, IMM, AUX); }
template <int AUX> DI void touch(rsrc_t r, LAS char* l, unsigned vo, unsigned so) { __builtin_amdgcn_raw_ptr_buffer_load_lds(r, (LAS void*)l, 4, vo, so, 0, AUX); }
template <int AUX, int N> DI void dma_lin(rsrc_t r, LAS char* l, unsigned vo, unsigned so) {
#pragma unroll
  for (int j = 0; j < N; j += 4) {
    dma<AUX, 0>(r, l + j * 1024, vo, so + j * 1024);
    if (j + 1 < N) dma<AUX, 1024>(r, l + j * 1024, vo, so + j * 1024);
    if (j + 2 < N) dma<AUX, 2048>(r, l + j * 1024, vo, so + j * 1024);
    if (j + 3 < N) dma<AUX, 3072>(r, l + j * 1024, vo, so + j * 1024);
  }
}
DI bf16x8 ld16(rsrc_t r, unsigned vo, unsigned so) { return __builtin_bit_cast(bf16x8, __builtin_amdgcn_raw_buffer_load_b128(r, vo, so, SC1)); }
DI void st16(rsrc_t r, u32x4 v, unsigned vo) { __builtin_amdgcn_raw_buffer_store_b128(v, r, vo, 0, SC1); }
DI bf16x8 lds16(LAS char* l, unsigned a) { return *(LAS bf16x8*)(l + a); }
DI unsigned pk(float lo, float hi) { f32x2 v = {lo, hi}; return __builtin_bit_cast(unsigned, __builtin_convertvector(v, bf2)); }
DI float blo(unsigned u) { return __uint_as_float(u << 16); }
DI float bhi(unsigned u) { return __uint_as_float(u & 0xffff0000u); }
DI f32x4 unpk(unsigned a, unsigned b) { return (f32x4){blo(a), bhi(a), blo(b), bhi(b)}; }
DI f32x4 mfma(bf16x8 a, bf16x8 b, f32x4 c) { return __builtin_amdgcn_mfma_f32_16x16x32_bf16(a, b, c, 0, 0, 0); }
DI float dot2(unsigned a, unsigned b, float s) { return __builtin_amdgcn_fdot2_f32_bf16(__builtin_bit_cast(bf2, a), __builtin_bit_cast(bf2, b), s, false); }
DI float dot_ones(bf16x8 f, float s) {
  const u32x4 u = __builtin_bit_cast(u32x4, f);
#pragma unroll
  for (int j = 0; j < 4; ++j) s = dot2(u[j], 0x3f803f80u, s);
  return s;
}
DI float sum_q(float s) { s += __shfl_xor(s, 16); s += __shfl_xor(s, 32); return s; }
DI float sum8(float s) {
  s += __builtin_bit_cast(float, __builtin_amdgcn_update_dpp(0, __builtin_bit_cast(int, s), 0xB1, 0xf, 0xf, true));
  s += __builtin_bit_cast(float, __builtin_amdgcn_update_dpp(0, __builtin_bit_cast(int, s), 0x4E, 0xf, 0xf, true));
  s += __builtin_bit_cast(float, __builtin_amdgcn_update_dpp(0, __builtin_bit_cast(int, s), 0x141, 0xf, 0xf, true));
  return s;
}
DI u32x4 swap_pair(unsigned a0, unsigned a1, unsigned b0, unsigned b1) {
  const auto r0 = __builtin_amdgcn_permlane16_swap(a0, b0, false, false);
  const auto r1 = __builtin_amdgcn_permlane16_swap(a1, b1, false, false);
  return (u32x4){r0[0], r1[0], r0[1], r1[1]};
}

struct Ctx {
  char* ws; LAS char* lds; int lane, b;
  int kq, P, np;
  int P1, mat, ct1;
};
#define STAMP_DECL
#define stamp(k, w, id) do { (void)(id); } while (0)
#define STAMP_OUT(k, w)

DI void x_store(rsrc_t rw, int lane, unsigned vx, unsigned vs, u32x4 o0, u32x4 o1, u32x4 o2, const unsigned (&res)[10]) {
  st16(rw, o0, vx); st16(rw, o1, vx + 1024);
  if (!(lane & 16)) st16(rw, o2, vx + 2048);
  float s = 0.f, ss = 0.f;
#pragma unroll
  for (int j = 0; j < 10; ++j) { s = dot2(res[j], 0x3f803f80u, s); ss = dot2(res[j], res[j], ss); }
  const auto r = __builtin_amdgcn_permlane16_swap(__float_as_uint(s), __float_as_uint(ss), false, false);
  const unsigned t = __float_as_uint(__uint_as_float(r[0]) + __uint_as_float(r[1]));
  const auto r2 = __builtin_amdgcn_permlane32_swap(t, t, false, false);
  const float u = __uint_as_float(r2[0]) + __uint_as_float(r2[1]);
  if (lane < 32) __builtin_amdgcn_raw_buffer_store_b32(__float_as_uint(u), rw, vs, 0, SC1);
}

DI int rot1(const Ctx& k) { const int r = ((k.ct1 * 5) >> 2) + (((k.P1 & 3) * 5) >> 1); return ROT1 ? (r >= 10 ? r - 10 : r) : 0; }

#define PHASE_LANE() Ctx k = k0; asm volatile("" : "+v"(k.lane), "+s"(k.P), "+s"(k.kq), "+s"(k.np), "+s"(k.P1), "+s"(k.ct1), "+s"(k.mat)); \
  const int tfo = 8 * k.P + 4 * (k.kq >> 1) + w, cto = 2 * k.np + (k.kq & 1); (void)tfo; (void)cto; \
  const int lane = k.lane, q = lane >> 4, c = lane & 15, qs = 2 * (q & 1) + (q >> 1); (void)qs; \
  const unsigned rowrd = (c >> 3) * 1024 + (c & 7) * 128 + ((q ^ (c >> 1)) * 16); (void)rowrd
#define X_ADDR(xb) unsigned vx = OFF_XT + (xb) * XT_BUF + tfo * 20480 + (10 * cto + qs) * 256 + c * 16, vs = OFF_ST + (xb) * ST_BUF + cto * 16384 + tfo * 128 + lane * 4; pin(vx); pin(vs)

DI void consumer(const Ctx& k0, const char* x0, char* out, const int w) {
  LAS char* const L = k0.lds;
  const rsrc_t rw = mk(k0.ws, WS_END);
  unsigned res[10];
  STAMP_DECL;
  stamp(k0, w, 0);
#pragma unroll 1
  for (int layer = 0; layer < 2; ++layer) {
    const int sb = 3 + layer * 24;
    {
      PHASE_LANE();
      unsigned vo = OFF_AB + layer * AB_BUF + ((k.mat * 4 + (k.ct1 >> 1)) * 64 + 4 * k.P1 + w) * 10240 + 5 * (k.ct1 & 1) * 1024 + (qs * 16 + c) * 16;
      unsigned ta = (32 * w + 4 * q) * 4, tc = c * 4;
      pin(vo); pin(ta); pin(tc);
      f32x4 acc[2][5], mean[2], rstd[2]; float cs1[5];
      bf16x8 wf[2][5];
      auto tables = [&]() {
#pragma unroll
        for (int i = 0; i < 5; ++i) cs1[i] = *(LAS float*)(L + L_CS1 + tc + i * 64);
#pragma unroll
        for (int t = 0; t < 2; ++t) { mean[t] = *(LAS f32x4*)(L + ta + L_MEAN + 64 * t); rstd[t] = *(LAS f32x4*)(L + ta + L_RSTD + 64 * t); }
      };
#pragma unroll
      for (int i = 0; i < 5; ++i) { acc[0][i] = (f32x4){0.f, 0.f, 0.f, 0.f}; acc[1][i] = (f32x4){0.f, 0.f, 0.f, 0.f}; }
      if (layer == 0) {
        bf16x8 xf[2][2], cf[2], ones = {0x3f80, 0x3f80, 0x3f80, 0x3f80, 0x3f80, 0x3f80, 0x3f80, 0x3f80};
        f32x4 as[2], aq[2], ac[2];
        unsigned wt = rot1(k) * 10240, wa0 = L_D + rowrd, wc0 = wa0 + w * 2048;
        pin(wa0); pin(wc0);
        unsigned wa = wa0 + wt, xr = L_XR + w * 4096 + rowrd, x_slot = 32768;
#pragma unroll
        for (int i = 0; i < 5; ++i) { pin(acc[0][i]); pin(acc[1][i]); }
#pragma unroll
        for (int t = 0; t < 2; ++t) { as[t] = (f32x4){0.f, 0.f, 0.f, 0.f}; aq[t] = as[t]; ac[t] = as[t]; pin(as[t]); pin(aq[t]); pin(ac[t]); }
        pin(wa); pin(xr); pin(ones);
        bar();
        stamp(k, w, sb + 0);
#pragma unroll
        for (int i = 0; i < 5; ++i) wf[0][i] = lds16(L, wa + i * 2048);
        xf[0][0] = lds16(L, xr + 32768); xf[0][1] = lds16(L, xr + 32768 + 2048); cf[0] = lds16(L, wa + w * 2048);
        unsigned xn = (xr + 32768) ^ 64, wn = wa ^ 64, cn = wn + w * 2048;
        lgk0();
#pragma unroll 1
        for (int kt = 0; kt < 10; ++kt) {
#pragma unroll
          for (int h = 0; h < 2; ++h) {
            xf[h ^ 1][0] = lds16(L, xn); xf[h ^ 1][1] = lds16(L, xn + 2048); cf[h ^ 1] = lds16(L, cn);
#pragma unroll
            for (int i = 0; i < 5; ++i) wf[h ^ 1][i] = lds16(L, wn + i * 2048);
#pragma unroll
            for (int i = 0; i < 5; ++i) { acc[0][i] = mfma(xf[h][0], wf[h][i], acc[0][i]); acc[1][i] = mfma(xf[h][1], wf[h][i], acc[1][i]); }
#pragma unroll
            for (int t = 0; t < 2; ++t) { as[t] = mfma(xf[h][t], ones, as[t]); aq[t] = mfma(xf[h][t], xf[h][t], aq[t]); }
            ac[0] = mfma(ones, cf[h], ac[0]); ac[1] = mfma(ones, wf[h][4], ac[1]);
            if (h == 0) {
              x_slot = x_slot == 32768 ? 0 : x_slot + 16384; wt = wt == 92160 ? 0 : wt + 10240;
              xn = xr + x_slot; wn = wa0 + wt; cn = wc0 + wt;
            } else { xn ^= 64; wn ^= 64; cn ^= 64; }
            weave<16, 8>();
            if (h == 0) { barv(); if (FINE) stamp(k, w, 52 + kt); } else lgk0();
          }
        }
        stamp(k, w, 50);
        if (q == 0) { *(LAS float*)(L + L_CS1 + (16 * w + c) * 4) = ac[0][0]; *(LAS float*)(L + L_CS1 + (64 + c) * 4) = ac[1][0]; }
#pragma unroll
        for (int t = 0; t < 2; ++t) {
          const float s = ((c & 2) ? ((c & 1) ? as[t][3] : as[t][2]) : ((c & 1) ? as[t][1] : as[t][0])) * (1.f / 640.f);
          const float ss = ((c & 2) ? ((c & 1) ? aq[t][3] : aq[t][2]) : ((c & 1) ? aq[t][1] : aq[t][0])) * (1.f / 640.f);
          const float r = __builtin_amdgcn_rsqf(fmaxf(ss - s * s, 0.f) + 1e-5f);
          if ((c >> 2) == q) { *(LAS float*)(L + L_MEAN + (32 * w + 16 * t + c) * 4) = -s; *(LAS float*)(L + L_RSTD + (32 * w + 16 * t + c) * 4) = k.mat ? -r : r; }
        }
        bar();
        tables();
      } else {
        constexpr int WIN = WIN1;
#define KS(s) (((s) + 12) % 20)
        unsigned wb[2][2] = {{L_D + rowrd, L_D + rowrd + 51200}, {L_D + (rowrd ^ 64), L_D + (rowrd ^ 64) + 51200}};
        unsigned xv[2] = {OFF_XT + 2 * XT_BUF + (8 * k.P1 + 2 * w) * 20480 + lane * 16, 0}; xv[1] = xv[0] + 20480;
        pin(wb[0][0]); pin(wb[0][1]); pin(wb[1][0]); pin(wb[1][1]); pin(xv[0]); pin(xv[1]);
        bf16x8 xa[WIN][2];
        bar();
        stamp(k, w, sb + 0);
#pragma unroll
        for (int s = 0; s < WIN; ++s) { xa[s][0] = ld16(rw, xv[0] + (KS(s) & 3) * 1024, (KS(s) >> 2) * 4096); xa[s][1] = ld16(rw, xv[1] + (KS(s) & 3) * 1024, (KS(s) >> 2) * 4096); sched(); }
#pragma unroll
        for (int i = 0; i < 5; ++i) wf[0][i] = lds16(L, wb[0][1] + 10240 + i * 2048);
        sched();
#pragma unroll
        for (int s = 0; s < 20; ++s) {
          if (s == 20 - WIN) bar();
          if (s + 1 < 20) {
#pragma unroll
            for (int i = 0; i < 5; ++i) wf[(s + 1) & 1][i] = lds16(L, wb[(s + 1) & 1][KS(s + 1) / 10] + ((KS(s + 1) >> 1) % 5) * 10240 + i * 2048);
          } else { bar(); tables(); sched(); }
#pragma unroll
          for (int i = 0; i < 5; ++i) acc[0][i] = mfma(xa[s % WIN][0], wf[s & 1][i], acc[0][i]);
          if (s + 1 < 20) {
#pragma unroll
            for (int j = 0; j < 5; ++j) { __builtin_amdgcn_sched_group_barrier(0x100, 1, 0); __builtin_amdgcn_sched_group_barrier(0x8, 1, 0); }
            sched();
          }
          if (s + WIN < 20) { xa[s % WIN][0] = ld16(rw, xv[0] + (KS(s + WIN) & 3) * 1024, (KS(s + WIN) >> 2) * 4096); sched(); }
#pragma unroll
          for (int i = 0; i < 5; ++i) acc[1][i] = mfma(xa[s % WIN][1], wf[s & 1][i], acc[1][i]);
          if (s + 1 < 20) sched();
          if (s + WIN < 20) { xa[s % WIN][1] = ld16(rw, xv[1] + (KS(s + WIN) & 3) * 1024, (KS(s + WIN) >> 2) * 4096); sched(); }
        }
      }
      stamp(k, w, sb + 1);
#pragma unroll
      for (int i = 0; i < 5; ++i) {
        const float cs = cs1[i];
        const f32x4 a = (mean[0] * cs + acc[0][i]) * rstd[0], bb = (mean[1] * cs + acc[1][i]) * rstd[1];
        st16(rw, swap_pair(pk(a[0], a[1]), pk(a[2], a[3]), pk(bb[0], bb[1]), pk(bb[2], bb[3])), vo + i * 1024);
      }
      stamp(k, w, sb + 2);
      drain_bar();
      stamp(k, w, sb + 3);
    }
    {
      PHASE_LANE();
      const int wm = w >> 1, wn = w & 1;
      f32x4 acc[4][5];
      bf16x8 fa[2][5], fb[2][4];
#pragma unroll
      for (int t = 0; t < 4; ++t)
#pragma unroll
        for (int i = 0; i < 5; ++i) { acc[t][i] = (f32x4){0.f, 0.f, 0.f, 0.f}; pin(acc[t][i]); }
      unsigned wb = L_WR + wm * 8192 + rowrd, ab = L_D + wn * 5120 + lane * 16;
      unsigned ws_slot = 49152, ab_slot = 0;
      unsigned ev = OFF_EX + layer * EX_BUF + ((k.P * 16 + w * 4 + k.np) * 3 + ((k.kq - w) & 3) - 1) * 10240 + lane * 16;
      pin(wb); pin(ab); pin(ev);
      bar();
#pragma unroll
      for (int t = 0; t < 4; ++t) fb[0][t] = lds16(L, wb + 49152 + t * 2048);
      bar();
      stamp(k, w, sb + 4);
#pragma unroll
      for (int i = 0; i < 5; ++i) fa[0][i] = lds16(L, ab + i * 1024);
      unsigned an = ab + 10240, wn2 = (wb + 49152) ^ 64;
      lgk0();
#pragma unroll 1
      for (int kt = 0; kt < 16; ++kt) {
#pragma unroll
        for (int h = 0; h < 2; ++h) {
#pragma unroll
          for (int i = 0; i < 5; ++i) fa[h ^ 1][i] = lds16(L, an + i * 1024);
#pragma unroll
          for (int t = 0; t < 4; ++t) fb[h ^ 1][t] = lds16(L, wn2 + t * 2048);
#pragma unroll
          for (int t = 0; t < 4; ++t)
#pragma unroll
            for (int i = 0; i < 5; ++i) acc[t][i] = mfma(fa[h][i], fb[h][t], acc[t][i]);
          if (h == 0) {
            ab_slot = ab_slot == 40960 ? 0 : ab_slot + 20480; ws_slot = ws_slot == 81920 ? 0 : ws_slot + 16384;
            an = ab + ab_slot; wn2 = wb + ws_slot;
          } else { an += 10240; wn2 ^= 64; }
          weave<20, 9>();
          if (h == 0) barv(); else lgk0();
        }
      }
      stamp(k, w, sb + 5);
      if (w == k.kq) {
#pragma unroll
        for (int t = 0; t < 4; ++t)
#pragma unroll
          for (int i = 0; i < 5; ++i) *(LAS f32x4*)(L + L_D + (t * 5 + i) * 1024 + lane * 16) = acc[t][i];
      } else {
        const unsigned ev8 = ev - lane * 8;
#pragma unroll
        for (int t = 0; t < 4; ++t) {
          st16(rw, (u32x4){pk(acc[t][0][0], acc[t][0][1]), pk(acc[t][0][2], acc[t][0][3]), pk(acc[t][1][0], acc[t][1][1]), pk(acc[t][1][2], acc[t][1][3])}, ev + t * 2560);
          st16(rw, (u32x4){pk(acc[t][2][0], acc[t][2][1]), pk(acc[t][2][2], acc[t][2][3]), pk(acc[t][3][0], acc[t][3][1]), pk(acc[t][3][2], acc[t][3][3])}, ev + t * 2560 + 1024);
          __builtin_amdgcn_raw_buffer_store_b64((u32x2){pk(acc[t][4][0], acc[t][4][1]), pk(acc[t][4][2], acc[t][4][3])}, rw, ev8, t * 2560 + 2048, SC1);
        }
      }
      stamp(k, w, sb + 6);
      drain_bar();
      stamp(k, w, sb + 7);
      if (layer == 0) {
        const rsrc_t rx = mk(x0, 2048 * 640 * 2);
#pragma unroll
        for (int j = 0; j < 3; ++j) {
          const int u = 64 * j + lane, r = (u * 205) >> 11;
          dma<0, 0>(rx, L + L_D + 20480 + w * 3072 + j * 1024, u < 160 ? r * 1280 + (u - 10 * r) * 16 : 0x7fffffff, 16 * tfo * 1280 + cto * 160);
        }
        unsigned xa = L_D + 20480 + w * 3072 + c * 160 + 8 * q;
        pin(xa);
        __builtin_amdgcn_s_waitcnt(0);
        asm volatile("" ::: "memory");
#pragma unroll
        for (int i = 0; i < 5; ++i) { const u32x2 t = *(LAS u32x2*)(L + xa + 32 * i); res[2 * i] = t[0]; res[2 * i + 1] = t[1]; }
      }
      f32x4 v[5];
#pragma unroll
      for (int i = 0; i < 5; ++i) { v[i] = *(LAS f32x4*)(L + L_D + (w * 5 + i) * 1024 + lane * 16) + unpk(res[2 * i], res[2 * i + 1]); pin(v[i]); }
      unsigned rv = OFF_EX + layer * EX_BUF + (k.P * 16 + k.kq * 4 + k.np) * 30720 + w * 2560 + lane * 16, rv8 = rv - lane * 8;
      pin(rv); pin(rv8);
      X_ADDR(2 * layer + 1);
      bar();
      stamp(k, w, sb + 8);
      u32x4 e[3][2]; u32x2 e8[3];
#pragma unroll
      for (int n = 0; n < 3; ++n) {
        e[n][0] = __builtin_amdgcn_raw_buffer_load_b128(rw, rv, n * 10240, SC1);
        e[n][1] = __builtin_amdgcn_raw_buffer_load_b128(rw, rv + 1024, n * 10240, SC1);
        e8[n] = __builtin_amdgcn_raw_buffer_load_b64(rw, rv8 + 2048, n * 10240, SC1);
        sched();
      }
#pragma unroll
      for (int n = 0; n < 3; ++n) {
#pragma unroll
        for (int j = 0; j < 2; ++j) { v[2 * j] += unpk(e[n][j][0], e[n][j][1]); v[2 * j + 1] += unpk(e[n][j][2], e[n][j][3]); }
        v[4] += unpk(e8[n][0], e8[n][1]);
        sched();
      }
#pragma unroll
      for (int i = 0; i < 5; ++i) { res[2 * i] = pk(v[i][0], v[i][1]); res[2 * i + 1] = pk(v[i][2], v[i][3]); }
      x_store(rw, lane, vx, vs, swap_pair(res[0], res[1], res[2], res[3]), swap_pair(res[4], res[5], res[6], res[7]), swap_pair(res[8], res[9], res[8], res[9]), res);
      stamp(k, w, sb + 9);
      drain_bar();
      stamp(k, w, sb + 10);
    }
    {
      PHASE_LANE();
      const int wm = w >> 1, wn = w & 1;
      f32x4 acc[4][6];
      bf16x8 fa[2][6], fb[2][4];
#pragma unroll
      for (int t = 0; t < 4; ++t)
#pragma unroll
        for (int i = 0; i < 6; ++i) { acc[t][i] = (f32x4){0.f, 0.f, 0.f, 0.f}; pin(acc[t][i]); }
      unsigned wb = L_WR + wn * 6144 + lane * 16, xb = L_D + wm * 8192 + lane * 16;
      unsigned w_slot = 49152, x_slot = 0;
      unsigned vo = OFF_HT + layer * HT_BUF + ((8 * k.P + 4 * wm + (q & 1)) * 48 + 3 * (4 * k.kq + k.np)) * 1024 + (6 * wn + (q >> 1)) * 256 + c * 16, vo2 = vo + 98304;
      unsigned ta = (64 * wm + c) * 4, tc = L_CS3 + (48 * wn + 4 * q) * 4;
      pin(wb); pin(xb); pin(vo); pin(vo2); pin(ta); pin(tc);
      bar();
#pragma unroll
      for (int i = 0; i < 6; ++i) fa[0][i] = lds16(L, wb + 49152 + i * 1024);
      bar();
      stamp(k, w, sb + 11);
#pragma unroll
      for (int t = 0; t < 4; ++t) fb[0][t] = lds16(L, xb + t * 2048);
      unsigned wn3 = wb + 49152 + 12288, xn = xb + 1024;
      lgk0();
#pragma unroll 1
      for (int kt = 0; kt < 10; ++kt) {
#pragma unroll
        for (int h = 0; h < 2; ++h) {
#pragma unroll
          for (int i = 0; i < 6; ++i) fa[h ^ 1][i] = lds16(L, wn3 + i * 1024);
#pragma unroll
          for (int t = 0; t < 4; ++t) fb[h ^ 1][t] = lds16(L, xn + t * 2048);
#pragma unroll
          for (int t = 0; t < 4; ++t)
#pragma unroll
            for (int i = 0; i < 6; ++i) acc[t][i] = mfma(fa[h][i], fb[h][t], acc[t][i]);
          if (h == 0) {
            x_slot = x_slot == 32768 ? 0 : x_slot + 16384; w_slot = w_slot == 73728 ? 0 : w_slot + 24576;
            wn3 = wb + w_slot; xn = xb + x_slot;
          } else { wn3 += 12288; xn += 1024; }
          weave<24, 10>();
          if (h == 0) barv(); else lgk0();
        }
      }
      stamp(k, w, sb + 12);
      float mean[4], rstd[4], rl2[4];
#pragma unroll
      for (int t = 0; t < 4; ++t) { mean[t] = *(LAS float*)(L + ta + L_MEAN + 64 * t); rstd[t] = *(LAS float*)(L + ta + L_RSTD + 64 * t); rl2[t] = rstd[t] * -1.44269504089f; }
#pragma unroll
      for (int f = 0; f < 3; ++f) {
        const f32x4 cg = *(LAS f32x4*)(L + tc + 64 * f), cu = *(LAS f32x4*)(L + tc + 384 + 64 * f);
        unsigned h[4][2];
#pragma unroll
        for (int t = 0; t < 4; ++t) {
          f32x4 g = (mean[t] * cg + acc[t][2 * f]) * rl2[t];
          const f32x4 u = (mean[t] * cu + acc[t][2 * f + 1]) * rstd[t];
          asm("" : "+v"(g));
          f32x4 o;
#pragma unroll
          for (int r = 0; r < 4; ++r) o[r] = __builtin_amdgcn_exp2f(g[r]);
          o += 1.f;
#pragma unroll
          for (int r = 0; r < 4; ++r) o[r] = __builtin_amdgcn_rcpf(o[r]);
          o *= u;
          h[t][0] = pk(o[0], o[1]); h[t][1] = pk(o[2], o[3]);
        }
        st16(rw, swap_pair(h[0][0], h[0][1], h[1][0], h[1][1]), vo + f * 512);
        st16(rw, swap_pair(h[2][0], h[2][1], h[3][0], h[3][1]), vo2 + f * 512);
      }
      stamp(k, w, sb + 13);
      drain_bar();
      stamp(k, w, sb + 14);
    }
    {
      PHASE_LANE();
      constexpr int WIN = WIN4;
      static_assert(24 - WIN > 17, "barrier order");
      const int kh = w & 1;
      unsigned wb[3] = {L_W4 + kh * 5120 + lane * 16, 0, 0}; wb[1] = wb[0] + 51200; wb[2] = wb[0] + 102400;
      unsigned hv[2] = {OFF_HT + layer * HT_BUF + tfo * 49152 + kh * 1024 + lane * 16, OFF_HT + layer * HT_BUF + (tfo ^ 1) * 49152 + kh * 1024 + lane * 16};
      unsigned rd = L_RED + w * 5120 + lane * 16, wr = L_RED + (w ^ 1) * 5120 + lane * 16;
      pin(wb[0]); pin(wb[1]); pin(wb[2]); pin(hv[0]); pin(hv[1]); pin(rd); pin(wr);
      X_ADDR(2);
      unsigned vout = (16 * tfo + c) * 1280 + (10 * cto + qs) * 16;
      pin(vout);
      bf16x8 ha[WIN][2], wf[2][5];
      f32x4 acc[2][5];
#pragma unroll
      for (int i = 0; i < 5; ++i) { acc[0][i] = unpk(res[2 * i], res[2 * i + 1]); pin(acc[0][i]); acc[1][i] = (f32x4){0.f, 0.f, 0.f, 0.f}; }
#pragma unroll
      for (int i = 0; i < 5; ++i) wf[0][i] = lds16(L, wb[0] + i * 1024);
      bar();
      stamp(k, w, sb + 15);
#pragma unroll
      for (int s = 0; s < WIN; ++s) { ha[s][1] = ld16(rw, hv[1] + (s & 1) * 2048, (s >> 1) * 4096); sched(); ha[s][0] = ld16(rw, hv[0] + (s & 1) * 2048, (s >> 1) * 4096); sched(); }
#pragma unroll
      for (int s = 0; s < 24; ++s) {
        if (s == 2 || s == 8 || s == 17 || s == 24 - WIN) bar();
        if (s + 1 < 24) {
#pragma unroll
          for (int i = 0; i < 5; ++i) wf[(s + 1) & 1][i] = lds16(L, wb[((s + 1) % 15) / 5] + ((s + 1) % 5) * 10240 + i * 1024);
        }
#pragma unroll
        for (int i = 0; i < 5; ++i) acc[1][i] = mfma(wf[s & 1][i], ha[s % WIN][1], acc[1][i]);
        if (s + 1 < 24) {
#pragma unroll
          for (int j = 0; j < 5; ++j) { __builtin_amdgcn_sched_group_barrier(0x100, 1, 0); __builtin_amdgcn_sched_group_barrier(0x8, 1, 0); }
          sched();
        }
        if (s + WIN < 24) { ha[s % WIN][1] = ld16(rw, hv[1] + ((s + WIN) & 1) * 2048, ((s + WIN) >> 1) * 4096); sched(); }
#pragma unroll
        for (int i = 0; i < 5; ++i) acc[0][i] = mfma(wf[s & 1][i], ha[s % WIN][0], acc[0][i]);
        if (s + 1 < 24) sched();
        if (s + WIN < 24) { ha[s % WIN][0] = ld16(rw, hv[0] + ((s + WIN) & 1) * 2048, ((s + WIN) >> 1) * 4096); sched(); }
      }
#pragma unroll
      for (int i = 0; i < 5; ++i) *(LAS f32x4*)(L + wr + i * 1024) = acc[1][i];
      bar();
      stamp(k, w, sb + 16);
#pragma unroll
      for (int i = 0; i < 5; ++i) { const f32x4 v = acc[0][i] + *(LAS f32x4*)(L + rd + i * 1024); res[2 * i] = pk(v[0], v[1]); res[2 * i + 1] = pk(v[2], v[3]); }
      const u32x4 o0 = swap_pair(res[0], res[1], res[2], res[3]), o1 = swap_pair(res[4], res[5], res[6], res[7]), o2 = swap_pair(res[8], res[9], res[8], res[9]);
      if (layer == 1) {
        const rsrc_t ro = mk(out, 2048 * 640 * 2);
        __builtin_amdgcn_raw_buffer_store_b128(o0, ro, vout, 0, 0);
        __builtin_amdgcn_raw_buffer_store_b128(o1, ro, vout + 64, 0, 0);
        if (!(q & 1)) __builtin_amdgcn_raw_buffer_store_b128(o2, ro, vout + 128, 0, 0);
        stamp(k, w, sb + 17);
      } else {
        x_store(rw, lane, vx, vs, o0, o1, o2, res);
        bar();
        stamp(k, w, sb + 17);
        drain_bar();
      }
      stamp(k, w, sb + 18);
    }
  }
  STAMP_OUT(k0, w);
}

DI void stats_load(const Ctx& k, rsrc_t rw, int xb, int tf0, u32x2 (&sv)[8]) {
#pragma unroll
  for (int j = 0; j < 8; ++j) sv[j] = __builtin_amdgcn_raw_buffer_load_b64(rw, k.lane * 8, OFF_ST + xb * ST_BUF + j * 16384 + tf0 * 128, SC1);
}
DI void stats_table(const Ctx& k, int a, u32x2 (&sv)[8], float sign) {
  f32x2 s = {0.f, 0.f};
#pragma unroll
  for (int j = 0; j < 8; ++j) { pin(sv[j]); s += __builtin_bit_cast(f32x2, sv[j]); }
  s *= 1.f / 640.f;
  const f32x2 ss = {__shfl_xor(s[0], 8), __shfl_xor(s[1], 8)};
  if (!(k.lane & 8)) {
    const unsigned t = (64 * a + (k.lane & 48) + 2 * (k.lane & 7)) * 4;
    *(LAS f32x2*)(k.lds + L_MEAN + t) = -s;
    *(LAS f32x2*)(k.lds + L_RSTD + t) = (f32x2){sign * __builtin_amdgcn_rsqf(fmaxf(ss[0] - s[0] * s[0], 0.f) + 1e-5f), sign * __builtin_amdgcn_rsqf(fmaxf(ss[1] - s[1] * s[1], 0.f) + 1e-5f)};
  }
}

DI void loader(const Ctx& k0, const char* x0, const char* wsp, const char* wdp, const char* wg, const char* wu, const char* wdn, int a4) {
  Ctx k = k0;
  const bool isW = a4 < 2;
  LAS char* const L = k.lds;
  const rsrc_t rw = mk(k.ws, WS_END);
  const rsrc_t rs = mk(wsp, 2048 * 2048 * 2);
  auto fresh = [&]() { asm volatile("" : "+v"(k.lane), "+s"(k.P), "+s"(k.kq), "+s"(k.np), "+s"(k.P1), "+s"(k.ct1), "+s"(k.mat), "+s"(a4)); };
#define lane k.lane
#define a (a4 & 1)
#define me (k.P * 16 + k.kq * 4 + k.np)
#define n3 (4 * k.kq + k.np)
#define sw (((lane & 7) ^ (4 * a + (lane >> 4))) * 16)
#define vo2 ((lane >> 3) * 4096 + sw)
#define so2 ((128 * k.P + 8 * a) * 4096 + (k.kq & 1) * 2048)
#define so3 (OFF_W3 + n3 * 20 * 12288 + a * 12288)
#define so4 (OFF_W4 + (2 * k.np + (k.kq & 1)) * 48 * 5120)
  bf16x8 k1[25], k4[3][8];
  u64 tag = 0;
  int rot = ROT0 * k.np;
  STAMP_DECL;
  auto w2 = [&](int t, int slot) {
#pragma unroll
    for (int j = 0; j < 8; ++j) dma<0, 0>(rs, L + L_WR + slot * 16384 + (2 * j + a) * 1024, vo2, so2 + j * 16 * 4096 + ((t + rot) & 15) * 128);
  };
  auto w2n = [&](int t0, int t1) {
#pragma unroll 1
    for (int t = t0; t < t1; ++t) w2(t, t < 3 ? t + 3 : t - 3);
  };
  auto w3 = [&](int t, int slot) { dma_lin<SC1, 12>(rw, L + L_WR + slot * 24576 + a * 12288, lane * 16, so3 + t * 24576); };
  auto w4 = [&](int ch) { dma_lin<SC1, 15>(rw, L + L_W4 + ch * 30720 + a * 15360, lane * 16, so4 + ch * 30720 + a * 15360); };
  auto push = [&](int ev, int wg0, int wgstep, int n, int slot) {
    GAS u64* dst = (GAS u64*)(k.ws + OFF_MB + ev * MB_EV + (wg0 + (lane & (n - 1)) * wgstep) * 128) + slot;
    pin(dst); pin(tag);
    bar();
    if (a4 == 2 && lane < n) __hip_atomic_store(dst, tag, __ATOMIC_RELAXED, __HIP_MEMORY_SCOPE_AGENT);
    fresh();
  };
  auto wait_mb = [&](int ev, int wg, int n) {
    GAS u64* src = (GAS u64*)(k.ws + OFF_MB + ev * MB_EV + wg * 128) + (lane & (n - 1));
#pragma unroll 1
    for (unsigned spins = 0; spins < SPIN_CAP; ++spins) {
      const u64 v = __hip_atomic_load(src, __ATOMIC_RELAXED, __HIP_MEMORY_SCOPE_AGENT);
      if (__all(v == tag)) break;
      __builtin_amdgcn_s_sleep(1);
    }
  };
  auto poll = [&](int ev, int wg, int n, bool nap) {
    if (a4 == 2) { if (nap) __builtin_amdgcn_s_sleep(NAP); wait_mb(ev, wg, n); }
    drain_bar();
  };

  auto get_tag = [&]() { const char* e = k.ws; pins(e); tag = *(const GAS u64*)e; };
  auto mix_tag = [&]() { pin(tag); tag = tag * 6364136223846793005ull + 1442695040888963407ull; };
  if (isW) __builtin_amdgcn_s_setprio(WPRIO); else __builtin_amdgcn_s_setprio(3);
  if (isW) {
    const rsrc_t rd = mk(wdp, 640 * 640 * 2), rx = mk(x0, 2048 * 640 * 2);
    const unsigned v1 = (lane >> 3) * 1280 + sw, sd = (80 * k.ct1 + 8 * a) * 1280, sx = (128 * k.P1 + 8 * a) * 1280;
    auto xw = [&](int t, int slot) {
#pragma unroll
      for (int j = 0; j < 8; ++j) dma<0, 0>(rx, L + L_XR + slot * 16384 + (2 * j + a) * 1024, v1, sx + 16 * j * 1280 + t * 128);
#pragma unroll
      for (int j = 0; j < 5; ++j) dma<0, 0>(rd, L + L_D + t * 10240 + (2 * j + a) * 1024, v1, sd + 16 * j * 1280 + t * 128);
    };
    int slot = 2, wt = rot1(k);
#pragma unroll 1
    for (int t = 0; t < 10; ++t) {
      if (t >= 2) { waitv<13>(); bar(); }
      xw(wt, slot); slot = slot == 2 ? 0 : slot + 1; wt = wt == 9 ? 0 : wt + 1;
    }
    waitv<13>(); bar(); w2(0, 3);
    waitv<8>(); bar(); w2(1, 4);
    bar();
    w2(2, 5);
  } else {
    LAS char* const t = L + L_RT + a * 4096;
    const unsigned tr = (unsigned)(size_t)t + (lane >> 4) * 1024 + ((lane >> 2) & 3) * 128 + (lane & 3) * 8;
    const bf16x8 ones = {0x3f80, 0x3f80, 0x3f80, 0x3f80, 0x3f80, 0x3f80, 0x3f80, 0x3f80};
    const int e = k.b * 2 + a + NORT * 500;
    const rsrc_t ro = mk(k.ws, e < 480 ? WS_END : 0);
    int ks = (e * 2731) >> 16, stp = e - ks * 24;
    unsigned sv = (lane >> 3) * 3072 + (lane & 7) * 16 + 32 * ks * 3072 + 128 * stp, st8 = 8 * 3072;
    const char* src = wg;
    __builtin_amdgcn_s_sleep(8);
#pragma unroll 1
    for (int m = 0; m < 3; ++m) {
      const rsrc_t rr = mk(src, e < 480 ? 640 * 1536 * 2 : 0);
#pragma unroll
      for (int j = 0; j < 4; ++j) dma<0, 0>(rr, t + j * 1024, sv, j * st8);
      bar();
      pins(stp);
      unsigned dst[4], dcs = OFF_CS + (ks * 3072 + m * 1536 + 64 * stp) * 4;
      const unsigned d0 = m < 2 ? OFF_W3 + m * 1024 + ks * 12288 : OFF_W4 + ks * 5120, mul = m < 2 ? 171 : 205, sf = m < 2 ? 2048 : 1024, sg = m < 2 ? 19 * 12288 : 47 * 5120;
#pragma unroll
      for (int n = 0; n < 4; ++n) { const int nf = 4 * stp + n; dst[n] = lane * 16 + d0 + ((nf * mul) >> 10) * sg + nf * sf; pin(dst[n]); }
      pins(dcs);
      bar();
      src = m == 0 ? wu : wdn;
      if (m == 1) { ks = (e * 6554) >> 16; stp = e - ks * 10; sv = (lane >> 3) * 1280 + (lane & 7) * 16 + 32 * ks * 1280 + 128 * stp; st8 = 8 * 1280; }
      pin(sv);
      bar();
      u32x2 r[8];
      asm volatile("s_waitcnt vmcnt(0)\n\tds_read_b64_tr_b16 %0, %8\n\tds_read_b64_tr_b16 %1, %8 offset:512\n\tds_read_b64_tr_b16 %2, %8 offset:32\n\tds_read_b64_tr_b16 %3, %8 offset:544\n\t"
                   "ds_read_b64_tr_b16 %4, %8 offset:64\n\tds_read_b64_tr_b16 %5, %8 offset:576\n\tds_read_b64_tr_b16 %6, %8 offset:96\n\tds_read_b64_tr_b16 %7, %8 offset:608\n\ts_waitcnt lgkmcnt(0)"
                   : "=&v"(r[0]), "=&v"(r[1]), "=&v"(r[2]), "=&v"(r[3]), "=&v"(r[4]), "=&v"(r[5]), "=&v"(r[6]), "=&v"(r[7]) : "v"(tr) : "memory");
      f32x4 cs[4];
#pragma unroll
      for (int n = 0; n < 4; ++n) {
        const u32x4 f = {r[2 * n][0], r[2 * n][1], r[2 * n + 1][0], r[2 * n + 1][1]};
        st16(ro, f, dst[n]);
        cs[n] = mfma(ones, __builtin_bit_cast(bf16x8, f), (f32x4){0.f, 0.f, 0.f, 0.f});
      }
      if (m < 2) __builtin_amdgcn_raw_buffer_store_b32(__float_as_uint((lane & 32) ? ((lane & 16) ? cs[3][0] : cs[2][0]) : ((lane & 16) ? cs[1][0] : cs[0][0])), ro, lane * 4, dcs, SC1);
    }
    get_tag();
    bar(); bar();
    mix_tag();
  }
  {
    unsigned kb[2] = {L_D + lane * 16 + ((a4 >> 1) * 10 + a) * 1024, 0}; kb[1] = kb[0] + 61440; pin(kb[0]); pin(kb[1]);
    bar();
#pragma unroll
    for (int j = 0; j < 25; ++j) k1[j] = lds16(L, kb[j / 15] + ((j / 5) % 3) * 20480 + (j % 5) * 2048);
    if (!isW) waitv<0>();
    stamp(k, 4 + a4, 51);
  }
#pragma unroll 1
  for (int layer = 0; layer < 2; ++layer) {
    const int ev0 = layer * EV_PER_LAYER, sb = 3 + layer * 24;
    if (layer) {
      stamp(k, 4 + a4, sb + 0);
      poll(ev0 + EV_X, k.P1 * 16 + k.mat * 8 + k.ct1, 16, false);
      if (!isW) { u32x2 sv[8]; stats_load(k, rw, 2, 8 * k.P1 + 4 * a, sv); stats_table(k, a, sv, k.mat ? -1.f : 1.f); }
      bar();
      if (isW) w2n(3, 5);
      bar();
      if (isW) w2n(5, 6);
    }
    push(ev0 + EV_AB, (2 * k.mat + (k.P1 >> 3)) * 4 + (k.ct1 >> 1), 16, 16, 2 * (k.P1 & 7) + (k.ct1 & 1));
    GAS u64* rtf = (GAS u64*)(k.ws + OFF_MB + EV_RT * MB_EV);
    if (layer == 0 && a4 == 3 && lane == 0) __hip_atomic_store(rtf + k.b, tag, __ATOMIC_RELAXED, __HIP_MEMORY_SCOPE_AGENT);
    if (isW && layer == 0) w2n(3, 6);
    stamp(k, 4 + a4, sb + 4);
    poll(ev0 + EV_AB, me, 16, layer == 0);
    const unsigned soab = OFF_AB + layer * AB_BUF + (((k.kq >> 1) * 4 + k.np) * 64 + 32 * (k.kq & 1)) * 10240;
    if (isW) {
      bar();
      int slot = 3;
#pragma unroll 1
      for (int t = 1; t <= 10; ++t) { waitv<32>(); bar(); w2(t + 5, slot); slot = slot == 5 ? 0 : slot + 1; }
      const bool chk = layer == 0 && a == 0;
      u64 f[4];
      if (chk) {
        get_tag();
#pragma unroll
        for (int j = 0; j < 4; ++j) f[j] = __hip_atomic_load(rtf + lane + 64 * j, __ATOMIC_RELAXED, __HIP_MEMORY_SCOPE_AGENT);
      }
      waitv<32>(); bar();
      if (chk) {
#pragma unroll
        for (int j = 0; j < 4; ++j) pin(f[j]);
        mix_tag();
#pragma unroll 1
        for (unsigned spins = 0; spins < SPIN_CAP && !__all(f[0] == tag && f[1] == tag && f[2] == tag && f[3] == tag); ++spins) {
#pragma unroll
          for (int j = 0; j < 4; ++j) f[j] = __hip_atomic_load(rtf + lane + 64 * j, __ATOMIC_RELAXED, __HIP_MEMORY_SCOPE_AGENT);
        }
      }
      waitv<0>(); bar();
      w3(3, 1); bar(); bar(); w3(0, 2); bar(); w3(1, 3);
      bar();
      w3(2, 0);
    } else {
      auto ab = [&](int t, int slot) { dma_lin<SC1, 10>(rw, L + L_D + slot * 20480 + a * 10240, lane * 16, soab + a * 10240 + ((t + rot) & 15) * 20480); };
      stamp(k, 4 + a4, sb + 3);
#pragma unroll 1
      for (int t = 0; t < 3; ++t) ab(t, t);
      waitv<20>();
      bar();
      int slot = 0;
#pragma unroll 1
      for (int t = 1; t <= 13; ++t) { waitv<10>(); bar(); ab(t + 2, slot); slot = slot == 2 ? 0 : slot + 1; }
      waitv<10>(); bar(); waitv<0>(); bar();
      bar();
    }
    auto k4load = [&](int n) {
#pragma unroll
      for (int j = 0; j < 8; ++j) k4[n][j] = ld16(rw, lane * 16 + a4 * 1024, so4 + (5 + n) * 30720 + j * 4096);
    };
    push(ev0 + EV_EX, k.P * 16 + k.np, 4, 4, k.kq);
    if (layer == 0) k4load(0);
    poll(ev0 + EV_EX, me, 4, false);
    push(ev0 + EV_X1, k.P * 16, 1, 16, n3);
    if (layer == 0) {
      const int hh = lane >= 24;
#pragma unroll
      for (int j = 0; j < 5; ++j) k4[2][j] = ld16(rw, lane < 48 ? hh * (10 * 3072 * 4 - 384) + lane * 16 : 0x7fffffff, OFF_CS + ((j + 5 * (a4 >> 1)) * 3072 + a * 1536 + 96 * n3) * 4);
      k4load(1);
      sched();
      f32x4 s = {0.f, 0.f, 0.f, 0.f};
#pragma unroll
      for (int j = 0; j < 5; ++j) s += __builtin_bit_cast(f32x4, k4[2][j]);
      if (lane < 48) *(LAS f32x4*)(L + L_D + 49152 + a4 * 768 + lane * 16) = s;
    }
    stamp(k, 4 + a4, sb + 11);
    poll(ev0 + EV_X1, me, 16, false);
    if (isW) {
      bar();
#pragma unroll 1
      for (int t = 1; t <= 6; ++t) { waitv<24>(); bar(); w3(t + 3, (t + 1) & 3); }
      u32x2 sv[8];
      stats_load(k, rw, 2 * layer + 1, 8 * k.P + 4 * a, sv);
      waitv<24>(); bar(); waitv<12>(); bar();
      stats_table(k, a, sv, 1.f);
      if (layer == 0 && lane < 24) {
        LAS char* t = L + L_D + 49152 + a * 768 + lane * 16;
        *(LAS f32x4*)(L + L_CS3 + (96 * a + 4 * lane) * 4) = (*(LAS f32x4*)t + *(LAS f32x4*)(t + 384)) + (*(LAS f32x4*)(t + 1536) + *(LAS f32x4*)(t + 1920));
      }
      waitv<0>();
      w4(2);
      bar();
      w4(3);
      bar();
      w4(0); waitv<30>(); w4(1); waitv<30>(); w4(4);
      waitv<0>();
      stamp(k, 4 + a4, sb + 15);
    } else {
      const unsigned so = OFF_XT + (2 * layer + 1) * XT_BUF + (8 * k.P + 4 * a) * 20480;
      auto xt = [&](int t, int slot) {
#pragma unroll
        for (int j = 0; j < 4; ++j) dma_lin<SC1, 2>(rw, L + L_D + slot * 16384 + (4 * a + j) * 2048, lane * 16, so + j * 20480 + t * 2048);
      };
      stamp(k, 4 + a4, sb + 10);
#pragma unroll 1
      for (int t = 0; t < 3; ++t) xt(t, t);
      waitv<16>();
      bar();
      int slot = 0;
#pragma unroll 1
      for (int t = 1; t <= 7; ++t) { waitv<8>(); bar(); xt(t + 2, slot); slot = slot == 2 ? 0 : slot + 1; }
      waitv<8>(); bar(); waitv<0>(); bar();
      bar();
    }
    push(ev0 + EV_H, k.P * 16, 1, 16, n3);
    if (layer == 0) k4load(2);
    poll(ev0 + EV_H, me, 16, false);
#pragma unroll
    for (int n = 0; n < 3; ++n) {
      unsigned kb = L_W4 + n * 30720 + a4 * 1024 + lane * 16; pin(kb);
      if (n < 2) bar();
#pragma unroll
      for (int j = 0; j < 8; ++j) if (j < 7 || isW) *(LAS bf16x8*)(L + kb + j * 4096) = k4[n][j];
    }
    bar();
    bar();
    rot = 0;
    if (isW && layer == 0) w2n(0, 2);
    bar();
    if (layer == 0) {
      if (isW) w2n(2, 3);
      unsigned kb[2] = {L_D + lane * 16 + ((a4 >> 1) * 10 + a) * 1024, 0}; kb[1] = kb[0] + 61440; pin(kb[0]); pin(kb[1]);
      bar();
#pragma unroll
      for (int j = 0; j < 7; ++j) *(LAS bf16x8*)(L + kb[j / 15] + ((j / 5) % 3) * 20480 + (j % 5) * 2048) = k1[j];
      push(EV_PER_LAYER + EV_X, k.P * 16, 1, 16, n3);
#pragma unroll
      for (int j = 7; j < 25; ++j) *(LAS bf16x8*)(L + kb[j / 15] + ((j / 5) % 3) * 20480 + (j % 5) * 2048) = k1[j];
    }
  }
#undef lane
  STAMP_OUT(k, 4 + a4);
#define lane k.lane
  if (k.b == 0 && a4 == 2 && lane == 0) *(GAS u64*)k.ws = tag;
#undef lane
#undef a
#undef me
#undef n3
#undef sw
#undef vo2
#undef so2
#undef so3
#undef so4
}

__global__ void __launch_bounds__(512, 2) fnet_glu_kernel(const char* x0, const char* wsr, const char* wsi, const char* wdr, const char* wdi, const char* wg, const char* wu, const char* wdn, char* out, char* ws) {
  __shared__ __attribute__((aligned(16))) char smem[163840];
  Ctx k;
  k.lds = (LAS char*)smem; k.lane = threadIdx.x & 63; k.b = blockIdx.x;
  const int w = __builtin_amdgcn_readfirstlane(threadIdx.x >> 6);
  if (CODE_BYTES && w < 4) {
    const char* pc; asm volatile("s_getpc_b64 %0" : "=s"(pc));
    touch<0>(mk(pc, CODE_BYTES), k.lds + L_JUNK, threadIdx.x * 128, 0);
  }
  asm volatile("" : "+s"(x0), "+s"(wsr), "+s"(wsi), "+s"(wdr), "+s"(wdi), "+s"(wg), "+s"(wu), "+s"(wdn), "+s"(out), "+s"(ws));
  k.ws = ws;
  const int x = k.b & 7, i = k.b >> 3;
  k.kq = x >> 1; k.P = 8 * (x & 1) + (i >> 2); k.np = i & 3;
  k.P1 = 4 * (x >> 1) + (i >> 3); k.mat = x & 1; k.ct1 = i & 7;
  if (w < 4) consumer(k, x0, out, w);
  else loader(k, x0, (x >> 2) ? wsi : wsr, (x & 1) ? wdi : wdr, wg, wu, wdn, w - 4);
}
}

extern "C" void kernel_launch(void* const* d_in, const int* in_sizes, int n_in, void* d_out, int out_size, void* d_ws, size_t ws_size, hipStream_t stream) {
  fnet_glu_kernel<<<256, 512, 0, stream>>>((const char*)d_in[0], (const char*)d_in[1], (const char*)d_in[2], (const char*)d_in[3], (const char*)d_in[4],
                                           (const char*)d_in[6], (const char*)d_in[7], (const char*)d_in[8], (char*)d_out, (char*)d_ws);
}
```

```cpp
#include <hip/hip_runtime.h>

typedef unsigned long long u64;
typedef short bf16x8 __attribute__((ext_vector_type(8)));
typedef short s16x4 __attribute__((ext_vector_type(4)));
typedef float f32x4 __attribute__((ext_vector_type(4)));
typedef float f32x2 __attribute__((ext_vector_type(2)));
typedef unsigned u32x4 __attribute__((ext_vector_type(4)));
typedef unsigned u32x2 __attribute__((ext_vector_type(2)));
typedef __bf16 bf2 __attribute__((ext_vector_type(2)));
#define LAS __attribute__((address_space(3)))
#define GAS __attribute__((address_space(1)))
#define DI __device__ __forceinline__
typedef __amdgpu_buffer_rsrc_t rsrc_t;

#define STAMPS 0
#define WIN1 4
#define WIN4 4
#define ROT0 2
#define LAG 12
#define LAGX 12
#define LAG2 0
#define W3POS 0
#define FINE 0
#define WPRIO 0
#define ROT1 1
#define NORT 0
#define G1EXP 0
#define NAP 10
#define CODE_BYTES 32768

namespace {
constexpr unsigned OFF_MB = 4096, MB_EV = 256 * 128;
constexpr int EV_X = 0, EV_AB = 1, EV_EX = 2, EV_X1 = 3, EV_H = 4, EV_PER_LAYER = 5, EV_RT = 10;
constexpr unsigned OFF_CS = 0x60000;
constexpr unsigned OFF_ST = 0xA0000, ST_BUF = 8 * 2048 * 8;
constexpr unsigned OFF_XT = 0x120000, XT_BUF = 2048 * 640 * 2;
constexpr unsigned OFF_AB = OFF_XT + 4 * XT_BUF, AB_BUF = 2048 * 1280 * 2;
constexpr unsigned OFF_HT = OFF_AB + 2 * AB_BUF, HT_BUF = 2048 * 1536 * 2;
constexpr unsigned OFF_EX = OFF_HT + 2 * HT_BUF, EX_BUF = 256 * 12 * 2560;
constexpr unsigned OFF_W3 = OFF_EX + 2 * EX_BUF;
constexpr unsigned OFF_W4 = OFF_W3 + 16 * 20 * 12288;
constexpr unsigned WS_END = OFF_W4 + 8 * 48 * 5120;
constexpr unsigned OFF_STAMP = 0x6000000;
static_assert(OFF_MB + 11 * MB_EV <= OFF_CS && OFF_CS + 20 * 3072 * 4 <= OFF_ST && OFF_ST + 4 * ST_BUF <= OFF_XT && WS_END <= OFF_STAMP, "ws map");
constexpr unsigned L_MEAN = 0, L_RSTD = 512, L_CS1 = 1024, L_CS3 = 2304, L_JUNK = 3072;
constexpr unsigned L_D = 4096;
constexpr unsigned L_WR = L_D + 61440, L_W4 = L_D + 6144, L_XR = L_D + 110592, L_RED = L_W4;
constexpr int SC1 = 16;
constexpr unsigned SPIN_CAP = 1u << 16;

DI rsrc_t mk(const void* p, unsigned bytes) { return __builtin_amdgcn_make_buffer_rsrc((void*)p, 0, bytes, 0x00020000); }
template <int N> DI void waitv() { asm volatile("s_waitcnt vmcnt(%0)" ::"n"(N) : "memory"); }
DI void bar() { asm volatile("s_waitcnt lgkmcnt(0)\n\ts_barrier" ::: "memory"); }
DI void lgk0() { __builtin_amdgcn_sched_barrier(0); __builtin_amdgcn_s_waitcnt(0xC07F); __builtin_amdgcn_sched_barrier(0); }
DI void barv() { lgk0(); asm volatile("s_barrier" ::: "memory"); __builtin_amdgcn_sched_barrier(0); }
template <int NM, int NR> DI void weave() {
#pragma unroll
  for (int j = 0; j < NR; ++j) { __builtin_amdgcn_sched_group_barrier(0x8, 1, 0); __builtin_amdgcn_sched_group_barrier(0x100, 1, 0); }
#pragma unroll
  for (int j = NR; j < NM; ++j) { __builtin_amdgcn_sched_group_barrier(0x8, 1, 0); __builtin_amdgcn_sched_group_barrier(0x6, 2, 0); }
}
DI void drain_bar() {
  asm volatile("s_waitcnt vmcnt(0) lgkmcnt(0)\n\ts_barrier" ::: "memory");
  __builtin_amdgcn_s_waitcnt(0);
  asm volatile("" ::: "memory");
}
DI void sched() { __builtin_amdgcn_sched_barrier(0); }
template <class T> DI void pin(T& x) { asm volatile("" : "+v"(x)); }
template <class T> DI void pins(T& x) { asm volatile("" : "+s"(x)); }
template <int AUX, int IMM> DI void dma(rsrc_t r, LAS char* l, unsigned vo, unsigned so) { __builtin_amdgcn_raw_ptr_buffer_load_lds(r, (LAS void*)l, 16, vo, so, IMM, AUX); }
template <int AUX> DI void touch(rsrc_t r, LAS char* l, unsigned vo, unsigned so) { __builtin_amdgcn_raw_ptr_buffer_load_lds(r, (LAS void*)l, 4, vo, so, 0, AUX); }
template <int AUX, int N> DI void dma_lin(rsrc_t r, LAS char* l, unsigned vo, unsigned so) {
#pragma unroll
  for (int j = 0; j < N; j += 4) {
    dma<AUX, 0>(r, l + j * 1024, vo, so + j * 1024);
    if (j + 1 < N) dma<AUX, 1024>(r, l + j * 1024, vo, so + j * 1024);
    if (j + 2 < N) dma<AUX, 2048>(r, l + j * 1024, vo, so + j * 1024);
    if (j + 3 < N) dma<AUX, 3072>(r, l + j * 1024, vo, so + j * 1024);
  }
}
DI bf16x8 ld16(rsrc_t r, unsigned vo, unsigned so) { return __builtin_bit_cast(bf16x8, __builtin_amdgcn_raw_buffer_load_b128(r, vo, so, SC1)); }
DI void st16(rsrc_t r, u32x4 v, unsigned vo) { __builtin_amdgcn_raw_buffer_store_b128(v, r, vo, 0, SC1); }
DI bf16x8 lds16(LAS char* l, unsigned a) { return *(LAS bf16x8*)(l + a); }
DI unsigned pk(float lo, float hi) { f32x2 v = {lo, hi}; return __builtin_bit_cast(unsigned, __builtin_convertvector(v, bf2)); }
DI float blo(unsigned u) { return __uint_as_float(u << 16); }
DI float bhi(unsigned u) { return __uint_as_float(u & 0xffff0000u); }
DI f32x4 unpk(unsigned a, unsigned b) { return (f32x4){blo(a), bhi(a), blo(b), bhi(b)}; }
DI f32x4 mfma(bf16x8 a, bf16x8 b, f32x4 c) { return __builtin_amdgcn_mfma_f32_16x16x32_bf16(a, b, c, 0, 0, 0); }
DI float dot2(unsigned a, unsigned b, float s) { return __builtin_amdgcn_fdot2_f32_bf16(__builtin_bit_cast(bf2, a), __builtin_bit_cast(bf2, b), s, false); }
DI float dot_ones(bf16x8 f, float s) {
  const u32x4 u = __builtin_bit_cast(u32x4, f);
#pragma unroll
  for (int j = 0; j < 4; ++j) s = dot2(u[j], 0x3f803f80u, s);
  return s;
}
DI float sum_q(float s) { s += __shfl_xor(s, 16); s += __shfl_xor(s, 32); return s; }
DI float sum8(float s) {
  s += __builtin_bit_cast(float, __builtin_amdgcn_update_dpp(0, __builtin_bit_cast(int, s), 0xB1, 0xf, 0xf, true));
  s += __builtin_bit_cast(float, __builtin_amdgcn_update_dpp(0, __builtin_bit_cast(int, s), 0x4E, 0xf, 0xf, true));
  s += __builtin_bit_cast(float, __builtin_amdgcn_update_dpp(0, __builtin_bit_cast(int, s), 0x141, 0xf, 0xf, true));
  return s;
}
DI u32x4 swap_pair(unsigned a0, unsigned a1, unsigned b0, unsigned b1) {
  const auto r0 = __builtin_amdgcn_permlane16_swap(a0, b0, false, false);
  const auto r1 = __builtin_amdgcn_permlane16_swap(a1, b1, false, false);
  return (u32x4){r0[0], r1[0], r0[1], r1[1]};
}

struct Ctx {
  char* ws; LAS char* lds; int lane, b;
  int kq, P, np;
  int P1, mat, ct1;
};
#define STAMP_DECL
#define stamp(k, w, id) do { (void)(id); } while (0)
#define STAMP_OUT(k, w)

DI void x_store(rsrc_t rw, int lane, unsigned vx, unsigned vs, u32x4 o0, u32x4 o1, u32x4 o2, const unsigned (&res)[10]) {
  st16(rw, o0, vx); st16(rw, o1, vx + 1024);
  if (!(lane & 16)) st16(rw, o2, vx + 2048);
  float s = 0.f, ss = 0.f;
#pragma unroll
  for (int j = 0; j < 10; ++j) { s = dot2(res[j], 0x3f803f80u, s); ss = dot2(res[j], res[j], ss); }
  const auto r = __builtin_amdgcn_permlane16_swap(__float_as_uint(s), __float_as_uint(ss), false, false);
  const unsigned t = __float_as_uint(__uint_as_float(r[0]) + __uint_as_float(r[1]));
  const auto r2 = __builtin_amdgcn_permlane32_swap(t, t, false, false);
  const float u = __uint_as_float(r2[0]) + __uint_as_float(r2[1]);
  if (lane < 32) __builtin_amdgcn_raw_buffer_store_b32(__float_as_uint(u), rw, vs, 0, SC1);
}

DI int rot1(const Ctx& k) { const int r = ((k.ct1 * 5) >> 2) + (((k.P1 & 3) * 5) >> 1); return ROT1 ? (r >= 10 ? r - 10 : r) : 0; }

#define PHASE_LANE() Ctx k = k0; int w = w0; asm volatile("" : "+v"(k.lane), "+s"(w), "+s"(k.P), "+s"(k.kq), "+s"(k.np), "+s"(k.P1), "+s"(k.ct1), "+s"(k.mat)); \
  const int tfo = 8 * k.P + 4 * (k.kq >> 1) + w, cto = 2 * k.np + (k.kq & 1); (void)tfo; (void)cto; \
  const int lane = k.lane, q = lane >> 4, c = lane & 15, qs = 2 * (q & 1) + (q >> 1); (void)qs; \
  const unsigned rowrd = (c >> 3) * 1024 + (c & 7) * 128 + ((q ^ (c >> 1)) * 16); (void)rowrd
#define X_ADDR(xb) unsigned vx = OFF_XT + (xb) * XT_BUF + tfo * 20480 + (10 * cto + qs) * 256 + c * 16, vs = OFF_ST + (xb) * ST_BUF + cto * 16384 + tfo * 128 + lane * 4; pin(vx); pin(vs)

DI void consumer(const Ctx& k0, const char* x0, const char* wg, const char* wu, const char* wdn, char* out, const int w0) {
  LAS char* const L = k0.lds;
  const rsrc_t rw = mk(k0.ws, WS_END);
  unsigned res[10];
  STAMP_DECL;
  stamp(k0, w0, 0);
  auto retile = [&](const Ctx& k0, const int w) {
    int e = ((k0.b >> 7) * 64 + (k0.b & 63)) * 4 + w;
    pins(e);
    const int lane = k0.lane;
    const int ks3 = (e * 2731) >> 16, st3 = e - ks3 * 24, ks4 = (e * 6554) >> 16, st4 = e - ks4 * 10;
    LAS char* t[3];
#pragma unroll
    for (int m = 0; m < 3; ++m) { const int p = 3 * w + m, u = rot1(k0) + 4 + (p >> 1); t[m] = L + L_D + (u >= 10 ? u - 10 : u) * 10240 + (p & 1) * 4096; }
    if (LAG) __builtin_amdgcn_s_sleep(LAG);
#pragma unroll
    for (int m = 0; m < 3; ++m) {
      const unsigned stride = m < 2 ? 3072 : 1280;
      const rsrc_t rr = mk(m == 0 ? wg : (m == 1 ? wu : wdn), e < 480 ? 640 * 1536 * 2 : 0);
#pragma unroll
      for (int j = 0; j < 4; ++j) dma<0, 0>(rr, t[m] + j * 1024, (lane >> 3) * stride + (lane & 7) * 16, (32 * (m < 2 ? ks3 : ks4) + 8 * j) * stride + 128 * (m < 2 ? st3 : st4));
    }
    const rsrc_t ro = mk(k0.ws, e < 480 ? WS_END : 0);
    const bf16x8 ones = {0x3f80, 0x3f80, 0x3f80, 0x3f80, 0x3f80, 0x3f80, 0x3f80, 0x3f80};
    const unsigned tr = (lane >> 4) * 1024 + ((lane >> 2) & 3) * 128 + (lane & 3) * 8;
    __builtin_amdgcn_s_waitcnt(0);
    asm volatile("" ::: "memory");
    u32x4 f[3][4];
#pragma unroll
    for (int m = 0; m < 3; ++m)
#pragma unroll
      for (int n = 0; n < 4; ++n) {
        const u32x2 lo = __builtin_bit_cast(u32x2, __builtin_amdgcn_ds_read_tr16_b64_v4i16((LAS s16x4*)(t[m] + tr + 32 * n)));
        const u32x2 hi = __builtin_bit_cast(u32x2, __builtin_amdgcn_ds_read_tr16_b64_v4i16((LAS s16x4*)(t[m] + tr + 32 * n + 512)));
        f[m][n] = (u32x4){lo[0], lo[1], hi[0], hi[1]};
      }
    stamp(k0, w, 61);
#pragma unroll
    for (int m = 0; m < 3; ++m) {
      f32x4 cs[4];
#pragma unroll
      for (int n = 0; n < 4; ++n) {
        const int nf = 4 * (m < 2 ? st3 : st4) + n;
        st16(ro, f[m][n], lane * 16 + (m < 2 ? OFF_W3 + m * 1024 + ks3 * 12288 + ((nf * 171) >> 10) * 19 * 12288 + nf * 2048 : OFF_W4 + ks4 * 5120 + ((nf * 205) >> 10) * 47 * 5120 + nf * 1024));
        if (m < 2) cs[n] = mfma(ones, __builtin_bit_cast(bf16x8, f[m][n]), (f32x4){0.f, 0.f, 0.f, 0.f});
      }
      if (m < 2) __builtin_amdgcn_raw_buffer_store_b32(__float_as_uint((lane & 32) ? ((lane & 16) ? cs[3][0] : cs[2][0]) : ((lane & 16) ? cs[1][0] : cs[0][0])), ro, lane * 4, OFF_CS + (ks3 * 3072 + m * 1536 + 64 * st3) * 4, SC1);
    }
    if (LAG2) __builtin_amdgcn_s_sleep(LAG2);
    stamp(k0, w, 62);
  };
#pragma unroll 1
  for (int layer = 0; layer < 2; ++layer) {
    const int sb = 3 + layer * 24;
    {
      PHASE_LANE();
      unsigned vo = OFF_AB + layer * AB_BUF + ((k.mat * 4 + (k.ct1 >> 1)) * 64 + 4 * k.P1 + w) * 10240 + 5 * (k.ct1 & 1) * 1024 + (qs * 16 + c) * 16;
      unsigned ta = (32 * w + 4 * q) * 4, tc = c * 4;
      pin(vo); pin(ta); pin(tc);
      f32x4 acc[2][5], mean[2], rstd[2]; float cs1[5];
      bf16x8 wf[2][5];
      auto tables = [&]() {
#pragma unroll
        for (int i = 0; i < 5; ++i) cs1[i] = *(LAS float*)(L + L_CS1 + tc + i * 64);
#pragma unroll
        for (int t = 0; t < 2; ++t) { mean[t] = *(LAS f32x4*)(L + ta + L_MEAN + 64 * t); rstd[t] = *(LAS f32x4*)(L + ta + L_RSTD + 64 * t); }
      };
#pragma unroll
      for (int i = 0; i < 5; ++i) { acc[0][i] = (f32x4){0.f, 0.f, 0.f, 0.f}; acc[1][i] = (f32x4){0.f, 0.f, 0.f, 0.f}; }
      if (layer == 0) {
        bf16x8 xf[2][2], cf[2], ones = {0x3f80, 0x3f80, 0x3f80, 0x3f80, 0x3f80, 0x3f80, 0x3f80, 0x3f80};
        f32x4 as[2], aq[2], ac[2];
        unsigned wt = rot1(k) * 10240, wa0 = L_D + rowrd, wc0 = wa0 + w * 2048;
        pin(wa0); pin(wc0);
        unsigned wa = wa0 + wt, xr = L_XR + w * 4096 + rowrd, x_slot = 32768;
#pragma unroll
        for (int i = 0; i < 5; ++i) { pin(acc[0][i]); pin(acc[1][i]); }
#pragma unroll
        for (int t = 0; t < 2; ++t) { as[t] = (f32x4){0.f, 0.f, 0.f, 0.f}; aq[t] = as[t]; ac[t] = as[t]; pin(as[t]); pin(aq[t]); pin(ac[t]); }
        pin(wa); pin(xr); pin(ones);
        if (k.P & 1) retile(k, w);
        bar();
        stamp(k, w, sb + 0);
#pragma unroll
        for (int i = 0; i < 5; ++i) wf[0][i] = lds16(L, wa + i * 2048);
        xf[0][0] = lds16(L, xr + 32768); xf[0][1] = lds16(L, xr + 32768 + 2048); cf[0] = lds16(L, wa + w * 2048);
        unsigned xn = (xr + 32768) ^ 64, wn = wa ^ 64, cn = wn + w * 2048;
        lgk0();
#pragma unroll 1
        for (int kt = 0; kt < 10; ++kt) {
#pragma unroll
          for (int h = 0; h < 2; ++h) {
            xf[h ^ 1][0] = lds16(L, xn); xf[h ^ 1][1] = lds16(L, xn + 2048); cf[h ^ 1] = lds16(L, cn);
#pragma unroll
            for (int i = 0; i < 5; ++i) wf[h ^ 1][i] = lds16(L, wn + i * 2048);
#pragma unroll
            for (int i = 0; i < 5; ++i) { acc[0][i] = mfma(xf[h][0], wf[h][i], acc[0][i]); acc[1][i] = mfma(xf[h][1], wf[h][i], acc[1][i]); }
#pragma unroll
            for (int t = 0; t < 2; ++t) { as[t] = mfma(xf[h][t], ones, as[t]); aq[t] = mfma(xf[h][t], xf[h][t], aq[t]); }
            ac[0] = mfma(ones, cf[h], ac[0]); ac[1] = mfma(ones, wf[h][4], ac[1]);
            if (h == 0) {
              x_slot = x_slot == 32768 ? 0 : x_slot + 16384; wt = wt == 92160 ? 0 : wt + 10240;
              xn = xr + x_slot; wn = wa0 + wt; cn = wc0 + wt;
            } else { xn ^= 64; wn ^= 64; cn ^= 64; }
            weave<16, 8>();
            if (h == 0) { barv(); if (FINE) stamp(k, w, 52 + kt); } else lgk0();
          }
        }
        stamp(k, w, 50);
        if (q == 0) { *(LAS float*)(L + L_CS1 + (16 * w + c) * 4) = ac[0][0]; *(LAS float*)(L + L_CS1 + (64 + c) * 4) = ac[1][0]; }
#pragma unroll
        for (int t = 0; t < 2; ++t) {
          const float s = ((c & 2) ? ((c & 1) ? as[t][3] : as[t][2]) : ((c & 1) ? as[t][1] : as[t][0])) * (1.f / 640.f);
          const float ss = ((c & 2) ? ((c & 1) ? aq[t][3] : aq[t][2]) : ((c & 1) ? aq[t][1] : aq[t][0])) * (1.f / 640.f);
          const float r = __builtin_amdgcn_rsqf(fmaxf(ss - s * s, 0.f) + 1e-5f);
          if ((c >> 2) == q) { *(LAS float*)(L + L_MEAN + (32 * w + 16 * t + c) * 4) = -s; *(LAS float*)(L + L_RSTD + (32 * w + 16 * t + c) * 4) = k.mat ? -r : r; }
        }
        bar();
        tables();
      } else {
        constexpr int WIN = WIN1;
#define KS(s) (((s) + 12) % 20)
        unsigned wb[2][2] = {{L_D + rowrd, L_D + rowrd + 51200}, {L_D + (rowrd ^ 64), L_D + (rowrd ^ 64) + 51200}};
        unsigned xv[2] = {OFF_XT + 2 * XT_BUF + (8 * k.P1 + 2 * w) * 20480 + lane * 16, 0}; xv[1] = xv[0] + 20480;
        pin(wb[0][0]); pin(wb[0][1]); pin(wb[1][0]); pin(wb[1][1]); pin(xv[0]); pin(xv[1]);
        bf16x8 xa[WIN][2];
        bar();
        stamp(k, w, sb + 0);
#pragma unroll
        for (int s = 0; s < WIN; ++s) { xa[s][0] = ld16(rw, xv[0] + (KS(s) & 3) * 1024, (KS(s) >> 2) * 4096); xa[s][1] = ld16(rw, xv[1] + (KS(s) & 3) * 1024, (KS(s) >> 2) * 4096); sched(); }
#pragma unroll
        for (int i = 0; i < 5; ++i) wf[0][i] = lds16(L, wb[0][1] + 10240 + i * 2048);
        sched();
#pragma unroll
        for (int s = 0; s < 20; ++s) {
          if (s == 20 - WIN) bar();
          if (s + 1 < 20) {
#pragma unroll
            for (int i = 0; i < 5; ++i) wf[(s + 1) & 1][i] = lds16(L, wb[(s + 1) & 1][KS(s + 1) / 10] + ((KS(s + 1) >> 1) % 5) * 10240 + i * 2048);
          } else { bar(); tables(); sched(); }
#pragma unroll
          for (int i = 0; i < 5; ++i) acc[0][i] = mfma(xa[s % WIN][0], wf[s & 1][i], acc[0][i]);
          if (s + 1 < 20) {
#pragma unroll
            for (int j = 0; j < 5; ++j) { __builtin_amdgcn_sched_group_barrier(0x100, 1, 0); __builtin_amdgcn_sched_group_barrier(0x8, 1, 0); }
            sched();
          }
          if (s + WIN < 20) { xa[s % WIN][0] = ld16(rw, xv[0] + (KS(s + WIN) & 3) * 1024, (KS(s + WIN) >> 2) * 4096); sched(); }
#pragma unroll
          for (int i = 0; i < 5; ++i) acc[1][i] = mfma(xa[s % WIN][1], wf[s & 1][i], acc[1][i]);
          if (s + 1 < 20) lgk0();
          if (s + WIN < 20) { xa[s % WIN][1] = ld16(rw, xv[1] + (KS(s + WIN) & 3) * 1024, (KS(s + WIN) >> 2) * 4096); sched(); }
        }
      }
      stamp(k, w, sb + 1);
#pragma unroll
      for (int i = 0; i < 5; ++i) {
        const float cs = cs1[i];
        const f32x4 a = (mean[0] * cs + acc[0][i]) * rstd[0], bb = (mean[1] * cs + acc[1][i]) * rstd[1];
        st16(rw, swap_pair(pk(a[0], a[1]), pk(a[2], a[3]), pk(bb[0], bb[1]), pk(bb[2], bb[3])), vo + i * 1024);
      }
      stamp(k, w, sb + 2);
      drain_bar();
      stamp(k, w, sb + 3);
    }
    {
      PHASE_LANE();
      const int wm = w >> 1, wn = w & 1;
      f32x4 acc[4][5];
      bf16x8 fa[2][5], fb[2][4];
#pragma unroll
      for (int t = 0; t < 4; ++t)
#pragma unroll
        for (int i = 0; i < 5; ++i) { acc[t][i] = (f32x4){0.f, 0.f, 0.f, 0.f}; pin(acc[t][i]); }
      unsigned wb = L_WR + wm * 8192 + rowrd, ab = L_D + wn * 5120 + lane * 16;
      unsigned ws_slot = 49152, ab_slot = 0;
      unsigned ev[2], ev8[2], en = w == k.kq ? 0 : WS_END;
      ev[0] = OFF_EX + layer * EX_BUF + ((k.P * 16 + w * 4 + k.np) * 3 + ((k.kq - w) & 3) - 1) * 10240 + lane * 16; ev[1] = ev[0] + 5120;
      ev8[0] = ev[0] - lane * 8 + 2048; ev8[1] = ev8[0] + 5120;
      pin(wb); pin(ab); pin(ev[0]); pin(ev[1]); pin(ev8[0]); pin(ev8[1]); pins(en);
      const rsrc_t re = mk(k.ws, en);
      bar();
#pragma unroll
      for (int t = 0; t < 4; ++t) fb[0][t] = lds16(L, wb + 49152 + t * 2048);
      bar();
      stamp(k, w, sb + 4);
#pragma unroll
      for (int i = 0; i < 5; ++i) fa[0][i] = lds16(L, ab + i * 1024);
      unsigned an = ab + 10240, wn2 = (wb + 49152) ^ 64;
      lgk0();
#pragma unroll 1
      for (int kt = 0; kt < 14; ++kt) {
#pragma unroll
        for (int h = 0; h < 2; ++h) {
#pragma unroll
          for (int i = 0; i < 5; ++i) fa[h ^ 1][i] = lds16(L, an + i * 1024);
#pragma unroll
          for (int t = 0; t < 4; ++t) fb[h ^ 1][t] = lds16(L, wn2 + t * 2048);
#pragma unroll
          for (int t = 0; t < 4; ++t)
#pragma unroll
            for (int i = 0; i < 5; ++i) acc[t][i] = mfma(fa[h][i], fb[h][t], acc[t][i]);
          if (h == 0) {
            ab_slot = ab_slot == 40960 ? 0 : ab_slot + 20480; ws_slot = ws_slot == 81920 ? 0 : ws_slot + 16384;
            an = ab + ab_slot; wn2 = wb + ws_slot;
          } else { an += 10240; wn2 ^= 64; }
          weave<20, 9>();
          if (h == 0) barv(); else lgk0();
        }
      }
      {
#pragma unroll
        for (int i = 0; i < 5; ++i) fa[1][i] = lds16(L, an + i * 1024);
#pragma unroll
        for (int t = 0; t < 4; ++t) fb[1][t] = lds16(L, wn2 + t * 2048);
#pragma unroll
        for (int t = 0; t < 4; ++t)
#pragma unroll
          for (int i = 0; i < 5; ++i) acc[t][i] = mfma(fa[0][i], fb[0][t], acc[t][i]);
        wn2 = wb ^ 64;
        weave<20, 9>();
        barv();
      }
      bf16x8 fc[5], fd[4];
      {
        unsigned o[10];
        auto send = [&](int t, int j) {
          if (j < 5) { o[2 * j] = pk(acc[t][j][0], acc[t][j][1]); o[2 * j + 1] = pk(acc[t][j][2], acc[t][j][3]); }
          if (j == 5) st16(re, (u32x4){o[0], o[1], o[2], o[3]}, ev[t >> 1] + (t & 1) * 2560);
          if (j == 7) st16(re, (u32x4){o[4], o[5], o[6], o[7]}, ev[t >> 1] + (t & 1) * 2560 + 1024);
          if (j == 8) __builtin_amdgcn_raw_buffer_store_b64((u32x2){o[8], o[9]}, re, ev8[t >> 1] + (t & 1) * 2560, 0, SC1);
        };
        auto step = [&](const bf16x8 (&a)[5], const bf16x8 (&b)[4], int t) {
#pragma unroll
          for (int i = 0; i < 5; ++i) acc[t][i] = mfma(a[i], b[t], acc[t][i]);
        };
        auto step_send = [&](const bf16x8 (&a)[5], const bf16x8 (&b)[4], int t, int j0) {
#pragma unroll
          for (int i = 0; i < 5; ++i) { acc[t][i] = mfma(a[i], b[t], acc[t][i]); sched(); if (j0 + i >= 0) send(t - 1, j0 + i); sched(); }
        };
#pragma unroll
        for (int i = 0; i < 5; ++i) fa[0][i] = lds16(L, ab + i * 1024);
        fb[0][0] = lds16(L, wb);
#pragma unroll
        for (int i = 0; i < 5; ++i) fc[i] = lds16(L, ab + 10240 + i * 1024);
        fd[0] = lds16(L, wn2);
#pragma unroll
        for (int t = 1; t < 4; ++t) { fb[0][t] = lds16(L, wb + t * 2048); fd[t] = lds16(L, wn2 + t * 2048); }
        step(fa[1], fb[1], 0); step(fa[1], fb[1], 1);
#pragma unroll
        for (int j = 0; j < 9; ++j) { __builtin_amdgcn_sched_group_barrier(0x8, 1, 0); __builtin_amdgcn_sched_group_barrier(0x100, 2, 0); }
        sched();
        step(fa[0], fb[0], 0); step(fc, fd, 0);
        barv();
        step_send(fa[0], fb[0], 1, -1); step_send(fc, fd, 1, 4);
        step_send(fa[1], fb[1], 2, -3); step_send(fa[0], fb[0], 2, 2); step_send(fc, fd, 2, 7);
        step_send(fa[1], fb[1], 3, -3); step_send(fa[0], fb[0], 3, 2); step_send(fc, fd, 3, 7);
        stamp(k, w, sb + 5);
        if (w == k.kq) {
#pragma unroll
          for (int t = 0; t < 4; ++t)
#pragma unroll
            for (int i = 0; i < 5; ++i) *(LAS f32x4*)(L + L_D + 20480 + (t * 5 + i) * 1024 + lane * 16) = acc[t][i];
        } else {
#pragma unroll
          for (int j = 0; j < 9; ++j) send(3, j);
        }
      }
      stamp(k, w, sb + 6);
      drain_bar();
      stamp(k, w, sb + 7);
      if (layer == 0) {
        const rsrc_t rx = mk(x0, 2048 * 640 * 2);
#pragma unroll
        for (int j = 0; j < 3; ++j) {
          const int u = 64 * j + lane, r = (u * 205) >> 11;
          dma<0, 0>(rx, L + L_D + w * 3072 + j * 1024, u < 160 ? r * 1280 + (u - 10 * r) * 16 : 0x7fffffff, 16 * tfo * 1280 + cto * 160);
        }
        unsigned xa = L_D + w * 3072 + c * 160 + 8 * q;
        pin(xa);
        __builtin_amdgcn_s_waitcnt(0);
        asm volatile("" ::: "memory");
#pragma unroll
        for (int i = 0; i < 5; ++i) { const u32x2 t = *(LAS u32x2*)(L + xa + 32 * i); res[2 * i] = t[0]; res[2 * i + 1] = t[1]; }
      }
      f32x4 v[5];
#pragma unroll
      for (int i = 0; i < 5; ++i) { v[i] = *(LAS f32x4*)(L + L_D + 20480 + (w * 5 + i) * 1024 + lane * 16) + unpk(res[2 * i], res[2 * i + 1]); pin(v[i]); }
      unsigned rv = OFF_EX + layer * EX_BUF + (k.P * 16 + k.kq * 4 + k.np) * 30720 + w * 2560 + lane * 16, rv8 = rv - lane * 8;
      pin(rv); pin(rv8);
      X_ADDR(2 * layer + 1);
      bar();
      stamp(k, w, sb + 8);
      u32x4 e[3][2]; u32x2 e8[3];
#pragma unroll
      for (int n = 0; n < 3; ++n) {
        e[n][0] = __builtin_amdgcn_raw_buffer_load_b128(rw, rv, n * 10240, SC1);
        e[n][1] = __builtin_amdgcn_raw_buffer_load_b128(rw, rv + 1024, n * 10240, SC1);
        e8[n] = __builtin_amdgcn_raw_buffer_load_b64(rw, rv8 + 2048, n * 10240, SC1);
        sched();
      }
#pragma unroll
      for (int n = 0; n < 3; ++n) {
#pragma unroll
        for (int j = 0; j < 2; ++j) { v[2 * j] += unpk(e[n][j][0], e[n][j][1]); v[2 * j + 1] += unpk(e[n][j][2], e[n][j][3]); }
        v[4] += unpk(e8[n][0], e8[n][1]);
        sched();
      }
#pragma unroll
      for (int i = 0; i < 5; ++i) { res[2 * i] = pk(v[i][0], v[i][1]); res[2 * i + 1] = pk(v[i][2], v[i][3]); }
      x_store(rw, lane, vx, vs, swap_pair(res[0], res[1], res[2], res[3]), swap_pair(res[4], res[5], res[6], res[7]), swap_pair(res[8], res[9], res[8], res[9]), res);
      stamp(k, w, sb + 9);
      drain_bar();
      stamp(k, w, sb + 10);
    }
    {
      PHASE_LANE();
      const int wm = w >> 1, wn = w & 1;
      f32x4 acc[4][6];
      bf16x8 fa[2][6], fb[2][4];
#pragma unroll
      for (int t = 0; t < 4; ++t)
#pragma unroll
        for (int i = 0; i < 6; ++i) { acc[t][i] = (f32x4){0.f, 0.f, 0.f, 0.f}; pin(acc[t][i]); }
      unsigned wb = L_WR + wn * 6144 + lane * 16, xb = L_D + wm * 8192 + lane * 16;
      unsigned w_slot = 49152, x_slot = 0;
      unsigned vo = OFF_HT + layer * HT_BUF + ((8 * k.P + 4 * wm + (q & 1)) * 48 + 3 * (4 * k.kq + k.np)) * 1024 + (6 * wn + (q >> 1)) * 256 + c * 16, vo2 = vo + 98304;
      unsigned ta = (64 * wm + c) * 4, tc = L_CS3 + (48 * wn + 4 * q) * 4;
      pin(wb); pin(xb); pin(vo); pin(vo2); pin(ta); pin(tc);
      bar();
#pragma unroll
      for (int i = 0; i < 6; ++i) fa[0][i] = lds16(L, wb + 49152 + i * 1024);
      bar();
      stamp(k, w, sb + 11);
#pragma unroll
      for (int t = 0; t < 4; ++t) fb[0][t] = lds16(L, xb + t * 2048);
      unsigned wn3 = wb + 49152 + 12288, xn = xb + 1024;
      lgk0();
#pragma unroll 1
      for (int kt = 0; kt < 10; ++kt) {
#pragma unroll
        for (int h = 0; h < 2; ++h) {
#pragma unroll
          for (int i = 0; i < 6; ++i) fa[h ^ 1][i] = lds16(L, wn3 + i * 1024);
#pragma unroll
          for (int t = 0; t < 4; ++t) fb[h ^ 1][t] = lds16(L, xn + t * 2048);
#pragma unroll
          for (int t = 0; t < 4; ++t)
#pragma unroll
            for (int i = 0; i < 6; ++i) acc[t][i] = mfma(fa[h][i], fb[h][t], acc[t][i]);
          if (h == 0) {
            x_slot = x_slot == 32768 ? 0 : x_slot + 16384; w_slot = w_slot == 73728 ? 0 : w_slot + 24576;
            wn3 = wb + w_slot; xn = xb + x_slot;
          } else { wn3 += 12288; xn += 1024; }
          weave<24, 10>();
          if (h == 0) barv(); else lgk0();
        }
      }
      stamp(k, w, sb + 12);
      float mean[4], rstd[4], rl2[4];
#pragma unroll
      for (int t = 0; t < 4; ++t) { mean[t] = *(LAS float*)(L + ta + L_MEAN + 64 * t); rstd[t] = *(LAS float*)(L + ta + L_RSTD + 64 * t); rl2[t] = rstd[t] * -1.44269504089f; }
#pragma unroll
      for (int f = 0; f < 3; ++f) {
        const f32x4 cg = *(LAS f32x4*)(L + tc + 64 * f), cu = *(LAS f32x4*)(L + tc + 384 + 64 * f);
        unsigned h[4][2];
#pragma unroll
        for (int t = 0; t < 4; ++t) {
          f32x4 g = (mean[t] * cg + acc[t][2 * f]) * rl2[t];
          const f32x4 u = (mean[t] * cu + acc[t][2 * f + 1]) * rstd[t];
          asm("" : "+v"(g));
          f32x4 o;
#pragma unroll
          for (int r = 0; r < 4; ++r) o[r] = __builtin_amdgcn_exp2f(g[r]);
          o += 1.f;
#pragma unroll
          for (int r = 0; r < 4; ++r) o[r] = __builtin_amdgcn_rcpf(o[r]);
          o *= u;
          h[t][0] = pk(o[0], o[1]); h[t][1] = pk(o[2], o[3]);
        }
        st16(rw, swap_pair(h[0][0], h[0][1], h[1][0], h[1][1]), vo + f * 512);
        st16(rw, swap_pair(h[2][0], h[2][1], h[3][0], h[3][1]), vo2 + f * 512);
      }
      stamp(k, w, sb + 13);
      bar();
    }
    {
      PHASE_LANE();
      constexpr int WIN = WIN4;
      static_assert(24 - WIN >= 17 && 24 - WIN <= 20, "that barrier frees chunk 5's slot and publishes chunk 7");
      const int kh = w & 1;
      unsigned wb[3] = {L_W4 + kh * 5120 + lane * 16, 0, 0}; wb[1] = wb[0] + 51200; wb[2] = wb[0] + 102400;
      unsigned hv[2] = {OFF_HT + layer * HT_BUF + tfo * 49152 + kh * 1024 + lane * 16, OFF_HT + layer * HT_BUF + (tfo ^ 1) * 49152 + kh * 1024 + lane * 16};
      unsigned rd = L_RED + w * 5120 + lane * 16, wr = L_RED + (w ^ 1) * 5120 + lane * 16;
      pin(wb[0]); pin(wb[1]); pin(wb[2]); pin(hv[0]); pin(hv[1]); pin(rd); pin(wr);
      X_ADDR(2);
      unsigned vout = (16 * tfo + c) * 1280 + (10 * cto + qs) * 16;
      pin(vout);
      bf16x8 ha[WIN][2], wf[7][5];
      f32x4 acc[2][5];
#define WF(s) ((s) < 6 ? (s) : 5 + (((s) + 1) & 1))
#pragma unroll
      for (int s = 0; s < 6; ++s)
#pragma unroll
        for (int i = 0; i < 5; ++i) wf[s][i] = lds16(L, wb[s / 5] + (s % 5) * 10240 + i * 1024);
      lgk0();
      drain_bar();
      stamp(k, w, sb + 14);
#pragma unroll
      for (int i = 0; i < 5; ++i) { acc[0][i] = unpk(res[2 * i], res[2 * i + 1]); pin(acc[0][i]); acc[1][i] = (f32x4){0.f, 0.f, 0.f, 0.f}; }
      bar();
      stamp(k, w, sb + 15);
#pragma unroll
      for (int s = 0; s < WIN; ++s) { ha[s][1] = ld16(rw, hv[1] + (s & 1) * 2048, (s >> 1) * 4096); sched(); ha[s][0] = ld16(rw, hv[0] + (s & 1) * 2048, (s >> 1) * 4096); sched(); }
#pragma unroll
      for (int s = 0; s < 24; ++s) {
        if (s == 8 || s == 24 - WIN) barv();
        if (s >= 5 && s + 1 < 24) {
#pragma unroll
          for (int i = 0; i < 5; ++i) wf[WF(s + 1)][i] = lds16(L, wb[((s + 1) % 15) / 5] + ((s + 1) % 5) * 10240 + i * 1024);
        }
#pragma unroll
        for (int i = 0; i < 5; ++i) acc[1][i] = mfma(wf[WF(s)][i], ha[s % WIN][1], acc[1][i]);
        if (s >= 5 && s + 1 < 24) {
#pragma unroll
          for (int j = 0; j < 5; ++j) { __builtin_amdgcn_sched_group_barrier(0x8, 1, 0); __builtin_amdgcn_sched_group_barrier(0x100, 1, 0); }
        }
        if (s + 1 < 24) sched();
        if (s + WIN < 24) { ha[s % WIN][1] = ld16(rw, hv[1] + ((s + WIN) & 1) * 2048, ((s + WIN) >> 1) * 4096); sched(); }
#pragma unroll
        for (int i = 0; i < 5; ++i) acc[0][i] = mfma(wf[WF(s)][i], ha[s % WIN][0], acc[0][i]);
        if (s + 1 < 24) { if (s >= 5) lgk0(); else sched(); }
        if (s + WIN < 24) { ha[s % WIN][0] = ld16(rw, hv[0] + ((s + WIN) & 1) * 2048, ((s + WIN) >> 1) * 4096); sched(); }
      }
#pragma unroll
      for (int i = 0; i < 5; ++i) *(LAS f32x4*)(L + wr + i * 1024) = acc[1][i];
      bar();
      stamp(k, w, sb + 16);
#pragma unroll
      for (int i = 0; i < 5; ++i) { const f32x4 v = acc[0][i] + *(LAS f32x4*)(L + rd + i * 1024); res[2 * i] = pk(v[0], v[1]); res[2 * i + 1] = pk(v[2], v[3]); }
      const u32x4 o0 = swap_pair(res[0], res[1], res[2], res[3]), o1 = swap_pair(res[4], res[5], res[6], res[7]), o2 = swap_pair(res[8], res[9], res[8], res[9]);
      if (layer == 1) {
        const rsrc_t ro = mk(out, 2048 * 640 * 2);
        __builtin_amdgcn_raw_buffer_store_b128(o0, ro, vout, 0, 0);
        __builtin_amdgcn_raw_buffer_store_b128(o1, ro, vout + 64, 0, 0);
        if (!(q & 1)) __builtin_amdgcn_raw_buffer_store_b128(o2, ro, vout + 128, 0, 0);
        stamp(k, w, sb + 17);
      } else {
        x_store(rw, lane, vx, vs, o0, o1, o2, res);
        bar();
        stamp(k, w, sb + 17);
        drain_bar();
      }
      stamp(k, w, sb + 18);
    }
  }
  STAMP_OUT(k0, w0);
}

DI void stats_load(const Ctx& k, rsrc_t rw, int xb, int tf0, u32x2 (&sv)[8]) {
#pragma unroll
  for (int j = 0; j < 8; ++j) sv[j] = __builtin_amdgcn_raw_buffer_load_b64(rw, k.lane * 8, OFF_ST + xb * ST_BUF + j * 16384 + tf0 * 128, SC1);
}
DI void stats_table(const Ctx& k, int a, u32x2 (&sv)[8], float sign) {
  f32x2 s = {0.f, 0.f};
#pragma unroll
  for (int j = 0; j < 8; ++j) { pin(sv[j]); s += __builtin_bit_cast(f32x2, sv[j]); }
  s *= 1.f / 640.f;
  const f32x2 ss = {__shfl_xor(s[0], 8), __shfl_xor(s[1], 8)};
  if (!(k.lane & 8)) {
    const unsigned t = (64 * a + (k.lane & 48) + 2 * (k.lane & 7)) * 4;
    *(LAS f32x2*)(k.lds + L_MEAN + t) = -s;
    *(LAS f32x2*)(k.lds + L_RSTD + t) = (f32x2){sign * __builtin_amdgcn_rsqf(fmaxf(ss[0] - s[0] * s[0], 0.f) + 1e-5f), sign * __builtin_amdgcn_rsqf(fmaxf(ss[1] - s[1] * s[1], 0.f) + 1e-5f)};
  }
}

DI void loader(const Ctx& k0, const char* x0, const char* wsp, const char* wdp, int a4) {
  Ctx k = k0;
  const bool isW = a4 < 2;
  LAS char* const L = k.lds;
  const rsrc_t rw = mk(k.ws, WS_END);
  const rsrc_t rs = mk(wsp, 2048 * 2048 * 2);
  auto fresh = [&]() { asm volatile("" : "+v"(k.lane), "+s"(k.P), "+s"(k.kq), "+s"(k.np), "+s"(k.P1), "+s"(k.ct1), "+s"(k.mat), "+s"(a4)); };
#define lane k.lane
#define a (a4 & 1)
#define me (k.P * 16 + k.kq * 4 + k.np)
#define n3 (4 * k.kq + k.np)
#define sw (((lane & 7) ^ (4 * a + (lane >> 4))) * 16)
#define vo2 ((lane >> 3) * 4096 + sw)
#define so2 ((128 * k.P + 8 * a) * 4096 + (k.kq & 1) * 2048)
#define so3 (OFF_W3 + n3 * 20 * 12288 + a * 12288)
#define so4 (OFF_W4 + (2 * k.np + (k.kq & 1)) * 48 * 5120)
  bf16x8 k1[25], k4[3][8];
  u64 tag = 0;
  int rot = ROT0 * k.np;
  auto ktile = [&](int t) { const int r = (t + rot) & 7; return 4 * (r >> 1) + 2 * (t >> 3) + (r & 1); };
  STAMP_DECL;
  auto w2 = [&](int t, int slot) {
#pragma unroll
    for (int j = 0; j < 8; ++j) dma<0, 0>(rs, L + L_WR + slot * 16384 + (2 * j + a) * 1024, vo2, so2 + j * 16 * 4096 + ktile(t) * 128);
  };
  auto w2n = [&](int t0, int t1) {
#pragma unroll 1
    for (int t = t0; t < t1; ++t) w2(t, t < 3 ? t + 3 : t - 3);
  };
  auto w3 = [&](int t, int slot) { dma_lin<SC1, 12>(rw, L + L_WR + slot * 24576 + a * 12288, lane * 16, so3 + t * 24576); };
  auto w4 = [&](int ch) { dma_lin<SC1, 15>(rw, L + L_W4 + ch * 30720 + a * 15360, lane * 16, so4 + ch * 30720 + a * 15360); };
  auto push = [&](int ev, int wg0, int wgstep, int n, int slot) {
    GAS u64* dst = (GAS u64*)(k.ws + OFF_MB + ev * MB_EV + (wg0 + (lane & (n - 1)) * wgstep) * 128) + slot;
    pin(dst); pin(tag);
    bar();
    if (a4 == 2 && lane < n) __hip_atomic_store(dst, tag, __ATOMIC_RELAXED, __HIP_MEMORY_SCOPE_AGENT);
    fresh();
  };
  auto wait_mb = [&](int ev, int wg, int n) {
    GAS u64* src = (GAS u64*)(k.ws + OFF_MB + ev * MB_EV + wg * 128) + (lane & (n - 1));
#pragma unroll 1
    for (unsigned spins = 0; spins < SPIN_CAP; ++spins) {
      const u64 v = __hip_atomic_load(src, __ATOMIC_RELAXED, __HIP_MEMORY_SCOPE_AGENT);
      if (__all(v == tag)) break;
      __builtin_amdgcn_s_sleep(1);
    }
  };
  auto poll = [&](int ev, int wg, int n, bool nap) {
    if (a4 == 2) { if (nap) __builtin_amdgcn_s_sleep(NAP); wait_mb(ev, wg, n); }
    drain_bar();
  };

  auto get_tag = [&]() { const char* e = k.ws; pins(e); tag = *(const GAS u64*)e; };
  auto mix_tag = [&]() { pin(tag); tag = tag * 6364136223846793005ull + 1442695040888963407ull; };
  if (isW) __builtin_amdgcn_s_setprio(WPRIO); else __builtin_amdgcn_s_setprio(3);
  {
    const rsrc_t r1 = isW ? mk(wdp, 640 * 640 * 2) : mk(x0, 2048 * 640 * 2);
    const unsigned v1 = (lane >> 3) * 1280 + sw, s1 = ((isW ? 80 * k.ct1 : 128 * k.P1) + 8 * a) * 1280;
    int slot = 2, wt = rot1(k);
    if (LAGX && (k.P & 1) && !isW) __builtin_amdgcn_s_sleep(LAGX);
#pragma unroll 1
    for (int t = 0; t < 10; ++t) {
      if (isW) {
        if (t >= 3) { waitv<10>(); bar(); }
#pragma unroll
        for (int j = 0; j < 5; ++j) dma<0, 0>(r1, L + L_D + wt * 10240 + (2 * j + a) * 1024, v1, s1 + 16 * j * 1280 + wt * 128);
      } else {
        if (t >= 2) { waitv<8>(); bar(); }
#pragma unroll
        for (int j = 0; j < 8; ++j) dma<0, 0>(r1, L + L_XR + slot * 16384 + (2 * j + a) * 1024, v1, s1 + 16 * j * 1280 + wt * 128);
      }
      slot = slot == 2 ? 0 : slot + 1; wt = wt == 9 ? 0 : wt + 1;
    }
    if (isW) {
      waitv<10>(); bar();
      waitv<5>(); bar(); w2(0, 3);
      waitv<8>(); bar(); w2(1, 4);
      bar();
      w2(2, 5);
    } else {
      waitv<8>(); bar(); waitv<0>();
      get_tag();
      bar(); bar();
      mix_tag();
    }
  }
  {
    unsigned kb[2] = {L_D + lane * 16 + ((a4 >> 1) * 10 + a) * 1024, 0}; kb[1] = kb[0] + 61440; pin(kb[0]); pin(kb[1]);
    bar();
#pragma unroll
    for (int j = 0; j < 25; ++j) k1[j] = lds16(L, kb[j / 15] + ((j / 5) % 3) * 20480 + (j % 5) * 2048);
    stamp(k, 4 + a4, 51);
  }
#pragma unroll 1
  for (int layer = 0; layer < 2; ++layer) {
    const int ev0 = layer * EV_PER_LAYER, sb = 3 + layer * 24;
    if (layer) {
      stamp(k, 4 + a4, sb + 0);
      poll(ev0 + EV_X, k.P1 * 16 + k.mat * 8 + k.ct1, 16, false);
      if (!isW) { u32x2 sv[8]; stats_load(k, rw, 2, 8 * k.P1 + 4 * a, sv); stats_table(k, a, sv, k.mat ? -1.f : 1.f); }
      bar();
      if (isW) w2n(3, 5);
      bar();
      if (isW) w2n(5, 6);
    }
    push(ev0 + EV_AB, (2 * k.mat + (k.P1 >> 3)) * 4 + (k.ct1 >> 1), 16, 16, 8 * (k.P1 & 1) + (k.P1 & 6) + (k.ct1 & 1));
    GAS u64* rtf = (GAS u64*)(k.ws + OFF_MB + EV_RT * MB_EV);
    if (layer == 0 && a4 == 3 && lane == 0) __hip_atomic_store(rtf + k.b, tag, __ATOMIC_RELAXED, __HIP_MEMORY_SCOPE_AGENT);
    if (isW && layer == 0) { get_tag(); w2n(3, 6); mix_tag(); }
    stamp(k, 4 + a4, sb + 4);
    poll(ev0 + EV_AB, me, 8, layer == 0);
    const unsigned soab = OFF_AB + layer * AB_BUF + (((k.kq >> 1) * 4 + k.np) * 64 + 32 * (k.kq & 1)) * 10240;
    if (isW) {
      bar();
      int slot = 3;
#pragma unroll 1
      for (int t = 1; t <= 10; ++t) { waitv<32>(); bar(); w2(t + 5, slot); slot = slot == 5 ? 0 : slot + 1; }
      const bool chk = layer == 0 && a == 0;
      u64 f[4];
      if (chk) {
#pragma unroll
        for (int j = 0; j < 4; ++j) f[j] = __hip_atomic_load(rtf + lane + 64 * j, __ATOMIC_RELAXED, __HIP_MEMORY_SCOPE_AGENT);
      }
      waitv<32>(); bar();
      if (chk) {
#pragma unroll
        for (int j = 0; j < 4; ++j) pin(f[j]);
#pragma unroll 1
        for (unsigned spins = 0; spins < SPIN_CAP && !__all(f[0] == tag && f[1] == tag && f[2] == tag && f[3] == tag); ++spins) {
#pragma unroll
          for (int j = 0; j < 4; ++j) f[j] = __hip_atomic_load(rtf + lane + 64 * j, __ATOMIC_RELAXED, __HIP_MEMORY_SCOPE_AGENT);
        }
      }
      waitv<0>(); bar();
      w3(3, 1); bar(); bar(); w3(0, 2); bar();
      if (W3POS == 0 || W3POS == 3) w3(1, 3);
      bar();
      if (W3POS == 0) w3(2, 0);
    } else {
      auto ab = [&](int t, int slot) { dma_lin<SC1, 10>(rw, L + L_D + slot * 20480 + a * 10240, lane * 16, soab + a * 10240 + ktile(t) * 20480); };
      stamp(k, 4 + a4, sb + 3);
#pragma unroll 1
      for (int t = 0; t < 3; ++t) ab(t, t);
      waitv<20>();
      bar();
      int slot = 0;
#pragma unroll 1
      for (int t = 1; t <= 13; ++t) {
        waitv<10>();
        if (t == 6 && a == 0) {
          unsigned ja = L_JUNK + 256 + layer * 256 + (lane & 15) * 8; pin(ja);
          if (!__all(*(LAS u64*)(L + ja) == tag)) wait_mb(ev0 + EV_AB, me, 16);
        }
        bar();
        if (t == 5 && a == 0) touch<SC1>(rw, L + L_JUNK + 256 + layer * 256, (lane & 31) * 4, OFF_MB + (ev0 + EV_AB) * MB_EV + me * 128);
        ab(t + 2, slot); slot = slot == 2 ? 0 : slot + 1;
      }
      waitv<10>(); bar(); waitv<0>(); bar();
      bar();
    }
    auto k4load = [&](int n) {
#pragma unroll
      for (int j = 0; j < 8; ++j) k4[n][j] = ld16(rw, lane * 16 + a4 * 1024, so4 + (5 + n) * 30720 + j * 4096);
    };
    push(ev0 + EV_EX, k.P * 16 + k.np, 4, 4, k.kq);
    if (isW && W3POS == 1) w3(1, 3);
    if (isW && (W3POS == 1 || W3POS == 3)) w3(2, 0);
    if (isW && W3POS == 4) w3(1, 3);
    if (layer == 0) k4load(0);
    poll(ev0 + EV_EX, me, 4, false);
    push(ev0 + EV_X1, k.P * 16, 1, 16, n3);
    if (isW && W3POS == 2) w3(1, 3);
    if (isW && (W3POS == 2 || W3POS == 4)) w3(2, 0);
    if (layer == 0) {
      const int hh = lane >= 24;
#pragma unroll
      for (int j = 0; j < 5; ++j) k4[2][j] = ld16(rw, lane < 48 ? hh * (10 * 3072 * 4 - 384) + lane * 16 : 0x7fffffff, OFF_CS + ((j + 5 * (a4 >> 1)) * 3072 + a * 1536 + 96 * n3) * 4);
      k4load(1);
      sched();
      f32x4 s = {0.f, 0.f, 0.f, 0.f};
#pragma unroll
      for (int j = 0; j < 5; ++j) s += __builtin_bit_cast(f32x4, k4[2][j]);
      if (lane < 48) *(LAS f32x4*)(L + L_D + 49152 + a4 * 768 + lane * 16) = s;
    }
    stamp(k, 4 + a4, sb + 11);
    poll(ev0 + EV_X1, me, 16, false);
    if (isW) {
      bar();
#pragma unroll 1
      for (int t = 1; t <= 6; ++t) { waitv<24>(); bar(); w3(t + 3, (t + 1) & 3); }
      u32x2 sv[8];
      stats_load(k, rw, 2 * layer + 1, 8 * k.P + 4 * a, sv);
      waitv<24>(); bar(); waitv<12>(); bar();
      stats_table(k, a, sv, 1.f);
      if (layer == 0 && lane < 24) {
        LAS char* t = L + L_D + 49152 + a * 768 + lane * 16;
        *(LAS f32x4*)(L + L_CS3 + (96 * a + 4 * lane) * 4) = (*(LAS f32x4*)t + *(LAS f32x4*)(t + 384)) + (*(LAS f32x4*)(t + 1536) + *(LAS f32x4*)(t + 1920));
      }
      waitv<0>();
      bar();
      bar();
      w4(0); w4(1); w4(4);
      waitv<0>();
      stamp(k, 4 + a4, sb + 15);
    } else {
      const unsigned so = OFF_XT + (2 * layer + 1) * XT_BUF + (8 * k.P + 4 * a) * 20480;
      auto xt = [&](int t, int slot) {
#pragma unroll
        for (int j = 0; j < 4; ++j) dma_lin<SC1, 2>(rw, L + L_D + slot * 16384 + (4 * a + j) * 2048, lane * 16, so + j * 20480 + t * 2048);
      };
      stamp(k, 4 + a4, sb + 10);
#pragma unroll 1
      for (int t = 0; t < 3; ++t) xt(t, t);
      waitv<16>();
      bar();
      int slot = 0;
#pragma unroll 1
      for (int t = 1; t <= 7; ++t) { waitv<8>(); bar(); xt(t + 2, slot); slot = slot == 2 ? 0 : slot + 1; }
      waitv<8>(); bar();
      w4(2);
      waitv<15>(); bar();
      w4(3);
      bar();
    }
    drain_bar();
    push(ev0 + EV_H, k.P * 16, 1, 16, n3);
    if (layer == 0) k4load(2);
    auto k4put = [&](int n, bool paced) {
      unsigned kb = L_W4 + n * 30720 + a4 * 1024 + lane * 16; pin(kb);
#pragma unroll
      for (int j = 0; j < 8; ++j) if (j < 7 || isW) { *(LAS bf16x8*)(L + kb + j * 4096) = k4[n][j]; if (paced) __builtin_amdgcn_s_sleep(3); }
    };
    k4put(0, false); k4put(1, false);
    poll(ev0 + EV_H, me, 16, false);
    bar();
    k4put(2, true);
    bar();
    rot = 0;
    if (isW && layer == 0) w2n(0, 2);
    bar();
    if (layer == 0) {
      if (isW) w2n(2, 3);
      unsigned kb[2] = {L_D + lane * 16 + ((a4 >> 1) * 10 + a) * 1024, 0}; kb[1] = kb[0] + 61440; pin(kb[0]); pin(kb[1]);
      bar();
#pragma unroll
      for (int j = 0; j < 7; ++j) *(LAS bf16x8*)(L + kb[j / 15] + ((j / 5) % 3) * 20480 + (j % 5) * 2048) = k1[j];
      push(EV_PER_LAYER + EV_X, k.P * 16, 1, 16, n3);
#pragma unroll
      for (int j = 7; j < 25; ++j) *(LAS bf16x8*)(L + kb[j / 15] + ((j / 5) % 3) * 20480 + (j % 5) * 2048) = k1[j];
    }
  }
#undef lane
  STAMP_OUT(k, 4 + a4);
#define lane k.lane
  if (k.b == 0 && a4 == 2 && lane == 0) *(GAS u64*)k.ws = tag;
#undef lane
#undef a
#undef me
#undef n3
#undef sw
#undef vo2
#undef so2
#undef so3
#undef so4
}

__global__ void __launch_bounds__(512, 2) fnet_glu_kernel(const char* x0, const char* wsr, const char* wsi, const char* wdr, const char* wdi, const char* wg, const char* wu, const char* wdn, char* out, char* ws) {
  __shared__ __attribute__((aligned(16))) char smem[163840];
  Ctx k;
  k.lds = (LAS char*)smem; k.lane = threadIdx.x & 63; k.b = blockIdx.x;
  const int w = __builtin_amdgcn_readfirstlane(threadIdx.x >> 6);
  if (CODE_BYTES && w < 4) {
    const char* pc; asm volatile("s_getpc_b64 %0" : "=s"(pc));
    touch<0>(mk(pc, CODE_BYTES), k.lds + L_JUNK, threadIdx.x * 128, 0);
  }
  asm volatile("" : "+s"(x0), "+s"(wsr), "+s"(wsi), "+s"(wdr), "+s"(wdi), "+s"(wg), "+s"(wu), "+s"(wdn), "+s"(out), "+s"(ws));
  k.ws = ws;
  const int x = k.b & 7, i = k.b >> 3;
  k.kq = x >> 1; k.P = 8 * (x & 1) + 4 * (i >> 4) + 2 * ((i >> 2) & 1) + ((i >> 3) & 1); k.np = i & 3;
  k.P1 = 4 * (x >> 1) + (i >> 3); k.mat = x & 1; k.ct1 = i & 7;
  if (w < 4) consumer(k, x0, wg, wu, wdn, out, w);
  else loader(k, x0, (x >> 2) ? wsi : wsr, (x & 1) ? wdi : wdr, w - 4);
}
}

extern "C" void kernel_launch(void* const* d_in, const int* in_sizes, int n_in, void* d_out, int out_size, void* d_ws, size_t ws_size, hipStream_t stream) {
  fnet_glu_kernel<<<256, 512, 0, stream>>>((const char*)d_in[0], (const char*)d_in[1], (const char*)d_in[2], (const char*)d_in[3], (const char*)d_in[4],
                                           (const char*)d_in[6], (const char*)d_in[7], (const char*)d_in[8], (char*)d_out, (char*)d_ws);
}
```

```cpp
#include <hip/hip_runtime.h>

typedef unsigned long long u64;
typedef short bf16x8 __attribute__((ext_vector_type(8)));
typedef short s16x4 __attribute__((ext_vector_type(4)));
typedef float f32x4 __attribute__((ext_vector_type(4)));
typedef float f32x2 __attribute__((ext_vector_type(2)));
typedef unsigned u32x4 __attribute__((ext_vector_type(4)));
typedef unsigned u32x2 __attribute__((ext_vector_type(2)));
typedef __bf16 bf2 __attribute__((ext_vector_type(2)));
#define LAS __attribute__((address_space(3)))
#define GAS __attribute__((address_space(1)))
#define DI __device__ __forceinline__
typedef __amdgpu_buffer_rsrc_t rsrc_t;

#define STAMPS 1
#define WIN1 4
#define WIN4 4
#define ROT0 2
#define LAG 12
#define LAGX 12
#define LAG2 0
#define W3POS 0
#define FINE 0
#define WPRIO 0
#define ROT1 1
#define NORT 0
#define G1EXP 0
#define NAP 10
#define CODE_BYTES 32768

namespace {
constexpr unsigned OFF_MB = 4096, MB_EV = 256 * 128;
constexpr int EV_X = 0, EV_AB = 1, EV_EX = 2, EV_X1 = 3, EV_H = 4, EV_PER_LAYER = 5, EV_RT = 10;
constexpr unsigned OFF_CS = 0x60000;
constexpr unsigned OFF_ST = 0xA0000, ST_BUF = 8 * 2048 * 8;
constexpr unsigned OFF_XT = 0x120000, XT_BUF = 2048 * 640 * 2;
constexpr unsigned OFF_AB = OFF_XT + 4 * XT_BUF, AB_BUF = 2048 * 1280 * 2;
constexpr unsigned OFF_HT = OFF_AB + 2 * AB_BUF, HT_BUF = 2048 * 1536 * 2;
constexpr unsigned OFF_EX = OFF_HT + 2 * HT_BUF, EX_BUF = 256 * 12 * 2560;
constexpr unsigned OFF_W3 = OFF_EX + 2 * EX_BUF;
constexpr unsigned OFF_W4 = OFF_W3 + 16 * 20 * 12288;
constexpr unsigned WS_END = OFF_W4 + 8 * 48 * 5120;
constexpr unsigned OFF_STAMP = 0x6000000;
static_assert(OFF_MB + 11 * MB_EV <= OFF_CS && OFF_CS + 20 * 3072 * 4 <= OFF_ST && OFF_ST + 4 * ST_BUF <= OFF_XT && WS_END <= OFF_STAMP, "ws map");
constexpr unsigned L_MEAN = 0, L_RSTD = 512, L_CS1 = 1024, L_CS3 = 2304, L_JUNK = 3072;
constexpr unsigned L_D = 4096;
constexpr unsigned L_WR = L_D + 61440, L_W4 = L_D + 6144, L_XR = L_D + 110592, L_RED = L_W4;
constexpr int SC1 = 16;
constexpr unsigned SPIN_CAP = 1u << 16;

DI rsrc_t mk(const void* p, unsigned bytes) { return __builtin_amdgcn_make_buffer_rsrc((void*)p, 0, bytes, 0x00020000); }
template <int N> DI void waitv() { asm volatile("s_waitcnt vmcnt(%0)" ::"n"(N) : "memory"); }
DI void bar() { asm volatile("s_waitcnt lgkmcnt(0)\n\ts_barrier" ::: "memory"); }
DI void lgk0() { __builtin_amdgcn_sched_barrier(0); __builtin_amdgcn_s_waitcnt(0xC07F); __builtin_amdgcn_sched_barrier(0); }
DI void barv() { lgk0(); asm volatile("s_barrier" ::: "memory"); __builtin_amdgcn_sched_barrier(0); }
template <int NM, int NR> DI void weave() {
#pragma unroll
  for (int j = 0; j < NR; ++j) { __builtin_amdgcn_sched_group_barrier(0x8, 1, 0); __builtin_amdgcn_sched_group_barrier(0x100, 1, 0); }
#pragma unroll
  for (int j = NR; j < NM; ++j) { __builtin_amdgcn_sched_group_barrier(0x8, 1, 0); __builtin_amdgcn_sched_group_barrier(0x6, 2, 0); }
}
DI void drain_bar() {
  asm volatile("s_waitcnt vmcnt(0) lgkmcnt(0)\n\ts_barrier" ::: "memory");
  __builtin_amdgcn_s_waitcnt(0);
  asm volatile("" ::: "memory");
}
DI void sched() { __builtin_amdgcn_sched_barrier(0); }
template <class T> DI void pin(T& x) { asm volatile("" : "+v"(x)); }
template <class T> DI void pins(T& x) { asm volatile("" : "+s"(x)); }
template <int AUX, int IMM> DI void dma(rsrc_t r, LAS char* l, unsigned vo, unsigned so) { __builtin_amdgcn_raw_ptr_buffer_load_lds(r, (LAS void*)l, 16, vo, so, IMM, AUX); }
template <int AUX> DI void touch(rsrc_t r, LAS char* l, unsigned vo, unsigned so) { __builtin_amdgcn_raw_ptr_buffer_load_lds(r, (LAS void*)l, 4, vo, so, 0, AUX); }
template <int AUX, int N> DI void dma_lin(rsrc_t r, LAS char* l, unsigned vo, unsigned so) {
#pragma unroll
  for (int j = 0; j < N; j += 4) {
    dma<AUX, 0>(r, l + j * 1024, vo, so + j * 1024);
    if (j + 1 < N) dma<AUX, 1024>(r, l + j * 1024, vo, so + j * 1024);
    if (j + 2 < N) dma<AUX, 2048>(r, l + j * 1024, vo, so + j * 1024);
    if (j + 3 < N) dma<AUX, 3072>(r, l + j * 1024, vo, so + j * 1024);
  }
}
DI bf16x8 ld16(rsrc_t r, unsigned vo, unsigned so) { return __builtin_bit_cast(bf16x8, __builtin_amdgcn_raw_buffer_load_b128(r, vo, so, SC1)); }
DI void st16(rsrc_t r, u32x4 v, unsigned vo) { __builtin_amdgcn_raw_buffer_store_b128(v, r, vo, 0, SC1); }
DI bf16x8 lds16(LAS char* l, unsigned a) { return *(LAS bf16x8*)(l + a); }
DI unsigned pk(float lo, float hi) { f32x2 v = {lo, hi}; return __builtin_bit_cast(unsigned, __builtin_convertvector(v, bf2)); }
DI float blo(unsigned u) { return __uint_as_float(u << 16); }
DI float bhi(unsigned u) { return __uint_as_float(u & 0xffff0000u); }
DI f32x4 unpk(unsigned a, unsigned b) { return (f32x4){blo(a), bhi(a), blo(b), bhi(b)}; }
DI f32x4 mfma(bf16x8 a, bf16x8 b, f32x4 c) { return __builtin_amdgcn_mfma_f32_16x16x32_bf16(a, b, c, 0, 0, 0); }
DI float dot2(unsigned a, unsigned b, float s) { return __builtin_amdgcn_fdot2_f32_bf16(__builtin_bit_cast(bf2, a), __builtin_bit_cast(bf2, b), s, false); }
DI float dot_ones(bf16x8 f, float s) {
  const u32x4 u = __builtin_bit_cast(u32x4, f);
#pragma unroll
  for (int j = 0; j < 4; ++j) s = dot2(u[j], 0x3f803f80u, s);
  return s;
}
DI float sum_q(float s) { s += __shfl_xor(s, 16); s += __shfl_xor(s, 32); return s; }
DI float sum8(float s) {
  s += __builtin_bit_cast(float, __builtin_amdgcn_update_dpp(0, __builtin_bit_cast(int, s), 0xB1, 0xf, 0xf, true));
  s += __builtin_bit_cast(float, __builtin_amdgcn_update_dpp(0, __builtin_bit_cast(int, s), 0x4E, 0xf, 0xf, true));
  s += __builtin_bit_cast(float, __builtin_amdgcn_update_dpp(0, __builtin_bit_cast(int, s), 0x141, 0xf, 0xf, true));
  return s;
}
DI u32x4 swap_pair(unsigned a0, unsigned a1, unsigned b0, unsigned b1) {
  const auto r0 = __builtin_amdgcn_permlane16_swap(a0, b0, false, false);
  const auto r1 = __builtin_amdgcn_permlane16_swap(a1, b1, false, false);
  return (u32x4){r0[0], r1[0], r0[1], r1[1]};
}

struct Ctx {
  char* ws; LAS char* lds; int lane, b;
  int kq, P, np;
  int P1, mat, ct1;
};
struct St { unsigned t, c; };
#define STAMP_DECL St st_ = {0u, 0u}
#define stamp(k, w, id) do { sched(); const unsigned t_ = (unsigned)__builtin_amdgcn_s_memrealtime(), c_ = (unsigned)__builtin_amdgcn_s_memtime(); const bool m_ = (int)(threadIdx.x & 63) == (id); st_.t = m_ ? t_ : st_.t; st_.c = m_ ? c_ : st_.c; pin(st_.t); pin(st_.c); sched(); } while (0)
#define STAMP_OUT(k, w) do { if ((w) == 0 && k.b == 64) { unsigned d_ = (unsigned)(__builtin_amdgcn_readlane(st_.t, 0) - __builtin_amdgcn_readlane(st_.t, 0)) ; d_ = d_ > 20000u ? 20000u : d_; const unsigned t0_ = (unsigned)__builtin_amdgcn_s_memrealtime(); while ((unsigned)__builtin_amdgcn_s_memrealtime() - t0_ < d_ * 20u) __builtin_amdgcn_s_sleep(8); } } while (0)

DI void x_store(rsrc_t rw, int lane, unsigned vx, unsigned vs, u32x4 o0, u32x4 o1, u32x4 o2, const unsigned (&res)[10]) {
  st16(rw, o0, vx); st16(rw, o1, vx + 1024);
  if (!(lane & 16)) st16(rw, o2, vx + 2048);
  float s = 0.f, ss = 0.f;
#pragma unroll
  for (int j = 0; j < 10; ++j) { s = dot2(res[j], 0x3f803f80u, s); ss = dot2(res[j], res[j], ss); }
  const auto r = __builtin_amdgcn_permlane16_swap(__float_as_uint(s), __float_as_uint(ss), false, false);
  const unsigned t = __float_as_uint(__uint_as_float(r[0]) + __uint_as_float(r[1]));
  const auto r2 = __builtin_amdgcn_permlane32_swap(t, t, false, false);
  const float u = __uint_as_float(r2[0]) + __uint_as_float(r2[1]);
  if (lane < 32) __builtin_amdgcn_raw_buffer_store_b32(__float_as_uint(u), rw, vs, 0, SC1);
}

DI int rot1(const Ctx& k) { const int r = ((k.ct1 * 5) >> 2) + (((k.P1 & 3) * 5) >> 1); return ROT1 ? (r >= 10 ? r - 10 : r) : 0; }

#define PHASE_LANE() Ctx k = k0; int w = w0; asm volatile("" : "+v"(k.lane), "+s"(w), "+s"(k.P), "+s"(k.kq), "+s"(k.np), "+s"(k.P1), "+s"(k.ct1), "+s"(k.mat)); \
  const int tfo = 8 * k.P + 4 * (k.kq >> 1) + w, cto = 2 * k.np + (k.kq & 1); (void)tfo; (void)cto; \
  const int lane = k.lane, q = lane >> 4, c = lane & 15, qs = 2 * (q & 1) + (q >> 1); (void)qs; \
  const unsigned rowrd = (c >> 3) * 1024 + (c & 7) * 128 + ((q ^ (c >> 1)) * 16); (void)rowrd
#define X_ADDR(xb) unsigned vx = OFF_XT + (xb) * XT_BUF + tfo * 20480 + (10 * cto + qs) * 256 + c * 16, vs = OFF_ST + (xb) * ST_BUF + cto * 16384 + tfo * 128 + lane * 4; pin(vx); pin(vs)

DI void consumer(const Ctx& k0, const char* x0, const char* wg, const char* wu, const char* wdn, char* out, const int w0) {
  LAS char* const L = k0.lds;
  const rsrc_t rw = mk(k0.ws, WS_END);
  unsigned res[10];
  STAMP_DECL;
  stamp(k0, w0, 0);
  auto retile = [&](const Ctx& k0, const int w) {
    int e = ((k0.b >> 7) * 64 + (k0.b & 63)) * 4 + w;
    pins(e);
    const int lane = k0.lane;
    const int ks3 = (e * 2731) >> 16, st3 = e - ks3 * 24, ks4 = (e * 6554) >> 16, st4 = e - ks4 * 10;
    LAS char* t[3];
#pragma unroll
    for (int m = 0; m < 3; ++m) { const int p = 3 * w + m, u = rot1(k0) + 4 + (p >> 1); t[m] = L + L_D + (u >= 10 ? u - 10 : u) * 10240 + (p & 1) * 4096; }
    if (LAG) __builtin_amdgcn_s_sleep(LAG);
#pragma unroll
    for (int m = 0; m < 3; ++m) {
      const unsigned stride = m < 2 ? 3072 : 1280;
      const rsrc_t rr = mk(m == 0 ? wg : (m == 1 ? wu : wdn), e < 480 ? 640 * 1536 * 2 : 0);
#pragma unroll
      for (int j = 0; j < 4; ++j) dma<0, 0>(rr, t[m] + j * 1024, (lane >> 3) * stride + (lane & 7) * 16, (32 * (m < 2 ? ks3 : ks4) + 8 * j) * stride + 128 * (m < 2 ? st3 : st4));
    }
    const rsrc_t ro = mk(k0.ws, e < 480 ? WS_END : 0);
    const bf16x8 ones = {0x3f80, 0x3f80, 0x3f80, 0x3f80, 0x3f80, 0x3f80, 0x3f80, 0x3f80};
    const unsigned tr = (lane >> 4) * 1024 + ((lane >> 2) & 3) * 128 + (lane & 3) * 8;
    __builtin_amdgcn_s_waitcnt(0);
    asm volatile("" ::: "memory");
    u32x4 f[3][4];
#pragma unroll
    for (int m = 0; m < 3; ++m)
#pragma unroll
      for (int n = 0; n < 4; ++n) {
        const u32x2 lo = __builtin_bit_cast(u32x2, __builtin_amdgcn_ds_read_tr16_b64_v4i16((LAS s16x4*)(t[m] + tr + 32 * n)));
        const u32x2 hi = __builtin_bit_cast(u32x2, __builtin_amdgcn_ds_read_tr16_b64_v4i16((LAS s16x4*)(t[m] + tr + 32 * n + 512)));
        f[m][n] = (u32x4){lo[0], lo[1], hi[0], hi[1]};
      }
    stamp(k0, w, 61);
#pragma unroll
    for (int m = 0; m < 3; ++m) {
      f32x4 cs[4];
#pragma unroll
      for (int n = 0; n < 4; ++n) {
        const int nf = 4 * (m < 2 ? st3 : st4) + n;
        st16(ro, f[m][n], lane * 16 + (m < 2 ? OFF_W3 + m * 1024 + ks3 * 12288 + ((nf * 171) >> 10) * 19 * 12288 + nf * 2048 : OFF_W4 + ks4 * 5120 + ((nf * 205) >> 10) * 47 * 5120 + nf * 1024));
        if (m < 2) cs[n] = mfma(ones, __builtin_bit_cast(bf16x8, f[m][n]), (f32x4){0.f, 0.f, 0.f, 0.f});
      }
      if (m < 2) __builtin_amdgcn_raw_buffer_store_b32(__float_as_uint((lane & 32) ? ((lane & 16) ? cs[3][0] : cs[2][0]) : ((lane & 16) ? cs[1][0] : cs[0][0])), ro, lane * 4, OFF_CS + (ks3 * 3072 + m * 1536 + 64 * st3) * 4, SC1);
    }
    if (LAG2) __builtin_amdgcn_s_sleep(LAG2);
    stamp(k0, w, 62);
  };
#pragma unroll 1
  for (int layer = 0; layer < 2; ++layer) {
    const int sb = 3 + layer * 24;
    {
      PHASE_LANE();
      unsigned vo = OFF_AB + layer * AB_BUF + ((k.mat * 4 + (k.ct1 >> 1)) * 64 + 4 * k.P1 + w) * 10240 + 5 * (k.ct1 & 1) * 1024 + (qs * 16 + c) * 16;
      unsigned ta = (32 * w + 4 * q) * 4, tc = c * 4;
      pin(vo); pin(ta); pin(tc);
      f32x4 acc[2][5], mean[2], rstd[2]; float cs1[5];
      bf16x8 wf[2][5];
      auto tables = [&]() {
#pragma unroll
        for (int i = 0; i < 5; ++i) cs1[i] = *(LAS float*)(L + L_CS1 + tc + i * 64);
#pragma unroll
        for (int t = 0; t < 2; ++t) { mean[t] = *(LAS f32x4*)(L + ta + L_MEAN + 64 * t); rstd[t] = *(LAS f32x4*)(L + ta + L_RSTD + 64 * t); }
      };
#pragma unroll
      for (int i = 0; i < 5; ++i) { acc[0][i] = (f32x4){0.f, 0.f, 0.f, 0.f}; acc[1][i] = (f32x4){0.f, 0.f, 0.f, 0.f}; }
      if (layer == 0) {
        bf16x8 xf[2][2], cf[2], ones = {0x3f80, 0x3f80, 0x3f80, 0x3f80, 0x3f80, 0x3f80, 0x3f80, 0x3f80};
        f32x4 as[2], aq[2], ac[2];
        unsigned wt = rot1(k) * 10240, wa0 = L_D + rowrd, wc0 = wa0 + w * 2048;
        pin(wa0); pin(wc0);
        unsigned wa = wa0 + wt, xr = L_XR + w * 4096 + rowrd, x_slot = 32768;
#pragma unroll
        for (int i = 0; i < 5; ++i) { pin(acc[0][i]); pin(acc[1][i]); }
#pragma unroll
        for (int t = 0; t < 2; ++t) { as[t] = (f32x4){0.f, 0.f, 0.f, 0.f}; aq[t] = as[t]; ac[t] = as[t]; pin(as[t]); pin(aq[t]); pin(ac[t]); }
        pin(wa); pin(xr); pin(ones);
        if (k.P & 1) retile(k, w);
        bar();
        stamp(k, w, sb + 0);
#pragma unroll
        for (int i = 0; i < 5; ++i) wf[0][i] = lds16(L, wa + i * 2048);
        xf[0][0] = lds16(L, xr + 32768); xf[0][1] = lds16(L, xr + 32768 + 2048); cf[0] = lds16(L, wa + w * 2048);
        unsigned xn = (xr + 32768) ^ 64, wn = wa ^ 64, cn = wn + w * 2048;
        lgk0();
#pragma unroll 1
        for (int kt = 0; kt < 10; ++kt) {
#pragma unroll
          for (int h = 0; h < 2; ++h) {
            xf[h ^ 1][0] = lds16(L, xn); xf[h ^ 1][1] = lds16(L, xn + 2048); cf[h ^ 1] = lds16(L, cn);
#pragma unroll
            for (int i = 0; i < 5; ++i) wf[h ^ 1][i] = lds16(L, wn + i * 2048);
#pragma unroll
            for (int i = 0; i < 5; ++i) { acc[0][i] = mfma(xf[h][0], wf[h][i], acc[0][i]); acc[1][i] = mfma(xf[h][1], wf[h][i], acc[1][i]); }
#pragma unroll
            for (int t = 0; t < 2; ++t) { as[t] = mfma(xf[h][t], ones, as[t]); aq[t] = mfma(xf[h][t], xf[h][t], aq[t]); }
            ac[0] = mfma(ones, cf[h], ac[0]); ac[1] = mfma(ones, wf[h][4], ac[1]);
            if (h == 0) {
              x_slot = x_slot == 32768 ? 0 : x_slot + 16384; wt = wt == 92160 ? 0 : wt + 10240;
              xn = xr + x_slot; wn = wa0 + wt; cn = wc0 + wt;
            } else { xn ^= 64; wn ^= 64; cn ^= 64; }
            weave<16, 8>();
            if (h == 0) { barv(); if (FINE) stamp(k, w, 52 + kt); } else lgk0();
          }
        }
        stamp(k, w, 50);
        if (q == 0) { *(LAS float*)(L + L_CS1 + (16 * w + c) * 4) = ac[0][0]; *(LAS float*)(L + L_CS1 + (64 + c) * 4) = ac[1][0]; }
#pragma unroll
        for (int t = 0; t < 2; ++t) {
          const float s = ((c & 2) ? ((c & 1) ? as[t][3] : as[t][2]) : ((c & 1) ? as[t][1] : as[t][0])) * (1.f / 640.f);
          const float ss = ((c & 2) ? ((c & 1) ? aq[t][3] : aq[t][2]) : ((c & 1) ? aq[t][1] : aq[t][0])) * (1.f / 640.f);
          const float r = __builtin_amdgcn_rsqf(fmaxf(ss - s * s, 0.f) + 1e-5f);
          if ((c >> 2) == q) { *(LAS float*)(L + L_MEAN + (32 * w + 16 * t + c) * 4) = -s; *(LAS float*)(L + L_RSTD + (32 * w + 16 * t + c) * 4) = k.mat ? -r : r; }
        }
        bar();
        tables();
      } else {
        constexpr int WIN = WIN1;
#define KS(s) (((s) + 12) % 20)
        unsigned wb[2][2] = {{L_D + rowrd, L_D + rowrd + 51200}, {L_D + (rowrd ^ 64), L_D + (rowrd ^ 64) + 51200}};
        unsigned xv[2] = {OFF_XT + 2 * XT_BUF + (8 * k.P1 + 2 * w) * 20480 + lane * 16, 0}; xv[1] = xv[0] + 20480;
        pin(wb[0][0]); pin(wb[0][1]); pin(wb[1][0]); pin(wb[1][1]); pin(xv[0]); pin(xv[1]);
        bf16x8 xa[WIN][2];
        bar();
        stamp(k, w, sb + 0);
#pragma unroll
        for (int s = 0; s < WIN; ++s) { xa[s][0] = ld16(rw, xv[0] + (KS(s) & 3) * 1024, (KS(s) >> 2) * 4096); xa[s][1] = ld16(rw, xv[1] + (KS(s) & 3) * 1024, (KS(s) >> 2) * 4096); sched(); }
#pragma unroll
        for (int i = 0; i < 5; ++i) wf[0][i] = lds16(L, wb[0][1] + 10240 + i * 2048);
        sched();
#pragma unroll
        for (int s = 0; s < 20; ++s) {
          if (s == 20 - WIN) bar();
          if (s + 1 < 20) {
#pragma unroll
            for (int i = 0; i < 5; ++i) wf[(s + 1) & 1][i] = lds16(L, wb[(s + 1) & 1][KS(s + 1) / 10] + ((KS(s + 1) >> 1) % 5) * 10240 + i * 2048);
          } else { bar(); tables(); sched(); }
#pragma unroll
          for (int i = 0; i < 5; ++i) acc[0][i] = mfma(xa[s % WIN][0], wf[s & 1][i], acc[0][i]);
          if (s + 1 < 20) {
#pragma unroll
            for (int j = 0; j < 5; ++j) { __builtin_amdgcn_sched_group_barrier(0x100, 1, 0); __builtin_amdgcn_sched_group_barrier(0x8, 1, 0); }
            sched();
          }
          if (s + WIN < 20) { xa[s % WIN][0] = ld16(rw, xv[0] + (KS(s + WIN) & 3) * 1024, (KS(s + WIN) >> 2) * 4096); sched(); }
#pragma unroll
          for (int i = 0; i < 5; ++i) acc[1][i] = mfma(xa[s % WIN][1], wf[s & 1][i], acc[1][i]);
          if (s + 1 < 20) lgk0();
          if (s + WIN < 20) { xa[s % WIN][1] = ld16(rw, xv[1] + (KS(s + WIN) & 3) * 1024, (KS(s + WIN) >> 2) * 4096); sched(); }
        }
      }
      stamp(k, w, sb + 1);
#pragma unroll
      for (int i = 0; i < 5; ++i) {
        const float cs = cs1[i];
        const f32x4 a = (mean[0] * cs + acc[0][i]) * rstd[0], bb = (mean[1] * cs + acc[1][i]) * rstd[1];
        st16(rw, swap_pair(pk(a[0], a[1]), pk(a[2], a[3]), pk(bb[0], bb[1]), pk(bb[2], bb[3])), vo + i * 1024);
      }
      stamp(k, w, sb + 2);
      drain_bar();
      stamp(k, w, sb + 3);
    }
    {
      PHASE_LANE();
      const int wm = w >> 1, wn = w & 1;
      f32x4 acc[4][5];
      bf16x8 fa[2][5], fb[2][4];
#pragma unroll
      for (int t = 0; t < 4; ++t)
#pragma unroll
        for (int i = 0; i < 5; ++i) { acc[t][i] = (f32x4){0.f, 0.f, 0.f, 0.f}; pin(acc[t][i]); }
      unsigned wb = L_WR + wm * 8192 + rowrd, ab = L_D + wn * 5120 + lane * 16;
      unsigned ws_slot = 49152, ab_slot = 0;
      unsigned ev[2], ev8[2], en = w == k.kq ? 0 : WS_END;
      ev[0] = OFF_EX + layer * EX_BUF + ((k.P * 16 + w * 4 + k.np) * 3 + ((k.kq - w) & 3) - 1) * 10240 + lane * 16; ev[1] = ev[0] + 5120;
      ev8[0] = ev[0] - lane * 8 + 2048; ev8[1] = ev8[0] + 5120;
      pin(wb); pin(ab); pin(ev[0]); pin(ev[1]); pin(ev8[0]); pin(ev8[1]); pins(en);
      const rsrc_t re = mk(k.ws, en);
      bar();
#pragma unroll
      for (int t = 0; t < 4; ++t) fb[0][t] = lds16(L, wb + 49152 + t * 2048);
      bar();
      stamp(k, w, sb + 4);
#pragma unroll
      for (int i = 0; i < 5; ++i) fa[0][i] = lds16(L, ab + i * 1024);
      unsigned an = ab + 10240, wn2 = (wb + 49152) ^ 64;
      lgk0();
#pragma unroll 1
      for (int kt = 0; kt < 14; ++kt) {
#pragma unroll
        for (int h = 0; h < 2; ++h) {
#pragma unroll
          for (int i = 0; i < 5; ++i) fa[h ^ 1][i] = lds16(L, an + i * 1024);
#pragma unroll
          for (int t = 0; t < 4; ++t) fb[h ^ 1][t] = lds16(L, wn2 + t * 2048);
#pragma unroll
          for (int t = 0; t < 4; ++t)
#pragma unroll
            for (int i = 0; i < 5; ++i) acc[t][i] = mfma(fa[h][i], fb[h][t], acc[t][i]);
          if (h == 0) {
            ab_slot = ab_slot == 40960 ? 0 : ab_slot + 20480; ws_slot = ws_slot == 81920 ? 0 : ws_slot + 16384;
            an = ab + ab_slot; wn2 = wb + ws_slot;
          } else { an += 10240; wn2 ^= 64; }
          weave<20, 9>();
          if (h == 0) barv(); else lgk0();
        }
      }
      {
#pragma unroll
        for (int i = 0; i < 5; ++i) fa[1][i] = lds16(L, an + i * 1024);
#pragma unroll
        for (int t = 0; t < 4; ++t) fb[1][t] = lds16(L, wn2 + t * 2048);
#pragma unroll
        for (int t = 0; t < 4; ++t)
#pragma unroll
          for (int i = 0; i < 5; ++i) acc[t][i] = mfma(fa[0][i], fb[0][t], acc[t][i]);
        wn2 = wb ^ 64;
        weave<20, 9>();
        barv();
      }
      bf16x8 fc[5], fd[4];
      {
        unsigned o[10];
        auto send = [&](int t, int j) {
          if (j < 5) { o[2 * j] = pk(acc[t][j][0], acc[t][j][1]); o[2 * j + 1] = pk(acc[t][j][2], acc[t][j][3]); }
          if (j == 5) st16(re, (u32x4){o[0], o[1], o[2], o[3]}, ev[t >> 1] + (t & 1) * 2560);
          if (j == 7) st16(re, (u32x4){o[4], o[5], o[6], o[7]}, ev[t >> 1] + (t & 1) * 2560 + 1024);
          if (j == 8) __builtin_amdgcn_raw_buffer_store_b64((u32x2){o[8], o[9]}, re, ev8[t >> 1] + (t & 1) * 2560, 0, SC1);
        };
        auto step = [&](const bf16x8 (&a)[5], const bf16x8 (&b)[4], int t) {
#pragma unroll
          for (int i = 0; i < 5; ++i) acc[t][i] = mfma(a[i], b[t], acc[t][i]);
        };
        auto step_send = [&](const bf16x8 (&a)[5], const bf16x8 (&b)[4], int t, int j0) {
#pragma unroll
          for (int i = 0; i < 5; ++i) { acc[t][i] = mfma(a[i], b[t], acc[t][i]); sched(); if (j0 + i >= 0) send(t - 1, j0 + i); sched(); }
        };
#pragma unroll
        for (int i = 0; i < 5; ++i) fa[0][i] = lds16(L, ab + i * 1024);
        fb[0][0] = lds16(L, wb);
#pragma unroll
        for (int i = 0; i < 5; ++i) fc[i] = lds16(L, ab + 10240 + i * 1024);
        fd[0] = lds16(L, wn2);
#pragma unroll
        for (int t = 1; t < 4; ++t) { fb[0][t] = lds16(L, wb + t * 2048); fd[t] = lds16(L, wn2 + t * 2048); }
        step(fa[1], fb[1], 0); step(fa[1], fb[1], 1);
#pragma unroll
        for (int j = 0; j < 9; ++j) { __builtin_amdgcn_sched_group_barrier(0x8, 1, 0); __builtin_amdgcn_sched_group_barrier(0x100, 2, 0); }
        sched();
        step(fa[0], fb[0], 0); step(fc, fd, 0);
        barv();
        step_send(fa[0], fb[0], 1, -1); step_send(fc, fd, 1, 4);
        step_send(fa[1], fb[1], 2, -3); step_send(fa[0], fb[0], 2, 2); step_send(fc, fd, 2, 7);
        step_send(fa[1], fb[1], 3, -3); step_send(fa[0], fb[0], 3, 2); step_send(fc, fd, 3, 7);
        stamp(k, w, sb + 5);
        if (w == k.kq) {
#pragma unroll
          for (int t = 0; t < 4; ++t)
#pragma unroll
            for (int i = 0; i < 5; ++i) *(LAS f32x4*)(L + L_D + 20480 + (t * 5 + i) * 1024 + lane * 16) = acc[t][i];
        } else {
#pragma unroll
          for (int j = 0; j < 9; ++j) send(3, j);
        }
      }
      stamp(k, w, sb + 6);
      drain_bar();
      stamp(k, w, sb + 7);
      if (layer == 0) {
        const rsrc_t rx = mk(x0, 2048 * 640 * 2);
#pragma unroll
        for (int j = 0; j < 3; ++j) {
          const int u = 64 * j + lane, r = (u * 205) >> 11;
          dma<0, 0>(rx, L + L_D + w * 3072 + j * 1024, u < 160 ? r * 1280 + (u - 10 * r) * 16 : 0x7fffffff, 16 * tfo * 1280 + cto * 160);
        }
        unsigned xa = L_D + w * 3072 + c * 160 + 8 * q;
        pin(xa);
        __builtin_amdgcn_s_waitcnt(0);
        asm volatile("" ::: "memory");
#pragma unroll
        for (int i = 0; i < 5; ++i) { const u32x2 t = *(LAS u32x2*)(L + xa + 32 * i); res[2 * i] = t[0]; res[2 * i + 1] = t[1]; }
      }
      f32x4 v[5];
#pragma unroll
      for (int i = 0; i < 5; ++i) { v[i] = *(LAS f32x4*)(L + L_D + 20480 + (w * 5 + i) * 1024 + lane * 16) + unpk(res[2 * i], res[2 * i + 1]); pin(v[i]); }
      unsigned rv = OFF_EX + layer * EX_BUF + (k.P * 16 + k.kq * 4 + k.np) * 30720 + w * 2560 + lane * 16, rv8 = rv - lane * 8;
      pin(rv); pin(rv8);
      X_ADDR(2 * layer + 1);
      bar();
      stamp(k, w, sb + 8);
      u32x4 e[3][2]; u32x2 e8[3];
#pragma unroll
      for (int n = 0; n < 3; ++n) {
        e[n][0] = __builtin_amdgcn_raw_buffer_load_b128(rw, rv, n * 10240, SC1);
        e[n][1] = __builtin_amdgcn_raw_buffer_load_b128(rw, rv + 1024, n * 10240, SC1);
        e8[n] = __builtin_amdgcn_raw_buffer_load_b64(rw, rv8 + 2048, n * 10240, SC1);
        sched();
      }
#pragma unroll
      for (int n = 0; n < 3; ++n) {
#pragma unroll
        for (int j = 0; j < 2; ++j) { v[2 * j] += unpk(e[n][j][0], e[n][j][1]); v[2 * j + 1] += unpk(e[n][j][2], e[n][j][3]); }
        v[4] += unpk(e8[n][0], e8[n][1]);
        sched();
      }
#pragma unroll
      for (int i = 0; i < 5; ++i) { res[2 * i] = pk(v[i][0], v[i][1]); res[2 * i + 1] = pk(v[i][2], v[i][3]); }
      x_store(rw, lane, vx, vs, swap_pair(res[0], res[1], res[2], res[3]), swap_pair(res[4], res[5], res[6], res[7]), swap_pair(res[8], res[9], res[8], res[9]), res);
      stamp(k, w, sb + 9);
      drain_bar();
      stamp(k, w, sb + 10);
    }
    {
      PHASE_LANE();
      const int wm = w >> 1, wn = w & 1;
      f32x4 acc[4][6];
      bf16x8 fa[2][6], fb[2][4];
#pragma unroll
      for (int t = 0; t < 4; ++t)
#pragma unroll
        for (int i = 0; i < 6; ++i) { acc[t][i] = (f32x4){0.f, 0.f, 0.f, 0.f}; pin(acc[t][i]); }
      unsigned wb = L_WR + wn * 6144 + lane * 16, xb = L_D + wm * 8192 + lane * 16;
      unsigned w_slot = 49152, x_slot = 0;
      unsigned vo = OFF_HT + layer * HT_BUF + ((8 * k.P + 4 * wm + (q & 1)) * 48 + 3 * (4 * k.kq + k.np)) * 1024 + (6 * wn + (q >> 1)) * 256 + c * 16, vo2 = vo + 98304;
      unsigned ta = (64 * wm + c) * 4, tc = L_CS3 + (48 * wn + 4 * q) * 4;
      pin(wb); pin(xb); pin(vo); pin(vo2); pin(ta); pin(tc);
      bar();
#pragma unroll
      for (int i = 0; i < 6; ++i) fa[0][i] = lds16(L, wb + 49152 + i * 1024);
      bar();
      stamp(k, w, sb + 11);
#pragma unroll
      for (int t = 0; t < 4; ++t) fb[0][t] = lds16(L, xb + t * 2048);
      unsigned wn3 = wb + 49152 + 12288, xn = xb + 1024;
      lgk0();
#pragma unroll 1
      for (int kt = 0; kt < 10; ++kt) {
#pragma unroll
        for (int h = 0; h < 2; ++h) {
#pragma unroll
          for (int i = 0; i < 6; ++i) fa[h ^ 1][i] = lds16(L, wn3 + i * 1024);
#pragma unroll
          for (int t = 0; t < 4; ++t) fb[h ^ 1][t] = lds16(L, xn + t * 2048);
#pragma unroll
          for (int t = 0; t < 4; ++t)
#pragma unroll
            for (int i = 0; i < 6; ++i) acc[t][i] = mfma(fa[h][i], fb[h][t], acc[t][i]);
          if (h == 0) {
            x_slot = x_slot == 32768 ? 0 : x_slot + 16384; w_slot = w_slot == 73728 ? 0 : w_slot + 24576;
            wn3 = wb + w_slot; xn = xb + x_slot;
          } else { wn3 += 12288; xn += 1024; }
          weave<24, 10>();
          if (h == 0) barv(); else lgk0();
        }
      }
      stamp(k, w, sb + 12);
      float mean[4], rstd[4], rl2[4];
#pragma unroll
      for (int t = 0; t < 4; ++t) { mean[t] = *(LAS float*)(L + ta + L_MEAN + 64 * t); rstd[t] = *(LAS float*)(L + ta + L_RSTD + 64 * t); rl2[t] = rstd[t] * -1.44269504089f; }
#pragma unroll
      for (int f = 0; f < 3; ++f) {
        const f32x4 cg = *(LAS f32x4*)(L + tc + 64 * f), cu = *(LAS f32x4*)(L + tc + 384 + 64 * f);
        unsigned h[4][2];
#pragma unroll
        for (int t = 0; t < 4; ++t) {
          f32x4 g = (mean[t] * cg + acc[t][2 * f]) * rl2[t];
          const f32x4 u = (mean[t] * cu + acc[t][2 * f + 1]) * rstd[t];
          asm("" : "+v"(g));
          f32x4 o;
#pragma unroll
          for (int r = 0; r < 4; ++r) o[r] = __builtin_amdgcn_exp2f(g[r]);
          o += 1.f;
#pragma unroll
          for (int r = 0; r < 4; ++r) o[r] = __builtin_amdgcn_rcpf(o[r]);
          o *= u;
          h[t][0] = pk(o[0], o[1]); h[t][1] = pk(o[2], o[3]);
        }
        st16(rw, swap_pair(h[0][0], h[0][1], h[1][0], h[1][1]), vo + f * 512);
        st16(rw, swap_pair(h[2][0], h[2][1], h[3][0], h[3][1]), vo2 + f * 512);
      }
      stamp(k, w, sb + 13);
      bar();
    }
    {
      PHASE_LANE();
      constexpr int WIN = WIN4;
      static_assert(24 - WIN >= 17 && 24 - WIN <= 20, "that barrier frees chunk 5's slot and publishes chunk 7");
      const int kh = w & 1;
      unsigned wb[3] = {L_W4 + kh * 5120 + lane * 16, 0, 0}; wb[1] = wb[0] + 51200; wb[2] = wb[0] + 102400;
      unsigned hv[2] = {OFF_HT + layer * HT_BUF + tfo * 49152 + kh * 1024 + lane * 16, OFF_HT + layer * HT_BUF + (tfo ^ 1) * 49152 + kh * 1024 + lane * 16};
      unsigned rd = L_RED + w * 5120 + lane * 16, wr = L_RED + (w ^ 1) * 5120 + lane * 16;
      pin(wb[0]); pin(wb[1]); pin(wb[2]); pin(hv[0]); pin(hv[1]); pin(rd); pin(wr);
      X_ADDR(2);
      unsigned vout = (16 * tfo + c) * 1280 + (10 * cto + qs) * 16;
      pin(vout);
      bf16x8 ha[WIN][2], wf[7][5];
      f32x4 acc[2][5];
#define WF(s) ((s) < 6 ? (s) : 5 + (((s) + 1) & 1))
#pragma unroll
      for (int s = 0; s < 6; ++s)
#pragma unroll
        for (int i = 0; i < 5; ++i) wf[s][i] = lds16(L, wb[s / 5] + (s % 5) * 10240 + i * 1024);
      lgk0();
      drain_bar();
      stamp(k, w, sb + 14);
#pragma unroll
      for (int i = 0; i < 5; ++i) { acc[0][i] = unpk(res[2 * i], res[2 * i + 1]); pin(acc[0][i]); acc[1][i] = (f32x4){0.f, 0.f, 0.f, 0.f}; }
      bar();
      stamp(k, w, sb + 15);
#pragma unroll
      for (int s = 0; s < WIN; ++s) { ha[s][1] = ld16(rw, hv[1] + (s & 1) * 2048, (s >> 1) * 4096); sched(); ha[s][0] = ld16(rw, hv[0] + (s & 1) * 2048, (s >> 1) * 4096); sched(); }
#pragma unroll
      for (int s = 0; s < 24; ++s) {
        if (s == 8 || s == 24 - WIN) barv();
        if (s >= 5 && s + 1 < 24) {
#pragma unroll
          for (int i = 0; i < 5; ++i) wf[WF(s + 1)][i] = lds16(L, wb[((s + 1) % 15) / 5] + ((s + 1) % 5) * 10240 + i * 1024);
        }
#pragma unroll
        for (int i = 0; i < 5; ++i) acc[1][i] = mfma(wf[WF(s)][i], ha[s % WIN][1], acc[1][i]);
        if (s >= 5 && s + 1 < 24) {
#pragma unroll
          for (int j = 0; j < 5; ++j) { __builtin_amdgcn_sched_group_barrier(0x8, 1, 0); __builtin_amdgcn_sched_group_barrier(0x100, 1, 0); }
        }
        if (s + 1 < 24) sched();
        if (s + WIN < 24) { ha[s % WIN][1] = ld16(rw, hv[1] + ((s + WIN) & 1) * 2048, ((s + WIN) >> 1) * 4096); sched(); }
#pragma unroll
        for (int i = 0; i < 5; ++i) acc[0][i] = mfma(wf[WF(s)][i], ha[s % WIN][0], acc[0][i]);
        if (s + 1 < 24) { if (s >= 5) lgk0(); else sched(); }
        if (s + WIN < 24) { ha[s % WIN][0] = ld16(rw, hv[0] + ((s + WIN) & 1) * 2048, ((s + WIN) >> 1) * 4096); sched(); }
      }
#pragma unroll
      for (int i = 0; i < 5; ++i) *(LAS f32x4*)(L + wr + i * 1024) = acc[1][i];
      bar();
      stamp(k, w, sb + 16);
#pragma unroll
      for (int i = 0; i < 5; ++i) { const f32x4 v = acc[0][i] + *(LAS f32x4*)(L + rd + i * 1024); res[2 * i] = pk(v[0], v[1]); res[2 * i + 1] = pk(v[2], v[3]); }
      const u32x4 o0 = swap_pair(res[0], res[1], res[2], res[3]), o1 = swap_pair(res[4], res[5], res[6], res[7]), o2 = swap_pair(res[8], res[9], res[8], res[9]);
      if (layer == 1) {
        const rsrc_t ro = mk(out, 2048 * 640 * 2);
        __builtin_amdgcn_raw_buffer_store_b128(o0, ro, vout, 0, 0);
        __builtin_amdgcn_raw_buffer_store_b128(o1, ro, vout + 64, 0, 0);
        if (!(q & 1)) __builtin_amdgcn_raw_buffer_store_b128(o2, ro, vout + 128, 0, 0);
        stamp(k, w, sb + 17);
      } else {
        x_store(rw, lane, vx, vs, o0, o1, o2, res);
        bar();
        stamp(k, w, sb + 17);
        drain_bar();
      }
      stamp(k, w, sb + 18);
    }
  }
  STAMP_OUT(k0, w0);
}

DI void stats_load(const Ctx& k, rsrc_t rw, int xb, int tf0, u32x2 (&sv)[8]) {
#pragma unroll
  for (int j = 0; j < 8; ++j) sv[j] = __builtin_amdgcn_raw_buffer_load_b64(rw, k.lane * 8, OFF_ST + xb * ST_BUF + j * 16384 + tf0 * 128, SC1);
}
DI void stats_table(const Ctx& k, int a, u32x2 (&sv)[8], float sign) {
  f32x2 s = {0.f, 0.f};
#pragma unroll
  for (int j = 0; j < 8; ++j) { pin(sv[j]); s += __builtin_bit_cast(f32x2, sv[j]); }
  s *= 1.f / 640.f;
  const f32x2 ss = {__shfl_xor(s[0], 8), __shfl_xor(s[1], 8)};
  if (!(k.lane & 8)) {
    const unsigned t = (64 * a + (k.lane & 48) + 2 * (k.lane & 7)) * 4;
    *(LAS f32x2*)(k.lds + L_MEAN + t) = -s;
    *(LAS f32x2*)(k.lds + L_RSTD + t) = (f32x2){sign * __builtin_amdgcn_rsqf(fmaxf(ss[0] - s[0] * s[0], 0.f) + 1e-5f), sign * __builtin_amdgcn_rsqf(fmaxf(ss[1] - s[1] * s[1], 0.f) + 1e-5f)};
  }
}

DI void loader(const Ctx& k0, const char* x0, const char* wsp, const char* wdp, int a4) {
  Ctx k = k0;
  const bool isW = a4 < 2;
  LAS char* const L = k.lds;
  const rsrc_t rw = mk(k.ws, WS_END);
  const rsrc_t rs = mk(wsp, 2048 * 2048 * 2);
  auto fresh = [&]() { asm volatile("" : "+v"(k.lane), "+s"(k.P), "+s"(k.kq), "+s"(k.np), "+s"(k.P1), "+s"(k.ct1), "+s"(k.mat), "+s"(a4)); };
#define lane k.lane
#define a (a4 & 1)
#define me (k.P * 16 + k.kq * 4 + k.np)
#define n3 (4 * k.kq + k.np)
#define sw (((lane & 7) ^ (4 * a + (lane >> 4))) * 16)
#define vo2 ((lane >> 3) * 4096 + sw)
#define so2 ((128 * k.P + 8 * a) * 4096 + (k.kq & 1) * 2048)
#define so3 (OFF_W3 + n3 * 20 * 12288 + a * 12288)
#define so4 (OFF_W4 + (2 * k.np + (k.kq & 1)) * 48 * 5120)
  bf16x8 k1[25], k4[3][8];
  u64 tag = 0;
  int rot = ROT0 * k.np;
  auto ktile = [&](int t) { const int r = (t + rot) & 7; return 4 * (r >> 1) + 2 * (t >> 3) + (r & 1); };
  STAMP_DECL;
  auto w2 = [&](int t, int slot) {
#pragma unroll
    for (int j = 0; j < 8; ++j) dma<0, 0>(rs, L + L_WR + slot * 16384 + (2 * j + a) * 1024, vo2, so2 + j * 16 * 4096 + ktile(t) * 128);
  };
  auto w2n = [&](int t0, int t1) {
#pragma unroll 1
    for (int t = t0; t < t1; ++t) w2(t, t < 3 ? t + 3 : t - 3);
  };
  auto w3 = [&](int t, int slot) { dma_lin<SC1, 12>(rw, L + L_WR + slot * 24576 + a * 12288, lane * 16, so3 + t * 24576); };
  auto w4 = [&](int ch) { dma_lin<SC1, 15>(rw, L + L_W4 + ch * 30720 + a * 15360, lane * 16, so4 + ch * 30720 + a * 15360); };
  auto push = [&](int ev, int wg0, int wgstep, int n, int slot) {
    GAS u64* dst = (GAS u64*)(k.ws + OFF_MB + ev * MB_EV + (wg0 + (lane & (n - 1)) * wgstep) * 128) + slot;
    pin(dst); pin(tag);
    bar();
    if (a4 == 2 && lane < n) __hip_atomic_store(dst, tag, __ATOMIC_RELAXED, __HIP_MEMORY_SCOPE_AGENT);
    fresh();
  };
  auto wait_mb = [&](int ev, int wg, int n) {
    GAS u64* src = (GAS u64*)(k.ws + OFF_MB + ev * MB_EV + wg * 128) + (lane & (n - 1));
#pragma unroll 1
    for (unsigned spins = 0; spins < SPIN_CAP; ++spins) {
      const u64 v = __hip_atomic_load(src, __ATOMIC_RELAXED, __HIP_MEMORY_SCOPE_AGENT);
      if (__all(v == tag)) break;
      __builtin_amdgcn_s_sleep(1);
    }
  };
  auto poll = [&](int ev, int wg, int n, bool nap) {
    if (a4 == 2) { if (nap) __builtin_amdgcn_s_sleep(NAP); wait_mb(ev, wg, n); }
    drain_bar();
  };

  auto get_tag = [&]() { const char* e = k.ws; pins(e); tag = *(const GAS u64*)e; };
  auto mix_tag = [&]() { pin(tag); tag = tag * 6364136223846793005ull + 1442695040888963407ull; };
  if (isW) __builtin_amdgcn_s_setprio(WPRIO); else __builtin_amdgcn_s_setprio(3);
  {
    const rsrc_t r1 = isW ? mk(wdp, 640 * 640 * 2) : mk(x0, 2048 * 640 * 2);
    const unsigned v1 = (lane >> 3) * 1280 + sw, s1 = ((isW ? 80 * k.ct1 : 128 * k.P1) + 8 * a) * 1280;
    int slot = 2, wt = rot1(k);
    if (LAGX && (k.P & 1) && !isW) __builtin_amdgcn_s_sleep(LAGX);
#pragma unroll 1
    for (int t = 0; t < 10; ++t) {
      if (isW) {
        if (t >= 3) { waitv<10>(); bar(); }
#pragma unroll
        for (int j = 0; j < 5; ++j) dma<0, 0>(r1, L + L_D + wt * 10240 + (2 * j + a) * 1024, v1, s1 + 16 * j * 1280 + wt * 128);
      } else {
        if (t >= 2) { waitv<8>(); bar(); }
#pragma unroll
        for (int j = 0; j < 8; ++j) dma<0, 0>(r1, L + L_XR + slot * 16384 + (2 * j + a) * 1024, v1, s1 + 16 * j * 1280 + wt * 128);
      }
      slot = slot == 2 ? 0 : slot + 1; wt = wt == 9 ? 0 : wt + 1;
    }
    if (isW) {
      waitv<10>(); bar();
      waitv<5>(); bar(); w2(0, 3);
      waitv<8>(); bar(); w2(1, 4);
      bar();
      w2(2, 5);
    } else {
      waitv<8>(); bar(); waitv<0>();
      get_tag();
      bar(); bar();
      mix_tag();
    }
  }
  {
    unsigned kb[2] = {L_D + lane * 16 + ((a4 >> 1) * 10 + a) * 1024, 0}; kb[1] = kb[0] + 61440; pin(kb[0]); pin(kb[1]);
    bar();
#pragma unroll
    for (int j = 0; j < 25; ++j) k1[j] = lds16(L, kb[j / 15] + ((j / 5) % 3) * 20480 + (j % 5) * 2048);
    stamp(k, 4 + a4, 51);
  }
#pragma unroll 1
  for (int layer = 0; layer < 2; ++layer) {
    const int ev0 = layer * EV_PER_LAYER, sb = 3 + layer * 24;
    if (layer) {
      stamp(k, 4 + a4, sb + 0);
      poll(ev0 + EV_X, k.P1 * 16 + k.mat * 8 + k.ct1, 16, false);
      if (!isW) { u32x2 sv[8]; stats_load(k, rw, 2, 8 * k.P1 + 4 * a, sv); stats_table(k, a, sv, k.mat ? -1.f : 1.f); }
      bar();
      if (isW) w2n(3, 5);
      bar();
      if (isW) w2n(5, 6);
    }
    push(ev0 + EV_AB, (2 * k.mat + (k.P1 >> 3)) * 4 + (k.ct1 >> 1), 16, 16, 8 * (k.P1 & 1) + (k.P1 & 6) + (k.ct1 & 1));
    GAS u64* rtf = (GAS u64*)(k.ws + OFF_MB + EV_RT * MB_EV);
    if (layer == 0 && a4 == 3 && lane == 0) __hip_atomic_store(rtf + k.b, tag, __ATOMIC_RELAXED, __HIP_MEMORY_SCOPE_AGENT);
    if (isW && layer == 0) { get_tag(); w2n(3, 6); mix_tag(); }
    stamp(k, 4 + a4, sb + 4);
    poll(ev0 + EV_AB, me, 8, layer == 0);
    const unsigned soab = OFF_AB + layer * AB_BUF + (((k.kq >> 1) * 4 + k.np) * 64 + 32 * (k.kq & 1)) * 10240;
    if (isW) {
      bar();
      int slot = 3;
#pragma unroll 1
      for (int t = 1; t <= 10; ++t) { waitv<32>(); bar(); w2(t + 5, slot); slot = slot == 5 ? 0 : slot + 1; }
      const bool chk = layer == 0 && a == 0;
      u64 f[4];
      if (chk) {
#pragma unroll
        for (int j = 0; j < 4; ++j) f[j] = __hip_atomic_load(rtf + lane + 64 * j, __ATOMIC_RELAXED, __HIP_MEMORY_SCOPE_AGENT);
      }
      waitv<32>(); bar();
      if (chk) {
#pragma unroll
        for (int j = 0; j < 4; ++j) pin(f[j]);
#pragma unroll 1
        for (unsigned spins = 0; spins < SPIN_CAP && !__all(f[0] == tag && f[1] == tag && f[2] == tag && f[3] == tag); ++spins) {
#pragma unroll
          for (int j = 0; j < 4; ++j) f[j] = __hip_atomic_load(rtf + lane + 64 * j, __ATOMIC_RELAXED, __HIP_MEMORY_SCOPE_AGENT);
        }
      }
      waitv<0>(); bar();
      w3(3, 1); bar(); bar(); w3(0, 2); bar();
      if (W3POS == 0 || W3POS == 3) w3(1, 3);
      bar();
      if (W3POS == 0) w3(2, 0);
    } else {
      auto ab = [&](int t, int slot) { dma_lin<SC1, 10>(rw, L + L_D + slot * 20480 + a * 10240, lane * 16, soab + a * 10240 + ktile(t) * 20480); };
      stamp(k, 4 + a4, sb + 3);
#pragma unroll 1
      for (int t = 0; t < 3; ++t) ab(t, t);
      waitv<20>();
      bar();
      int slot = 0;
#pragma unroll 1
      for (int t = 1; t <= 13; ++t) {
        waitv<10>();
        if (t == 6 && a == 0) {
          unsigned ja = L_JUNK + 256 + layer * 256 + (lane & 15) * 8; pin(ja);
          if (!__all(*(LAS u64*)(L + ja) == tag)) wait_mb(ev0 + EV_AB, me, 16);
        }
        bar();
        if (t == 5 && a == 0) touch<SC1>(rw, L + L_JUNK + 256 + layer * 256, (lane & 31) * 4, OFF_MB + (ev0 + EV_AB) * MB_EV + me * 128);
        ab(t + 2, slot); slot = slot == 2 ? 0 : slot + 1;
      }
      waitv<10>(); bar(); waitv<0>(); bar();
      bar();
    }
    auto k4load = [&](int n) {
#pragma unroll
      for (int j = 0; j < 8; ++j) k4[n][j] = ld16(rw, lane * 16 + a4 * 1024, so4 + (5 + n) * 30720 + j * 4096);
    };
    push(ev0 + EV_EX, k.P * 16 + k.np, 4, 4, k.kq);
    if (isW && W3POS == 1) w3(1, 3);
    if (isW && (W3POS == 1 || W3POS == 3)) w3(2, 0);
    if (isW && W3POS == 4) w3(1, 3);
    if (layer == 0) k4load(0);
    poll(ev0 + EV_EX, me, 4, false);
    push(ev0 + EV_X1, k.P * 16, 1, 16, n3);
    if (isW && W3POS == 2) w3(1, 3);
    if (isW && (W3POS == 2 || W3POS == 4)) w3(2, 0);
    if (layer == 0) {
      const int hh = lane >= 24;
#pragma unroll
      for (int j = 0; j < 5; ++j) k4[2][j] = ld16(rw, lane < 48 ? hh * (10 * 3072 * 4 - 384) + lane * 16 : 0x7fffffff, OFF_CS + ((j + 5 * (a4 >> 1)) * 3072 + a * 1536 + 96 * n3) * 4);
      k4load(1);
      sched();
      f32x4 s = {0.f, 0.f, 0.f, 0.f};
#pragma unroll
      for (int j = 0; j < 5; ++j) s += __builtin_bit_cast(f32x4, k4[2][j]);
      if (lane < 48) *(LAS f32x4*)(L + L_D + 49152 + a4 * 768 + lane * 16) = s;
    }
    stamp(k, 4 + a4, sb + 11);
    poll(ev0 + EV_X1, me, 16, false);
    if (isW) {
      bar();
#pragma unroll 1
      for (int t = 1; t <= 6; ++t) { waitv<24>(); bar(); w3(t + 3, (t + 1) & 3); }
      u32x2 sv[8];
      stats_load(k, rw, 2 * layer + 1, 8 * k.P + 4 * a, sv);
      waitv<24>(); bar(); waitv<12>(); bar();
      stats_table(k, a, sv, 1.f);
      if (layer == 0 && lane < 24) {
        LAS char* t = L + L_D + 49152 + a * 768 + lane * 16;
        *(LAS f32x4*)(L + L_CS3 + (96 * a + 4 * lane) * 4) = (*(LAS f32x4*)t + *(LAS f32x4*)(t + 384)) + (*(LAS f32x4*)(t + 1536) + *(LAS f32x4*)(t + 1920));
      }
      waitv<0>();
      bar();
      bar();
      w4(0); w4(1); w4(4);
      waitv<0>();
      stamp(k, 4 + a4, sb + 15);
    } else {
      const unsigned so = OFF_XT + (2 * layer + 1) * XT_BUF + (8 * k.P + 4 * a) * 20480;
      auto xt = [&](int t, int slot) {
#pragma unroll
        for (int j = 0; j < 4; ++j) dma_lin<SC1, 2>(rw, L + L_D + slot * 16384 + (4 * a + j) * 2048, lane * 16, so + j * 20480 + t * 2048);
      };
      stamp(k, 4 + a4, sb + 10);
#pragma unroll 1
      for (int t = 0; t < 3; ++t) xt(t, t);
      waitv<16>();
      bar();
      int slot = 0;
#pragma unroll 1
      for (int t = 1; t <= 7; ++t) { waitv<8>(); bar(); xt(t + 2, slot); slot = slot == 2 ? 0 : slot + 1; }
      waitv<8>(); bar();
      w4(2);
      waitv<15>(); bar();
      w4(3);
      bar();
    }
    drain_bar();
    push(ev0 + EV_H, k.P * 16, 1, 16, n3);
    if (layer == 0) k4load(2);
    auto k4put = [&](int n, bool paced) {
      unsigned kb = L_W4 + n * 30720 + a4 * 1024 + lane * 16; pin(kb);
#pragma unroll
      for (int j = 0; j < 8; ++j) if (j < 7 || isW) { *(LAS bf16x8*)(L + kb + j * 4096) = k4[n][j]; if (paced) __builtin_amdgcn_s_sleep(3); }
    };
    k4put(0, false); k4put(1, false);
    poll(ev0 + EV_H, me, 16, false);
    bar();
    k4put(2, true);
    bar();
    rot = 0;
    if (isW && layer == 0) w2n(0, 2);
    bar();
    if (layer == 0) {
      if (isW) w2n(2, 3);
      unsigned kb[2] = {L_D + lane * 16 + ((a4 >> 1) * 10 + a) * 1024, 0}; kb[1] = kb[0] + 61440; pin(kb[0]); pin(kb[1]);
      bar();
#pragma unroll
      for (int j = 0; j < 7; ++j) *(LAS bf16x8*)(L + kb[j / 15] + ((j / 5) % 3) * 20480 + (j % 5) * 2048) = k1[j];
      push(EV_PER_LAYER + EV_X, k.P * 16, 1, 16, n3);
#pragma unroll
      for (int j = 7; j < 25; ++j) *(LAS bf16x8*)(L + kb[j / 15] + ((j / 5) % 3) * 20480 + (j % 5) * 2048) = k1[j];
    }
  }
#undef lane
  STAMP_OUT(k, 4 + a4);
#define lane k.lane
  if (k.b == 0 && a4 == 2 && lane == 0) *(GAS u64*)k.ws = tag;
#undef lane
#undef a
#undef me
#undef n3
#undef sw
#undef vo2
#undef so2
#undef so3
#undef so4
}

__global__ void __launch_bounds__(512, 2) fnet_glu_kernel(const char* x0, const char* wsr, const char* wsi, const char* wdr, const char* wdi, const char* wg, const char* wu, const char* wdn, char* out, char* ws) {
  __shared__ __attribute__((aligned(16))) char smem[163840];
  Ctx k;
  k.lds = (LAS char*)smem; k.lane = threadIdx.x & 63; k.b = blockIdx.x;
  const int w = __builtin_amdgcn_readfirstlane(threadIdx.x >> 6);
  if (CODE_BYTES && w < 4) {
    const char* pc; asm volatile("s_getpc_b64 %0" : "=s"(pc));
    touch<0>(mk(pc, CODE_BYTES), k.lds + L_JUNK, threadIdx.x * 128, 0);
  }
  asm volatile("" : "+s"(x0), "+s"(wsr), "+s"(wsi), "+s"(wdr), "+s"(wdi), "+s"(wg), "+s"(wu), "+s"(wdn), "+s"(out), "+s"(ws));
  k.ws = ws;
  const int x = k.b & 7, i = k.b >> 3;
  k.kq = x >> 1; k.P = 8 * (x & 1) + 4 * (i >> 4) + 2 * ((i >> 2) & 1) + ((i >> 3) & 1); k.np = i & 3;
  k.P1 = 4 * (x >> 1) + (i >> 3); k.mat = x & 1; k.ct1 = i & 7;
  if (w < 4) consumer(k, x0, wg, wu, wdn, out, w);
  else loader(k, x0, (x >> 2) ? wsi : wsr, (x & 1) ? wdi : wdr, w - 4);
}
}

extern "C" void kernel_launch(void* const* d_in, const int* in_sizes, int n_in, void* d_out, int out_size, void* d_ws, size_t ws_size, hipStream_t stream) {
  fnet_glu_kernel<<<256, 512, 0, stream>>>((const char*)d_in[0], (const char*)d_in[1], (const char*)d_in[2], (const char*)d_in[3], (const char*)d_in[4],
                                           (const char*)d_in[6], (const char*)d_in[7], (const char*)d_in[8], (char*)d_out, (char*)d_ws);
}
```

```cpp
#include <hip/hip_runtime.h>

typedef unsigned long long u64;
typedef short bf16x8 __attribute__((ext_vector_type(8)));
typedef short s16x4 __attribute__((ext_vector_type(4)));
typedef float f32x4 __attribute__((ext_vector_type(4)));
typedef float f32x2 __attribute__((ext_vector_type(2)));
typedef unsigned u32x4 __attribute__((ext_vector_type(4)));
typedef unsigned u32x2 __attribute__((ext_vector_type(2)));
typedef __bf16 bf2 __attribute__((ext_vector_type(2)));
#define LAS __attribute__((address_space(3)))
#define GAS __attribute__((address_space(1)))
#define DI __device__ __forceinline__
typedef __amdgpu_buffer_rsrc_t rsrc_t;

#define STAMPS 0
#define WIN1 4
#define WIN4 4
#define ROT0 2
#define LAG 12
#define LAGX 12
#define LAG2 0
#define FINE 0
#define APRIO 1
#define CPRIO 2
#define W3POS 0
#define WPRIO 0
#define ROT1 1
#define NORT 0
#define G1EXP 0
#define NAP 4
#define CODE_BYTES 32768

namespace {
constexpr unsigned OFF_MB = 4096, MB_EV = 256 * 128;
constexpr int EV_X = 0, EV_AB = 1, EV_EX = 2, EV_X1 = 3, EV_H = 4, EV_PER_LAYER = 5, EV_RT = 10;
constexpr unsigned OFF_CS = 0x60000;
constexpr unsigned OFF_ST = 0xA0000, ST_BUF = 8 * 2048 * 8;
constexpr unsigned OFF_XT = 0x120000, XT_BUF = 2048 * 640 * 2;
constexpr unsigned OFF_AB = OFF_XT + 4 * XT_BUF, AB_BUF = 2048 * 1280 * 2;
constexpr unsigned OFF_HT = OFF_AB + 2 * AB_BUF, HT_BUF = 2048 * 1536 * 2;
constexpr unsigned OFF_EX = OFF_HT + 2 * HT_BUF, EX_BUF = 256 * 12 * 2560;
constexpr unsigned OFF_W3 = OFF_EX + 2 * EX_BUF;
constexpr unsigned OFF_W4 = OFF_W3 + 16 * 20 * 12288;
constexpr unsigned WS_END = OFF_W4 + 8 * 48 * 5120;
constexpr unsigned OFF_STAMP = 0x6000000;
static_assert(OFF_MB + 11 * MB_EV <= OFF_CS && OFF_CS + 20 * 3072 * 4 <= OFF_ST && OFF_ST + 4 * ST_BUF <= OFF_XT && WS_END <= OFF_STAMP, "ws map");
constexpr unsigned L_MEAN = 0, L_RSTD = 512, L_CS1 = 1024, L_STD = 1536, L_CS3 = 2304, L_JUNK = 3072;
constexpr unsigned L_D = 4096;
constexpr unsigned L_WR = L_D + 61440, L_W4 = L_D + 6144, L_CP = L_D + 102400, L_XR = L_D + 110592, L_RED = L_W4 + 30720;
constexpr int w4slot(int ch) { return ch == 4 ? 4 : ((ch < 5 ? ch : ch - 5) + 2) & 3; }
constexpr int w4off(int s) { return w4slot(s / 3) * 30720 + (s % 3) * 10240; }
constexpr int SC1 = 16;
constexpr unsigned SPIN_CAP = 1u << 16;

DI rsrc_t mk(const void* p, unsigned bytes) { return __builtin_amdgcn_make_buffer_rsrc((void*)p, 0, bytes, 0x00020000); }
template <int N> DI void waitv() { asm volatile("s_waitcnt vmcnt(%0)" ::"n"(N) : "memory"); }
DI void bar() { asm volatile("s_waitcnt lgkmcnt(0)\n\ts_barrier" ::: "memory"); }
DI void lgk0() { __builtin_amdgcn_sched_barrier(0); __builtin_amdgcn_s_waitcnt(0xC07F); __builtin_amdgcn_sched_barrier(0); }
DI void barv() { lgk0(); asm volatile("s_barrier" ::: "memory"); __builtin_amdgcn_sched_barrier(0); }
DI void drain_bar() {
  asm volatile("s_waitcnt vmcnt(0) lgkmcnt(0)\n\ts_barrier" ::: "memory");
  __builtin_amdgcn_s_waitcnt(0);
  asm volatile("" ::: "memory");
}
DI void sched() { __builtin_amdgcn_sched_barrier(0); }
template <class T> DI void pin(T& x) { asm volatile("" : "+v"(x)); }
template <class T> DI void pins(T& x) { asm volatile("" : "+s"(x)); }
template <int AUX, int IMM> DI void dma(rsrc_t r, LAS char* l, unsigned vo, unsigned so) { __builtin_amdgcn_raw_ptr_buffer_load_lds(r, (LAS void*)l, 16, vo, so, IMM, AUX); }
template <int AUX> DI void touch(rsrc_t r, LAS char* l, unsigned vo, unsigned so) { __builtin_amdgcn_raw_ptr_buffer_load_lds(r, (LAS void*)l, 4, vo, so, 0, AUX); }
template <int AUX, int N> DI void dma_lin(rsrc_t r, LAS char* l, unsigned vo, unsigned so) {
#pragma unroll
  for (int j = 0; j < N; j += 4) {
    dma<AUX, 0>(r, l + j * 1024, vo, so + j * 1024);
    if (j + 1 < N) dma<AUX, 1024>(r, l + j * 1024, vo, so + j * 1024);
    if (j + 2 < N) dma<AUX, 2048>(r, l + j * 1024, vo, so + j * 1024);
    if (j + 3 < N) dma<AUX, 3072>(r, l + j * 1024, vo, so + j * 1024);
  }
}
DI unsigned uni(unsigned x) { x = __builtin_amdgcn_readfirstlane(x); asm volatile("" : "+s"(x)); return x; }
DI bf16x8 ld16(rsrc_t r, unsigned vo, unsigned so) { return __builtin_bit_cast(bf16x8, __builtin_amdgcn_raw_buffer_load_b128(r, vo, so, SC1)); }
DI void st16(rsrc_t r, u32x4 v, unsigned vo) { __builtin_amdgcn_raw_buffer_store_b128(v, r, vo, 0, SC1); }
DI bf16x8 lds16(LAS char* l, unsigned a) { return *(LAS bf16x8*)(l + a); }
DI unsigned pk(float lo, float hi) { f32x2 v = {lo, hi}; return __builtin_bit_cast(unsigned, __builtin_convertvector(v, bf2)); }
DI float blo(unsigned u) { return __uint_as_float(u << 16); }
DI float bhi(unsigned u) { return __uint_as_float(u & 0xffff0000u); }
DI f32x4 unpk(unsigned a, unsigned b) { return (f32x4){blo(a), bhi(a), blo(b), bhi(b)}; }
DI f32x4 mfma(bf16x8 a, bf16x8 b, f32x4 c) { return __builtin_amdgcn_mfma_f32_16x16x32_bf16(a, b, c, 0, 0, 0); }
DI void mma(f32x4& c, const bf16x8& a, const bf16x8& b) { asm volatile("v_mfma_f32_16x16x32_bf16 %0, %1, %2, %0" : "+v"(c) : "v"(a), "v"(b)); __builtin_amdgcn_sched_barrier(0); }
DI void mma0(f32x4& c, const bf16x8& a, const bf16x8& b) { asm volatile("v_mfma_f32_16x16x32_bf16 %0, %1, %2, 0" : "=&v"(c) : "v"(a), "v"(b)); __builtin_amdgcn_sched_barrier(0); }
DI void settle() { asm volatile("s_nop 7"); __builtin_amdgcn_sched_barrier(0); }
DI float dot2(unsigned a, unsigned b, float s) { return __builtin_amdgcn_fdot2_f32_bf16(__builtin_bit_cast(bf2, a), __builtin_bit_cast(bf2, b), s, false); }
DI float dot_ones(bf16x8 f, float s) {
  const u32x4 u = __builtin_bit_cast(u32x4, f);
#pragma unroll
  for (int j = 0; j < 4; ++j) s = dot2(u[j], 0x3f803f80u, s);
  return s;
}
DI float sum_q(float s) { s += __shfl_xor(s, 16); s += __shfl_xor(s, 32); return s; }
DI float sum8(float s) {
  s += __builtin_bit_cast(float, __builtin_amdgcn_update_dpp(0, __builtin_bit_cast(int, s), 0xB1, 0xf, 0xf, true));
  s += __builtin_bit_cast(float, __builtin_amdgcn_update_dpp(0, __builtin_bit_cast(int, s), 0x4E, 0xf, 0xf, true));
  s += __builtin_bit_cast(float, __builtin_amdgcn_update_dpp(0, __builtin_bit_cast(int, s), 0x141, 0xf, 0xf, true));
  return s;
}
DI u32x4 swap_pair(unsigned a0, unsigned a1, unsigned b0, unsigned b1) {
  const auto r0 = __builtin_amdgcn_permlane16_swap(a0, b0, false, false);
  const auto r1 = __builtin_amdgcn_permlane16_swap(a1, b1, false, false);
  return (u32x4){r0[0], r1[0], r0[1], r1[1]};
}

struct Ctx {
  char* ws; LAS char* lds; int lane, b;
  int kq, P, np;
  int P1, mat, ct1;
};
#define STAMP_DECL
#define stamp(k, w, id) do { (void)(id); } while (0)
#define STAMP_OUT(k, w)

DI u32x4 x_pack(int j, const f32x4 (&v)[5], unsigned (&res)[10]) {
  const int i1 = j < 2 ? 2 * j + 1 : 4;
#pragma unroll
  for (int i = 2 * j; i <= i1; ++i) { res[2 * i] = pk(v[i][0], v[i][1]); res[2 * i + 1] = pk(v[i][2], v[i][3]); }
  return swap_pair(res[4 * j], res[4 * j + 1], res[2 * i1], res[2 * i1 + 1]);
}
DI void x_part(rsrc_t rw, unsigned vx, unsigned vx2, int j, const f32x4 (&v)[5], unsigned (&res)[10], float& s, float& ss) {
  const u32x4 o = x_pack(j, v, res);
  st16(rw, o, j < 2 ? vx + j * 1024 : vx2);
#pragma unroll
  for (int e = 4 * j; e < (j < 2 ? 4 * j + 4 : 10); ++e) { s = dot2(res[e], 0x3f803f80u, s); ss = dot2(res[e], res[e], ss); }
  pin(s); pin(ss);
}
DI void x_stats(rsrc_t rw, unsigned vs, float s, float ss) {
  const auto r = __builtin_amdgcn_permlane16_swap(__float_as_uint(s), __float_as_uint(ss), false, false);
  const unsigned t = __float_as_uint(__uint_as_float(r[0]) + __uint_as_float(r[1]));
  const auto r2 = __builtin_amdgcn_permlane32_swap(t, t, false, false);
  const float u = __uint_as_float(r2[0]) + __uint_as_float(r2[1]);
  __builtin_amdgcn_raw_buffer_store_b32(__float_as_uint(u), rw, vs, 0, SC1);
}

DI int rot1(const Ctx& k) { const int r = ((k.ct1 * 5) >> 2) + (((k.P1 & 3) * 5) >> 1); return ROT1 ? (r >= 10 ? r - 10 : r) : 0; }

#define PHASE_LANE() Ctx k = k0; int w = w0; asm volatile("" : "+v"(k.lane), "+s"(w), "+s"(k.P), "+s"(k.kq), "+s"(k.np), "+s"(k.P1), "+s"(k.ct1), "+s"(k.mat)); \
  const int tfo = 8 * k.P + 4 * (k.kq >> 1) + w, cto = 2 * k.np + (k.kq & 1); (void)tfo; (void)cto; \
  const int lane = k.lane, q = lane >> 4, c = lane & 15, qs = 2 * (q & 1) + (q >> 1); (void)qs; \
  const unsigned rowrd = (c >> 3) * 1024 + (c & 7) * 128 + ((q ^ (c >> 1)) * 16); (void)rowrd
#define X_ADDR(xb) unsigned vx = OFF_XT + (xb) * XT_BUF + tfo * 20480 + (10 * cto + qs) * 256 + c * 16, vx2 = vx + 2048 + ((lane & 16) << 26), \
  vs = OFF_ST + (xb) * ST_BUF + tfo * 1024 + cto * 128 + lane * 4 + ((lane & 32) << 25); pin(vx); pin(vx2); pin(vs)

DI void consumer(const Ctx& k0, const char* x0, const char* wg, const char* wu, const char* wdn, char* out, const int w0) {
  LAS char* const L = k0.lds;
  const rsrc_t rw = mk(k0.ws, WS_END);
  unsigned res[10];
  STAMP_DECL;
  if (CPRIO) __builtin_amdgcn_s_setprio(CPRIO);
  stamp(k0, w0, 0);
  auto retile = [&](const Ctx& k0, const int w) {
    int e = ((k0.b >> 7) * 64 + (k0.b & 63)) * 4 + w;
    pins(e);
    const int lane = k0.lane;
    const int g3 = ((e >> 2) * 2731) >> 16, ks3 = 4 * g3 + w, st3 = (e >> 2) - g3 * 24, ks4 = (e * 6554) >> 16, st4 = e - ks4 * 10;
    LAS char* t[3];
#pragma unroll
    for (int m = 0; m < 3; ++m) { const int u = rot1(k0) + (m < 2 ? 4 + w : 8 + (w >> 1)); t[m] = L + L_D + (u >= 10 ? u - 10 : u) * 10240 + (m < 2 ? m : w & 1) * 4096; }
    if (LAG) __builtin_amdgcn_s_sleep(LAG);
#pragma unroll
    for (int m = 0; m < 3; ++m) {
      const unsigned stride = m < 2 ? 3072 : 1280;
      const rsrc_t rr = mk(m == 0 ? wg : (m == 1 ? wu : wdn), e < 480 ? 640 * 1536 * 2 : 0);
#pragma unroll
      for (int j = 0; j < 4; ++j) dma<0, 0>(rr, t[m] + j * 1024, (lane >> 3) * stride + (lane & 7) * 16, (32 * (m < 2 ? ks3 : ks4) + 8 * j) * stride + 128 * (m < 2 ? st3 : st4));
    }
    const rsrc_t ro = mk(k0.ws, e < 480 ? WS_END : 0);
    const bf16x8 ones = {0x3f80, 0x3f80, 0x3f80, 0x3f80, 0x3f80, 0x3f80, 0x3f80, 0x3f80};
    const unsigned tr = (lane >> 4) * 1024 + ((lane >> 2) & 3) * 128 + (lane & 3) * 8;
    __builtin_amdgcn_s_waitcnt(0);
    asm volatile("" ::: "memory");
    u32x4 f[3][4];
#pragma unroll
    for (int m = 0; m < 3; ++m)
#pragma unroll
      for (int n = 0; n < 4; ++n) {
        const u32x2 lo = __builtin_bit_cast(u32x2, __builtin_amdgcn_ds_read_tr16_b64_v4i16((LAS s16x4*)(t[m] + tr + 32 * n)));
        const u32x2 hi = __builtin_bit_cast(u32x2, __builtin_amdgcn_ds_read_tr16_b64_v4i16((LAS s16x4*)(t[m] + tr + 32 * n + 512)));
        f[m][n] = (u32x4){lo[0], lo[1], hi[0], hi[1]};
      }
    stamp(k0, w, 61);
#pragma unroll
    for (int m = 0; m < 3; ++m) {
      f32x4 cs[4];
#pragma unroll
      for (int n = 0; n < 4; ++n) {
        const int nf = 4 * (m < 2 ? st3 : st4) + n;
        st16(ro, f[m][n], lane * 16 + (m < 2 ? OFF_W3 + m * 1024 + ks3 * 12288 + ((nf * 171) >> 10) * 19 * 12288 + nf * 2048 : OFF_W4 + ks4 * 5120 + ((nf * 205) >> 10) * 47 * 5120 + nf * 1024));
        if (m < 2) cs[n] = mfma(ones, __builtin_bit_cast(bf16x8, f[m][n]), (f32x4){0.f, 0.f, 0.f, 0.f});
      }
      if (m < 2) *(LAS float*)(L + L_CP + (m * 64 + lane) * 16 + w * 4) = (lane & 32) ? ((lane & 16) ? cs[3][0] : cs[2][0]) : ((lane & 16) ? cs[1][0] : cs[0][0]);
    }
    if (LAG2) __builtin_amdgcn_s_sleep(LAG2);
    stamp(k0, w, 62);
  };
#pragma unroll 1
  for (int layer = 0; layer < 2; ++layer) {
    const int sb = 3 + layer * 24;
    {
      PHASE_LANE();
      unsigned vo = OFF_AB + layer * AB_BUF + ((k.mat * 4 + (k.ct1 >> 1)) * 64 + 4 * k.P1 + w) * 10240 + 5 * (k.ct1 & 1) * 1024 + (qs * 16 + c) * 16;
      unsigned ta = (32 * w + 4 * q) * 4, tc = c * 4;
      pin(vo); pin(ta); pin(tc);
      f32x4 acc[2][5], mean[2], rstd[2]; float cs1[5];
      bf16x8 wf[2][5];
      auto tables = [&]() {
#pragma unroll
        for (int i = 0; i < 5; ++i) cs1[i] = *(LAS float*)(L + L_CS1 + tc + i * 64);
#pragma unroll
        for (int t = 0; t < 2; ++t) { mean[t] = *(LAS f32x4*)(L + ta + L_MEAN + 64 * t); rstd[t] = *(LAS f32x4*)(L + ta + L_RSTD + 64 * t); }
      };
#pragma unroll
      for (int i = 0; i < 5; ++i) { acc[0][i] = (f32x4){0.f, 0.f, 0.f, 0.f}; acc[1][i] = (f32x4){0.f, 0.f, 0.f, 0.f}; }
      if (layer == 0) {
        bf16x8 xf[2][2], cf[2], ones = {0x3f80, 0x3f80, 0x3f80, 0x3f80, 0x3f80, 0x3f80, 0x3f80, 0x3f80};
        f32x4 as[2], aq[2], ac[2];
        unsigned wt = rot1(k) * 10240, wa0 = L_D + rowrd, wc0 = wa0 + w * 2048;
        pin(wa0); pin(wc0);
        unsigned wa = wa0 + wt, xr = L_XR + w * 4096 + rowrd, x_slot = 32768;
#pragma unroll
        for (int i = 0; i < 5; ++i) { pin(acc[0][i]); pin(acc[1][i]); }
#pragma unroll
        for (int t = 0; t < 2; ++t) { as[t] = (f32x4){0.f, 0.f, 0.f, 0.f}; aq[t] = as[t]; ac[t] = as[t]; pin(as[t]); pin(aq[t]); pin(ac[t]); }
        pin(wa); pin(xr); pin(ones);
        if (k.P & 1) retile(k, w);
        bar();
        stamp(k, w, sb + 0);
#pragma unroll
        for (int i = 0; i < 5; ++i) wf[0][i] = lds16(L, wa + i * 2048);
        xf[0][0] = lds16(L, xr + 32768); xf[0][1] = lds16(L, xr + 32768 + 2048); cf[0] = lds16(L, wa + w * 2048);
        unsigned xn = (xr + 32768) ^ 64, wn = wa ^ 64, cn = wn + w * 2048;
        lgk0();
        int left = 10;
#pragma unroll 1
        do {
#pragma unroll
          for (int h = 0; h < 2; ++h) {
#pragma unroll
            for (int n = 0; n < 16; ++n) {
              if (n < 10) { const int t = n / 5, i = t ? 9 - n : n; mma(acc[t][i], xf[h][t], wf[h][i]); }
              else if (n == 10) mma(aq[1], xf[h][1], xf[h][1]);
              else if (n == 11) mma(as[1], xf[h][1], ones);
              else if (n == 12) mma(as[0], xf[h][0], ones);
              else if (n == 13) mma(aq[0], xf[h][0], xf[h][0]);
              else if (n == 14) mma(ac[0], ones, cf[h]);
              else mma(ac[1], ones, wf[h][4]);
              if (n < 2) xf[h ^ 1][n] = lds16(L, xn + n * 2048);
              else if (n < 7) wf[h ^ 1][n - 2] = lds16(L, wn + (n - 2) * 2048);
              else if (n == 7) cf[h ^ 1] = lds16(L, cn);
              else if (h == 0) {
                if (n == 8) { x_slot = x_slot == 32768 ? 0 : x_slot + 16384; pins(x_slot); }
                if (n == 9) { wt = wt == 92160 ? 0 : wt + 10240; pins(wt); }
                if (n == 10) { xn = xr + x_slot; wn = wa0 + wt; pin(xn); pin(wn); }
                if (n == 11) { cn = wc0 + wt; pin(cn); }
              } else {
                if (n == 8) { xn ^= 64; wn ^= 64; pin(xn); pin(wn); }
                if (n == 9) { cn ^= 64; pin(cn); --left; pins(left); }
              }
              sched();
            }
            if (h == 0) barv(); else lgk0();
          }
        } while (left);
        settle();
        stamp(k, w, 50);
        if (q == 0) { *(LAS float*)(L + L_CS1 + (16 * w + c) * 4) = ac[0][0]; *(LAS float*)(L + L_CS1 + (64 + c) * 4) = ac[1][0]; }
#pragma unroll
        for (int t = 0; t < 2; ++t) {
          const float s = ((c & 2) ? ((c & 1) ? as[t][3] : as[t][2]) : ((c & 1) ? as[t][1] : as[t][0])) * (1.f / 640.f);
          const float ss = ((c & 2) ? ((c & 1) ? aq[t][3] : aq[t][2]) : ((c & 1) ? aq[t][1] : aq[t][0])) * (1.f / 640.f);
          const float r = __builtin_amdgcn_rsqf(fmaxf(ss - s * s, 0.f) + 1e-5f);
          if ((c >> 2) == q) { *(LAS float*)(L + L_MEAN + (32 * w + 16 * t + c) * 4) = -s; *(LAS float*)(L + L_RSTD + (32 * w + 16 * t + c) * 4) = k.mat ? -r : r; }
        }
        bar();
        tables();
      } else {
        constexpr int WIN = WIN1;
#define KS(s) (((s) + 12) % 20)
        unsigned wb[2][2] = {{L_D + rowrd, L_D + rowrd + 51200}, {L_D + (rowrd ^ 64), L_D + (rowrd ^ 64) + 51200}};
        unsigned xv[2] = {OFF_XT + 2 * XT_BUF + (8 * k.P1 + 2 * w) * 20480 + lane * 16, 0}; xv[1] = xv[0] + 20480;
        pin(wb[0][0]); pin(wb[0][1]); pin(wb[1][0]); pin(wb[1][1]); pin(xv[0]); pin(xv[1]);
        bf16x8 xa[WIN][2];
        bar();
        stamp(k, w, sb + 0);
#pragma unroll
        for (int s = 0; s < WIN; ++s) { xa[s][0] = ld16(rw, xv[0] + (KS(s) & 3) * 1024, (KS(s) >> 2) * 4096); xa[s][1] = ld16(rw, xv[1] + (KS(s) & 3) * 1024, (KS(s) >> 2) * 4096); sched(); }
#pragma unroll
        for (int i = 0; i < 5; ++i) wf[0][i] = lds16(L, wb[0][1] + 10240 + i * 2048);
        sched();
        lgk0();
#pragma unroll
        for (int s = 0; s < 20; ++s) {
          if (s == 20 - WIN) barv();
          if (s == 19) { barv(); tables(); sched(); }
#pragma unroll
          for (int n = 0; n < 10; ++n) {
            const int t = n / 5, i = t ? 9 - n : n;
            if (s == 0) mma0(acc[t][i], xa[0][t], wf[0][i]); else mma(acc[t][i], xa[s % WIN][t], wf[s & 1][i]);
            if (n < 5 && s + 1 < 20) wf[(s + 1) & 1][n] = lds16(L, wb[(s + 1) & 1][KS(s + 1) / 10] + ((KS(s + 1) >> 1) % 5) * 10240 + n * 2048);
            if (n == 5 && s + WIN < 20) xa[s % WIN][0] = ld16(rw, xv[0] + (KS(s + WIN) & 3) * 1024, (KS(s + WIN) >> 2) * 4096);
            if (n == 9 && s + WIN < 20) xa[s % WIN][1] = ld16(rw, xv[1] + (KS(s + WIN) & 3) * 1024, (KS(s + WIN) >> 2) * 4096);
            if (n == 8 && s + 1 < 20) lgk0();
            sched();
          }
        }
        settle();
      }
      stamp(k, w, sb + 1);
#pragma unroll
      for (int i = 0; i < 5; ++i) {
        const float cs = cs1[i];
        const f32x4 a = (mean[0] * cs + acc[0][i]) * rstd[0], bb = (mean[1] * cs + acc[1][i]) * rstd[1];
        st16(rw, swap_pair(pk(a[0], a[1]), pk(a[2], a[3]), pk(bb[0], bb[1]), pk(bb[2], bb[3])), vo + i * 1024);
      }
      if (layer == 0 && (k.P & 1) && w < 2) {
        const int j = (k.b >> 7) * 64 + (k.b & 63), g3 = (j * 2731) >> 16;
        const f32x4 t = *(LAS f32x4*)(L + L_CP + (w * 64 + lane) * 16);
        if (j < 120) __builtin_amdgcn_raw_buffer_store_b32(__float_as_uint((t[0] + t[1]) + (t[2] + t[3])), rw, lane * 4, OFF_CS + (g3 * 3072 + w * 1536 + 64 * (j - g3 * 24)) * 4, SC1);
      }
      stamp(k, w, sb + 2);
      drain_bar();
      stamp(k, w, sb + 3);
    }
    {
      PHASE_LANE();
      const int wm = w >> 1, wn = w & 1;
      f32x4 acc[4][5];
      bf16x8 fa[2][5], fb[2][4];
#pragma unroll
      for (int t = 0; t < 4; ++t)
#pragma unroll
        for (int i = 0; i < 5; ++i) { acc[t][i] = (f32x4){0.f, 0.f, 0.f, 0.f}; pin(acc[t][i]); }
      unsigned wb = L_WR + wm * 8192 + rowrd, ab = L_D + wn * 5120 + lane * 16;
      unsigned ws_slot = 49152, ab_slot = 0;
      unsigned ev[2], ev8[2], en = w == k.kq ? 0 : WS_END;
      ev[0] = OFF_EX + layer * EX_BUF + ((k.P * 16 + w * 4 + k.np) * 3 + ((k.kq - w) & 3) - 1) * 10240 + lane * 16; ev[1] = ev[0] + 5120;
      ev8[0] = ev[0] - lane * 8 + 2048; ev8[1] = ev8[0] + 5120;
      unsigned lo = L_D + 20480 + w * 10240 + lane * 16, lo8 = lo - lane * 8 + 2048;
      pin(lo); pin(lo8);
      pin(wb); pin(ab); pin(ev[0]); pin(ev[1]); pin(ev8[0]); pin(ev8[1]); pins(en);
      const rsrc_t re = mk(k.ws, en);
      bar();
#pragma unroll
      for (int t = 0; t < 4; ++t) fb[0][t] = lds16(L, wb + 49152 + t * 2048);
      bar();
      stamp(k, w, sb + 4);
#pragma unroll
      for (int i = 0; i < 5; ++i) fa[0][i] = lds16(L, ab + i * 1024);
      unsigned an = ab + 10240, wn2 = (wb + 49152) ^ 64;
      lgk0();
      int left = 14;
      auto half = [&](int h, bool last) {
#pragma unroll
        for (int n = 0; n < 20; ++n) {
          const int i = n / 4, t = (i & 1) ? 3 - n % 4 : n % 4;
          mma(acc[t][i], fa[h][i], fb[h][t]);
          if (n < 4) fb[h ^ 1][n] = lds16(L, wn2 + n * 2048);
          else if (n < 9) fa[h ^ 1][n - 4] = lds16(L, an + (n - 4) * 1024);
          else if (last) { if (n == 9) { wn2 = wb ^ 64; pin(wn2); } }
          else if (h == 0) {
            if (n == 9) { ab_slot = ab_slot == 40960 ? 0 : ab_slot + 20480; pins(ab_slot); }
            if (n == 10) { ws_slot = ws_slot == 81920 ? 0 : ws_slot + 16384; pins(ws_slot); }
            if (n == 11) { an = ab + ab_slot; wn2 = wb + ws_slot; pin(an); pin(wn2); }
          } else {
            if (n == 9) { an += 10240; wn2 ^= 64; pin(an); pin(wn2); }
            if (n == 10) { --left; pins(left); }
          }
          sched();
        }
        if (h == 0) barv(); else lgk0();
      };
#pragma unroll 1
      do { half(0, false); half(1, false); } while (left);
      half(0, true);
      bf16x8 fc[5], fd[4];
      {
        unsigned o[10];
        auto send = [&](int t, int j) {
          if (j < 5) { o[2 * j] = pk(acc[t][j][0], acc[t][j][1]); o[2 * j + 1] = pk(acc[t][j][2], acc[t][j][3]); }
          if (j == 5) st16(re, (u32x4){o[0], o[1], o[2], o[3]}, ev[t >> 1] + (t & 1) * 2560);
          if (j == 6) { *(LAS u32x4*)(L + lo + t * 2560) = (u32x4){o[0], o[1], o[2], o[3]}; *(LAS u32x2*)(L + lo8 + t * 2560) = (u32x2){o[8], o[9]}; }
          if (j == 7) st16(re, (u32x4){o[4], o[5], o[6], o[7]}, ev[t >> 1] + (t & 1) * 2560 + 1024);
          if (j == 8) { __builtin_amdgcn_raw_buffer_store_b64((u32x2){o[8], o[9]}, re, ev8[t >> 1] + (t & 1) * 2560, 0, SC1); *(LAS u32x4*)(L + lo + t * 2560 + 1024) = (u32x4){o[4], o[5], o[6], o[7]}; }
        };
        auto step_send = [&](const bf16x8 (&a)[5], const bf16x8 (&b)[4], int t, int j0) {
#pragma unroll
          for (int i = 0; i < 5; ++i) { mma(acc[t][i], a[i], b[t]); if (j0 + i >= 0) send(t - 1, j0 + i); sched(); }
        };
#pragma unroll
        for (int n = 0; n < 20; ++n) {
          const int i = n % 5;
          if (n < 10) mma(acc[n / 5][i], fa[1][i], fb[1][n / 5]);
          else if (n < 15) mma(acc[0][i], fa[0][i], fb[0][0]);
          else mma(acc[0][i], fc[i], fd[0]);
          if (n == 0) fb[0][0] = lds16(L, wb);
          else if (n < 6) fa[0][n - 1] = lds16(L, ab + (n - 1) * 1024);
          else if (n == 6) fd[0] = lds16(L, wn2);
          else if (n < 12) fc[n - 7] = lds16(L, ab + 10240 + (n - 7) * 1024);
          else if (n < 18) { if (n & 1) fd[(n - 10) / 2] = lds16(L, wn2 + (n - 10) / 2 * 2048); else fb[0][(n - 10) / 2] = lds16(L, wb + (n - 10) / 2 * 2048); }
          sched();
        }
        step_send(fa[0], fb[0], 1, -1);
        barv();
        step_send(fc, fd, 1, 4);
        step_send(fa[1], fb[1], 2, -3); step_send(fa[0], fb[0], 2, 2); step_send(fc, fd, 2, 7);
        step_send(fa[1], fb[1], 3, -3); step_send(fa[0], fb[0], 3, 2); step_send(fc, fd, 3, 7);
        stamp(k, w, sb + 5);
        asm volatile("s_nop 3"); sched();
#pragma unroll
        for (int j = 0; j < 9; ++j) { send(3, j); sched(); }
      }
      stamp(k, w, sb + 6);
      drain_bar();
      stamp(k, w, sb + 7);
      if (layer == 0) {
        const rsrc_t rx = mk(x0, 2048 * 640 * 2);
#pragma unroll
        for (int j = 0; j < 3; ++j) {
          const int u = 64 * j + lane, r = (u * 205) >> 11;
          dma<0, 0>(rx, L + L_D + w * 3072 + j * 1024, r * 1120 + u * 16 + (j == 2 ? (lane & 32) << 25 : 0), 16 * tfo * 1280 + cto * 160);
        }
        __builtin_amdgcn_s_waitcnt(0);
        asm volatile("" ::: "memory");
#pragma unroll
        for (int i = 0; i < 5; ++i) { const u32x2 t = *(LAS u32x2*)(L + L_D + w * 3072 + c * 160 + 8 * q + 32 * i); res[2 * i] = t[0]; res[2 * i + 1] = t[1]; }
      }
      f32x4 v[5];
      {
        LAS char* t = L + L_D + 20480 + k.kq * 10240 + w * 2560;
        const u32x4 a = *(LAS u32x4*)(t + lane * 16), bb = *(LAS u32x4*)(t + 1024 + lane * 16); const u32x2 d = *(LAS u32x2*)(t + 2048 + lane * 8);
        v[0] = unpk(a[0], a[1]); v[1] = unpk(a[2], a[3]); v[2] = unpk(bb[0], bb[1]); v[3] = unpk(bb[2], bb[3]); v[4] = unpk(d[0], d[1]);
#pragma unroll
        for (int i = 0; i < 5; ++i) { v[i] += unpk(res[2 * i], res[2 * i + 1]); pin(v[i]); }
      }
      unsigned rv = OFF_EX + layer * EX_BUF + (k.P * 16 + k.kq * 4 + k.np) * 30720 + w * 2560 + lane * 16, rv8 = rv - lane * 8;
      pin(rv); pin(rv8);
      X_ADDR(2 * layer + 1);
      bar();
      stamp(k, w, sb + 8);
      u32x4 e[3][2]; u32x2 e8[3];
#pragma unroll
      for (int j = 0; j < 3; ++j)
#pragma unroll
        for (int n = 0; n < 3; ++n) {
          if (j < 2) e[n][j] = __builtin_amdgcn_raw_buffer_load_b128(rw, rv + j * 1024, n * 10240, SC1);
          else e8[n] = __builtin_amdgcn_raw_buffer_load_b64(rw, rv8 + 2048, n * 10240, SC1);
          sched();
        }
      float s = 0.f, ss = 0.f;
#pragma unroll
      for (int j = 0; j < 3; ++j) {
#pragma unroll
        for (int n = 0; n < 3; ++n) {
          if (j < 2) { v[2 * j] += unpk(e[n][j][0], e[n][j][1]); v[2 * j + 1] += unpk(e[n][j][2], e[n][j][3]); }
          else v[4] += unpk(e8[n][0], e8[n][1]);
        }
        x_part(rw, vx, vx2, j, v, res, s, ss);
        sched();
      }
      x_stats(rw, vs, s, ss);
      stamp(k, w, sb + 9);
      drain_bar();
      stamp(k, w, sb + 10);
    }
    {
      PHASE_LANE();
      const int wm = w >> 1, wn = w & 1;
      f32x4 acc[4][6];
      bf16x8 fa[2][6], fb[2][4];
#pragma unroll
      for (int t = 0; t < 4; ++t)
#pragma unroll
        for (int i = 0; i < 6; ++i) { acc[t][i] = (f32x4){0.f, 0.f, 0.f, 0.f}; pin(acc[t][i]); }
      unsigned wb = L_WR + wn * 6144 + lane * 16, xb = L_D + wm * 8192 + lane * 16;
      unsigned w_slot = 49152, x_slot = 0;
      unsigned vo = OFF_HT + layer * HT_BUF + ((8 * k.P + 4 * wm + (q & 1)) * 48 + 3 * (4 * k.kq + k.np)) * 1024 + (6 * wn + (q >> 1)) * 256 + c * 16, vo2 = vo + 98304;
      unsigned ta = (64 * wm + c) * 4, tc = L_CS3 + (48 * wn + 4 * q) * 4;
      pin(wb); pin(xb); pin(vo); pin(vo2); pin(ta); pin(tc);
      bar();
#pragma unroll
      for (int i = 0; i < 6; ++i) fa[0][i] = lds16(L, wb + 49152 + i * 1024);
      bar();
      stamp(k, w, sb + 11);
#pragma unroll
      for (int t = 0; t < 4; ++t) fb[0][t] = lds16(L, xb + t * 2048);
      unsigned wn3 = wb + 49152 + 12288, xn = xb + 1024;
      lgk0();
      int left = 8;
#pragma unroll 1
      do {
#pragma unroll
        for (int h = 0; h < 2; ++h) {
#pragma unroll
          for (int n = 0; n < 24; ++n) {
            const int i = n / 4, t = (i & 1) ? 3 - n % 4 : n % 4;
            mma(acc[t][i], fa[h][i], fb[h][t]);
            if (n < 4) fb[h ^ 1][n] = lds16(L, xn + n * 2048);
            else if (n < 10) fa[h ^ 1][n - 4] = lds16(L, wn3 + (n - 4) * 1024);
            else if (h == 0) {
              if (n == 10) { x_slot = x_slot == 32768 ? 0 : x_slot + 16384; pins(x_slot); }
              if (n == 11) { w_slot = w_slot == 73728 ? 0 : w_slot + 24576; pins(w_slot); }
              if (n == 12) { wn3 = wb + w_slot; xn = xb + x_slot; pin(wn3); pin(xn); }
            } else {
              if (n == 10) { wn3 += 12288; xn += 1024; pin(wn3); pin(xn); }
              if (n == 11) { --left; pins(left); }
            }
            sched();
          }
          if (h == 0) barv(); else lgk0();
        }
      } while (left);
      float mean[4], rl2[4], sd[4];
      f32x4 cs[6];
      bf16x8 x9[2][4], w9[2][6];
      unsigned h[4][2];
      f32x4 o[4];
      auto one = [](float v) { asm("" : "+v"(v)); return v; };
      auto corr1 = [&](int f, int n) { const int t = n >> 3, i = 2 * f + ((n >> 2) & 1), e = n & 3; if (n < 32) acc[t][i][e] = one(fmaf(cs[i][e], mean[t], acc[t][i][e])); };
      auto glu1 = [&](int n) {
        const int t = n / 15, j = n % 15;
        if (j < 2) { o[t][2 * j] = one(acc[t][0][2 * j] * rl2[t]); o[t][2 * j + 1] = one(acc[t][0][2 * j + 1] * rl2[t]); }
        else if (j < 6) o[t][j - 2] = __builtin_amdgcn_exp2f(o[t][j - 2]);
        else if (j < 8) { o[t][2 * j - 12] = one(fmaf(o[t][2 * j - 12], sd[t], sd[t])); o[t][2 * j - 11] = one(fmaf(o[t][2 * j - 11], sd[t], sd[t])); }
        else if (j < 12) o[t][j - 8] = __builtin_amdgcn_rcpf(o[t][j - 8]);
        else if (j < 14) { o[t][2 * j - 24] = one(o[t][2 * j - 24] * acc[t][1][2 * j - 24]); o[t][2 * j - 23] = one(o[t][2 * j - 23] * acc[t][1][2 * j - 23]); }
        else { h[t][0] = pk(o[t][0], o[t][1]); h[t][1] = pk(o[t][2], o[t][3]); }
      };
      auto glu = [&](int f, int t, int from) {
        if (from < 1) { o[t] = acc[t][2 * f] * rl2[t]; asm("" : "+v"(o[t])); }
        if (from < 2) {
#pragma unroll
          for (int r = 0; r < 4; ++r) o[t][r] = __builtin_amdgcn_exp2f(o[t][r]);
        }
        o[t] = o[t] * sd[t] + sd[t]; asm("" : "+v"(o[t]));
#pragma unroll
        for (int r = 0; r < 4; ++r) o[t][r] = __builtin_amdgcn_rcpf(o[t][r]);
        o[t] *= acc[t][2 * f + 1];
        h[t][0] = pk(o[t][0], o[t][1]); h[t][1] = pk(o[t][2], o[t][3]);
      };
      auto mm = [&](const bf16x8 (&a)[6], const bf16x8 (&b)[4], int f, int n) { const int t = n >> 1, i = 2 * f + ((n ^ t) & 1); mma(acc[t][i], a[i], b[t]); };
      auto tab2 = [&](float (&d)[4], unsigned off, int j) { d[2 * j] = *(LAS float*)(L + ta + off + 128 * j); d[2 * j + 1] = *(LAS float*)(L + ta + off + 128 * j + 64); };
#pragma unroll
      for (int n = 0; n < 24; ++n) {
        mm(fa[0], fb[0], n >> 3, n & 7);
        if (n < 2) { tab2(mean, L_MEAN, n); cs[n] = *(LAS f32x4*)(L + tc + 384 * n); }
        else if (n < 6) fb[1][n - 2] = lds16(L, xn + (n - 2) * 2048);
        else if (n < 12) fa[1][n - 6] = lds16(L, wn3 + (n - 6) * 1024);
        if (n >= 8 && n < 12) corr1(0, n - 8);
        if (n >= 12) { corr1(0, 2 * n - 20); corr1(0, 2 * n - 19); }
        sched();
      }
      wn3 = wb + 73728; pin(wn3);
      barv();
#pragma unroll
      for (int n = 0; n < 16; ++n) {
        mm(fa[1], fb[1], n >> 3, n & 7);
        if (n < 12) { const int kk = n / 6, j = n % 6; if (j < 4) x9[kk][j] = lds16(L, xb + kk * 1024 + j * 2048); else w9[kk][j - 4] = lds16(L, wn3 + kk * 12288 + (j - 4) * 1024); }
        else if (n < 14) cs[n - 10] = *(LAS f32x4*)(L + tc + 64 + 384 * (n - 12));
        if (n >= 10 && n < 14) corr1(0, 18 + n);
        sched();
      }
#pragma unroll
      for (int n = 0; n < 16; ++n) {
        mm(w9[n >> 3], x9[n >> 3], 0, n & 7);
        if (n < 8) w9[(n >> 1) & 1][2 + 2 * (n >> 2) + (n & 1)] = lds16(L, wn3 + ((n >> 1) & 1) * 12288 + (2 + 2 * (n >> 2) + (n & 1)) * 1024);
        else if (n < 10) { tab2(rl2, L_RSTD, n & 1); tab2(sd, L_STD, n & 1); }
        if (n >= 4 && n < 8) corr1(1, n - 4);
        if (n >= 10) { corr1(1, 2 * n - 16); corr1(1, 2 * n - 15); }
        sched();
      }
      barv();
      u32x4 sp;
#pragma unroll
      for (int n = 0; n < 40; ++n) {
        if (n < 16) mm(w9[n >> 3], x9[n >> 3], 1, n & 7);
        else if (n < 24) mm(fa[1], fb[1], 2, n & 7);
        else mm(w9[(n >> 3) - 3], x9[(n >> 3) - 3], 2, n & 7);
        if (n < 36) glu1(n);
        else if (n == 36) { const auto r = __builtin_amdgcn_permlane16_swap(h[0][0], h[1][0], false, false); sp[0] = r[0]; sp[2] = r[1]; }
        else if (n == 37) { const auto r = __builtin_amdgcn_permlane16_swap(h[0][1], h[1][1], false, false); sp[1] = r[0]; sp[3] = r[1]; }
        else if (n == 38) { st16(rw, sp, vo); cs[4] = *(LAS f32x4*)(L + tc + 128); }
        else cs[5] = *(LAS f32x4*)(L + tc + 384 + 128);
        sched();
      }
      stamp(k, w, sb + 12);
      glu(0, 2, 2); glu(0, 3, 0);
      st16(rw, swap_pair(h[2][0], h[2][1], h[3][0], h[3][1]), vo2);
      sched();
#pragma unroll
      for (int f = 1; f < 3; ++f)
#pragma unroll
        for (int t = 0; t < 4; t += 2) {
          if (f == 2 || t == 2) { acc[t][2 * f] += cs[2 * f] * mean[t]; acc[t][2 * f + 1] += cs[2 * f + 1] * mean[t]; acc[t + 1][2 * f] += cs[2 * f] * mean[t + 1]; acc[t + 1][2 * f + 1] += cs[2 * f + 1] * mean[t + 1]; }
          glu(f, t, 0); glu(f, t + 1, 0);
          st16(rw, swap_pair(h[t][0], h[t][1], h[t + 1][0], h[t + 1][1]), (t ? vo2 : vo) + f * 512);
          sched();
        }
      stamp(k, w, sb + 13);
    }
    {
      PHASE_LANE();
      constexpr int WIN = WIN4;
      static_assert(24 - WIN == 20, "that barrier frees chunk 6's slot and publishes chunk 7");
      const int kh = w & 1;
      unsigned wb[3] = {L_W4 + kh * 5120 + lane * 16, 0, 0}; wb[1] = wb[0] + 51200; wb[2] = wb[0] + 102400;
      pin(wb[0]); pin(wb[1]); pin(wb[2]);
      bar();
      bf16x8 ha[WIN][2], wf[7][5];
      f32x4 acc[2][5];
#define WF(s) ((s) < 6 ? (s) : 5 + (((s) + 1) & 1))
#pragma unroll
      for (int s = 0; s < 6; ++s)
#pragma unroll
        for (int i = 0; i < 5; ++i) wf[s][i] = lds16(L, wb[w4off(s) / 51200] + w4off(s) % 51200 + i * 1024);
      lgk0();
      drain_bar();
      stamp(k, w, sb + 14);
      sched();
      unsigned hv[2] = {OFF_HT + layer * HT_BUF + tfo * 49152 + kh * 1024 + lane * 16, OFF_HT + layer * HT_BUF + (tfo ^ 1) * 49152 + kh * 1024 + lane * 16};
      unsigned rd = L_RED + w * 5120 + lane * 16, wr = L_RED + (w ^ 1) * 5120 + lane * 16;
      pin(hv[0]); pin(hv[1]); pin(rd); pin(wr);
      X_ADDR(2);
      unsigned vout = (16 * tfo + c) * 1280 + (10 * cto + qs) * 16;
      unsigned vout2 = vout + 128 + ((lane & 16) << 26);
      pin(vout); pin(vout2);
#pragma unroll
      for (int i = 0; i < 5; ++i) { acc[0][i] = unpk(res[2 * i], res[2 * i + 1]); pin(acc[0][i]); }
      bar();
      stamp(k, w, sb + 15);
#pragma unroll
      for (int s = 0; s < WIN; ++s) { ha[s][1] = ld16(rw, hv[1] + (s & 1) * 2048, (s >> 1) * 4096); sched(); ha[s][0] = ld16(rw, hv[0] + (s & 1) * 2048, (s >> 1) * 4096); sched(); }
#pragma unroll
      for (int s = 0; s < 22; ++s) {
        if (s == 8 || s == 24 - WIN) barv();
#pragma unroll
        for (int n = 0; n < 10; ++n) {
          const int u = n < 5, i = u ? n : 9 - n;
          if (s == 0 && u) mma0(acc[1][i], wf[0][i], ha[0][1]); else mma(acc[u][i], wf[WF(s)][i], ha[s % WIN][u]);
          if (n < 5 && s >= 5) wf[WF(s + 1)][n] = lds16(L, wb[w4off(s + 1) / 51200] + w4off(s + 1) % 51200 + n * 1024);
          if (n == 5 && s + WIN < 24) ha[s % WIN][1] = ld16(rw, hv[1] + ((s + WIN) & 1) * 2048, ((s + WIN) >> 1) * 4096);
          if (n == 9 && s + WIN < 24) ha[s % WIN][0] = ld16(rw, hv[0] + ((s + WIN) & 1) * 2048, ((s + WIN) >> 1) * 4096);
          if (n == 8 && s >= 5) lgk0();
          sched();
        }
      }
      f32x4 pt[5];
#pragma unroll
      for (int n = 0; n < 20; ++n) {
        const int i = n % 5, s = 22 + ((n / 5) & 1), u = n < 10;
        if (n == 17) barv();
        mma(acc[u][i], wf[WF(s)][i], ha[s % WIN][u]);
        if (n < 3) { wf[WF(23)][2 * n] = lds16(L, wb[w4off(23) / 51200] + w4off(23) % 51200 + 2 * n * 1024); if (n < 2) wf[WF(23)][2 * n + 1] = lds16(L, wb[w4off(23) / 51200] + w4off(23) % 51200 + (2 * n + 1) * 1024); }
        if (n >= 12 && n < 17) *(LAS f32x4*)(L + wr + (n - 12) * 1024) = acc[1][n - 12];
        if (n >= 17) { pt[2 * n - 34] = *(LAS f32x4*)(L + rd + (2 * n - 34) * 1024); if (n < 19) pt[2 * n - 33] = *(LAS f32x4*)(L + rd + (2 * n - 33) * 1024); }
        sched();
      }
      stamp(k, w, sb + 16);
#pragma unroll
      for (int i = 0; i < 5; ++i) { acc[0][i] += pt[i]; pin(acc[0][i]); sched(); }
      if (layer == 1) {
        const rsrc_t ro = mk(out, 2048 * 640 * 2);
#pragma unroll
        for (int j = 0; j < 3; ++j) { pin(acc[0][2 * j]); __builtin_amdgcn_raw_buffer_store_b128(x_pack(j, acc[0], res), ro, j < 2 ? vout + j * 64 : vout2, 0, 0); }
        stamp(k, w, sb + 17);
      } else {
        float s = 0.f, ss = 0.f;
#pragma unroll
        for (int j = 0; j < 3; ++j) x_part(rw, vx, vx2, j, acc[0], res, s, ss);
        x_stats(rw, vs, s, ss);
        bar();
        stamp(k, w, sb + 17);
        drain_bar();
      }
      stamp(k, w, sb + 18);
    }
  }
  STAMP_OUT(k0, w0);
}

DI void stats_load(const Ctx& k, rsrc_t rw, int xb, int tf0, u32x2 (&sv)[8]) {
#pragma unroll
  for (int j = 0; j < 8; ++j) sv[j] = __builtin_amdgcn_raw_buffer_load_b64(rw, (k.lane >> 4) * 1024 + (k.lane & 15) * 8 + j * 128, OFF_ST + xb * ST_BUF + tf0 * 1024, SC1);
}
DI float one(float v) { asm("" : "+v"(v)); return v; }
DI void stats_sum(u32x2 (&sv)[8], float (&s)[2]) {
#pragma unroll
  for (int j = 0; j < 8; ++j) pin(sv[j]);
#pragma unroll
  for (int e = 0; e < 2; ++e) {
    s[e] = __uint_as_float(sv[0][e]);
#pragma unroll
    for (int j = 1; j < 8; ++j) s[e] = one(s[e] + __uint_as_float(sv[j][e]));
    s[e] *= 1.f / 640.f; pin(s[e]);
  }
}
DI void stats_table(const Ctx& k, int a, float (&s)[2], float scale) {
  pin(s[0]); pin(s[1]);
  float r[2], d[2];
#pragma unroll
  for (int e = 0; e < 2; ++e) {
    const float ss = __builtin_bit_cast(float, __builtin_amdgcn_update_dpp(0, __builtin_bit_cast(int, s[e]), 0x128, 0xf, 0xf, true));
    const float v = one(fmaxf(one(fmaf(-s[e], s[e], ss)), 0.f) + 1e-5f), q = __builtin_amdgcn_rsqf(v);
    r[e] = one(q * scale); d[e] = one(q * v);
  }
  if (!(k.lane & 8)) {
    const unsigned t = (64 * a + (k.lane & 48) + 2 * (k.lane & 7)) * 4;
    *(LAS f32x2*)(k.lds + L_MEAN + t) = (f32x2){one(-s[0]), one(-s[1])};
    *(LAS f32x2*)(k.lds + L_RSTD + t) = (f32x2){r[0], r[1]};
    *(LAS f32x2*)(k.lds + L_STD + t) = (f32x2){d[0], d[1]};
  }
}

DI void loader(const Ctx& k0, const char* x0, const char* wsp, const char* wdp, int a4) {
  Ctx k = k0;
  const bool isW = a4 < 2;
  LAS char* const L = k.lds;
  const rsrc_t rw = mk(k.ws, WS_END);
  const rsrc_t rs = mk(wsp, 2048 * 2048 * 2);
  auto fresh = [&]() { asm volatile("" : "+v"(k.lane), "+s"(k.P), "+s"(k.kq), "+s"(k.np), "+s"(k.P1), "+s"(k.ct1), "+s"(k.mat), "+s"(a4)); };
#define lane k.lane
#define a (a4 & 1)
#define me (k.P * 16 + k.kq * 4 + k.np)
#define n3 (4 * k.kq + k.np)
#define sw (((lane & 7) ^ (4 * a + (lane >> 4))) * 16)
#define vo2 ((lane >> 3) * 4096 + sw)
#define so2 ((128 * k.P + 8 * a) * 4096 + (k.kq & 1) * 2048)
#define so3 (OFF_W3 + n3 * 20 * 12288 + a * 12288)
#define so4 (OFF_W4 + (2 * k.np + (k.kq & 1)) * 48 * 5120)
  bf16x8 k1[25], k4[3][8];
  u64 tag = 0;
  int rot = ROT0 * k.np;
  auto ktile = [&](int t) { const int r = (t + rot) & 7; return 4 * (r >> 1) + 2 * (t >> 3) + (r & 1); };
  STAMP_DECL;
  auto w2 = [&](int t, int slot) {
#pragma unroll
    for (int j = 0; j < 8; ++j) dma<0, 0>(rs, L + L_WR + slot * 16384 + (2 * j + a) * 1024, vo2, so2 + j * 16 * 4096 + ktile(t) * 128);
  };
  auto w2n = [&](int t0, int t1) {
#pragma unroll 1
    for (int t = t0; t < t1; ++t) w2(t, t < 3 ? t + 3 : t - 3);
  };
  auto w3 = [&](int t, int slot) { dma_lin<SC1, 12>(rw, L + L_WR + slot * 24576 + a * 12288, lane * 16, so3 + t * 24576); };
  auto w4 = [&](int ch) { dma_lin<SC1, 15>(rw, L + L_W4 + w4slot(ch) * 30720 + a * 15360, lane * 16, so4 + ch * 30720 + a * 15360); };
  auto push = [&](int ev, int wg0, int wgstep, int n, int slot) {
    if (a4 == 2) {
      unsigned dst = lane < n ? OFF_MB + ev * MB_EV + (wg0 + lane * wgstep) * 128 + slot * 8 : 0x7fffffff;
      pin(dst); pin(tag);
      bar();
      __builtin_amdgcn_raw_buffer_store_b64(__builtin_bit_cast(u32x2, tag), rw, dst, 0, SC1);
    } else bar();
    fresh();
  };
  auto wait_mb = [&](int ev, int wg, int n) {
    GAS u64* src = (GAS u64*)(k.ws + OFF_MB + ev * MB_EV + wg * 128) + (lane & (n - 1));
#pragma unroll 1
    for (unsigned spins = 0; spins < SPIN_CAP; ++spins) {
      const u64 v = __hip_atomic_load(src, __ATOMIC_RELAXED, __HIP_MEMORY_SCOPE_AGENT);
      if (__all(v == tag)) break;
      __builtin_amdgcn_s_sleep(1);
    }
  };
  auto poll = [&](int ev, int wg, int n, bool nap) {
    if (a4 == 2) { if (nap) __builtin_amdgcn_s_sleep(NAP); wait_mb(ev, wg, n); }
    drain_bar();
  };

  auto get_tag = [&]() { const char* e = k.ws; pins(e); tag = *(const GAS u64*)e; };
  auto mix_tag = [&]() { pin(tag); tag = tag * 6364136223846793005ull + 1442695040888963407ull; };
  if (isW) __builtin_amdgcn_s_setprio(WPRIO); else __builtin_amdgcn_s_setprio(APRIO);
  {
    const rsrc_t r1 = isW ? mk(wdp, 640 * 640 * 2) : mk(x0, 2048 * 640 * 2);
    const unsigned v1 = (lane >> 3) * 1280 + sw, s1 = ((isW ? 80 * k.ct1 : 128 * k.P1) + 8 * a) * 1280;
    int slot = 2, wt = rot1(k);
    if (LAGX && (k.P & 1) && !isW) __builtin_amdgcn_s_sleep(LAGX);
#pragma unroll 1
    for (int t = 0; t < 10; ++t) {
      if (isW) {
        if (t >= 3) { waitv<10>(); bar(); }
#pragma unroll
        for (int j = 0; j < 5; ++j) dma<0, 0>(r1, L + L_D + wt * 10240 + (2 * j + a) * 1024, v1, s1 + 16 * j * 1280 + wt * 128);
      } else {
        if (t >= 2) { waitv<8>(); bar(); }
#pragma unroll
        for (int j = 0; j < 8; ++j) dma<0, 0>(r1, L + L_XR + slot * 16384 + (2 * j + a) * 1024, v1, s1 + 16 * j * 1280 + wt * 128);
      }
      slot = slot == 2 ? 0 : slot + 1; wt = wt == 9 ? 0 : wt + 1;
    }
    if (isW) {
      waitv<10>(); bar();
      waitv<5>(); bar(); w2(0, 3);
      waitv<8>(); bar(); w2(1, 4);
      bar();
      w2(2, 5);
    } else {
      waitv<8>(); bar(); waitv<0>();
      get_tag();
      bar(); bar();
      mix_tag();
    }
  }
  {
    unsigned kb[2] = {L_D + lane * 16 + ((a4 >> 1) * 10 + a) * 1024, 0}; kb[1] = kb[0] + 61440; pin(kb[0]); pin(kb[1]);
    bar();
#pragma unroll
    for (int j = 0; j < 25; ++j) k1[j] = lds16(L, kb[j / 15] + ((j / 5) % 3) * 20480 + (j % 5) * 2048);
    stamp(k, 4 + a4, 51);
  }
#pragma unroll 1
  for (int layer = 0; layer < 2; ++layer) {
    const int ev0 = layer * EV_PER_LAYER, sb = 3 + layer * 24;
    if (layer) {
      stamp(k, 4 + a4, sb + 0);
      poll(ev0 + EV_X, k.P1 * 16 + k.mat * 8 + k.ct1, 16, false);
      if (!isW) { u32x2 sv[8]; float sm[2]; stats_load(k, rw, 2, 8 * k.P1 + 4 * a, sv); stats_sum(sv, sm); stats_table(k, a, sm, k.mat ? -1.f : 1.f); }
      bar();
      if (isW) w2n(3, 5);
      bar();
      if (isW) w2n(5, 6);
    }
    push(ev0 + EV_AB, (2 * k.mat + (k.P1 >> 3)) * 4 + (k.ct1 >> 1), 16, 16, 8 * (k.P1 & 1) + (k.P1 & 6) + (k.ct1 & 1));
    GAS u64* rtf = (GAS u64*)(k.ws + OFF_MB + EV_RT * MB_EV);
    if (layer == 0 && a4 == 3 && lane == 0) __hip_atomic_store(rtf + k.b, tag, __ATOMIC_RELAXED, __HIP_MEMORY_SCOPE_AGENT);
    if (isW && layer == 0) { get_tag(); w2n(3, 6); mix_tag(); }
    stamp(k, 4 + a4, sb + 4);
    const unsigned soab = OFF_AB + layer * AB_BUF + (((k.kq >> 1) * 4 + k.np) * 64 + 32 * (k.kq & 1)) * 10240;
    unsigned l16 = lane * 16, so0 = soab + a * 10240 + ktile(0) * 20480, la0 = L_D + a * 10240;
    so0 = __builtin_amdgcn_readfirstlane(so0); la0 = __builtin_amdgcn_readfirstlane(la0);
    pin(l16); pins(so0); pins(la0);
    poll(ev0 + EV_AB, me, 8, layer == 0);
    if (!isW) dma_lin<SC1, 10>(rw, L + la0, l16, so0);
    if (isW) {
      bar();
      const unsigned sb2 = uni(so2), lb2 = uni(L_WR + a * 1024);
      unsigned v2 = vo2, so = uni(sb2 + ktile(6) * 128), la = uni(lb2 + 49152), t = 7;
      pin(v2);
#pragma unroll 1
      do {
        waitv<32>(); bar();
#pragma unroll
        for (int j = 0; j < 2; ++j) dma<0, 0>(rs, L + la + j * 2048, v2, so + j * 16 * 4096);
        sched();
        const unsigned sn = uni(sb2 + ktile(t) * 128), ln = uni(la == lb2 + 81920 ? lb2 : la + 16384);
        t = uni(t + 1);
        sched();
#pragma unroll
        for (int j = 2; j < 8; ++j) dma<0, 0>(rs, L + la + j * 2048, v2, so + j * 16 * 4096);
        so = sn; la = ln;
      } while (t != 17);
      const bool chk = layer == 0 && a == 0;
      u64 f[4];
      if (chk) {
#pragma unroll
        for (int j = 0; j < 4; ++j) f[j] = __hip_atomic_load(rtf + lane + 64 * j, __ATOMIC_RELAXED, __HIP_MEMORY_SCOPE_AGENT);
      }
      waitv<32>(); bar();
      if (chk) {
#pragma unroll
        for (int j = 0; j < 4; ++j) pin(f[j]);
#pragma unroll 1
        for (unsigned spins = 0; spins < SPIN_CAP && !__all(f[0] == tag && f[1] == tag && f[2] == tag && f[3] == tag); ++spins) {
#pragma unroll
          for (int j = 0; j < 4; ++j) f[j] = __hip_atomic_load(rtf + lane + 64 * j, __ATOMIC_RELAXED, __HIP_MEMORY_SCOPE_AGENT);
        }
      }
      waitv<0>(); bar();
      w3(3, 1); bar(); bar(); w3(0, 2); bar();
      if (W3POS == 0 || W3POS == 3 || W3POS == 5 || W3POS == 6) w3(1, 3);
      bar();
      if (W3POS == 6) __builtin_amdgcn_s_sleep(10);
      if (W3POS == 0 || W3POS == 6) w3(2, 0);
    } else {
      stamp(k, 4 + a4, sb + 3);
      const unsigned sba = uni(soab + a * 10240);
      dma_lin<SC1, 10>(rw, L + la0 + 20480, l16, sba + ktile(1) * 20480);
      unsigned so = uni(sba + ktile(2) * 20480), la = uni(la0 + 40960), t = 3;
      auto step = [&]() {
        bar();
        if (FINE == 2 && layer == 1) stamp(k, 4 + a4, 43 + t);
        dma_lin<SC1, 4>(rw, L + la, l16, so);
        sched();
        const unsigned sn = uni(sba + ktile(t) * 20480), ln = uni(la == la0 + 40960 ? la0 : la + 20480);
        t = uni(t + 1);
        sched();
        dma_lin<SC1, 6>(rw, L + la + 4096, l16, so + 4096);
        so = sn; la = ln;
      };
#pragma unroll 1
      do { waitv<10>(); step(); } while (t != 8);
      unsigned ja = L_JUNK + 256 + layer * 256 + a * 128;
      if (lane < 8) dma<SC1, 0>(rw, L + ja, l16, OFF_MB + (ev0 + EV_AB) * MB_EV + me * 128);
      ja += (lane & 15) * 8; pin(ja);
      waitv<11>(); step();
      waitv<10>();
      if (!__all(*(LAS u64*)(L + ja) == tag)) wait_mb(ev0 + EV_AB, me, 16);
      step();
#pragma unroll 1
      do { waitv<10>(); step(); } while (t != 17);
      waitv<10>(); bar(); if (FINE == 2 && layer == 1) stamp(k, 4 + a4, 60);
      waitv<0>(); bar(); if (FINE == 2 && layer == 1) stamp(k, 4 + a4, 61);
      bar();
      if (FINE == 2 && layer == 1) stamp(k, 4 + a4, 62);
    }
    auto k4load = [&](int n) {
#pragma unroll
      for (int j = 0; j < 8; ++j) k4[n][j] = ld16(rw, lane * 16 + a4 * 1024, so4 + (5 + n) * 30720 + j * 4096);
    };
    push(ev0 + EV_EX, k.P * 16 + k.np, 4, 4, k.kq);
    if (isW && (W3POS == 1 || W3POS == 4)) w3(1, 3);
    if (isW && (W3POS == 1 || W3POS == 3)) w3(2, 0);
    if (layer == 0) k4load(0);
    poll(ev0 + EV_EX, me, 4, false);
    push(ev0 + EV_X1, k.P * 16, 1, 16, n3);
    if (isW && W3POS == 2) w3(1, 3);
    if (isW && (W3POS == 2 || W3POS == 4 || W3POS == 5)) w3(2, 0);
    if (layer == 0) {
      u32x2 cv[5];
      if (isW) {
#pragma unroll
        for (int j = 0; j < 5; ++j) cv[j] = __builtin_amdgcn_raw_buffer_load_b64(rw, lane * 8, OFF_CS + (j * 3072 + a * 1536 + 96 * n3) * 4, SC1);
      }
      k4load(1);
      if (isW) {
        sched();
        f32x2 s = {0.f, 0.f};
#pragma unroll
        for (int j = 0; j < 5; ++j) s += __builtin_bit_cast(f32x2, cv[j]);
        if (lane < 48) *(LAS f32x2*)(L + L_CS3 + (96 * a + 2 * lane) * 4) = s;
      }
    }
    stamp(k, 4 + a4, sb + 11);
    l16 = lane * 16; so0 = OFF_XT + (2 * layer + 1) * XT_BUF + (8 * k.P + 4 * a) * 20480; la0 = L_D + a * 8192;
    so0 = __builtin_amdgcn_readfirstlane(so0); la0 = __builtin_amdgcn_readfirstlane(la0);
    pin(l16); pins(so0); pins(la0);
    poll(ev0 + EV_X1, me, 16, false);
    if (!isW) {
#pragma unroll
      for (int j = 0; j < 4; ++j) dma_lin<SC1, 2>(rw, L + la0 + j * 2048, l16, so0 + j * 20480);
    }
    if (isW) {
      bar();
      __builtin_amdgcn_s_sleep(3);
      dma_lin<SC1, 4>(rw, L + L_D + 49152 + a * 4096, lane * 16, OFF_ST + (2 * layer + 1) * ST_BUF + (8 * k.P + 4 * a) * 1024);
      const unsigned lb3 = uni(L_WR + a * 12288), se = uni(so3 + 10 * 24576);
      unsigned so = uni(so3 + 4 * 24576), la = uni(lb3 + 49152);
#pragma unroll 1
      do {
        waitv<24>(); bar();
        dma_lin<SC1, 4>(rw, L + la, l16, so);
        sched();
        const unsigned sn = uni(so + 24576), ln = uni(la == lb3 + 73728 ? lb3 : la + 24576);
        sched();
        dma_lin<SC1, 8>(rw, L + la + 4096, l16, so + 4096);
        so = sn; la = ln;
      } while (so != se);
      waitv<24>(); bar();
      u32x2 sv[8]; float sm[2];
      unsigned sa = L_D + 49152 + a * 4096 + (lane >> 4) * 1024 + (lane & 15) * 8; pin(sa);
#pragma unroll
      for (int j = 0; j < 8; ++j) sv[j] = *(LAS u32x2*)(L + sa + j * 128);
      stats_sum(sv, sm);
      stats_table(k, a, sm, -1.44269504089f);
      waitv<12>(); bar(); waitv<0>(); bar();
      bar();
      w4(2); w4(3); w4(4);
      stamp(k, 4 + a4, sb + 15);
    } else {
      stamp(k, 4 + a4, sb + 10);
#pragma unroll
      for (int j = 0; j < 4; ++j) dma_lin<SC1, 2>(rw, L + la0 + 16384 + j * 2048, l16, so0 + 2048 + j * 20480);
      unsigned so = uni(so0 + 4096), la = uni(la0 + 32768);
#pragma unroll 1
      do {
        waitv<8>(); bar();
        if (FINE == 3 && layer == 1) stamp(k, 4 + a4, 44 + ((so - so0) >> 11));
        dma_lin<SC1, 2>(rw, L + la, l16, so);
        sched();
        const unsigned sn = uni(so + 2048), ln = uni(la == la0 + 32768 ? la0 : la + 16384);
        sched();
#pragma unroll
        for (int j = 1; j < 4; ++j) dma_lin<SC1, 2>(rw, L + la + j * 2048, l16, so + j * 20480);
        so = sn; la = ln;
      } while (so != so0 + 20480);
      waitv<8>(); bar();
      if (FINE == 3 && layer == 1) stamp(k, 4 + a4, 54);
      w4(0);
      waitv<15>(); bar();
      if (FINE == 3 && layer == 1) stamp(k, 4 + a4, 55);
      w4(1);
      bar();
      if (FINE == 3 && layer == 1) stamp(k, 4 + a4, 56);
      waitv<0>();
    }
    bar();
    push(ev0 + EV_H, k.P * 16, 1, 16, n3);
    if (layer == 0) k4load(2);
    auto k4put = [&](int n, bool paced) {
      unsigned kb = L_W4 + w4slot(5 + n) * 30720 + a4 * 1024 + lane * 16; pin(kb);
#pragma unroll
      for (int j = 0; j < 8; ++j) if (j < 7 || isW) { *(LAS bf16x8*)(L + kb + j * 4096) = k4[n][j]; if (paced) __builtin_amdgcn_s_sleep(3); }
    };
    k4put(0, false); k4put(1, false);
    poll(ev0 + EV_H, me, 16, false);
    bar();
    k4put(2, true);
    bar();
    rot = 0;
    if (isW && layer == 0) w2n(0, 2);
    bar();
    if (layer == 0) {
      if (isW) w2n(2, 3);
      unsigned kb[2] = {L_D + lane * 16 + ((a4 >> 1) * 10 + a) * 1024, 0}; kb[1] = kb[0] + 61440; pin(kb[0]); pin(kb[1]);
      bar();
#pragma unroll
      for (int j = 0; j < 7; ++j) *(LAS bf16x8*)(L + kb[j / 15] + ((j / 5) % 3) * 20480 + (j % 5) * 2048) = k1[j];
      push(EV_PER_LAYER + EV_X, k.P * 16, 1, 16, n3);
#pragma unroll
      for (int j = 7; j < 25; ++j) *(LAS bf16x8*)(L + kb[j / 15] + ((j / 5) % 3) * 20480 + (j % 5) * 2048) = k1[j];
    }
  }
#undef lane
  STAMP_OUT(k, 4 + a4);
#define lane k.lane
  if (k.b == 0 && a4 == 2 && lane == 0) *(GAS u64*)k.ws = tag;
#undef lane
#undef a
#undef me
#undef n3
#undef sw
#undef vo2
#undef so2
#undef so3
#undef so4
}

__global__ void __launch_bounds__(512, 2) fnet_glu_kernel(const char* x0, const char* wsr, const char* wsi, const char* wdr, const char* wdi, const char* wg, const char* wu, const char* wdn, char* out, char* ws) {
  __shared__ __attribute__((aligned(16))) char smem[163840];
  Ctx k;
  k.lds = (LAS char*)smem; k.lane = threadIdx.x & 63; k.b = blockIdx.x;
  const int w = __builtin_amdgcn_readfirstlane(threadIdx.x >> 6);
  if (CODE_BYTES && w < 4) {
    const char* pc; asm volatile("s_getpc_b64 %0" : "=s"(pc));
    touch<0>(mk(pc, CODE_BYTES), k.lds + L_JUNK, threadIdx.x * 128, 0);
  }
  asm volatile("" : "+s"(x0), "+s"(wsr), "+s"(wsi), "+s"(wdr), "+s"(wdi), "+s"(wg), "+s"(wu), "+s"(wdn), "+s"(out), "+s"(ws));
  k.ws = ws;
  const int x = k.b & 7, i = k.b >> 3;
  k.kq = x >> 1; k.P = 8 * (x & 1) + 4 * (i >> 4) + 2 * ((i >> 2) & 1) + ((i >> 3) & 1); k.np = i & 3;
  k.P1 = 4 * (x >> 1) + (i >> 3); k.mat = x & 1; k.ct1 = i & 7;
  if (w < 4) consumer(k, x0, wg, wu, wdn, out, w);
  else loader(k, x0, (x >> 2) ? wsi : wsr, (x & 1) ? wdi : wdr, w - 4);
}
}

extern "C" void kernel_launch(void* const* d_in, const int* in_sizes, int n_in, void* d_out, int out_size, void* d_ws, size_t ws_size, hipStream_t stream) {
  fnet_glu_kernel<<<256, 512, 0, stream>>>((const char*)d_in[0], (const char*)d_in[1], (const char*)d_in[2], (const char*)d_in[3], (const char*)d_in[4],
                                           (const char*)d_in[6], (const char*)d_in[7], (const char*)d_in[8], (char*)d_out, (char*)d_ws);
}
```

```cpp
#include <hip/hip_runtime.h>

typedef unsigned long long u64;
typedef short bf16x8 __attribute__((ext_vector_type(8)));
typedef short s16x4 __attribute__((ext_vector_type(4)));
typedef float f32x4 __attribute__((ext_vector_type(4)));
typedef float f32x2 __attribute__((ext_vector_type(2)));
typedef unsigned u32x4 __attribute__((ext_vector_type(4)));
typedef unsigned u32x2 __attribute__((ext_vector_type(2)));
typedef __bf16 bf2 __attribute__((ext_vector_type(2)));
#define LAS __attribute__((address_space(3)))
#define GAS __attribute__((address_space(1)))
#define DI __device__ __forceinline__
typedef __amdgpu_buffer_rsrc_t rsrc_t;

namespace {
constexpr int AHEAD = 10;
constexpr int LAG = 12;
constexpr unsigned CODE_BYTES = 34688;
constexpr unsigned OFF_MB = 4096, MB_EV = 256 * 128;
constexpr int EV_X = 0, EV_AB = 1, EV_EX = 2, EV_X1 = 3, EV_H = 4, EV_PER_LAYER = 5, EV_RT = 0;
constexpr unsigned OFF_CS = 0x60000;
constexpr unsigned OFF_ST = 0x80000, ST_BUF = 8 * 2048 * 8;
constexpr unsigned OFF_XT = 0x100000, XT_BUF = 2048 * 640 * 2;
constexpr unsigned OFF_AB = OFF_XT + 3 * XT_BUF, AB_BUF = 2048 * 1280 * 2;
constexpr unsigned OFF_HT = OFF_AB + 2 * AB_BUF, HT_BUF = 2048 * 1536 * 2;
constexpr unsigned OFF_EX = OFF_HT + 2 * HT_BUF, EX_BUF = 256 * 12 * 2560;
constexpr unsigned OFF_W3 = OFF_EX + 2 * EX_BUF;
constexpr unsigned OFF_W4 = OFF_W3 + 16 * 20 * 12288;
constexpr unsigned WS_END = OFF_W4 + 8 * 48 * 5120;
static_assert(OFF_MB + 10 * MB_EV <= OFF_CS && OFF_CS + 5 * 3072 * 4 <= OFF_ST && OFF_ST + 3 * ST_BUF <= OFF_XT && WS_END <= 0x4000000, "ws map");
constexpr unsigned L_MEAN = 0, L_RSTD = 512, L_CS1 = 1024, L_STD = 1536, L_CS3 = 2304, L_JUNK = 3072;
constexpr unsigned L_D = 4096;
constexpr unsigned L_WR = L_D + 61440, L_W4 = L_D + 6144, L_CP = L_D + 102400, L_XR = L_D + 110592, L_RED = L_W4 + 30720;
constexpr int w4slot(int ch) { return ch == 4 ? 4 : ((ch < 5 ? ch : ch - 5) + 2) & 3; }
constexpr int w4off(int s) { return w4slot(s / 3) * 30720 + (s % 3) * 10240; }
constexpr int SC1 = 16;
constexpr unsigned SPIN_CAP = 1u << 21;

DI rsrc_t mk(const void* p, unsigned bytes) { return __builtin_amdgcn_make_buffer_rsrc((void*)p, 0, bytes, 0x00020000); }
template <int N> DI void waitv() { asm volatile("s_waitcnt vmcnt(%0)" ::"n"(N) : "memory"); }
DI void bar() { asm volatile("s_waitcnt lgkmcnt(0)\n\ts_barrier" ::: "memory"); }
DI void lgk0() { __builtin_amdgcn_sched_barrier(0); __builtin_amdgcn_s_waitcnt(0xC07F); __builtin_amdgcn_sched_barrier(0); }
DI void barv() { lgk0(); asm volatile("s_barrier" ::: "memory"); __builtin_amdgcn_sched_barrier(0); }
DI void drain_bar() {
  asm volatile("s_waitcnt vmcnt(0) lgkmcnt(0)\n\ts_barrier" ::: "memory");
  __builtin_amdgcn_s_waitcnt(0);
  asm volatile("" ::: "memory");
}
DI void sched() { __builtin_amdgcn_sched_barrier(0); }
template <class T> DI void pin(T& x) { asm volatile("" : "+v"(x)); }
template <class T> DI void pins(T& x) { asm volatile("" : "+s"(x)); }
template <int AUX, int IMM> DI void dma(rsrc_t r, LAS char* l, unsigned vo, unsigned so) { __builtin_amdgcn_raw_ptr_buffer_load_lds(r, (LAS void*)l, 16, vo, so, IMM, AUX); }
template <int AUX> DI void touch(rsrc_t r, LAS char* l, unsigned vo, unsigned so) { __builtin_amdgcn_raw_ptr_buffer_load_lds(r, (LAS void*)l, 4, vo, so, 0, AUX); }
template <int AUX, int N> DI void dma_lin(rsrc_t r, LAS char* l, unsigned vo, unsigned so) {
#pragma unroll
  for (int j = 0; j < N; j += 4) {
    dma<AUX, 0>(r, l + j * 1024, vo, so + j * 1024);
    if (j + 1 < N) dma<AUX, 1024>(r, l + j * 1024, vo, so + j * 1024);
    if (j + 2 < N) dma<AUX, 2048>(r, l + j * 1024, vo, so + j * 1024);
    if (j + 3 < N) dma<AUX, 3072>(r, l + j * 1024, vo, so + j * 1024);
  }
}
DI unsigned uni(unsigned x) { x = __builtin_amdgcn_readfirstlane(x); asm volatile("" : "+s"(x)); return x; }
DI bf16x8 ld16(rsrc_t r, unsigned vo, unsigned so) { return __builtin_bit_cast(bf16x8, __builtin_amdgcn_raw_buffer_load_b128(r, vo, so, SC1)); }
DI void st16(rsrc_t r, u32x4 v, unsigned vo) { __builtin_amdgcn_raw_buffer_store_b128(v, r, vo, 0, SC1); }
DI bf16x8 lds16(LAS char* l, unsigned a) { return *(LAS bf16x8*)(l + a); }
DI unsigned pk(float lo, float hi) { f32x2 v = {lo, hi}; return __builtin_bit_cast(unsigned, __builtin_convertvector(v, bf2)); }
DI float blo(unsigned u) { return __uint_as_float(u << 16); }
DI float bhi(unsigned u) { return __uint_as_float(u & 0xffff0000u); }
DI f32x4 unpk(unsigned a, unsigned b) { return (f32x4){blo(a), bhi(a), blo(b), bhi(b)}; }
DI f32x4 mfma(bf16x8 a, bf16x8 b, f32x4 c) { return __builtin_amdgcn_mfma_f32_16x16x32_bf16(a, b, c, 0, 0, 0); }
DI void mma(f32x4& c, const bf16x8& a, const bf16x8& b) { asm volatile("v_mfma_f32_16x16x32_bf16 %0, %1, %2, %0" : "+v"(c) : "v"(a), "v"(b)); __builtin_amdgcn_sched_barrier(0); }
DI void mma0(f32x4& c, const bf16x8& a, const bf16x8& b) { asm volatile("v_mfma_f32_16x16x32_bf16 %0, %1, %2, 0" : "=&v"(c) : "v"(a), "v"(b)); __builtin_amdgcn_sched_barrier(0); }
DI void settle() { asm volatile("s_nop 7"); __builtin_amdgcn_sched_barrier(0); }
DI float dot2(unsigned a, unsigned b, float s) { return __builtin_amdgcn_fdot2_f32_bf16(__builtin_bit_cast(bf2, a), __builtin_bit_cast(bf2, b), s, false); }
DI float one(float v) { asm("" : "+v"(v)); return v; }
DI u32x4 swap_pair(unsigned a0, unsigned a1, unsigned b0, unsigned b1) {
  const auto r0 = __builtin_amdgcn_permlane16_swap(a0, b0, false, false);
  const auto r1 = __builtin_amdgcn_permlane16_swap(a1, b1, false, false);
  return (u32x4){r0[0], r1[0], r0[1], r1[1]};
}

struct Ctx {
  char* ws; LAS char* lds; int lane, b;
  int kq, P, np;
  int P1, mat, ct1;
};

DI u32x4 x_pack(int j, const f32x4 (&v)[5], unsigned (&res)[10]) {
  const int i1 = j < 2 ? 2 * j + 1 : 4;
#pragma unroll
  for (int i = 2 * j; i <= i1; ++i) { res[2 * i] = pk(v[i][0], v[i][1]); res[2 * i + 1] = pk(v[i][2], v[i][3]); }
  return swap_pair(res[4 * j], res[4 * j + 1], res[2 * i1], res[2 * i1 + 1]);
}
DI void x_part(rsrc_t rw, unsigned vx, unsigned vx2, int j, const f32x4 (&v)[5], unsigned (&res)[10], float& s, float& ss) {
  const u32x4 o = x_pack(j, v, res);
  st16(rw, o, j < 2 ? vx + j * 1024 : vx2);
#pragma unroll
  for (int e = 4 * j; e < (j < 2 ? 4 * j + 4 : 10); ++e) { s = dot2(res[e], 0x3f803f80u, s); ss = dot2(res[e], res[e], ss); }
  pin(s); pin(ss);
}
DI void x_stats(rsrc_t rw, unsigned vs, float s, float ss) {
  const auto r = __builtin_amdgcn_permlane16_swap(__float_as_uint(s), __float_as_uint(ss), false, false);
  const unsigned t = __float_as_uint(__uint_as_float(r[0]) + __uint_as_float(r[1]));
  const auto r2 = __builtin_amdgcn_permlane32_swap(t, t, false, false);
  const float u = __uint_as_float(r2[0]) + __uint_as_float(r2[1]);
  __builtin_amdgcn_raw_buffer_store_b32(__float_as_uint(u), rw, vs, 0, SC1);
}

DI int rot1(const Ctx& k) { const int r = ((k.ct1 * 5) >> 2) + (((k.P1 & 3) * 5) >> 1); return r >= 10 ? r - 10 : r; }

#define PHASE_LANE() Ctx k = k0; int w = w0; asm volatile("" : "+v"(k.lane), "+s"(w), "+s"(k.P), "+s"(k.kq), "+s"(k.np), "+s"(k.P1), "+s"(k.ct1), "+s"(k.mat)); \
  const int tfo = 8 * k.P + 4 * (k.kq >> 1) + w, cto = 2 * k.np + (k.kq & 1); (void)tfo; (void)cto; \
  const int lane = k.lane, q = lane >> 4, c = lane & 15, qs = 2 * (q & 1) + (q >> 1); (void)qs; \
  const unsigned rowrd = (c >> 3) * 1024 + (c & 7) * 128 + ((q ^ (c >> 1)) * 16); (void)rowrd
#define X_ADDR(xb) unsigned vx = OFF_XT + (xb) * XT_BUF + tfo * 20480 + (10 * cto + qs) * 256 + c * 16, vx2 = vx + 2048 + ((lane & 16) << 26), \
  vs = OFF_ST + (xb) * ST_BUF + tfo * 1024 + cto * 128 + lane * 4 + ((lane & 32) << 25); pin(vx); pin(vx2); pin(vs)

DI void consumer(const Ctx& k0, const char* x0, const char* wg, const char* wu, const char* wdn, char* out, const int w0) {
  LAS char* const L = k0.lds;
  const rsrc_t rw = mk(k0.ws, WS_END);
  unsigned res[10];
  __builtin_amdgcn_s_setprio(2);
  auto retile = [&](const Ctx& k0, const int w) {
    int e = ((k0.b >> 7) * 64 + (k0.b & 63)) * 4 + w;
    pins(e);
    const int lane = k0.lane;
    const int g3 = ((e >> 2) * 2731) >> 16, ks3 = 4 * g3 + w, st3 = (e >> 2) - g3 * 24, ks4 = (e * 6554) >> 16, st4 = e - ks4 * 10;
    LAS char* t[3];
#pragma unroll
    for (int m = 0; m < 3; ++m) { const int u = rot1(k0) + (m < 2 ? 4 + w : 8 + (w >> 1)); t[m] = L + L_D + (u >= 10 ? u - 10 : u) * 10240 + (m < 2 ? m : w & 1) * 4096; }
    __builtin_amdgcn_s_sleep(LAG);
#pragma unroll
    for (int m = 0; m < 3; ++m) {
      const unsigned stride = m < 2 ? 3072 : 1280;
      const rsrc_t rr = mk(m == 0 ? wg : (m == 1 ? wu : wdn), e < 480 ? 640 * 1536 * 2 : 0);
#pragma unroll
      for (int j = 0; j < 4; ++j) dma<0, 0>(rr, t[m] + j * 1024, (lane >> 3) * stride + (lane & 7) * 16, (32 * (m < 2 ? ks3 : ks4) + 8 * j) * stride + 128 * (m < 2 ? st3 : st4));
    }
    const rsrc_t ro = mk(k0.ws, e < 480 ? WS_END : 0);
    const bf16x8 ones = {0x3f80, 0x3f80, 0x3f80, 0x3f80, 0x3f80, 0x3f80, 0x3f80, 0x3f80};
    const unsigned tr = (lane >> 4) * 1024 + ((lane >> 2) & 3) * 128 + (lane & 3) * 8;
    __builtin_amdgcn_s_waitcnt(0);
    asm volatile("" ::: "memory");
    u32x4 f[3][4];
#pragma unroll
    for (int m = 0; m < 3; ++m)
#pragma unroll
      for (int n = 0; n < 4; ++n) {
        const u32x2 lo = __builtin_bit_cast(u32x2, __builtin_amdgcn_ds_read_tr16_b64_v4i16((LAS s16x4*)(t[m] + tr + 32 * n)));
        const u32x2 hi = __builtin_bit_cast(u32x2, __builtin_amdgcn_ds_read_tr16_b64_v4i16((LAS s16x4*)(t[m] + tr + 32 * n + 512)));
        f[m][n] = (u32x4){lo[0], lo[1], hi[0], hi[1]};
      }
#pragma unroll
    for (int m = 0; m < 3; ++m) {
      f32x4 cs[4];
#pragma unroll
      for (int n = 0; n < 4; ++n) {
        const int nf = 4 * (m < 2 ? st3 : st4) + n;
        st16(ro, f[m][n], lane * 16 + (m < 2 ? OFF_W3 + m * 1024 + ks3 * 12288 + ((nf * 171) >> 10) * 19 * 12288 + nf * 2048 : OFF_W4 + ks4 * 5120 + ((nf * 205) >> 10) * 47 * 5120 + nf * 1024));
        if (m < 2) cs[n] = mfma(ones, __builtin_bit_cast(bf16x8, f[m][n]), (f32x4){0.f, 0.f, 0.f, 0.f});
      }
      if (m < 2) *(LAS float*)(L + L_CP + (m * 64 + lane) * 16 + w * 4) = (lane & 32) ? ((lane & 16) ? cs[3][0] : cs[2][0]) : ((lane & 16) ? cs[1][0] : cs[0][0]);
    }
  };
#pragma unroll 1
  for (int layer = 0; layer < 2; ++layer) {
    {
      PHASE_LANE();
      unsigned vo = OFF_AB + layer * AB_BUF + ((k.mat * 4 + (k.ct1 >> 1)) * 64 + 4 * k.P1 + w) * 10240 + 5 * (k.ct1 & 1) * 1024 + (qs * 16 + c) * 16;
      unsigned ta = (32 * w + 4 * q) * 4, tc = c * 4;
      pin(vo); pin(ta); pin(tc);
      f32x4 acc[2][5], mean[2], rstd[2]; float cs1[5];
      bf16x8 wf[4][5];
      auto tables = [&]() {
#pragma unroll
        for (int i = 0; i < 5; ++i) cs1[i] = *(LAS float*)(L + L_CS1 + tc + i * 64);
#pragma unroll
        for (int t = 0; t < 2; ++t) { mean[t] = *(LAS f32x4*)(L + ta + L_MEAN + 64 * t); rstd[t] = *(LAS f32x4*)(L + ta + L_RSTD + 64 * t); }
      };
#pragma unroll
      for (int i = 0; i < 5; ++i) { acc[0][i] = (f32x4){0.f, 0.f, 0.f, 0.f}; acc[1][i] = (f32x4){0.f, 0.f, 0.f, 0.f}; }
      if (layer == 0) {
        bf16x8 xf[4][2], cf[4], ones = {0x3f80, 0x3f80, 0x3f80, 0x3f80, 0x3f80, 0x3f80, 0x3f80, 0x3f80};
        f32x4 as[2], aq[2], ac[2];
        unsigned wt = rot1(k) * 10240, wa0 = L_D + rowrd;
        pin(wa0);
        unsigned wa = wa0 + wt, xr = L_XR + w * 4096 + rowrd, x_slot = 32768;
#pragma unroll
        for (int i = 0; i < 5; ++i) { pin(acc[0][i]); pin(acc[1][i]); }
#pragma unroll
        for (int t = 0; t < 2; ++t) { as[t] = (f32x4){0.f, 0.f, 0.f, 0.f}; aq[t] = as[t]; ac[t] = as[t]; pin(as[t]); pin(aq[t]); pin(ac[t]); }
        pin(wa); pin(xr); pin(ones);
        if (k.P & 1) retile(k, w);
        bar();
        unsigned xn[2] = {xr + 32768, (xr + 32768) ^ 64}, wn[2] = {wa, wa ^ 64};
        const unsigned wo = w * 2048;
        auto rd = [&](int p, int n) {
          const int e = n & 1, m = n >> 1;
          if (m < 2) xf[2 * p + e][m] = lds16(L, xn[e] + m * 2048);
          else if (m < 7) wf[2 * p + e][m - 2] = lds16(L, wn[e] + (m - 2) * 2048);
          else cf[2 * p + e] = lds16(L, wn[e] + wo);
        };
#pragma unroll
        for (int n = 0; n < 16; ++n) rd(0, n);
        lgk0();
        int left = 5;
#pragma unroll 1
        do {
#pragma unroll
          for (int p = 0; p < 2; ++p) {
#pragma unroll
            for (int n = 0; n < 32; ++n) {
              const int m = n >> 1, h = 2 * p + ((n ^ m) & 1);
              if (n == 10) barv();
              if (m < 10) { const int t = m / 5, i = t ? 9 - m : m; mma(acc[t][i], xf[h][t], wf[h][i]); }
              else if (m == 10) mma(aq[1], xf[h][1], xf[h][1]);
              else if (m == 11) mma(as[1], xf[h][1], ones);
              else if (m == 12) mma(as[0], xf[h][0], ones);
              else if (m == 13) mma(aq[0], xf[h][0], xf[h][0]);
              else if (m == 14) mma(ac[0], ones, cf[h]);
              else mma(ac[1], ones, wf[h][4]);
              if (n == 0) { x_slot = x_slot == 32768 ? 0 : x_slot + 16384; pins(x_slot); }
              if (n == 1) { wt = wt == 92160 ? 0 : wt + 10240; pins(wt); }
              if (n == 2) { xn[0] = xr + x_slot; wn[0] = wa0 + wt; pin(xn[0]); pin(wn[0]); }
              if (n == 4) { xn[1] = xn[0] ^ 64; wn[1] = wn[0] ^ 64; pin(xn[1]); pin(wn[1]); }
              if (n == 6 && p) { --left; pins(left); }
              if (n >= 10 && n < 26) rd(p ^ 1, n - 10);
              sched();
            }
            lgk0();
          }
        } while (left);
        settle();
        if (q == 0) { *(LAS float*)(L + L_CS1 + (16 * w + c) * 4) = ac[0][0]; *(LAS float*)(L + L_CS1 + (64 + c) * 4) = ac[1][0]; }
#pragma unroll
        for (int t = 0; t < 2; ++t) {
          const float s = ((c & 2) ? ((c & 1) ? as[t][3] : as[t][2]) : ((c & 1) ? as[t][1] : as[t][0])) * (1.f / 640.f);
          const float ss = ((c & 2) ? ((c & 1) ? aq[t][3] : aq[t][2]) : ((c & 1) ? aq[t][1] : aq[t][0])) * (1.f / 640.f);
          const float r = __builtin_amdgcn_rsqf(fmaxf(ss - s * s, 0.f) + 1e-5f);
          if ((c >> 2) == q) { *(LAS float*)(L + L_MEAN + (32 * w + 16 * t + c) * 4) = -s; *(LAS float*)(L + L_RSTD + (32 * w + 16 * t + c) * 4) = k.mat ? -r : r; }
        }
        bar();
        tables();
      } else {
#define KS(s) (((s) + 12) % 20)
        unsigned wb[2][2] = {{L_D + rowrd, L_D + rowrd + 51200}, {L_D + (rowrd ^ 64), L_D + (rowrd ^ 64) + 51200}};
        unsigned xv[2] = {OFF_XT + XT_BUF + (8 * k.P1 + 2 * w) * 20480 + lane * 16, 0}; xv[1] = xv[0] + 20480;
        pin(wb[0][0]); pin(wb[0][1]); pin(wb[1][0]); pin(wb[1][1]); pin(xv[0]); pin(xv[1]);
        constexpr int R = AHEAD + 2;
        bf16x8 xa[R];
        auto req = [&](int l) { const int ks = KS(2 * (l >> 2) + (l & 1)); if (l < 40) xa[l % R] = ld16(rw, xv[(l >> 1) & 1] + (ks & 3) * 1024, (ks >> 2) * 4096); };
        auto rd = [&](int d, int n) { const int s = 2 * d + (n & 1); wf[s & 3][n >> 1] = lds16(L, wb[s & 1][KS(s) / 10] + ((KS(s) >> 1) % 5) * 10240 + (n >> 1) * 2048); };
        bar();
#pragma unroll
        for (int l = 0; l < AHEAD; ++l) { req(l); sched(); }
#pragma unroll
        for (int n = 0; n < 10; ++n) rd(0, n);
        lgk0();
#pragma unroll
        for (int d = 0; d < 10; ++d) {
          if (d == 8) barv();
          if (d == 9) { tables(); sched(); }
#pragma unroll
          for (int n = 0; n < 20; ++n) {
            const int m = n >> 1, t = m / 5, i = t ? 9 - m : m, e = (n ^ m) & 1;
            if (d == 0 && !(n & 1)) mma0(acc[t][i], xa[(2 * t + e) % R], wf[e][i]); else mma(acc[t][i], xa[(4 * d + 2 * t + e) % R], wf[(2 * d + e) & 3][i]);
            if (n % 5 == 4) req(4 * d + n / 5 + AHEAD);
            else if (d < 9 && n < 13) { const int j = n - n / 5; if (j < 10) rd(d + 1, j); }
            if (n == 18 && d < 9) lgk0();
            sched();
          }
        }
        settle();
      }
#pragma unroll
      for (int i = 0; i < 5; ++i) {
        const float cs = cs1[i];
        const f32x4 a = (mean[0] * cs + acc[0][i]) * rstd[0], bb = (mean[1] * cs + acc[1][i]) * rstd[1];
        st16(rw, swap_pair(pk(a[0], a[1]), pk(a[2], a[3]), pk(bb[0], bb[1]), pk(bb[2], bb[3])), vo + i * 1024);
      }
      if (layer == 0 && (k.P & 1) && w < 2) {
        const int j = (k.b >> 7) * 64 + (k.b & 63), g3 = (j * 2731) >> 16;
        const f32x4 t = *(LAS f32x4*)(L + L_CP + (w * 64 + lane) * 16);
        if (j < 120) __builtin_amdgcn_raw_buffer_store_b32(__float_as_uint((t[0] + t[1]) + (t[2] + t[3])), rw, lane * 4, OFF_CS + (g3 * 3072 + w * 1536 + 64 * (j - g3 * 24)) * 4, SC1);
      }
      drain_bar();
    }
    {
      PHASE_LANE();
      const int wm = w >> 1, wn = w & 1;
      f32x4 acc[4][5];
      bf16x8 fa[2][4], fl[2][2], fb[2][2][4];
#pragma unroll
      for (int t = 0; t < 4; ++t)
#pragma unroll
        for (int i = 0; i < 5; ++i) { acc[t][i] = (f32x4){0.f, 0.f, 0.f, 0.f}; pin(acc[t][i]); }
      unsigned wb = L_WR + wm * 8192 + rowrd, ab = L_D + wn * 5120 + lane * 16;
      unsigned ws_slot = 65536, ab_slot = 20480;
      unsigned ev[2], ev8[2], en = w == k.kq ? 0 : WS_END;
      ev[0] = OFF_EX + layer * EX_BUF + ((k.P * 16 + w * 4 + k.np) * 3 + ((k.kq - w) & 3) - 1) * 10240 + lane * 16; ev[1] = ev[0] + 5120;
      ev8[0] = ev[0] - lane * 8 + 2048; ev8[1] = ev8[0] + 5120;
      unsigned lo = L_D + 20480 + w * 10240 + lane * 16, lo8 = lo - lane * 8 + 2048;
      pin(lo); pin(lo8);
      pin(wb); pin(ab); pin(ev[0]); pin(ev[1]); pin(ev8[0]); pin(ev8[1]); pins(en);
      const rsrc_t re = mk(k.ws, en);
      bar();
#pragma unroll
      for (int n = 0; n < 8; ++n) fb[0][n & 1][n >> 1] = lds16(L, (wb ^ ((n & 1) * 64)) + 49152 + (n >> 1) * 2048);
      unsigned an = ab + 20480, w0 = wb + 65536, w1 = w0 ^ 64;
      pin(an); pin(w0); pin(w1);
      bar();
#pragma unroll
      for (int n = 0; n < 10; ++n) { if (n < 8) fa[n & 1][n >> 1] = lds16(L, ab + (n & 1) * 10240 + (n >> 1) * 1024); else fl[0][n & 1] = lds16(L, ab + (n & 1) * 10240 + 4096); }
      lgk0();
      int left = 7;
      auto tile = [&](int p, bool last) {
#pragma unroll
        for (int n = 0; n < 40; ++n) {
          const int m = n >> 1, i = m >> 2, t = (i & 1) ? 3 - (m & 3) : m & 3, kk = (n ^ m) & 1, e = n & 1;
          if (n == 16) barv();
          mma(acc[t][i], i < 4 ? fa[kk][i & 3] : fl[p][kk], fb[p][kk][t]);
          if (n < 8) fb[p ^ 1][e][m] = lds16(L, (e ? w1 : w0) + m * 2048);
          else if (n >= 16 && n < 20) fa[e][m - 8] = lds16(L, an + e * 10240 + (m - 8) * 1024);
          else if (m == 10) fl[p ^ 1][e] = lds16(L, an + e * 10240 + 4096);
          else if (m == 12 || m == 16) fa[e][m / 4 - 1] = lds16(L, an + e * 10240 + (m / 4 - 1) * 1024);
          else if (!last) {
            if (n == 8) { ws_slot = ws_slot == 81920 ? 0 : ws_slot + 16384; pins(ws_slot); }
            if (n == 9) { ab_slot = ab_slot == 40960 ? 0 : ab_slot + 20480; pins(ab_slot); }
            if (n == 10) { w0 = wb + ws_slot; pin(w0); }
            if (n == 11) { w1 = w0 ^ 64; pin(w1); }
            if (n == 34) { an = ab + ab_slot; pin(an); if (p) { --left; pins(left); } }
          }
          sched();
        }
        lgk0();
      };
#pragma unroll 1
      do { tile(0, false); tile(1, false); } while (left);
      tile(0, true);
      {
        unsigned o[10];
        auto send = [&](int t, int j) {
          if (j < 5) { o[2 * j] = pk(acc[t][j][0], acc[t][j][1]); o[2 * j + 1] = pk(acc[t][j][2], acc[t][j][3]); }
          if (j == 5) st16(re, (u32x4){o[0], o[1], o[2], o[3]}, ev[t >> 1] + (t & 1) * 2560);
          if (j == 6) { *(LAS u32x4*)(L + lo + t * 2560) = (u32x4){o[0], o[1], o[2], o[3]}; *(LAS u32x2*)(L + lo8 + t * 2560) = (u32x2){o[8], o[9]}; }
          if (j == 7) st16(re, (u32x4){o[4], o[5], o[6], o[7]}, ev[t >> 1] + (t & 1) * 2560 + 1024);
          if (j == 8) { __builtin_amdgcn_raw_buffer_store_b64((u32x2){o[8], o[9]}, re, ev8[t >> 1] + (t & 1) * 2560, 0, SC1); *(LAS u32x4*)(L + lo + t * 2560 + 1024) = (u32x4){o[4], o[5], o[6], o[7]}; }
        };
#pragma unroll
        for (int n = 0; n < 40; ++n) {
          const int t = n / 10, i = (n % 10) >> 1, kk = (n ^ i) & 1;
          mma(acc[t][i], i < 4 ? fa[kk][i & 3] : fl[1][kk], fb[1][kk][t]);
          if (t) send(t - 1, n % 10);
          sched();
        }
#pragma unroll
        for (int j = 0; j < 9; ++j) { send(3, j); sched(); }
      }
      drain_bar();
      if (layer == 0) {
        const rsrc_t rx = mk(x0, 2048 * 640 * 2);
#pragma unroll
        for (int j = 0; j < 3; ++j) {
          const int u = 64 * j + lane, r = (u * 205) >> 11;
          dma<0, 0>(rx, L + L_D + w * 3072 + j * 1024, r * 1120 + u * 16 + (j == 2 ? (lane & 32) << 25 : 0), 16 * tfo * 1280 + cto * 160);
        }
        __builtin_amdgcn_s_waitcnt(0);
        asm volatile("" ::: "memory");
#pragma unroll
        for (int i = 0; i < 5; ++i) { const u32x2 t = *(LAS u32x2*)(L + L_D + w * 3072 + c * 160 + 8 * q + 32 * i); res[2 * i] = t[0]; res[2 * i + 1] = t[1]; }
      }
      f32x4 v[5];
      {
        LAS char* t = L + L_D + 20480 + k.kq * 10240 + w * 2560;
        const u32x4 a = *(LAS u32x4*)(t + lane * 16), bb = *(LAS u32x4*)(t + 1024 + lane * 16); const u32x2 d = *(LAS u32x2*)(t + 2048 + lane * 8);
        v[0] = unpk(a[0], a[1]); v[1] = unpk(a[2], a[3]); v[2] = unpk(bb[0], bb[1]); v[3] = unpk(bb[2], bb[3]); v[4] = unpk(d[0], d[1]);
#pragma unroll
        for (int i = 0; i < 5; ++i) { v[i] += unpk(res[2 * i], res[2 * i + 1]); pin(v[i]); }
      }
      unsigned rv = OFF_EX + layer * EX_BUF + (k.P * 16 + k.kq * 4 + k.np) * 30720 + w * 2560 + lane * 16, rv8 = rv - lane * 8;
      pin(rv); pin(rv8);
      X_ADDR(2 * layer);
      bar();
      u32x4 e[3][2]; u32x2 e8[3];
#pragma unroll
      for (int j = 0; j < 3; ++j)
#pragma unroll
        for (int n = 0; n < 3; ++n) {
          if (j < 2) e[n][j] = __builtin_amdgcn_raw_buffer_load_b128(rw, rv + j * 1024, n * 10240, SC1);
          else e8[n] = __builtin_amdgcn_raw_buffer_load_b64(rw, rv8 + 2048, n * 10240, SC1);
          sched();
        }
      float s = 0.f, ss = 0.f;
#pragma unroll
      for (int j = 0; j < 3; ++j) {
#pragma unroll
        for (int n = 0; n < 3; ++n) {
          if (j < 2) { v[2 * j] += unpk(e[n][j][0], e[n][j][1]); v[2 * j + 1] += unpk(e[n][j][2], e[n][j][3]); }
          else v[4] += unpk(e8[n][0], e8[n][1]);
        }
        x_part(rw, vx, vx2, j, v, res, s, ss);
        sched();
      }
      x_stats(rw, vs, s, ss);
      drain_bar();
    }
    {
      PHASE_LANE();
      const int wm = w >> 1, wn = w & 1;
      f32x4 acc[4][6];
      bf16x8 fa[2][5], fl[2][2], fb[2][2][4];
#pragma unroll
      for (int t = 0; t < 4; ++t)
#pragma unroll
        for (int i = 0; i < 6; ++i) { acc[t][i] = (f32x4){0.f, 0.f, 0.f, 0.f}; pin(acc[t][i]); }
      unsigned wb = L_WR + wn * 6144 + lane * 16, xb = L_D + wm * 8192 + lane * 16;
      unsigned w_slot = 73728, x_slot = 16384;
      unsigned vo = OFF_HT + layer * HT_BUF + ((8 * k.P + 4 * wm + (q & 1)) * 48 + 3 * (4 * k.kq + k.np)) * 1024 + (6 * wn + (q >> 1)) * 256 + c * 16, vo2 = vo + 98304;
      unsigned ta = (64 * wm + c) * 4, tc = L_CS3 + (48 * wn + 4 * q) * 4;
      unsigned sa = L_D + 49152 + (2 * w + (lane >> 5)) * 1024 + (lane & 31) * 4, sw = (32 * w + 16 * (lane >> 5) + c) * 4;
      pin(wb); pin(xb); pin(vo); pin(vo2); pin(ta); pin(tc); pin(sa); pin(sw);
      bar();
#pragma unroll
      for (int n = 0; n < 12; ++n) { if (n < 10) fa[n & 1][n >> 1] = lds16(L, wb + 49152 + (n & 1) * 12288 + (n >> 1) * 1024); else fl[0][n & 1] = lds16(L, wb + 49152 + (n & 1) * 12288 + 5120); }
      unsigned wn3 = wb + 73728, xn = xb + 16384;
      pin(wn3); pin(xn);
      bar();
#pragma unroll
      for (int n = 0; n < 8; ++n) fb[0][n & 1][n >> 1] = lds16(L, xb + n * 1024);
      lgk0();
      int left = 4;
      auto mm = [&](int p, int n) { const int m = n >> 1, i = m >> 2, t = (i & 1) ? 3 - (m & 3) : m & 3, kk = (n ^ m) & 1; mma(acc[t][i], i < 5 ? fa[kk][i < 5 ? i : 0] : fl[p][kk], fb[p][kk][t]); };
      auto rd = [&](int p, int n) {
        const int m = n >> 1, e = n & 1;
        if (m == 8 || m == 9) fa[e][m - 8] = lds16(L, wn3 + e * 12288 + (m - 8) * 1024);
        else if (m == 10) fl[p ^ 1][e] = lds16(L, wn3 + e * 12288 + 5120);
        else if (m == 12 || m == 16 || m == 20) fa[e][m / 4 - 1] = lds16(L, wn3 + e * 12288 + (m / 4 - 1) * 1024);
        else if (m == 11) fb[p ^ 1][e][0] = lds16(L, xn + e * 1024);
        else if (m >= 13 && m <= 15) fb[p ^ 1][e][m - 12] = lds16(L, xn + e * 1024 + (m - 12) * 2048);
      };
#pragma unroll 1
      do {
#pragma unroll
        for (int p = 0; p < 2; ++p) {
#pragma unroll
          for (int n = 0; n < 48; ++n) {
            if (n == 16) barv();
            mm(p, n); rd(p, n);
            if (n == 34) { x_slot = x_slot == 32768 ? 0 : x_slot + 16384; pins(x_slot); }
            if (n == 35) { w_slot = w_slot == 73728 ? 0 : w_slot + 24576; pins(w_slot); }
            if (n == 42) { wn3 = wb + w_slot; xn = xb + x_slot; pin(wn3); pin(xn); }
            if (n == 43 && p) { --left; pins(left); }
            sched();
          }
          lgk0();
        }
      } while (left);
      float mean[4], rl2[4], sd[4];
      f32x4 cs[6];
      unsigned h[4][2];
      f32x4 o[4];
      auto corr1 = [&](int f, int n) { const int t = n >> 3, i = 2 * f + ((n >> 2) & 1), e = n & 3; if (n < 32) acc[t][i][e] = one(fmaf(cs[i][e], mean[t], acc[t][i][e])); };
      auto glu1 = [&](int n) {
        const int t = n / 15, j = n % 15;
        if (j < 2) { o[t][2 * j] = one(acc[t][0][2 * j] * rl2[t]); o[t][2 * j + 1] = one(acc[t][0][2 * j + 1] * rl2[t]); }
        else if (j < 6) o[t][j - 2] = __builtin_amdgcn_exp2f(o[t][j - 2]);
        else if (j < 8) { o[t][2 * j - 12] = one(fmaf(o[t][2 * j - 12], sd[t], sd[t])); o[t][2 * j - 11] = one(fmaf(o[t][2 * j - 11], sd[t], sd[t])); }
        else if (j < 12) o[t][j - 8] = __builtin_amdgcn_rcpf(o[t][j - 8]);
        else if (j < 14) { o[t][2 * j - 24] = one(o[t][2 * j - 24] * acc[t][1][2 * j - 24]); o[t][2 * j - 23] = one(o[t][2 * j - 23] * acc[t][1][2 * j - 23]); }
        else { h[t][0] = pk(o[t][0], o[t][1]); h[t][1] = pk(o[t][2], o[t][3]); }
      };
      auto glu = [&](int f, int t, int from) {
        if (from < 1) { o[t] = acc[t][2 * f] * rl2[t]; asm("" : "+v"(o[t])); }
        if (from < 2) {
#pragma unroll
          for (int r = 0; r < 4; ++r) o[t][r] = __builtin_amdgcn_exp2f(o[t][r]);
        }
        o[t] = o[t] * sd[t] + sd[t]; asm("" : "+v"(o[t]));
#pragma unroll
        for (int r = 0; r < 4; ++r) o[t][r] = __builtin_amdgcn_rcpf(o[t][r]);
        o[t] *= acc[t][2 * f + 1];
        h[t][0] = pk(o[t][0], o[t][1]); h[t][1] = pk(o[t][2], o[t][3]);
      };
      auto tab2 = [&](float (&d)[4], unsigned off, int j) { d[2 * j] = *(LAS float*)(L + ta + off + 128 * j); d[2 * j + 1] = *(LAS float*)(L + ta + off + 128 * j + 64); };
      float pv[8], s1 = 0.f, s2 = 0.f;
#pragma unroll
      for (int n = 0; n < 26; ++n) {
        if (n == 16) barv();
        mm(0, n); rd(0, n);
        if (n < 2) {
#pragma unroll
          for (int j = 0; j < 4; ++j) pv[4 * n + j] = *(LAS float*)(L + sa + 128 * (4 * n + j));
        }
        if (n == 2) { cs[0] = *(LAS f32x4*)(L + tc); cs[1] = *(LAS f32x4*)(L + tc + 384); }
        if (n == 6) { s1 = one(pv[0] + pv[1]); s2 = one(pv[2] + pv[3]); }
        if (n == 7 || n == 8) { s1 = one(s1 + pv[2 * n - 10]); s2 = one(s2 + pv[2 * n - 9]); }
        if (n == 9) s1 = one(s1 + s2);
        if (n == 10) { s2 = one(s1 * (1.f / 640.f)); s1 = one(s1 * (1.f / 640.f)); }
        if (n == 11) { const auto r = __builtin_amdgcn_permlane16_swap(__float_as_uint(s1), __float_as_uint(s2), false, false); s1 = one(__uint_as_float(r[0])); s2 = one(__uint_as_float(r[1])); }
        if (n == 12) { s2 = one(fmaf(-s1, s1, s2)); s1 = one(-s1); }
        if (n == 13) { *(LAS float*)(L + sw + L_MEAN) = s1; s2 = one(fmaxf(s2, 0.f)); }
        if (n == 14) s2 = one(s2 + 1e-5f);
        if (n == 15) s1 = __builtin_amdgcn_rsqf(s2);
        if (n == 16 || n == 17) tab2(mean, L_MEAN, n & 1);
        if (n == 18) s2 = one(s2 * s1);
        if (n == 19) s1 = one(s1 * -1.44269504089f);
        if (n == 20) *(LAS float*)(L + sw + L_STD) = s2;
        if (n == 21) *(LAS float*)(L + sw + L_RSTD) = s1;
        sched();
      }
#pragma unroll
      for (int n = 26; n < 48; ++n) {
        mm(0, n); rd(0, n);
        if (n < 34) corr1(0, n - 26);
        if (n >= 34 && n < 40) { corr1(0, 2 * n - 60); corr1(0, 2 * n - 59); }
        if (n == 40 || n == 41) corr1(0, n - 20);
        if (n == 42) { cs[2] = *(LAS f32x4*)(L + tc + 64); cs[3] = *(LAS f32x4*)(L + tc + 384 + 64); }
        if (n > 42) { corr1(0, 2 * n - 64); corr1(0, 2 * n - 63); }
        sched();
      }
      barv();
#pragma unroll
      for (int n = 0; n < 48; ++n) {
        mm(1, n);
        if (n < 16) { corr1(1, 2 * n); corr1(1, 2 * n + 1); }
        if (n == 12 || n == 13) tab2(rl2, L_RSTD, n & 1);
        if (n == 14 || n == 15) tab2(sd, L_STD, n & 1);
        if (n == 16 || n == 17) cs[n - 12] = *(LAS f32x4*)(L + tc + 128 + 384 * (n & 1));
        if (n >= 18) glu1(n - 18);
        sched();
      }
      st16(rw, swap_pair(h[0][0], h[0][1], h[1][0], h[1][1]), vo);
      sched();
      glu(0, 2, 0); glu(0, 3, 0);
      st16(rw, swap_pair(h[2][0], h[2][1], h[3][0], h[3][1]), vo2);
      sched();
#pragma unroll
      for (int f = 1; f < 3; ++f)
#pragma unroll
        for (int t = 0; t < 4; t += 2) {
          if (f == 2) { acc[t][4] += cs[4] * mean[t]; acc[t][5] += cs[5] * mean[t]; acc[t + 1][4] += cs[4] * mean[t + 1]; acc[t + 1][5] += cs[5] * mean[t + 1]; }
          glu(f, t, 0); glu(f, t + 1, 0);
          st16(rw, swap_pair(h[t][0], h[t][1], h[t + 1][0], h[t + 1][1]), (t ? vo2 : vo) + f * 512);
          sched();
        }
    }
    {
      PHASE_LANE();
      const int kh = w & 1;
      unsigned wb[3] = {L_W4 + kh * 5120 + lane * 16, 0, 0}; wb[1] = wb[0] + 51200; wb[2] = wb[0] + 102400;
      pin(wb[0]); pin(wb[1]); pin(wb[2]);
      bar();
      constexpr int R = AHEAD + 2;
      bf16x8 ha[R], wf[6][5];
      f32x4 acc[2][5];
      auto rdw = [&](int s, int i) { wf[s % 6][i] = lds16(L, wb[w4off(s) / 51200] + w4off(s) % 51200 + i * 1024); };
#pragma unroll
      for (int n = 0; n < 30; ++n) rdw(n / 5, n % 5);
      lgk0();
      drain_bar();
      sched();
      unsigned hv[2] = {OFF_HT + layer * HT_BUF + tfo * 49152 + kh * 1024 + lane * 16, OFF_HT + layer * HT_BUF + (tfo ^ 1) * 49152 + kh * 1024 + lane * 16};
      unsigned rd = L_RED + w * 5120 + lane * 16, wr = L_RED + (w ^ 1) * 5120 + lane * 16;
      pin(hv[0]); pin(hv[1]); pin(rd); pin(wr);
      X_ADDR(1);
      unsigned ow = L_W4 + 61440 + w * 3072 + c * 160 + 8 * q, orr = L_W4 + 61440 + w * 3072 + lane * 16, vo3[3];
#pragma unroll
      for (int j = 0; j < 3; ++j) { const int u = 64 * j + lane; vo3[j] = 16 * tfo * 1280 + cto * 160 + ((u * 205) >> 11) * 1120 + u * 16 + (j == 2 ? (lane & 32) << 25 : 0); pin(vo3[j]); }
      pin(ow); pin(orr);
#pragma unroll
      for (int i = 0; i < 5; ++i) { acc[0][i] = unpk(res[2 * i], res[2 * i + 1]); pin(acc[0][i]); }
      bar();
      auto req = [&](int l) { if (l < 48) ha[l % R] = ld16(rw, hv[1 - ((l >> 1) & 1)] + (l & 1) * 2048, (l >> 2) * 4096); };
#pragma unroll
      for (int l = 0; l < AHEAD; ++l) { req(l); sched(); }
      f32x4 pt[5];
#pragma unroll
      for (int d = 0; d < 12; ++d) {
        if (d == 4 || d == 9 || d == 10) barv();
#pragma unroll
        for (int n = 0; n < 20; ++n) {
          const int m = n >> 1, v = m / 5, i = v && d < 11 ? 9 - m : m % 5, e = (n ^ m) & 1;
          if (d == 11 && n == 17) barv();
          if (d == 0 && !v && !(n & 1)) mma0(acc[1][i], wf[e][i], ha[e]); else mma(acc[1 - v][i], wf[(2 * d + e) % 6][i], ha[(4 * d + 2 * v + e) % R]);
          if (n % 5 == 4) req(4 * d + n / 5 + AHEAD);
          else if (d >= 2 && d < 11 && n < 13) { const int j = n - n / 5; if (j < 10) rdw(2 * d + 2 + (j & 1), j >> 1); }
          if (n == 18 && d >= 2 && d < 11) lgk0();
          if (d == 11) {
            if (n >= 10 && n < 15) *(LAS f32x4*)(L + wr + (n - 10) * 1024) = acc[1][n - 10];
            if (n >= 17) { pt[2 * n - 34] = *(LAS f32x4*)(L + rd + (2 * n - 34) * 1024); if (n < 19) pt[2 * n - 33] = *(LAS f32x4*)(L + rd + (2 * n - 33) * 1024); }
          }
          sched();
        }
      }
#pragma unroll
      for (int i = 0; i < 5; ++i) { acc[0][i] += pt[i]; pin(acc[0][i]); sched(); }
      if (layer == 1) {
        const rsrc_t ro = mk(out, 2048 * 640 * 2);
#pragma unroll
        for (int i = 0; i < 5; ++i) *(LAS u32x2*)(L + ow + 32 * i) = (u32x2){pk(acc[0][i][0], acc[0][i][1]), pk(acc[0][i][2], acc[0][i][3])};
#pragma unroll
        for (int j = 0; j < 3; ++j) st16(ro, *(LAS u32x4*)(L + orr + j * 1024), vo3[j]);
      } else {
        float s = 0.f, ss = 0.f;
#pragma unroll
        for (int j = 0; j < 3; ++j) x_part(rw, vx, vx2, j, acc[0], res, s, ss);
        x_stats(rw, vs, s, ss);
        bar();
        drain_bar();
      }
    }
  }
}

DI void stats_load(const Ctx& k, rsrc_t rw, int xb, int tf0, u32x2 (&sv)[8]) {
#pragma unroll
  for (int j = 0; j < 8; ++j) sv[j] = __builtin_amdgcn_raw_buffer_load_b64(rw, (k.lane >> 4) * 1024 + (k.lane & 15) * 8 + j * 128, OFF_ST + xb * ST_BUF + tf0 * 1024, SC1);
}
DI void stats_sum(u32x2 (&sv)[8], float (&s)[2]) {
#pragma unroll
  for (int j = 0; j < 8; ++j) pin(sv[j]);
#pragma unroll
  for (int e = 0; e < 2; ++e) {
    s[e] = __uint_as_float(sv[0][e]);
#pragma unroll
    for (int j = 1; j < 8; ++j) s[e] = one(s[e] + __uint_as_float(sv[j][e]));
    s[e] *= 1.f / 640.f; pin(s[e]);
  }
}
DI void stats_table(const Ctx& k, int a, float (&s)[2], float scale) {
  pin(s[0]); pin(s[1]);
  float r[2];
#pragma unroll
  for (int e = 0; e < 2; ++e) {
    const float ss = __builtin_bit_cast(float, __builtin_amdgcn_update_dpp(0, __builtin_bit_cast(int, s[e]), 0x128, 0xf, 0xf, true));
    const float v = one(fmaxf(one(fmaf(-s[e], s[e], ss)), 0.f) + 1e-5f), q = __builtin_amdgcn_rsqf(v);
    r[e] = one(q * scale);
  }
  if (!(k.lane & 8)) {
    const unsigned t = (64 * a + (k.lane & 48) + 2 * (k.lane & 7)) * 4;
    *(LAS f32x2*)(k.lds + L_MEAN + t) = (f32x2){one(-s[0]), one(-s[1])};
    *(LAS f32x2*)(k.lds + L_RSTD + t) = (f32x2){r[0], r[1]};
  }
}

DI void loader(const Ctx& k0, const char* x0, const char* wsp, const char* wdp, int a4) {
  Ctx k = k0;
  const bool isW = a4 < 2;
  LAS char* const L = k.lds;
  const rsrc_t rw = mk(k.ws, WS_END);
  const rsrc_t rs = mk(wsp, 2048 * 2048 * 2);
  auto fresh = [&]() { asm volatile("" : "+v"(k.lane), "+s"(k.P), "+s"(k.kq), "+s"(k.np), "+s"(k.P1), "+s"(k.ct1), "+s"(k.mat), "+s"(a4)); };
#define lane k.lane
#define a (a4 & 1)
#define me (k.P * 16 + k.kq * 4 + k.np)
#define n3 (4 * k.kq + k.np)
#define sw (((lane & 7) ^ (4 * a + (lane >> 4))) * 16)
#define vo2 ((lane >> 3) * 4096 + sw)
#define so2 ((128 * k.P + 8 * a) * 4096 + (k.kq & 1) * 2048)
#define so3 (OFF_W3 + n3 * 20 * 12288 + a * 12288)
#define so4 (OFF_W4 + (2 * k.np + (k.kq & 1)) * 48 * 5120)
  bf16x8 k1[25], k4[3][8];
  u64 tag = 0;
  int rot = 2 * k.np;
  auto ktile = [&](int t) { const int r = (t + rot) & 7; return 4 * (r >> 1) + 2 * (t >> 3) + (r & 1); };
  auto w2 = [&](int t, int slot) {
#pragma unroll
    for (int j = 0; j < 8; ++j) dma<0, 0>(rs, L + L_WR + slot * 16384 + (2 * j + a) * 1024, vo2, so2 + j * 16 * 4096 + ktile(t) * 128);
  };
  auto w2n = [&](int t0, int t1) {
#pragma unroll 1
    for (int t = t0; t < t1; ++t) w2(t, t < 3 ? t + 3 : t - 3);
  };
  auto w3 = [&](int t, int slot) { dma_lin<SC1, 12>(rw, L + L_WR + slot * 24576 + a * 12288, lane * 16, so3 + t * 24576); };
  auto w4 = [&](int ch) { dma_lin<SC1, 15>(rw, L + L_W4 + w4slot(ch) * 30720 + a * 15360, lane * 16, so4 + ch * 30720 + a * 15360); };
  auto push = [&](int ev, int wg0, int wgstep, int n, int slot) {
    if (a4 == 2) {
      unsigned dst = lane < n ? OFF_MB + ev * MB_EV + (wg0 + lane * wgstep) * 128 + slot * 8 : 0x7fffffff;
      pin(dst); pin(tag);
      bar();
      __builtin_amdgcn_raw_buffer_store_b64(__builtin_bit_cast(u32x2, tag), rw, dst, 0, SC1);
    } else bar();
    fresh();
  };
  unsigned budget = SPIN_CAP;
  auto wait_mb = [&](int ev, int wg, int n) {
    GAS u64* src = (GAS u64*)(k.ws + OFF_MB + ev * MB_EV + wg * 128) + (lane & (n - 1));
#pragma unroll 1
    for (; budget; budget = uni(budget - 1)) {
      const u64 v = __hip_atomic_load(src, __ATOMIC_RELAXED, __HIP_MEMORY_SCOPE_AGENT);
      if (__all(v == tag)) break;
      __builtin_amdgcn_s_sleep(1);
    }
  };
  auto poll = [&](int ev, int wg, int n) {
    if (a4 == 2) wait_mb(ev, wg, n);
    drain_bar();
  };
  auto get_tag = [&]() { const char* e = k.ws; pins(e); tag = *(const GAS u64*)e; };
  auto mix_tag = [&]() { pin(tag); tag = tag * 6364136223846793005ull + 1442695040888963407ull; };
  if (!isW) __builtin_amdgcn_s_setprio(1);
  {
    const rsrc_t r1 = isW ? mk(wdp, 640 * 640 * 2) : mk(x0, 2048 * 640 * 2);
    const unsigned v1 = (lane >> 3) * 1280 + sw, s1 = ((isW ? 80 * k.ct1 : 128 * k.P1) + 8 * a) * 1280;
    int slot = 2, wt = rot1(k);
    if ((k.P & 1) && !isW) __builtin_amdgcn_s_sleep(LAG);
#pragma unroll 1
    for (int t = 0; t < 10; ++t) {
      if (isW) {
        if (t >= 3) { waitv<10>(); bar(); }
#pragma unroll
        for (int j = 0; j < 5; ++j) dma<0, 0>(r1, L + L_D + wt * 10240 + (2 * j + a) * 1024, v1, s1 + 16 * j * 1280 + wt * 128);
      } else {
        if (t >= 2) { waitv<8>(); bar(); }
#pragma unroll
        for (int j = 0; j < 8; ++j) dma<0, 0>(r1, L + L_XR + slot * 16384 + (2 * j + a) * 1024, v1, s1 + 16 * j * 1280 + wt * 128);
      }
      slot = slot == 2 ? 0 : slot + 1; wt = wt == 9 ? 0 : wt + 1;
    }
    if (isW) {
      waitv<10>(); bar();
      waitv<5>(); bar(); w2(0, 3);
      waitv<8>(); bar(); w2(1, 4);
      bar();
      w2(2, 5);
    } else {
      waitv<8>(); bar(); waitv<0>();
      get_tag();
      bar(); bar();
      mix_tag();
    }
  }
  {
    unsigned kb[2] = {L_D + lane * 16 + ((a4 >> 1) * 10 + a) * 1024, 0}; kb[1] = kb[0] + 61440; pin(kb[0]); pin(kb[1]);
    bar();
#pragma unroll
    for (int j = 0; j < 25; ++j) k1[j] = lds16(L, kb[j / 15] + ((j / 5) % 3) * 20480 + (j % 5) * 2048);
  }
#pragma unroll 1
  for (int layer = 0; layer < 2; ++layer) {
    const int ev0 = layer * EV_PER_LAYER;
    if (layer) {
      poll(ev0 + EV_X, k.P1 * 16 + k.mat * 8 + k.ct1, 16);
      if (!isW) { u32x2 sv[8]; float sm[2]; stats_load(k, rw, 1, 8 * k.P1 + 4 * a, sv); stats_sum(sv, sm); stats_table(k, a, sm, k.mat ? -1.f : 1.f); }
      bar();
      if (isW) w2n(3, 6);
    }
    push(ev0 + EV_AB, (2 * k.mat + (k.P1 >> 3)) * 4 + (k.ct1 >> 1), 16, 16, 8 * (k.P1 & 1) + (k.P1 & 6) + (k.ct1 & 1));
    GAS u64* rtf = (GAS u64*)(k.ws + OFF_MB + EV_RT * MB_EV);
    if (layer == 0 && a4 == 3 && lane == 0) __hip_atomic_store(rtf + k.b, tag, __ATOMIC_RELAXED, __HIP_MEMORY_SCOPE_AGENT);
    if (isW && layer == 0) { get_tag(); w2n(3, 6); mix_tag(); }
    const unsigned soab = OFF_AB + layer * AB_BUF + (((k.kq >> 1) * 4 + k.np) * 64 + 32 * (k.kq & 1)) * 10240;
    unsigned l16 = lane * 16, so0 = soab + a * 10240 + ktile(0) * 20480, la0 = L_D + a * 10240;
    so0 = __builtin_amdgcn_readfirstlane(so0); la0 = __builtin_amdgcn_readfirstlane(la0);
    pin(l16); pins(so0); pins(la0);
    poll(ev0 + EV_AB, me, 8);
    if (!isW) dma_lin<SC1, 10>(rw, L + la0, l16, so0);
    if (isW) {
      const unsigned sb2 = uni(so2), lb2 = uni(L_WR + a * 1024);
      unsigned v2 = vo2, so = uni(sb2 + ktile(6) * 128), la = uni(lb2 + 49152), t = 7;
      pin(v2);
#pragma unroll 1
      do {
        waitv<32>(); bar();
#pragma unroll
        for (int j = 0; j < 2; ++j) dma<0, 0>(rs, L + la + j * 2048, v2, so + j * 16 * 4096);
        sched();
        const unsigned sn = uni(sb2 + ktile(t) * 128), ln = uni(la == lb2 + 81920 ? lb2 : la + 16384);
        t = uni(t + 1);
        sched();
#pragma unroll
        for (int j = 2; j < 8; ++j) dma<0, 0>(rs, L + la + j * 2048, v2, so + j * 16 * 4096);
        so = sn; la = ln;
      } while (t != 17);
      const bool chk = layer == 0 && a == 0;
      u64 f[4];
      if (chk) {
#pragma unroll
        for (int j = 0; j < 4; ++j) f[j] = __hip_atomic_load(rtf + lane + 64 * j, __ATOMIC_RELAXED, __HIP_MEMORY_SCOPE_AGENT);
      }
      waitv<32>(); bar();
      if (chk) {
#pragma unroll
        for (int j = 0; j < 4; ++j) pin(f[j]);
#pragma unroll 1
        for (unsigned spins = 0; spins < SPIN_CAP && !__all(f[0] == tag && f[1] == tag && f[2] == tag && f[3] == tag); ++spins) {
#pragma unroll
          for (int j = 0; j < 4; ++j) f[j] = __hip_atomic_load(rtf + lane + 64 * j, __ATOMIC_RELAXED, __HIP_MEMORY_SCOPE_AGENT);
        }
      }
      waitv<0>(); bar();
      w3(3, 1); bar(); bar(); w3(0, 2); bar();
      w3(1, 3); bar(); w3(2, 0);
    } else {
      const unsigned sba = uni(soab + a * 10240);
      dma_lin<SC1, 10>(rw, L + la0 + 20480, l16, sba + ktile(1) * 20480);
      unsigned so = uni(sba + ktile(2) * 20480), la = uni(la0 + 40960), t = 3;
      auto step = [&]() {
        bar();
        dma_lin<SC1, 4>(rw, L + la, l16, so);
        sched();
        const unsigned sn = uni(sba + ktile(t) * 20480), ln = uni(la == la0 + 40960 ? la0 : la + 20480);
        t = uni(t + 1);
        sched();
        dma_lin<SC1, 6>(rw, L + la + 4096, l16, so + 4096);
        so = sn; la = ln;
      };
#pragma unroll 1
      do { waitv<10>(); step(); } while (t != 8);
      unsigned ja = L_JUNK + 256 + layer * 256 + a * 128;
      if (lane < 8) dma<SC1, 0>(rw, L + ja, l16, OFF_MB + (ev0 + EV_AB) * MB_EV + me * 128);
      ja += (lane & 15) * 8; pin(ja);
      waitv<11>(); step();
      waitv<10>();
      if (!__all(*(LAS u64*)(L + ja) == tag)) wait_mb(ev0 + EV_AB, me, 16);
      step();
#pragma unroll 1
      do { waitv<10>(); step(); } while (t != 17);
      waitv<10>(); bar();
      waitv<0>(); bar();
    }
    auto k4load = [&](int n) {
#pragma unroll
      for (int j = 0; j < 8; ++j) k4[n][j] = ld16(rw, lane * 16 + a4 * 1024, so4 + (5 + n) * 30720 + j * 4096);
    };
    push(ev0 + EV_EX, k.P * 16 + k.np, 4, 4, k.kq);
    if (layer == 0) k4load(0);
    poll(ev0 + EV_EX, me, 4);
    push(ev0 + EV_X1, k.P * 16, 1, 16, n3);
    if (layer == 0) {
      u32x2 cv[5];
      if (isW) {
#pragma unroll
        for (int j = 0; j < 5; ++j) cv[j] = __builtin_amdgcn_raw_buffer_load_b64(rw, lane * 8, OFF_CS + (j * 3072 + a * 1536 + 96 * n3) * 4, SC1);
      }
      k4load(1);
      if (isW) {
        sched();
        f32x2 s = {0.f, 0.f};
#pragma unroll
        for (int j = 0; j < 5; ++j) s += __builtin_bit_cast(f32x2, cv[j]);
        if (lane < 48) *(LAS f32x2*)(L + L_CS3 + (96 * a + 2 * lane) * 4) = s;
      }
    }
    l16 = lane * 16; so0 = OFF_XT + 2 * layer * XT_BUF + (8 * k.P + 4 * a) * 20480; la0 = L_D + a * 8192;
    so0 = __builtin_amdgcn_readfirstlane(so0); la0 = __builtin_amdgcn_readfirstlane(la0);
    pin(l16); pins(so0); pins(la0);
    poll(ev0 + EV_X1, me, 16);
    if (!isW) {
#pragma unroll
      for (int j = 0; j < 4; ++j) dma_lin<SC1, 2>(rw, L + la0 + j * 2048, l16, so0 + j * 20480);
    }
    if (isW) {
      bar();
      __builtin_amdgcn_s_sleep(3);
      dma_lin<SC1, 4>(rw, L + L_D + 49152 + a * 4096, lane * 16, OFF_ST + 2 * layer * ST_BUF + (8 * k.P + 4 * a) * 1024);
      const unsigned lb3 = uni(L_WR + a * 12288), se = uni(so3 + 10 * 24576);
      unsigned so = uni(so3 + 4 * 24576), la = uni(lb3 + 49152);
#pragma unroll 1
      do {
        waitv<24>(); bar();
        dma_lin<SC1, 4>(rw, L + la, l16, so);
        sched();
        const unsigned sn = uni(so + 24576), ln = uni(la == lb3 + 73728 ? lb3 : la + 24576);
        sched();
        dma_lin<SC1, 8>(rw, L + la + 4096, l16, so + 4096);
        so = sn; la = ln;
      } while (so != se);
      waitv<24>(); bar();
      waitv<12>(); bar(); waitv<0>(); bar();
      bar();
      w4(2); w4(3); w4(4);
    } else {
#pragma unroll
      for (int j = 0; j < 4; ++j) dma_lin<SC1, 2>(rw, L + la0 + 16384 + j * 2048, l16, so0 + 2048 + j * 20480);
      unsigned so = uni(so0 + 4096), la = uni(la0 + 32768);
#pragma unroll 1
      do {
        waitv<8>(); bar();
        dma_lin<SC1, 2>(rw, L + la, l16, so);
        sched();
        const unsigned sn = uni(so + 2048), ln = uni(la == la0 + 32768 ? la0 : la + 16384);
        sched();
#pragma unroll
        for (int j = 1; j < 4; ++j) dma_lin<SC1, 2>(rw, L + la + j * 2048, l16, so + j * 20480);
        so = sn; la = ln;
      } while (so != so0 + 20480);
      waitv<8>(); bar();

      w4(0);
      waitv<15>(); bar();

      w4(1);
      bar();

      waitv<0>();
    }
    bar();
    push(ev0 + EV_H, k.P * 16, 1, 16, n3);
    if (layer == 0) k4load(2);
    auto k4put = [&](int n, bool paced) {
      unsigned kb = L_W4 + w4slot(5 + n) * 30720 + a4 * 1024 + lane * 16; pin(kb);
#pragma unroll
      for (int j = 0; j < 8; ++j) if (j < 7 || isW) { *(LAS bf16x8*)(L + kb + j * 4096) = k4[n][j]; if (paced) __builtin_amdgcn_s_sleep(2); }
    };
    k4put(0, false); k4put(1, false);
    poll(ev0 + EV_H, me, 16);
    bar();
    k4put(2, true);
    bar(); bar();
    rot = 0;
    if (isW && layer == 0) w2n(0, 2);
    bar();
    if (layer == 0) {
      if (isW) w2n(2, 3);
      unsigned kb[2] = {L_D + lane * 16 + ((a4 >> 1) * 10 + a) * 1024, 0}; kb[1] = kb[0] + 61440; pin(kb[0]); pin(kb[1]);
      bar();
#pragma unroll
      for (int j = 0; j < 7; ++j) *(LAS bf16x8*)(L + kb[j / 15] + ((j / 5) % 3) * 20480 + (j % 5) * 2048) = k1[j];
      push(EV_PER_LAYER + EV_X, k.P * 16, 1, 16, n3);
#pragma unroll
      for (int j = 7; j < 25; ++j) *(LAS bf16x8*)(L + kb[j / 15] + ((j / 5) % 3) * 20480 + (j % 5) * 2048) = k1[j];
    }
  }
  if (k.b == 0 && a4 == 2 && lane == 0) *(GAS u64*)k.ws = tag;
#undef lane
#undef a
#undef me
#undef n3
#undef sw
#undef vo2
#undef so2
#undef so3
#undef so4
}

__global__ void __launch_bounds__(512, 2) fnet_glu_kernel(const char* x0, const char* wsr, const char* wsi, const char* wdr, const char* wdi, const char* wg, const char* wu, const char* wdn, char* out, char* ws) {
  __shared__ __attribute__((aligned(16))) char smem[163840];
  Ctx k;
  k.lds = (LAS char*)smem; k.lane = threadIdx.x & 63; k.b = blockIdx.x;
  const int w = __builtin_amdgcn_readfirstlane(threadIdx.x >> 6);
  if (w < 4 || w == 7) {
    const char* pc; asm volatile("s_getpc_b64 %0" : "=s"(pc));
    touch<0>(mk(pc, CODE_BYTES), k.lds + L_JUNK, (w < 4 ? threadIdx.x : threadIdx.x - 192) * 128, 0);
  }
  asm volatile("" : "+s"(x0), "+s"(wsr), "+s"(wsi), "+s"(wdr), "+s"(wdi), "+s"(wg), "+s"(wu), "+s"(wdn), "+s"(out), "+s"(ws));
  k.ws = ws;
  const int x = k.b & 7, i = k.b >> 3;
  k.kq = x >> 1; k.P = 8 * (x & 1) + 4 * (i >> 4) + 2 * ((i >> 2) & 1) + ((i >> 3) & 1); k.np = i & 3;
  k.P1 = 4 * (x >> 1) + (i >> 3); k.mat = x & 1; k.ct1 = i & 7;
  if (w < 4) consumer(k, x0, wg, wu, wdn, out, w);
  else loader(k, x0, (x >> 2) ? wsi : wsr, (x & 1) ? wdi : wdr, w - 4);
}
}

extern "C" void kernel_launch(void* const* d_in, const int* in_sizes, int n_in, void* d_out, int out_size, void* d_ws, size_t ws_size, hipStream_t stream) {
  fnet_glu_kernel<<<256, 512, 0, stream>>>((const char*)d_in[0], (const char*)d_in[1], (const char*)d_in[2], (const char*)d_in[3], (const char*)d_in[4],
                                           (const char*)d_in[6], (const char*)d_in[7], (const char*)d_in[8], (char*)d_out, (char*)d_ws);
}
```

```cpp
#include <hip/hip_runtime.h>

typedef unsigned long long u64;
typedef short bf16x8 __attribute__((ext_vector_type(8)));
typedef short s16x4 __attribute__((ext_vector_type(4)));
typedef float f32x4 __attribute__((ext_vector_type(4)));
typedef float f32x2 __attribute__((ext_vector_type(2)));
typedef unsigned u32x4 __attribute__((ext_vector_type(4)));
typedef unsigned u32x2 __attribute__((ext_vector_type(2)));
typedef __bf16 bf2 __attribute__((ext_vector_type(2)));
#define LAS __attribute__((address_space(3)))
#define GAS __attribute__((address_space(1)))
#define DI __device__ __forceinline__
typedef __amdgpu_buffer_rsrc_t rsrc_t;

namespace {
constexpr int AHEAD = 10;
constexpr int LAG = 12, LAG2 = 48;
constexpr unsigned CODE_BYTES = 34816;
constexpr unsigned OFF_MB = 4096, MB_EV = 256 * 128;
constexpr int EV_X = 0, EV_AB = 1, EV_EX = 2, EV_X1 = 3, EV_H = 4, EV_PER_LAYER = 5, EV_RT = 0;
constexpr unsigned OFF_CS = 0x60000;
constexpr unsigned OFF_ST = 0x80000, ST_BUF = 8 * 2048 * 8;
constexpr unsigned OFF_XT = 0x100000, XT_BUF = 2048 * 640 * 2;
constexpr unsigned OFF_AB = OFF_XT + 3 * XT_BUF, AB_BUF = 2048 * 1280 * 2;
constexpr unsigned OFF_HT = OFF_AB + 2 * AB_BUF, HT_BUF = 2048 * 1536 * 2;
constexpr unsigned OFF_EX = OFF_HT + 2 * HT_BUF, EX_BUF = 256 * 12 * 2560;
constexpr unsigned OFF_W3 = OFF_EX + 2 * EX_BUF;
constexpr unsigned OFF_W4 = OFF_W3 + 16 * 20 * 12288;
constexpr unsigned WS_END = OFF_W4 + 8 * 48 * 5120;
static_assert(OFF_MB + 10 * MB_EV <= OFF_CS && OFF_CS + 5 * 3072 * 4 <= OFF_ST && OFF_ST + 3 * ST_BUF <= OFF_XT && WS_END <= 0x4000000, "ws map");
constexpr unsigned L_MEAN = 0, L_RSTD = 512, L_CS1 = 1024, L_STD = 1536, L_CS3 = 2304, L_JUNK = 3072;
constexpr unsigned L_D = 4096;
constexpr unsigned L_WR = L_D + 61440, L_W4 = L_D + 6144, L_CP = L_D + 102400, L_XR = L_D + 110592, L_RED = L_W4 + 30720;
constexpr int w4slot(int ch) { return ch == 4 ? 4 : ((ch < 5 ? ch : ch - 5) + 2) & 3; }
constexpr int w4off(int s) { return w4slot(s / 3) * 30720 + (s % 3) * 10240; }
constexpr int SC1 = 16;
constexpr unsigned SPIN_CAP = 1u << 21;

DI rsrc_t mk(const void* p, unsigned bytes) { return __builtin_amdgcn_make_buffer_rsrc((void*)p, 0, bytes, 0x00020000); }
template <int N> DI void waitv() { asm volatile("s_waitcnt vmcnt(%0)" ::"n"(N) : "memory"); }
DI void bar() { asm volatile("s_waitcnt lgkmcnt(0)\n\ts_barrier" ::: "memory"); }
DI void lgk0() { __builtin_amdgcn_sched_barrier(0); __builtin_amdgcn_s_waitcnt(0xC07F); __builtin_amdgcn_sched_barrier(0); }
DI void barv() { lgk0(); asm volatile("s_barrier" ::: "memory"); __builtin_amdgcn_sched_barrier(0); }
DI void drain_bar() {
  asm volatile("s_waitcnt vmcnt(0) lgkmcnt(0)\n\ts_barrier" ::: "memory");
  __builtin_amdgcn_s_waitcnt(0);
  asm volatile("" ::: "memory");
}
DI void sched() { __builtin_amdgcn_sched_barrier(0); }
template <class T> DI void pin(T& x) { asm volatile("" : "+v"(x)); }
template <class T> DI void pins(T& x) { asm volatile("" : "+s"(x)); }
template <int AUX, int IMM> DI void dma(rsrc_t r, LAS char* l, unsigned vo, unsigned so) { __builtin_amdgcn_raw_ptr_buffer_load_lds(r, (LAS void*)l, 16, vo, so, IMM, AUX); }
template <int AUX> DI void touch(rsrc_t r, LAS char* l, unsigned vo, unsigned so) { __builtin_amdgcn_raw_ptr_buffer_load_lds(r, (LAS void*)l, 4, vo, so, 0, AUX); }
template <int AUX, int N> DI void dma_lin(rsrc_t r, LAS char* l, unsigned vo, unsigned so) {
#pragma unroll
  for (int j = 0; j < N; j += 4) {
    dma<AUX, 0>(r, l + j * 1024, vo, so + j * 1024);
    if (j + 1 < N) dma<AUX, 1024>(r, l + j * 1024, vo, so + j * 1024);
    if (j + 2 < N) dma<AUX, 2048>(r, l + j * 1024, vo, so + j * 1024);
    if (j + 3 < N) dma<AUX, 3072>(r, l + j * 1024, vo, so + j * 1024);
  }
}
DI unsigned uni(unsigned x) { x = __builtin_amdgcn_readfirstlane(x); asm volatile("" : "+s"(x)); return x; }
DI bf16x8 ld16(rsrc_t r, unsigned vo, unsigned so) { return __builtin_bit_cast(bf16x8, __builtin_amdgcn_raw_buffer_load_b128(r, vo, so, SC1)); }
DI void st16(rsrc_t r, u32x4 v, unsigned vo) { __builtin_amdgcn_raw_buffer_store_b128(v, r, vo, 0, SC1); }
DI bf16x8 lds16(LAS char* l, unsigned a) { return *(LAS bf16x8*)(l + a); }
DI unsigned pk(float lo, float hi) { f32x2 v = {lo, hi}; return __builtin_bit_cast(unsigned, __builtin_convertvector(v, bf2)); }
DI float blo(unsigned u) { return __uint_as_float(u << 16); }
DI float bhi(unsigned u) { return __uint_as_float(u & 0xffff0000u); }
DI f32x4 unpk(unsigned a, unsigned b) { return (f32x4){blo(a), bhi(a), blo(b), bhi(b)}; }
DI f32x4 mfma(bf16x8 a, bf16x8 b, f32x4 c) { return __builtin_amdgcn_mfma_f32_16x16x32_bf16(a, b, c, 0, 0, 0); }
DI void mma(f32x4& c, const bf16x8& a, const bf16x8& b) { asm volatile("v_mfma_f32_16x16x32_bf16 %0, %1, %2, %0" : "+v"(c) : "v"(a), "v"(b)); __builtin_amdgcn_sched_barrier(0); }
DI void mma0(f32x4& c, const bf16x8& a, const bf16x8& b) { asm volatile("v_mfma_f32_16x16x32_bf16 %0, %1, %2, 0" : "=&v"(c) : "v"(a), "v"(b)); __builtin_amdgcn_sched_barrier(0); }
DI void settle() { asm volatile("s_nop 7"); __builtin_amdgcn_sched_barrier(0); }
DI float dot2(unsigned a, unsigned b, float s) { return __builtin_amdgcn_fdot2_f32_bf16(__builtin_bit_cast(bf2, a), __builtin_bit_cast(bf2, b), s, false); }
DI float one(float v) { asm("" : "+v"(v)); return v; }
DI u32x4 swap_pair(unsigned a0, unsigned a1, unsigned b0, unsigned b1) {
  const auto r0 = __builtin_amdgcn_permlane16_swap(a0, b0, false, false);
  const auto r1 = __builtin_amdgcn_permlane16_swap(a1, b1, false, false);
  return (u32x4){r0[0], r1[0], r0[1], r1[1]};
}

struct Ctx {
  char* ws; LAS char* lds; int lane, b;
  int kq, P, np;
  int P1, mat, ct1;
};

DI u32x4 x_pack(int j, const f32x4 (&v)[5], unsigned (&res)[10]) {
  const int i1 = j < 2 ? 2 * j + 1 : 4;
#pragma unroll
  for (int i = 2 * j; i <= i1; ++i) { res[2 * i] = pk(v[i][0], v[i][1]); res[2 * i + 1] = pk(v[i][2], v[i][3]); }
  return swap_pair(res[4 * j], res[4 * j + 1], res[2 * i1], res[2 * i1 + 1]);
}
DI void x_part(rsrc_t rw, unsigned vx, unsigned vx2, int j, const f32x4 (&v)[5], unsigned (&res)[10], float& s, float& ss) {
  const u32x4 o = x_pack(j, v, res);
  st16(rw, o, j < 2 ? vx + j * 1024 : vx2);
#pragma unroll
  for (int e = 4 * j; e < (j < 2 ? 4 * j + 4 : 10); ++e) { s = dot2(res[e], 0x3f803f80u, s); ss = dot2(res[e], res[e], ss); }
  pin(s); pin(ss);
}
DI void x_stats(rsrc_t rw, unsigned vs, float s, float ss) {
  const auto r = __builtin_amdgcn_permlane16_swap(__float_as_uint(s), __float_as_uint(ss), false, false);
  const unsigned t = __float_as_uint(__uint_as_float(r[0]) + __uint_as_float(r[1]));
  const auto r2 = __builtin_amdgcn_permlane32_swap(t, t, false, false);
  const float u = __uint_as_float(r2[0]) + __uint_as_float(r2[1]);
  __builtin_amdgcn_raw_buffer_store_b32(__float_as_uint(u), rw, vs, 0, SC1);
}

DI int rot1(const Ctx& k) { const int r = ((k.ct1 * 5) >> 2) + (((k.P1 & 3) * 5) >> 1); return r >= 10 ? r - 10 : r; }

#define PHASE_LANE() Ctx k = k0; int w = w0; asm volatile("" : "+v"(k.lane), "+s"(w), "+s"(k.P), "+s"(k.kq), "+s"(k.np), "+s"(k.P1), "+s"(k.ct1), "+s"(k.mat)); \
  const int tfo = 8 * k.P + 4 * (k.kq >> 1) + w, cto = 2 * k.np + (k.kq & 1); (void)tfo; (void)cto; \
  const int lane = k.lane, q = lane >> 4, c = lane & 15, qs = 2 * (q & 1) + (q >> 1); (void)qs; \
  const unsigned rowrd = (c >> 3) * 1024 + (c & 7) * 128 + ((q ^ (c >> 1)) * 16); (void)rowrd
#define X_ADDR(xb) unsigned vx = OFF_XT + (xb) * XT_BUF + tfo * 20480 + (10 * cto + qs) * 256 + c * 16, vx2 = vx + 2048 + ((lane & 16) << 26), \
  vs = OFF_ST + (xb) * ST_BUF + tfo * 1024 + cto * 128 + lane * 4 + ((lane & 32) << 25); pin(vx); pin(vx2); pin(vs)

DI void consumer(const Ctx& k0, const char* x0, const char* wg, const char* wu, const char* wdn, char* out, const int w0) {
  LAS char* const L = k0.lds;
  const rsrc_t rw = mk(k0.ws, WS_END);
  unsigned res[10];
  __builtin_amdgcn_s_setprio(2);
  auto retile = [&](const Ctx& k0, const int w) {
    int e = ((k0.b >> 7) * 64 + (k0.b & 63)) * 4 + w;
    pins(e);
    const int lane = k0.lane;
    const int g3 = ((e >> 2) * 2731) >> 16, ks3 = 4 * g3 + w, st3 = (e >> 2) - g3 * 24, ks4 = (e * 6554) >> 16, st4 = e - ks4 * 10;
    LAS char* t[3];
#pragma unroll
    for (int m = 0; m < 3; ++m) { const int u = rot1(k0) + (m < 2 ? 4 + w : 8 + (w >> 1)); t[m] = L + L_D + (u >= 10 ? u - 10 : u) * 10240 + (m < 2 ? m : w & 1) * 4096; }
    __builtin_amdgcn_s_sleep(LAG);
#pragma unroll
    for (int m = 0; m < 3; ++m) {
      const unsigned stride = m < 2 ? 3072 : 1280;
      const rsrc_t rr = mk(m == 0 ? wg : (m == 1 ? wu : wdn), e < 480 ? 640 * 1536 * 2 : 0);
#pragma unroll
      for (int j = 0; j < 4; ++j) dma<0, 0>(rr, t[m] + j * 1024, (lane >> 3) * stride + (lane & 7) * 16, (32 * (m < 2 ? ks3 : ks4) + 8 * j) * stride + 128 * (m < 2 ? st3 : st4));
    }
    const rsrc_t ro = mk(k0.ws, e < 480 ? WS_END : 0);
    const bf16x8 ones = {0x3f80, 0x3f80, 0x3f80, 0x3f80, 0x3f80, 0x3f80, 0x3f80, 0x3f80};
    const unsigned tr = (lane >> 4) * 1024 + ((lane >> 2) & 3) * 128 + (lane & 3) * 8;
    __builtin_amdgcn_s_waitcnt(0);
    asm volatile("" ::: "memory");
    u32x4 f[3][4];
#pragma unroll
    for (int m = 0; m < 3; ++m)
#pragma unroll
      for (int n = 0; n < 4; ++n) {
        const u32x2 lo = __builtin_bit_cast(u32x2, __builtin_amdgcn_ds_read_tr16_b64_v4i16((LAS s16x4*)(t[m] + tr + 32 * n)));
        const u32x2 hi = __builtin_bit_cast(u32x2, __builtin_amdgcn_ds_read_tr16_b64_v4i16((LAS s16x4*)(t[m] + tr + 32 * n + 512)));
        f[m][n] = (u32x4){lo[0], lo[1], hi[0], hi[1]};
      }
#pragma unroll
    for (int m = 0; m < 3; ++m) {
      f32x4 cs[4];
#pragma unroll
      for (int n = 0; n < 4; ++n) {
        const int nf = 4 * (m < 2 ? st3 : st4) + n;
        st16(ro, f[m][n], lane * 16 + (m < 2 ? OFF_W3 + m * 1024 + ks3 * 12288 + ((nf * 171) >> 10) * 19 * 12288 + nf * 2048 : OFF_W4 + ks4 * 5120 + ((nf * 205) >> 10) * 47 * 5120 + nf * 1024));
        if (m < 2) cs[n] = mfma(ones, __builtin_bit_cast(bf16x8, f[m][n]), (f32x4){0.f, 0.f, 0.f, 0.f});
      }
      if (m < 2) *(LAS float*)(L + L_CP + (m * 64 + lane) * 16 + w * 4) = (lane & 32) ? ((lane & 16) ? cs[3][0] : cs[2][0]) : ((lane & 16) ? cs[1][0] : cs[0][0]);
    }
  };
#pragma unroll 1
  for (int layer = 0; layer < 2; ++layer) {
    {
      PHASE_LANE();
      unsigned vo = OFF_AB + layer * AB_BUF + ((k.mat * 4 + (k.ct1 >> 1)) * 64 + 4 * k.P1 + w) * 10240 + 5 * (k.ct1 & 1) * 1024 + (qs * 16 + c) * 16;
      unsigned ta = (32 * w + 4 * q) * 4, tc = c * 4;
      pin(vo); pin(ta); pin(tc);
      f32x4 acc[2][5], mean[2], rstd[2]; float cs1[5];
      bf16x8 wf[4][5];
      auto tables = [&]() {
#pragma unroll
        for (int i = 0; i < 5; ++i) cs1[i] = *(LAS float*)(L + L_CS1 + tc + i * 64);
#pragma unroll
        for (int t = 0; t < 2; ++t) { mean[t] = *(LAS f32x4*)(L + ta + L_MEAN + 64 * t); rstd[t] = *(LAS f32x4*)(L + ta + L_RSTD + 64 * t); }
      };
#pragma unroll
      for (int i = 0; i < 5; ++i) { acc[0][i] = (f32x4){0.f, 0.f, 0.f, 0.f}; acc[1][i] = (f32x4){0.f, 0.f, 0.f, 0.f}; }
      if (layer == 0) {
        bf16x8 xf[4][2], cf[4], ones = {0x3f80, 0x3f80, 0x3f80, 0x3f80, 0x3f80, 0x3f80, 0x3f80, 0x3f80};
        f32x4 as[2], aq[2], ac[2];
        unsigned wt = rot1(k) * 10240, wa0 = L_D + rowrd;
        pin(wa0);
        unsigned wa = wa0 + wt, xr = L_XR + w * 4096 + rowrd, x_slot = 32768;
#pragma unroll
        for (int i = 0; i < 5; ++i) { pin(acc[0][i]); pin(acc[1][i]); }
#pragma unroll
        for (int t = 0; t < 2; ++t) { as[t] = (f32x4){0.f, 0.f, 0.f, 0.f}; aq[t] = as[t]; ac[t] = as[t]; pin(as[t]); pin(aq[t]); pin(ac[t]); }
        pin(wa); pin(xr); pin(ones);
        if (k.P & 1) retile(k, w);
        bar();
        unsigned xn[2] = {xr + 32768, (xr + 32768) ^ 64}, wn[2] = {wa, wa ^ 64};
        const unsigned wo = w * 2048;
        auto rd = [&](int p, int n) {
          const int e = n & 1, m = n >> 1;
          if (m < 2) xf[2 * p + e][m] = lds16(L, xn[e] + m * 2048);
          else if (m < 7) wf[2 * p + e][m - 2] = lds16(L, wn[e] + (m - 2) * 2048);
          else cf[2 * p + e] = lds16(L, wn[e] + wo);
        };
#pragma unroll
        for (int n = 0; n < 16; ++n) rd(0, n);
        lgk0();
        int left = 5;
#pragma unroll 1
        do {
#pragma unroll
          for (int p = 0; p < 2; ++p) {
#pragma unroll
            for (int n = 0; n < 32; ++n) {
              const int m = n >> 1, h = 2 * p + ((n ^ m) & 1);
              if (n == 10) barv();
              if (m < 10) { const int t = m / 5, i = t ? 9 - m : m; mma(acc[t][i], xf[h][t], wf[h][i]); }
              else if (m == 10) mma(aq[1], xf[h][1], xf[h][1]);
              else if (m == 11) mma(as[1], xf[h][1], ones);
              else if (m == 12) mma(as[0], xf[h][0], ones);
              else if (m == 13) mma(aq[0], xf[h][0], xf[h][0]);
              else if (m == 14) mma(ac[0], ones, cf[h]);
              else mma(ac[1], ones, wf[h][4]);
              if (n == 0) { x_slot = x_slot == 32768 ? 0 : x_slot + 16384; pins(x_slot); }
              if (n == 1) { wt = wt == 92160 ? 0 : wt + 10240; pins(wt); }
              if (n == 2) { xn[0] = xr + x_slot; wn[0] = wa0 + wt; pin(xn[0]); pin(wn[0]); }
              if (n == 4) { xn[1] = xn[0] ^ 64; wn[1] = wn[0] ^ 64; pin(xn[1]); pin(wn[1]); }
              if (n == 6 && p) { --left; pins(left); }
              if (n >= 10 && n < 26) rd(p ^ 1, n - 10);
              sched();
            }
            lgk0();
          }
        } while (left);
        settle();
        if (q == 0) { *(LAS float*)(L + L_CS1 + (16 * w + c) * 4) = ac[0][0]; *(LAS float*)(L + L_CS1 + (64 + c) * 4) = ac[1][0]; }
#pragma unroll
        for (int t = 0; t < 2; ++t) {
          const float s = ((c & 2) ? ((c & 1) ? as[t][3] : as[t][2]) : ((c & 1) ? as[t][1] : as[t][0])) * (1.f / 640.f);
          const float ss = ((c & 2) ? ((c & 1) ? aq[t][3] : aq[t][2]) : ((c & 1) ? aq[t][1] : aq[t][0])) * (1.f / 640.f);
          const float r = __builtin_amdgcn_rsqf(fmaxf(ss - s * s, 0.f) + 1e-5f);
          if ((c >> 2) == q) { *(LAS float*)(L + L_MEAN + (32 * w + 16 * t + c) * 4) = -s; *(LAS float*)(L + L_RSTD + (32 * w + 16 * t + c) * 4) = k.mat ? -r : r; }
        }
        bar();
        tables();
      } else {
#define KS(s) (((s) + 12) % 20)
        unsigned wb[2][2] = {{L_D + rowrd, L_D + rowrd + 51200}, {L_D + (rowrd ^ 64), L_D + (rowrd ^ 64) + 51200}};
        unsigned xv[2] = {OFF_XT + XT_BUF + (8 * k.P1 + 2 * w) * 20480 + lane * 16, 0}; xv[1] = xv[0] + 20480;
        pin(wb[0][0]); pin(wb[0][1]); pin(wb[1][0]); pin(wb[1][1]); pin(xv[0]); pin(xv[1]);
        constexpr int R = AHEAD + 2;
        bf16x8 xa[R];
        auto req = [&](int l) { const int ks = KS(2 * (l >> 2) + (l & 1)); if (l < 40) xa[l % R] = ld16(rw, xv[(l >> 1) & 1] + (ks & 3) * 1024, (ks >> 2) * 4096); };
        auto rd = [&](int d, int n) { const int s = 2 * d + (n & 1); wf[s & 3][n >> 1] = lds16(L, wb[s & 1][KS(s) / 10] + ((KS(s) >> 1) % 5) * 10240 + (n >> 1) * 2048); };
        bar();
#pragma unroll
        for (int l = 0; l < AHEAD; ++l) { req(l); sched(); }
#pragma unroll
        for (int n = 0; n < 10; ++n) rd(0, n);
        lgk0();
#pragma unroll
        for (int d = 0; d < 10; ++d) {
          if (d == 8) barv();
          if (d == 9) { tables(); sched(); }
#pragma unroll
          for (int n = 0; n < 20; ++n) {
            const int m = n >> 1, t = m / 5, i = t ? 9 - m : m, e = (n ^ m) & 1;
            if (d == 0 && !(n & 1)) mma0(acc[t][i], xa[(2 * t + e) % R], wf[e][i]); else mma(acc[t][i], xa[(4 * d + 2 * t + e) % R], wf[(2 * d + e) & 3][i]);
            if (n % 5 == 4) req(4 * d + n / 5 + AHEAD);
            else if (d < 9 && n < 13) { const int j = n - n / 5; if (j < 10) rd(d + 1, j); }
            if (n == 18 && d < 9) lgk0();
            sched();
          }
        }
        settle();
      }
#pragma unroll
      for (int i = 0; i < 5; ++i) {
        const float cs = cs1[i];
        const f32x4 a = (mean[0] * cs + acc[0][i]) * rstd[0], bb = (mean[1] * cs + acc[1][i]) * rstd[1];
        st16(rw, swap_pair(pk(a[0], a[1]), pk(a[2], a[3]), pk(bb[0], bb[1]), pk(bb[2], bb[3])), vo + i * 1024);
      }
      if (layer == 0 && (k.P & 1) && w < 2) {
        const int j = (k.b >> 7) * 64 + (k.b & 63), g3 = (j * 2731) >> 16;
        const f32x4 t = *(LAS f32x4*)(L + L_CP + (w * 64 + lane) * 16);
        if (j < 120) __builtin_amdgcn_raw_buffer_store_b32(__float_as_uint((t[0] + t[1]) + (t[2] + t[3])), rw, lane * 4, OFF_CS + (g3 * 3072 + w * 1536 + 64 * (j - g3 * 24)) * 4, SC1);
      }
      drain_bar();
    }
    {
      PHASE_LANE();
      const int wm = w >> 1, wn = w & 1;
      f32x4 acc[4][5];
      bf16x8 fa[2][4], fl[2][2], fb[2][2][4];
#pragma unroll
      for (int t = 0; t < 4; ++t)
#pragma unroll
        for (int i = 0; i < 5; ++i) { acc[t][i] = (f32x4){0.f, 0.f, 0.f, 0.f}; pin(acc[t][i]); }
      unsigned wb = L_WR + wm * 8192 + rowrd, ab = L_D + wn * 5120 + lane * 16;
      unsigned ws_slot = 65536, ab_slot = 20480;
      unsigned ev[2], ev8[2], en = w == k.kq ? 0 : WS_END;
      ev[0] = OFF_EX + layer * EX_BUF + ((k.P * 16 + w * 4 + k.np) * 3 + ((k.kq - w) & 3) - 1) * 10240 + lane * 16; ev[1] = ev[0] + 5120;
      ev8[0] = ev[0] - lane * 8 + 2048; ev8[1] = ev8[0] + 5120;
      unsigned lo = L_D + 20480 + w * 10240 + lane * 16, lo8 = lo - lane * 8 + 2048;
      pin(lo); pin(lo8);
      pin(wb); pin(ab); pin(ev[0]); pin(ev[1]); pin(ev8[0]); pin(ev8[1]); pins(en);
      const rsrc_t re = mk(k.ws, en);
      bar();
#pragma unroll
      for (int n = 0; n < 8; ++n) fb[0][n & 1][n >> 1] = lds16(L, (wb ^ ((n & 1) * 64)) + 49152 + (n >> 1) * 2048);
      unsigned an = ab + 20480, w0 = wb + 65536, w1 = w0 ^ 64;
      pin(an); pin(w0); pin(w1);
      bar();
#pragma unroll
      for (int n = 0; n < 10; ++n) { if (n < 8) fa[n & 1][n >> 1] = lds16(L, ab + (n & 1) * 10240 + (n >> 1) * 1024); else fl[0][n & 1] = lds16(L, ab + (n & 1) * 10240 + 4096); }
      lgk0();
      int left = 7;
      auto tile = [&](int p, bool last) {
#pragma unroll
        for (int n = 0; n < 40; ++n) {
          const int m = n >> 1, i = m >> 2, t = (i & 1) ? 3 - (m & 3) : m & 3, kk = (n ^ m) & 1, e = n & 1;
          if (n == 16) barv();
          mma(acc[t][i], i < 4 ? fa[kk][i & 3] : fl[p][kk], fb[p][kk][t]);
          if (n < 8) fb[p ^ 1][e][m] = lds16(L, (e ? w1 : w0) + m * 2048);
          else if (n >= 16 && n < 20) fa[e][m - 8] = lds16(L, an + e * 10240 + (m - 8) * 1024);
          else if (m == 10) fl[p ^ 1][e] = lds16(L, an + e * 10240 + 4096);
          else if (m == 12 || m == 16) fa[e][m / 4 - 1] = lds16(L, an + e * 10240 + (m / 4 - 1) * 1024);
          else if (!last) {
            if (n == 8) { ws_slot = ws_slot == 81920 ? 0 : ws_slot + 16384; pins(ws_slot); }
            if (n == 9) { ab_slot = ab_slot == 40960 ? 0 : ab_slot + 20480; pins(ab_slot); }
            if (n == 10) { w0 = wb + ws_slot; pin(w0); }
            if (n == 11) { w1 = w0 ^ 64; pin(w1); }
            if (n == 34) { an = ab + ab_slot; pin(an); if (p) { --left; pins(left); } }
          }
          sched();
        }
        lgk0();
      };
#pragma unroll 1
      do { tile(0, false); tile(1, false); } while (left);
      tile(0, true);
      {
        unsigned o[10];
        auto send = [&](int t, int j) {
          if (j < 5) { o[2 * j] = pk(acc[t][j][0], acc[t][j][1]); o[2 * j + 1] = pk(acc[t][j][2], acc[t][j][3]); }
          if (j == 5) st16(re, (u32x4){o[0], o[1], o[2], o[3]}, ev[t >> 1] + (t & 1) * 2560);
          if (j == 6) { *(LAS u32x4*)(L + lo + t * 2560) = (u32x4){o[0], o[1], o[2], o[3]}; *(LAS u32x2*)(L + lo8 + t * 2560) = (u32x2){o[8], o[9]}; }
          if (j == 7) st16(re, (u32x4){o[4], o[5], o[6], o[7]}, ev[t >> 1] + (t & 1) * 2560 + 1024);
          if (j == 8) { __builtin_amdgcn_raw_buffer_store_b64((u32x2){o[8], o[9]}, re, ev8[t >> 1] + (t & 1) * 2560, 0, SC1); *(LAS u32x4*)(L + lo + t * 2560 + 1024) = (u32x4){o[4], o[5], o[6], o[7]}; }
        };
#pragma unroll
        for (int n = 0; n < 40; ++n) {
          const int t = n / 10, i = (n % 10) >> 1, kk = (n ^ i) & 1;
          mma(acc[t][i], i < 4 ? fa[kk][i & 3] : fl[1][kk], fb[1][kk][t]);
          if (t) send(t - 1, n % 10);
          sched();
        }
#pragma unroll
        for (int j = 0; j < 9; ++j) { send(3, j); sched(); }
      }
      drain_bar();
      if (layer == 0) {
        const rsrc_t rx = mk(x0, 2048 * 640 * 2);
#pragma unroll
        for (int j = 0; j < 3; ++j) {
          const int u = 64 * j + lane, r = (u * 205) >> 11;
          dma<0, 0>(rx, L + L_D + w * 3072 + j * 1024, r * 1120 + u * 16 + (j == 2 ? (lane & 32) << 25 : 0), 16 * tfo * 1280 + cto * 160);
        }
        __builtin_amdgcn_s_waitcnt(0);
        asm volatile("" ::: "memory");
#pragma unroll
        for (int i = 0; i < 5; ++i) { const u32x2 t = *(LAS u32x2*)(L + L_D + w * 3072 + c * 160 + 8 * q + 32 * i); res[2 * i] = t[0]; res[2 * i + 1] = t[1]; }
      }
      f32x4 v[5];
      {
        LAS char* t = L + L_D + 20480 + k.kq * 10240 + w * 2560;
        const u32x4 a = *(LAS u32x4*)(t + lane * 16), bb = *(LAS u32x4*)(t + 1024 + lane * 16); const u32x2 d = *(LAS u32x2*)(t + 2048 + lane * 8);
        v[0] = unpk(a[0], a[1]); v[1] = unpk(a[2], a[3]); v[2] = unpk(bb[0], bb[1]); v[3] = unpk(bb[2], bb[3]); v[4] = unpk(d[0], d[1]);
#pragma unroll
        for (int i = 0; i < 5; ++i) { v[i] += unpk(res[2 * i], res[2 * i + 1]); pin(v[i]); }
      }
      unsigned rv = OFF_EX + layer * EX_BUF + (k.P * 16 + k.kq * 4 + k.np) * 30720 + w * 2560 + lane * 16, rv8 = rv - lane * 8;
      pin(rv); pin(rv8);
      X_ADDR(2 * layer);
      bar();
      u32x4 e[3][2]; u32x2 e8[3];
#pragma unroll
      for (int j = 0; j < 3; ++j)
#pragma unroll
        for (int n = 0; n < 3; ++n) {
          if (j < 2) e[n][j] = __builtin_amdgcn_raw_buffer_load_b128(rw, rv + j * 1024, n * 10240, SC1);
          else e8[n] = __builtin_amdgcn_raw_buffer_load_b64(rw, rv8 + 2048, n * 10240, SC1);
          sched();
        }
      float s = 0.f, ss = 0.f;
#pragma unroll
      for (int j = 0; j < 3; ++j) {
#pragma unroll
        for (int n = 0; n < 3; ++n) {
          if (j < 2) { v[2 * j] += unpk(e[n][j][0], e[n][j][1]); v[2 * j + 1] += unpk(e[n][j][2], e[n][j][3]); }
          else v[4] += unpk(e8[n][0], e8[n][1]);
        }
        x_part(rw, vx, vx2, j, v, res, s, ss);
        sched();
      }
      x_stats(rw, vs, s, ss);
      drain_bar();
    }
    {
      PHASE_LANE();
      const int wm = w >> 1, wn = w & 1;
      f32x4 acc[4][6];
      bf16x8 fa[2][5], fl[2][2], fb[2][2][4];
#pragma unroll
      for (int t = 0; t < 4; ++t)
#pragma unroll
        for (int i = 0; i < 6; ++i) { acc[t][i] = (f32x4){0.f, 0.f, 0.f, 0.f}; pin(acc[t][i]); }
      unsigned wb = L_WR + wn * 6144 + lane * 16, xb = L_D + wm * 8192 + lane * 16;
      unsigned w_slot = 73728, x_slot = 16384;
      unsigned vo = OFF_HT + layer * HT_BUF + ((8 * k.P + 4 * wm + (q & 1)) * 48 + 3 * (4 * k.kq + k.np)) * 1024 + (6 * wn + (q >> 1)) * 256 + c * 16, vo2 = vo + 98304;
      unsigned ta = (64 * wm + c) * 4, tc = L_CS3 + (48 * wn + 4 * q) * 4;
      unsigned sa = L_D + 49152 + (2 * w + (lane >> 5)) * 1024 + (lane & 31) * 4, sw = (32 * w + 16 * (lane >> 5) + c) * 4;
      pin(wb); pin(xb); pin(vo); pin(vo2); pin(ta); pin(tc); pin(sa); pin(sw);
      bar();
#pragma unroll
      for (int n = 0; n < 12; ++n) { if (n < 10) fa[n & 1][n >> 1] = lds16(L, wb + 49152 + (n & 1) * 12288 + (n >> 1) * 1024); else fl[0][n & 1] = lds16(L, wb + 49152 + (n & 1) * 12288 + 5120); }
      unsigned wn3 = wb + 73728, xn = xb + 16384;
      pin(wn3); pin(xn);
      bar();
#pragma unroll
      for (int n = 0; n < 8; ++n) fb[0][n & 1][n >> 1] = lds16(L, xb + n * 1024);
      lgk0();
      int left = 4;
      auto mm = [&](int p, int n) { const int m = n >> 1, i = m >> 2, t = (i & 1) ? 3 - (m & 3) : m & 3, kk = (n ^ m) & 1; mma(acc[t][i], i < 5 ? fa[kk][i < 5 ? i : 0] : fl[p][kk], fb[p][kk][t]); };
      auto rd = [&](int p, int n) {
        const int m = n >> 1, e = n & 1;
        if (m == 8 || m == 9) fa[e][m - 8] = lds16(L, wn3 + e * 12288 + (m - 8) * 1024);
        else if (m == 10) fl[p ^ 1][e] = lds16(L, wn3 + e * 12288 + 5120);
        else if (m == 12 || m == 16 || m == 20) fa[e][m / 4 - 1] = lds16(L, wn3 + e * 12288 + (m / 4 - 1) * 1024);
        else if (m == 11) fb[p ^ 1][e][0] = lds16(L, xn + e * 1024);
        else if (m >= 13 && m <= 15) fb[p ^ 1][e][m - 12] = lds16(L, xn + e * 1024 + (m - 12) * 2048);
      };
#pragma unroll 1
      do {
#pragma unroll
        for (int p = 0; p < 2; ++p) {
#pragma unroll
          for (int n = 0; n < 48; ++n) {
            if (n == 16) barv();
            mm(p, n); rd(p, n);
            if (n == 34) { x_slot = x_slot == 32768 ? 0 : x_slot + 16384; pins(x_slot); }
            if (n == 35) { w_slot = w_slot == 73728 ? 0 : w_slot + 24576; pins(w_slot); }
            if (n == 42) { wn3 = wb + w_slot; xn = xb + x_slot; pin(wn3); pin(xn); }
            if (n == 43 && p) { --left; pins(left); }
            sched();
          }
          lgk0();
        }
      } while (left);
      float mean[4], rl2[4], sd[4];
      f32x4 cs[6];
      unsigned h[4][2];
      f32x4 o[4];
      auto corr1 = [&](int f, int n) { const int t = n >> 3, i = 2 * f + ((n >> 2) & 1), e = n & 3; if (n < 32) acc[t][i][e] = one(fmaf(cs[i][e], mean[t], acc[t][i][e])); };
      auto glu1 = [&](int n) {
        const int t = n / 15, j = n % 15;
        if (j < 2) { o[t][2 * j] = one(acc[t][0][2 * j] * rl2[t]); o[t][2 * j + 1] = one(acc[t][0][2 * j + 1] * rl2[t]); }
        else if (j < 6) o[t][j - 2] = __builtin_amdgcn_exp2f(o[t][j - 2]);
        else if (j < 8) { o[t][2 * j - 12] = one(fmaf(o[t][2 * j - 12], sd[t], sd[t])); o[t][2 * j - 11] = one(fmaf(o[t][2 * j - 11], sd[t], sd[t])); }
        else if (j < 12) o[t][j - 8] = __builtin_amdgcn_rcpf(o[t][j - 8]);
        else if (j < 14) { o[t][2 * j - 24] = one(o[t][2 * j - 24] * acc[t][1][2 * j - 24]); o[t][2 * j - 23] = one(o[t][2 * j - 23] * acc[t][1][2 * j - 23]); }
        else { h[t][0] = pk(o[t][0], o[t][1]); h[t][1] = pk(o[t][2], o[t][3]); }
      };
      auto glu = [&](int f, int t, int from) {
        if (from < 1) { o[t] = acc[t][2 * f] * rl2[t]; asm("" : "+v"(o[t])); }
        if (from < 2) {
#pragma unroll
          for (int r = 0; r < 4; ++r) o[t][r] = __builtin_amdgcn_exp2f(o[t][r]);
        }
        o[t] = o[t] * sd[t] + sd[t]; asm("" : "+v"(o[t]));
#pragma unroll
        for (int r = 0; r < 4; ++r) o[t][r] = __builtin_amdgcn_rcpf(o[t][r]);
        o[t] *= acc[t][2 * f + 1];
        h[t][0] = pk(o[t][0], o[t][1]); h[t][1] = pk(o[t][2], o[t][3]);
      };
      auto tab2 = [&](float (&d)[4], unsigned off, int j) { d[2 * j] = *(LAS float*)(L + ta + off + 128 * j); d[2 * j + 1] = *(LAS float*)(L + ta + off + 128 * j + 64); };
      float pv[8], s1 = 0.f, s2 = 0.f;
#pragma unroll
      for (int n = 0; n < 26; ++n) {
        if (n == 16) barv();
        mm(0, n); rd(0, n);
        if (n < 2) {
#pragma unroll
          for (int j = 0; j < 4; ++j) pv[4 * n + j] = *(LAS float*)(L + sa + 128 * (4 * n + j));
        }
        if (n == 2) { cs[0] = *(LAS f32x4*)(L + tc); cs[1] = *(LAS f32x4*)(L + tc + 384); }
        if (n == 6) { s1 = one(pv[0] + pv[1]); s2 = one(pv[2] + pv[3]); }
        if (n == 7 || n == 8) { s1 = one(s1 + pv[2 * n - 10]); s2 = one(s2 + pv[2 * n - 9]); }
        if (n == 9) s1 = one(s1 + s2);
        if (n == 10) { s2 = one(s1 * (1.f / 640.f)); s1 = one(s1 * (1.f / 640.f)); }
        if (n == 11) { const auto r = __builtin_amdgcn_permlane16_swap(__float_as_uint(s1), __float_as_uint(s2), false, false); s1 = one(__uint_as_float(r[0])); s2 = one(__uint_as_float(r[1])); }
        if (n == 12) { s2 = one(fmaf(-s1, s1, s2)); s1 = one(-s1); }
        if (n == 13) { *(LAS float*)(L + sw + L_MEAN) = s1; s2 = one(fmaxf(s2, 0.f)); }
        if (n == 14) s2 = one(s2 + 1e-5f);
        if (n == 15) s1 = __builtin_amdgcn_rsqf(s2);
        if (n == 16 || n == 17) tab2(mean, L_MEAN, n & 1);
        if (n == 18) s2 = one(s2 * s1);
        if (n == 19) s1 = one(s1 * -1.44269504089f);
        if (n == 20) *(LAS float*)(L + sw + L_STD) = s2;
        if (n == 21) *(LAS float*)(L + sw + L_RSTD) = s1;
        sched();
      }
#pragma unroll
      for (int n = 26; n < 48; ++n) {
        mm(0, n); rd(0, n);
        if (n < 34) corr1(0, n - 26);
        if (n >= 34 && n < 40) { corr1(0, 2 * n - 60); corr1(0, 2 * n - 59); }
        if (n == 40 || n == 41) corr1(0, n - 20);
        if (n == 42) { cs[2] = *(LAS f32x4*)(L + tc + 64); cs[3] = *(LAS f32x4*)(L + tc + 384 + 64); }
        if (n > 42) { corr1(0, 2 * n - 64); corr1(0, 2 * n - 63); }
        sched();
      }
      barv();
#pragma unroll
      for (int n = 0; n < 48; ++n) {
        mm(1, n);
        if (n < 16) { corr1(1, 2 * n); corr1(1, 2 * n + 1); }
        if (n == 12 || n == 13) tab2(rl2, L_RSTD, n & 1);
        if (n == 14 || n == 15) tab2(sd, L_STD, n & 1);
        if (n == 16 || n == 17) cs[n - 12] = *(LAS f32x4*)(L + tc + 128 + 384 * (n & 1));
        if (n >= 18) glu1(n - 18);
        sched();
      }
      st16(rw, swap_pair(h[0][0], h[0][1], h[1][0], h[1][1]), vo);
      sched();
      glu(0, 2, 0); glu(0, 3, 0);
      st16(rw, swap_pair(h[2][0], h[2][1], h[3][0], h[3][1]), vo2);
      sched();
#pragma unroll
      for (int f = 1; f < 3; ++f)
#pragma unroll
        for (int t = 0; t < 4; t += 2) {
          if (f == 2) { acc[t][4] += cs[4] * mean[t]; acc[t][5] += cs[5] * mean[t]; acc[t + 1][4] += cs[4] * mean[t + 1]; acc[t + 1][5] += cs[5] * mean[t + 1]; }
          glu(f, t, 0); glu(f, t + 1, 0);
          st16(rw, swap_pair(h[t][0], h[t][1], h[t + 1][0], h[t + 1][1]), (t ? vo2 : vo) + f * 512);
          sched();
        }
    }
    {
      PHASE_LANE();
      const int kh = w & 1;
      unsigned wb[3] = {L_W4 + kh * 5120 + lane * 16, 0, 0}; wb[1] = wb[0] + 51200; wb[2] = wb[0] + 102400;
      pin(wb[0]); pin(wb[1]); pin(wb[2]);
      bar();
      constexpr int R = AHEAD + 2;
      bf16x8 ha[R], wf[6][5];
      f32x4 acc[2][5];
      auto rdw = [&](int s, int i) { wf[s % 6][i] = lds16(L, wb[w4off(s) / 51200] + w4off(s) % 51200 + i * 1024); };
#pragma unroll
      for (int n = 0; n < 30; ++n) rdw(n / 5, n % 5);
      lgk0();
      drain_bar();
      sched();
      unsigned hv[2] = {OFF_HT + layer * HT_BUF + tfo * 49152 + kh * 1024 + lane * 16, OFF_HT + layer * HT_BUF + (tfo ^ 1) * 49152 + kh * 1024 + lane * 16};
      unsigned rd = L_RED + w * 5120 + lane * 16, wr = L_RED + (w ^ 1) * 5120 + lane * 16;
      pin(hv[0]); pin(hv[1]); pin(rd); pin(wr);
      X_ADDR(1);
      unsigned ow = L_W4 + 61440 + w * 3072 + c * 160 + 8 * q, orr = L_W4 + 61440 + w * 3072 + lane * 16, vo3[3];
#pragma unroll
      for (int j = 0; j < 3; ++j) { const int u = 64 * j + lane; vo3[j] = 16 * tfo * 1280 + cto * 160 + ((u * 205) >> 11) * 1120 + u * 16 + (j == 2 ? (lane & 32) << 25 : 0); pin(vo3[j]); }
      pin(ow); pin(orr);
#pragma unroll
      for (int i = 0; i < 5; ++i) { acc[0][i] = unpk(res[2 * i], res[2 * i + 1]); pin(acc[0][i]); }
      bar();
      auto req = [&](int l) { if (l < 48) ha[l % R] = ld16(rw, hv[1 - ((l >> 1) & 1)] + (l & 1) * 2048, (l >> 2) * 4096); };
#pragma unroll
      for (int l = 0; l < AHEAD; ++l) { req(l); sched(); }
      f32x4 pt[5];
#pragma unroll
      for (int d = 0; d < 12; ++d) {
        if (d == 4 || d == 9 || d == 10) barv();
#pragma unroll
        for (int n = 0; n < 20; ++n) {
          const int m = n >> 1, v = m / 5, i = v && d < 11 ? 9 - m : m % 5, e = (n ^ m) & 1;
          if (d == 11 && n == 17) barv();
          if (d == 0 && !v && !(n & 1)) mma0(acc[1][i], wf[e][i], ha[e]); else mma(acc[1 - v][i], wf[(2 * d + e) % 6][i], ha[(4 * d + 2 * v + e) % R]);
          if (n % 5 == 4) req(4 * d + n / 5 + AHEAD);
          else if (d >= 2 && d < 11 && n < 13) { const int j = n - n / 5; if (j < 10) rdw(2 * d + 2 + (j & 1), j >> 1); }
          if (n == 18 && d >= 2 && d < 11) lgk0();
          if (d == 11) {
            if (n >= 10 && n < 15) *(LAS f32x4*)(L + wr + (n - 10) * 1024) = acc[1][n - 10];
            if (n >= 17) { pt[2 * n - 34] = *(LAS f32x4*)(L + rd + (2 * n - 34) * 1024); if (n < 19) pt[2 * n - 33] = *(LAS f32x4*)(L + rd + (2 * n - 33) * 1024); }
          }
          sched();
        }
      }
#pragma unroll
      for (int i = 0; i < 5; ++i) { acc[0][i] += pt[i]; pin(acc[0][i]); sched(); }
      if (layer == 1) {
        const rsrc_t ro = mk(out, 2048 * 640 * 2);
#pragma unroll
        for (int i = 0; i < 5; ++i) *(LAS u32x2*)(L + ow + 32 * i) = (u32x2){pk(acc[0][i][0], acc[0][i][1]), pk(acc[0][i][2], acc[0][i][3])};
#pragma unroll
        for (int j = 0; j < 3; ++j) st16(ro, *(LAS u32x4*)(L + orr + j * 1024), vo3[j]);
      } else {
        float s = 0.f, ss = 0.f;
#pragma unroll
        for (int j = 0; j < 3; ++j) x_part(rw, vx, vx2, j, acc[0], res, s, ss);
        x_stats(rw, vs, s, ss);
        bar();
        drain_bar();
      }
    }
  }
}

DI void stats_load(const Ctx& k, rsrc_t rw, int xb, int tf0, u32x2 (&sv)[8]) {
#pragma unroll
  for (int j = 0; j < 8; ++j) sv[j] = __builtin_amdgcn_raw_buffer_load_b64(rw, (k.lane >> 4) * 1024 + (k.lane & 15) * 8 + j * 128, OFF_ST + xb * ST_BUF + tf0 * 1024, SC1);
}
DI void stats_sum(u32x2 (&sv)[8], float (&s)[2]) {
#pragma unroll
  for (int j = 0; j < 8; ++j) pin(sv[j]);
#pragma unroll
  for (int e = 0; e < 2; ++e) {
    s[e] = __uint_as_float(sv[0][e]);
#pragma unroll
    for (int j = 1; j < 8; ++j) s[e] = one(s[e] + __uint_as_float(sv[j][e]));
    s[e] *= 1.f / 640.f; pin(s[e]);
  }
}
DI void stats_table(const Ctx& k, int a, float (&s)[2], float scale) {
  pin(s[0]); pin(s[1]);
  float r[2];
#pragma unroll
  for (int e = 0; e < 2; ++e) {
    const float ss = __builtin_bit_cast(float, __builtin_amdgcn_update_dpp(0, __builtin_bit_cast(int, s[e]), 0x128, 0xf, 0xf, true));
    const float v = one(fmaxf(one(fmaf(-s[e], s[e], ss)), 0.f) + 1e-5f), q = __builtin_amdgcn_rsqf(v);
    r[e] = one(q * scale);
  }
  if (!(k.lane & 8)) {
    const unsigned t = (64 * a + (k.lane & 48) + 2 * (k.lane & 7)) * 4;
    *(LAS f32x2*)(k.lds + L_MEAN + t) = (f32x2){one(-s[0]), one(-s[1])};
    *(LAS f32x2*)(k.lds + L_RSTD + t) = (f32x2){r[0], r[1]};
  }
}

DI void loader(const Ctx& k0, const char* x0, const char* wsp, const char* wdp, int a4) {
  Ctx k = k0;
  const bool isW = a4 < 2;
  LAS char* const L = k.lds;
  const rsrc_t rw = mk(k.ws, WS_END);
  const rsrc_t rs = mk(wsp, 2048 * 2048 * 2);
  auto fresh = [&]() { asm volatile("" : "+v"(k.lane), "+s"(k.P), "+s"(k.kq), "+s"(k.np), "+s"(k.P1), "+s"(k.ct1), "+s"(k.mat), "+s"(a4)); };
#define lane k.lane
#define a (a4 & 1)
#define me (k.P * 16 + k.kq * 4 + k.np)
#define n3 (4 * k.kq + k.np)
#define sw (((lane & 7) ^ (4 * a + (lane >> 4))) * 16)
#define vo2 ((lane >> 3) * 4096 + sw)
#define so2 ((128 * k.P + 8 * a) * 4096 + (k.kq & 1) * 2048)
#define so3 (OFF_W3 + n3 * 20 * 12288 + a * 12288)
#define so4 (OFF_W4 + (2 * k.np + (k.kq & 1)) * 48 * 5120)
  bf16x8 k1[25], k4[3][8];
  u64 tag = 0;
  int rot = 2 * k.np;
  auto ktile = [&](int t) { const int r = (t + rot) & 7; return 4 * (r >> 1) + 2 * (t >> 3) + (r & 1); };
  auto w2 = [&](int t, int slot) {
#pragma unroll
    for (int j = 0; j < 8; ++j) dma<0, 0>(rs, L + L_WR + slot * 16384 + (2 * j + a) * 1024, vo2, so2 + j * 16 * 4096 + ktile(t) * 128);
  };
  auto w2n = [&](int t0, int t1) {
#pragma unroll 1
    for (int t = t0; t < t1; ++t) w2(t, t < 3 ? t + 3 : t - 3);
  };
  auto w3 = [&](int t, int slot) { dma_lin<SC1, 12>(rw, L + L_WR + slot * 24576 + a * 12288, lane * 16, so3 + t * 24576); };
  auto w4 = [&](int ch) { dma_lin<SC1, 15>(rw, L + L_W4 + w4slot(ch) * 30720 + a * 15360, lane * 16, so4 + ch * 30720 + a * 15360); };
  auto push = [&](int ev, int wg0, int wgstep, int n, int slot) {
    if (a4 == 2) {
      unsigned dst = lane < n ? OFF_MB + ev * MB_EV + (wg0 + lane * wgstep) * 128 + slot * 8 : 0x7fffffff;
      pin(dst); pin(tag);
      bar();
      __builtin_amdgcn_raw_buffer_store_b64(__builtin_bit_cast(u32x2, tag), rw, dst, 0, SC1);
    } else bar();
    fresh();
  };
  unsigned budget = SPIN_CAP;
  auto wait_mb = [&](int ev, int wg, int n) {
    GAS u64* src = (GAS u64*)(k.ws + OFF_MB + ev * MB_EV + wg * 128) + (lane & (n - 1));
#pragma unroll 1
    for (; budget; budget = uni(budget - 1)) {
      const u64 v = __hip_atomic_load(src, __ATOMIC_RELAXED, __HIP_MEMORY_SCOPE_AGENT);
      if (__all(v == tag)) break;
      __builtin_amdgcn_s_sleep(1);
    }
  };
  auto poll = [&](int ev, int wg, int n) {
    if (a4 == 2) wait_mb(ev, wg, n);
    drain_bar();
  };
  auto get_tag = [&]() { const char* e = k.ws; pins(e); tag = *(const GAS u64*)e; };
  auto mix_tag = [&]() { pin(tag); tag = tag * 6364136223846793005ull + 1442695040888963407ull; };
  if (!isW) __builtin_amdgcn_s_setprio(1);
  {
    const rsrc_t r1 = isW ? mk(wdp, 640 * 640 * 2) : mk(x0, 2048 * 640 * 2);
    const unsigned v1 = (lane >> 3) * 1280 + sw, s1 = ((isW ? 80 * k.ct1 : 128 * k.P1) + 8 * a) * 1280;
    int slot = 2, wt = rot1(k);
    if ((k.P & 1) && !isW) __builtin_amdgcn_s_sleep(LAG);
#pragma unroll 1
    for (int t = 0; t < 10; ++t) {
      if (isW) {
        if (t >= 3) { waitv<10>(); bar(); }
#pragma unroll
        for (int j = 0; j < 5; ++j) dma<0, 0>(r1, L + L_D + wt * 10240 + (2 * j + a) * 1024, v1, s1 + 16 * j * 1280 + wt * 128);
      } else {
        if (t >= 2) { waitv<8>(); bar(); }
#pragma unroll
        for (int j = 0; j < 8; ++j) dma<0, 0>(r1, L + L_XR + slot * 16384 + (2 * j + a) * 1024, v1, s1 + 16 * j * 1280 + wt * 128);
      }
      slot = slot == 2 ? 0 : slot + 1; wt = wt == 9 ? 0 : wt + 1;
    }
    if (isW) {
      waitv<10>(); bar();
      waitv<5>(); bar(); w2(0, 3);
      waitv<8>(); bar(); w2(1, 4);
      bar();
      w2(2, 5);
    } else {
      get_tag();
      waitv<8>(); bar(); waitv<0>();
      mix_tag(); pin(tag);
      if (!(k.P & 1)) {
#pragma unroll
        for (int j = 0; j < 2; ++j) touch<0>(rs, L + L_JUNK, (lane >> 1) * 4096 + (lane & 1) * 128, (128 * k.P + 64 * a + 32 * j) * 4096 + (k.kq & 1) * 2048 + ktile(8) * 128);
      }
      bar(); bar();
    }
  }
  {
    unsigned kb[2] = {L_D + lane * 16 + ((a4 >> 1) * 10 + a) * 1024, 0}; kb[1] = kb[0] + 61440; pin(kb[0]); pin(kb[1]);
    bar();
#pragma unroll
    for (int j = 0; j < 25; ++j) k1[j] = lds16(L, kb[j / 15] + ((j / 5) % 3) * 20480 + (j % 5) * 2048);
  }
#pragma unroll 1
  for (int layer = 0; layer < 2; ++layer) {
    const int ev0 = layer * EV_PER_LAYER;
    if (layer) {
      poll(ev0 + EV_X, k.P1 * 16 + k.mat * 8 + k.ct1, 16);
      if (!isW) { u32x2 sv[8]; float sm[2]; stats_load(k, rw, 1, 8 * k.P1 + 4 * a, sv); stats_sum(sv, sm); stats_table(k, a, sm, k.mat ? -1.f : 1.f); }
      bar();
      if (isW) w2n(3, 6);
    }
    push(ev0 + EV_AB, (2 * k.mat + (k.P1 >> 3)) * 4 + (k.ct1 >> 1), 16, 16, 8 * (k.P1 & 1) + (k.P1 & 6) + (k.ct1 & 1));
    GAS u64* rtf = (GAS u64*)(k.ws + OFF_MB + EV_RT * MB_EV);
    if (layer == 0 && a4 == 3 && lane == 0) __hip_atomic_store(rtf + k.b, tag, __ATOMIC_RELAXED, __HIP_MEMORY_SCOPE_AGENT);
    if (isW && layer == 0) { get_tag(); w2n(3, 6); mix_tag(); }
    const unsigned soab = OFF_AB + layer * AB_BUF + (((k.kq >> 1) * 4 + k.np) * 64 + 32 * (k.kq & 1)) * 10240;
    unsigned l16 = lane * 16, so0 = soab + a * 10240 + ktile(0) * 20480, la0 = L_D + a * 10240;
    so0 = __builtin_amdgcn_readfirstlane(so0); la0 = __builtin_amdgcn_readfirstlane(la0);
    pin(l16); pins(so0); pins(la0);
    if (a4 == 3 && (k.P & 1) && layer == 0) __builtin_amdgcn_s_sleep(LAG2);
    poll(ev0 + EV_AB, me, 8);
    if (!isW) dma_lin<SC1, 10>(rw, L + la0, l16, so0);
    if (isW) {
      const unsigned sb2 = uni(so2), lb2 = uni(L_WR + a * 1024);
      unsigned v2 = vo2, so = uni(sb2 + ktile(6) * 128), la = uni(lb2 + 49152), t = 7;
      pin(v2);
#pragma unroll 1
      do {
        waitv<32>(); bar();
#pragma unroll
        for (int j = 0; j < 2; ++j) dma<0, 0>(rs, L + la + j * 2048, v2, so + j * 16 * 4096);
        sched();
        const unsigned sn = uni(sb2 + ktile(t) * 128), ln = uni(la == lb2 + 81920 ? lb2 : la + 16384);
        t = uni(t + 1);
        sched();
#pragma unroll
        for (int j = 2; j < 8; ++j) dma<0, 0>(rs, L + la + j * 2048, v2, so + j * 16 * 4096);
        so = sn; la = ln;
      } while (t != 17);
      const bool chk = layer == 0 && a == 0;
      u64 f[4];
      if (chk) {
#pragma unroll
        for (int j = 0; j < 4; ++j) f[j] = __hip_atomic_load(rtf + lane + 64 * j, __ATOMIC_RELAXED, __HIP_MEMORY_SCOPE_AGENT);
      }
      waitv<32>(); bar();
      if (chk) {
#pragma unroll
        for (int j = 0; j < 4; ++j) pin(f[j]);
#pragma unroll 1
        for (unsigned spins = 0; spins < SPIN_CAP && !__all(f[0] == tag && f[1] == tag && f[2] == tag && f[3] == tag); ++spins) {
#pragma unroll
          for (int j = 0; j < 4; ++j) f[j] = __hip_atomic_load(rtf + lane + 64 * j, __ATOMIC_RELAXED, __HIP_MEMORY_SCOPE_AGENT);
        }
      }
      waitv<0>(); bar();
      w3(3, 1); bar(); bar(); w3(0, 2); bar();
      w3(1, 3); bar(); w3(2, 0);
    } else {
      const unsigned sba = uni(soab + a * 10240);
      dma_lin<SC1, 10>(rw, L + la0 + 20480, l16, sba + ktile(1) * 20480);
      unsigned so = uni(sba + ktile(2) * 20480), la = uni(la0 + 40960), t = 3;
      auto step = [&]() {
        bar();
        dma_lin<SC1, 4>(rw, L + la, l16, so);
        sched();
        const unsigned sn = uni(sba + ktile(t) * 20480), ln = uni(la == la0 + 40960 ? la0 : la + 20480);
        t = uni(t + 1);
        sched();
        dma_lin<SC1, 6>(rw, L + la + 4096, l16, so + 4096);
        so = sn; la = ln;
      };
#pragma unroll 1
      do { waitv<10>(); step(); } while (t != 8);
      unsigned ja = L_JUNK + 256 + layer * 256 + a * 128;
      if (lane < 8) dma<SC1, 0>(rw, L + ja, l16, OFF_MB + (ev0 + EV_AB) * MB_EV + me * 128);
      ja += (lane & 15) * 8; pin(ja);
      waitv<11>(); step();
      waitv<10>();
      if (!__all(*(LAS u64*)(L + ja) == tag)) wait_mb(ev0 + EV_AB, me, 16);
      step();
#pragma unroll 1
      do { waitv<10>(); step(); } while (t != 17);
      waitv<10>(); bar();
      waitv<0>(); bar();
    }
    auto k4load = [&](int n) {
#pragma unroll
      for (int j = 0; j < 8; ++j) k4[n][j] = ld16(rw, lane * 16 + a4 * 1024, so4 + (5 + n) * 30720 + j * 4096);
    };
    push(ev0 + EV_EX, k.P * 16 + k.np, 4, 4, k.kq);
    if (layer == 0) k4load(0);
    poll(ev0 + EV_EX, me, 4);
    push(ev0 + EV_X1, k.P * 16, 1, 16, n3);
    if (layer == 0) {
      u32x2 cv[5];
      if (isW) {
#pragma unroll
        for (int j = 0; j < 5; ++j) cv[j] = __builtin_amdgcn_raw_buffer_load_b64(rw, lane * 8, OFF_CS + (j * 3072 + a * 1536 + 96 * n3) * 4, SC1);
      }
      k4load(1);
      if (isW) {
        sched();
        f32x2 s = {0.f, 0.f};
#pragma unroll
        for (int j = 0; j < 5; ++j) s += __builtin_bit_cast(f32x2, cv[j]);
        if (lane < 48) *(LAS f32x2*)(L + L_CS3 + (96 * a + 2 * lane) * 4) = s;
      }
    }
    l16 = lane * 16; so0 = OFF_XT + 2 * layer * XT_BUF + (8 * k.P + 4 * a) * 20480; la0 = L_D + a * 8192;
    so0 = __builtin_amdgcn_readfirstlane(so0); la0 = __builtin_amdgcn_readfirstlane(la0);
    pin(l16); pins(so0); pins(la0);
    poll(ev0 + EV_X1, me, 16);
    if (!isW) {
#pragma unroll
      for (int j = 0; j < 4; ++j) dma_lin<SC1, 2>(rw, L + la0 + j * 2048, l16, so0 + j * 20480);
    }
    if (isW) {
      bar();
      __builtin_amdgcn_s_sleep(3);
      dma_lin<SC1, 4>(rw, L + L_D + 49152 + a * 4096, lane * 16, OFF_ST + 2 * layer * ST_BUF + (8 * k.P + 4 * a) * 1024);
      const unsigned lb3 = uni(L_WR + a * 12288), se = uni(so3 + 10 * 24576);
      unsigned so = uni(so3 + 4 * 24576), la = uni(lb3 + 49152);
#pragma unroll 1
      do {
        waitv<24>(); bar();
        dma_lin<SC1, 4>(rw, L + la, l16, so);
        sched();
        const unsigned sn = uni(so + 24576), ln = uni(la == lb3 + 73728 ? lb3 : la + 24576);
        sched();
        dma_lin<SC1, 8>(rw, L + la + 4096, l16, so + 4096);
        so = sn; la = ln;
      } while (so != se);
      waitv<24>(); bar();
      waitv<12>(); bar(); waitv<0>(); bar();
      bar();
      w4(2); w4(3); w4(4);
    } else {
#pragma unroll
      for (int j = 0; j < 4; ++j) dma_lin<SC1, 2>(rw, L + la0 + 16384 + j * 2048, l16, so0 + 2048 + j * 20480);
      unsigned so = uni(so0 + 4096), la = uni(la0 + 32768);
#pragma unroll 1
      do {
        waitv<8>(); bar();
        dma_lin<SC1, 2>(rw, L + la, l16, so);
        sched();
        const unsigned sn = uni(so + 2048), ln = uni(la == la0 + 32768 ? la0 : la + 16384);
        sched();
#pragma unroll
        for (int j = 1; j < 4; ++j) dma_lin<SC1, 2>(rw, L + la + j * 2048, l16, so + j * 20480);
        so = sn; la = ln;
      } while (so != so0 + 20480);
      waitv<8>(); bar();

      w4(0);
      waitv<15>(); bar();

      w4(1);
      bar();

      waitv<0>();
    }
    bar();
    push(ev0 + EV_H, k.P * 16, 1, 16, n3);
    if (layer == 0) k4load(2);
    auto k4put = [&](int n, bool paced) {
      unsigned kb = L_W4 + w4slot(5 + n) * 30720 + a4 * 1024 + lane * 16; pin(kb);
#pragma unroll
      for (int j = 0; j < 8; ++j) if (j < 7 || isW) { *(LAS bf16x8*)(L + kb + j * 4096) = k4[n][j]; if (paced) __builtin_amdgcn_s_sleep(2); }
    };
    k4put(0, false); k4put(1, false);
    poll(ev0 + EV_H, me, 16);
    bar();
    k4put(2, true);
    bar(); bar();
    rot = 0;
    if (isW && layer == 0) w2n(0, 2);
    bar();
    if (layer == 0) {
      if (isW) w2n(2, 3);
      unsigned kb[2] = {L_D + lane * 16 + ((a4 >> 1) * 10 + a) * 1024, 0}; kb[1] = kb[0] + 61440; pin(kb[0]); pin(kb[1]);
      bar();
#pragma unroll
      for (int j = 0; j < 7; ++j) *(LAS bf16x8*)(L + kb[j / 15] + ((j / 5) % 3) * 20480 + (j % 5) * 2048) = k1[j];
      push(EV_PER_LAYER + EV_X, k.P * 16, 1, 16, n3);
#pragma unroll
      for (int j = 7; j < 25; ++j) *(LAS bf16x8*)(L + kb[j / 15] + ((j / 5) % 3) * 20480 + (j % 5) * 2048) = k1[j];
    }
  }
  if (k.b == 0 && a4 == 2 && lane == 0) *(GAS u64*)k.ws = tag;
#undef lane
#undef a
#undef me
#undef n3
#undef sw
#undef vo2
#undef so2
#undef so3
#undef so4
}

__global__ void __launch_bounds__(512, 2) fnet_glu_kernel(const char* x0, const char* wsr, const char* wsi, const char* wdr, const char* wdi, const char* wg, const char* wu, const char* wdn, char* out, char* ws) {
  __shared__ __attribute__((aligned(16))) char smem[163840];
  Ctx k;
  k.lds = (LAS char*)smem; k.lane = threadIdx.x & 63; k.b = blockIdx.x;
  const int w = __builtin_amdgcn_readfirstlane(threadIdx.x >> 6);
  if (w < 4 || w == 7) {
    const char* pc; asm volatile("s_getpc_b64 %0" : "=s"(pc));
    touch<0>(mk(pc, CODE_BYTES), k.lds + L_JUNK, (w < 4 ? threadIdx.x : threadIdx.x - 192) * 128, 0);
  }
  asm volatile("" : "+s"(x0), "+s"(wsr), "+s"(wsi), "+s"(wdr), "+s"(wdi), "+s"(wg), "+s"(wu), "+s"(wdn), "+s"(out), "+s"(ws));
  k.ws = ws;
  const int x = k.b & 7, i = k.b >> 3;
  k.kq = x >> 1; k.P = 8 * (x & 1) + 4 * (i >> 4) + 2 * ((i >> 2) & 1) + ((i >> 3) & 1); k.np = i & 3;
  k.P1 = 4 * (x >> 1) + (i >> 3); k.mat = x & 1; k.ct1 = i & 7;
  if (w < 4) consumer(k, x0, wg, wu, wdn, out, w);
  else loader(k, x0, (x >> 2) ? wsi : wsr, (x & 1) ? wdi : wdr, w - 4);
}
}

extern "C" void kernel_launch(void* const* d_in, const int* in_sizes, int n_in, void* d_out, int out_size, void* d_ws, size_t ws_size, hipStream_t stream) {
  fnet_glu_kernel<<<256, 512, 0, stream>>>((const char*)d_in[0], (const char*)d_in[1], (const char*)d_in[2], (const char*)d_in[3], (const char*)d_in[4],
                                           (const char*)d_in[6], (const char*)d_in[7], (const char*)d_in[8], (char*)d_out, (char*)d_ws);
}
```

```cpp
#include <hip/hip_runtime.h>

typedef unsigned long long u64;
typedef short bf16x8 __attribute__((ext_vector_type(8)));
typedef short s16x4 __attribute__((ext_vector_type(4)));
typedef float f32x4 __attribute__((ext_vector_type(4)));
typedef float f32x2 __attribute__((ext_vector_type(2)));
typedef unsigned u32x4 __attribute__((ext_vector_type(4)));
typedef unsigned u32x2 __attribute__((ext_vector_type(2)));
typedef __bf16 bf2 __attribute__((ext_vector_type(2)));
#define LAS __attribute__((address_space(3)))
#define GAS __attribute__((address_space(1)))
#define DI __device__ __forceinline__
typedef __amdgpu_buffer_rsrc_t rsrc_t;

namespace {
constexpr int AHEAD = 10;
constexpr int LAG = 12, LAG2 = 34;
constexpr unsigned CODE_BYTES = 34944;
constexpr unsigned OFF_MB = 4096, MB_EV = 256 * 128;
constexpr int EV_X = 0, EV_AB = 1, EV_EX = 2, EV_X1 = 3, EV_H = 4, EV_PER_LAYER = 5, EV_RT = 0;
constexpr unsigned OFF_CS = 0x60000;
constexpr unsigned OFF_ST = 0x80000, ST_BUF = 8 * 2048 * 8;
constexpr unsigned OFF_XT = 0x100000, XT_BUF = 2048 * 640 * 2;
constexpr unsigned OFF_AB = OFF_XT + 3 * XT_BUF, AB_BUF = 2048 * 1280 * 2;
constexpr unsigned OFF_HT = OFF_AB + 2 * AB_BUF, HT_BUF = 2048 * 1536 * 2;
constexpr unsigned OFF_EX = OFF_HT + 2 * HT_BUF, EX_BUF = 256 * 12 * 2560;
constexpr unsigned OFF_W3 = OFF_EX + 2 * EX_BUF;
constexpr unsigned OFF_W4 = OFF_W3 + 16 * 20 * 12288;
constexpr unsigned WS_END = OFF_W4 + 8 * 48 * 5120;
static_assert(OFF_MB + 10 * MB_EV <= OFF_CS && OFF_CS + 5 * 3072 * 4 <= OFF_ST && OFF_ST + 3 * ST_BUF <= OFF_XT && WS_END <= 0x4000000, "ws map");
constexpr unsigned L_MEAN = 0, L_RSTD = 512, L_CS1 = 1024, L_STD = 1536, L_CS3 = 2304, L_JUNK = 3072;
constexpr unsigned L_D = 4096;
constexpr unsigned L_WR = L_D + 61440, L_W4 = L_D + 6144, L_CP = L_D + 102400, L_XR = L_D + 110592, L_RED = L_W4 + 30720;
constexpr int w4slot(int ch) { return ch == 4 ? 4 : ((ch < 5 ? ch : ch - 5) + 2) & 3; }
constexpr int w4off(int s) { return w4slot(s / 3) * 30720 + (s % 3) * 10240; }
constexpr int SC1 = 16;
constexpr unsigned SPIN_CAP = 1u << 21;

DI rsrc_t mk(const void* p, unsigned bytes) { return __builtin_amdgcn_make_buffer_rsrc((void*)p, 0, bytes, 0x00020000); }
template <int N> DI void waitv() { asm volatile("s_waitcnt vmcnt(%0)" ::"n"(N) : "memory"); }
DI void bar() { asm volatile("s_waitcnt lgkmcnt(0)\n\ts_barrier" ::: "memory"); }
DI void lgk0() { __builtin_amdgcn_sched_barrier(0); __builtin_amdgcn_s_waitcnt(0xC07F); __builtin_amdgcn_sched_barrier(0); }
DI void barv() { lgk0(); asm volatile("s_barrier" ::: "memory"); __builtin_amdgcn_sched_barrier(0); }
DI void drain_bar() {
  asm volatile("s_waitcnt vmcnt(0) lgkmcnt(0)\n\ts_barrier" ::: "memory");
  __builtin_amdgcn_s_waitcnt(0);
  asm volatile("" ::: "memory");
}
DI void sched() { __builtin_amdgcn_sched_barrier(0); }
template <class T> DI void pin(T& x) { asm volatile("" : "+v"(x)); }
template <class T> DI void pins(T& x) { asm volatile("" : "+s"(x)); }
template <int AUX, int IMM> DI void dma(rsrc_t r, LAS char* l, unsigned vo, unsigned so) { __builtin_amdgcn_raw_ptr_buffer_load_lds(r, (LAS void*)l, 16, vo, so, IMM, AUX); }
template <int AUX> DI void touch(rsrc_t r, LAS char* l, unsigned vo, unsigned so) { __builtin_amdgcn_raw_ptr_buffer_load_lds(r, (LAS void*)l, 4, vo, so, 0, AUX); }
template <int AUX, int N> DI void dma_lin(rsrc_t r, LAS char* l, unsigned vo, unsigned so) {
#pragma unroll
  for (int j = 0; j < N; j += 4) {
    dma<AUX, 0>(r, l + j * 1024, vo, so + j * 1024);
    if (j + 1 < N) dma<AUX, 1024>(r, l + j * 1024, vo, so + j * 1024);
    if (j + 2 < N) dma<AUX, 2048>(r, l + j * 1024, vo, so + j * 1024);
    if (j + 3 < N) dma<AUX, 3072>(r, l + j * 1024, vo, so + j * 1024);
  }
}
DI unsigned uni(unsigned x) { x = __builtin_amdgcn_readfirstlane(x); asm volatile("" : "+s"(x)); return x; }
DI bf16x8 ld16(rsrc_t r, unsigned vo, unsigned so) { return __builtin_bit_cast(bf16x8, __builtin_amdgcn_raw_buffer_load_b128(r, vo, so, SC1)); }
DI void st16(rsrc_t r, u32x4 v, unsigned vo) { __builtin_amdgcn_raw_buffer_store_b128(v, r, vo, 0, SC1); }
DI bf16x8 lds16(LAS char* l, unsigned a) { return *(LAS bf16x8*)(l + a); }
DI unsigned pk(float lo, float hi) { f32x2 v = {lo, hi}; return __builtin_bit_cast(unsigned, __builtin_convertvector(v, bf2)); }
DI float blo(unsigned u) { return __uint_as_float(u << 16); }
DI float bhi(unsigned u) { return __uint_as_float(u & 0xffff0000u); }
DI f32x4 unpk(unsigned a, unsigned b) { return (f32x4){blo(a), bhi(a), blo(b), bhi(b)}; }
DI f32x4 mfma(bf16x8 a, bf16x8 b, f32x4 c) { return __builtin_amdgcn_mfma_f32_16x16x32_bf16(a, b, c, 0, 0, 0); }
DI void mma(f32x4& c, const bf16x8& a, const bf16x8& b) { asm volatile("v_mfma_f32_16x16x32_bf16 %0, %1, %2, %0" : "+v"(c) : "v"(a), "v"(b)); __builtin_amdgcn_sched_barrier(0); }
DI void mma0(f32x4& c, const bf16x8& a, const bf16x8& b) { asm volatile("v_mfma_f32_16x16x32_bf16 %0, %1, %2, 0" : "=&v"(c) : "v"(a), "v"(b)); __builtin_amdgcn_sched_barrier(0); }
DI void settle() { asm volatile("s_nop 7"); __builtin_amdgcn_sched_barrier(0); }
DI float dot2(unsigned a, unsigned b, float s) { return __builtin_amdgcn_fdot2_f32_bf16(__builtin_bit_cast(bf2, a), __builtin_bit_cast(bf2, b), s, false); }
DI float one(float v) { asm("" : "+v"(v)); return v; }
DI u32x4 swap_pair(unsigned a0, unsigned a1, unsigned b0, unsigned b1) {
  const auto r0 = __builtin_amdgcn_permlane16_swap(a0, b0, false, false);
  const auto r1 = __builtin_amdgcn_permlane16_swap(a1, b1, false, false);
  return (u32x4){r0[0], r1[0], r0[1], r1[1]};
}

struct Ctx {
  char* ws; LAS char* lds; int lane, b;
  int kq, P, np;
  int P1, mat, ct1;
};

DI u32x4 x_pack(int j, const f32x4 (&v)[5], unsigned (&res)[10]) {
  const int i1 = j < 2 ? 2 * j + 1 : 4;
#pragma unroll
  for (int i = 2 * j; i <= i1; ++i) { res[2 * i] = pk(v[i][0], v[i][1]); res[2 * i + 1] = pk(v[i][2], v[i][3]); }
  return swap_pair(res[4 * j], res[4 * j + 1], res[2 * i1], res[2 * i1 + 1]);
}
DI void x_part(rsrc_t rw, unsigned vx, unsigned vx2, int j, const f32x4 (&v)[5], unsigned (&res)[10], float& s, float& ss) {
  const u32x4 o = x_pack(j, v, res);
  st16(rw, o, j < 2 ? vx + j * 1024 : vx2);
#pragma unroll
  for (int e = 4 * j; e < (j < 2 ? 4 * j + 4 : 10); ++e) { s = dot2(res[e], 0x3f803f80u, s); ss = dot2(res[e], res[e], ss); }
  pin(s); pin(ss);
}
DI void x_stats(rsrc_t rw, unsigned vs, float s, float ss) {
  const auto r = __builtin_amdgcn_permlane16_swap(__float_as_uint(s), __float_as_uint(ss), false, false);
  const unsigned t = __float_as_uint(__uint_as_float(r[0]) + __uint_as_float(r[1]));
  const auto r2 = __builtin_amdgcn_permlane32_swap(t, t, false, false);
  const float u = __uint_as_float(r2[0]) + __uint_as_float(r2[1]);
  __builtin_amdgcn_raw_buffer_store_b32(__float_as_uint(u), rw, vs, 0, SC1);
}

DI int rot1(const Ctx& k) { const int r = ((k.ct1 * 5) >> 2) + (((k.P1 & 3) * 5) >> 1); return r >= 10 ? r - 10 : r; }

#define PHASE_LANE() Ctx k = k0; int w = w0; asm volatile("" : "+v"(k.lane), "+s"(w), "+s"(k.P), "+s"(k.kq), "+s"(k.np), "+s"(k.P1), "+s"(k.ct1), "+s"(k.mat)); \
  const int tfo = 8 * k.P + 4 * (k.kq >> 1) + w, cto = 2 * k.np + (k.kq & 1); (void)tfo; (void)cto; \
  const int lane = k.lane, q = lane >> 4, c = lane & 15, qs = 2 * (q & 1) + (q >> 1); (void)qs; \
  const unsigned rowrd = (c >> 3) * 1024 + (c & 7) * 128 + ((q ^ (c >> 1)) * 16); (void)rowrd
#define X_ADDR(xb) unsigned vx = OFF_XT + (xb) * XT_BUF + tfo * 20480 + (10 * cto + qs) * 256 + c * 16, vx2 = vx + 2048 + ((lane & 16) << 26), \
  vs = OFF_ST + (xb) * ST_BUF + tfo * 1024 + cto * 128 + lane * 4 + ((lane & 32) << 25); pin(vx); pin(vx2); pin(vs)

DI void consumer(const Ctx& k0, const char* x0, const char* wg, const char* wu, const char* wdn, char* out, const int w0) {
  LAS char* const L = k0.lds;
  const rsrc_t rw = mk(k0.ws, WS_END);
  unsigned res[10];
  __builtin_amdgcn_s_setprio(2);
  auto retile = [&](const Ctx& k0, const int w) {
    int e = ((k0.b >> 7) * 64 + (k0.b & 63)) * 4 + w;
    pins(e);
    const int lane = k0.lane;
    const int g3 = ((e >> 2) * 2731) >> 16, ks3 = 4 * g3 + w, st3 = (e >> 2) - g3 * 24, ks4 = (e * 6554) >> 16, st4 = e - ks4 * 10;
    LAS char* t[3];
#pragma unroll
    for (int m = 0; m < 3; ++m) { const int u = rot1(k0) + (m < 2 ? 4 + w : 8 + (w >> 1)); t[m] = L + L_D + (u >= 10 ? u - 10 : u) * 10240 + (m < 2 ? m : w & 1) * 4096; }
    __builtin_amdgcn_s_sleep(LAG);
#pragma unroll
    for (int m = 0; m < 3; ++m) {
      const unsigned stride = m < 2 ? 3072 : 1280;
      const rsrc_t rr = mk(m == 0 ? wg : (m == 1 ? wu : wdn), e < 480 ? 640 * 1536 * 2 : 0);
#pragma unroll
      for (int j = 0; j < 4; ++j) dma<0, 0>(rr, t[m] + j * 1024, (lane >> 3) * stride + (lane & 7) * 16, (32 * (m < 2 ? ks3 : ks4) + 8 * j) * stride + 128 * (m < 2 ? st3 : st4));
    }
    const rsrc_t ro = mk(k0.ws, e < 480 ? WS_END : 0);
    const bf16x8 ones = {0x3f80, 0x3f80, 0x3f80, 0x3f80, 0x3f80, 0x3f80, 0x3f80, 0x3f80};
    const unsigned tr = (lane >> 4) * 1024 + ((lane >> 2) & 3) * 128 + (lane & 3) * 8;
    __builtin_amdgcn_s_waitcnt(0);
    asm volatile("" ::: "memory");
    u32x4 f[3][4];
#pragma unroll
    for (int m = 0; m < 3; ++m)
#pragma unroll
      for (int n = 0; n < 4; ++n) {
        const u32x2 lo = __builtin_bit_cast(u32x2, __builtin_amdgcn_ds_read_tr16_b64_v4i16((LAS s16x4*)(t[m] + tr + 32 * n)));
        const u32x2 hi = __builtin_bit_cast(u32x2, __builtin_amdgcn_ds_read_tr16_b64_v4i16((LAS s16x4*)(t[m] + tr + 32 * n + 512)));
        f[m][n] = (u32x4){lo[0], lo[1], hi[0], hi[1]};
      }
#pragma unroll
    for (int m = 0; m < 3; ++m) {
      f32x4 cs[4];
#pragma unroll
      for (int n = 0; n < 4; ++n) {
        const int nf = 4 * (m < 2 ? st3 : st4) + n;
        st16(ro, f[m][n], lane * 16 + (m < 2 ? OFF_W3 + m * 1024 + ks3 * 12288 + ((nf * 171) >> 10) * 19 * 12288 + nf * 2048 : OFF_W4 + ks4 * 5120 + ((nf * 205) >> 10) * 47 * 5120 + nf * 1024));
        if (m < 2) cs[n] = mfma(ones, __builtin_bit_cast(bf16x8, f[m][n]), (f32x4){0.f, 0.f, 0.f, 0.f});
      }
      if (m < 2) *(LAS float*)(L + L_CP + (m * 64 + lane) * 16 + w * 4) = (lane & 32) ? ((lane & 16) ? cs[3][0] : cs[2][0]) : ((lane & 16) ? cs[1][0] : cs[0][0]);
    }
  };
#pragma unroll 1
  for (int layer = 0; layer < 2; ++layer) {
    {
      PHASE_LANE();
      unsigned vo = OFF_AB + layer * AB_BUF + ((k.mat * 4 + (k.ct1 >> 1)) * 64 + 4 * k.P1 + w) * 10240 + 5 * (k.ct1 & 1) * 1024 + (qs * 16 + c) * 16;
      unsigned ta = (32 * w + 4 * q) * 4, tc = c * 4;
      pin(vo); pin(ta); pin(tc);
      f32x4 acc[2][5], mean[2], rstd[2]; float cs1[5];
      bf16x8 wf[4][5];
      auto tables = [&]() {
#pragma unroll
        for (int i = 0; i < 5; ++i) cs1[i] = *(LAS float*)(L + L_CS1 + tc + i * 64);
#pragma unroll
        for (int t = 0; t < 2; ++t) { mean[t] = *(LAS f32x4*)(L + ta + L_MEAN + 64 * t); rstd[t] = *(LAS f32x4*)(L + ta + L_RSTD + 64 * t); }
      };
#pragma unroll
      for (int i = 0; i < 5; ++i) { acc[0][i] = (f32x4){0.f, 0.f, 0.f, 0.f}; acc[1][i] = (f32x4){0.f, 0.f, 0.f, 0.f}; }
      if (layer == 0) {
        bf16x8 xf[4][2], cf[4], ones = {0x3f80, 0x3f80, 0x3f80, 0x3f80, 0x3f80, 0x3f80, 0x3f80, 0x3f80};
        f32x4 as[2], aq[2], ac[2];
        unsigned wt = rot1(k) * 10240, wa0 = L_D + rowrd;
        pin(wa0);
        unsigned wa = wa0 + wt, xr = L_XR + w * 4096 + rowrd, x_slot = 32768;
#pragma unroll
        for (int i = 0; i < 5; ++i) { pin(acc[0][i]); pin(acc[1][i]); }
#pragma unroll
        for (int t = 0; t < 2; ++t) { as[t] = (f32x4){0.f, 0.f, 0.f, 0.f}; aq[t] = as[t]; ac[t] = as[t]; pin(as[t]); pin(aq[t]); pin(ac[t]); }
        pin(wa); pin(xr); pin(ones);
        if (k.P & 1) retile(k, w);
        bar();
        unsigned xn[2] = {xr + 32768, (xr + 32768) ^ 64}, wn[2] = {wa, wa ^ 64};
        const unsigned wo = w * 2048;
        auto rd = [&](int p, int n) {
          const int e = n & 1, m = n >> 1;
          if (m < 2) xf[2 * p + e][m] = lds16(L, xn[e] + m * 2048);
          else if (m < 7) wf[2 * p + e][m - 2] = lds16(L, wn[e] + (m - 2) * 2048);
          else cf[2 * p + e] = lds16(L, wn[e] + wo);
        };
#pragma unroll
        for (int n = 0; n < 16; ++n) rd(0, n);
        lgk0();
        int left = 5;
#pragma unroll 1
        do {
#pragma unroll
          for (int p = 0; p < 2; ++p) {
#pragma unroll
            for (int n = 0; n < 32; ++n) {
              const int m = n >> 1, h = 2 * p + ((n ^ m) & 1);
              if (n == 10) barv();
              if (m < 10) { const int t = m / 5, i = t ? 9 - m : m; mma(acc[t][i], xf[h][t], wf[h][i]); }
              else if (m == 10) mma(aq[1], xf[h][1], xf[h][1]);
              else if (m == 11) mma(as[1], xf[h][1], ones);
              else if (m == 12) mma(as[0], xf[h][0], ones);
              else if (m == 13) mma(aq[0], xf[h][0], xf[h][0]);
              else if (m == 14) mma(ac[0], ones, cf[h]);
              else mma(ac[1], ones, wf[h][4]);
              if (n == 0) { x_slot = x_slot == 32768 ? 0 : x_slot + 16384; pins(x_slot); }
              if (n == 1) { wt = wt == 92160 ? 0 : wt + 10240; pins(wt); }
              if (n == 2) { xn[0] = xr + x_slot; wn[0] = wa0 + wt; pin(xn[0]); pin(wn[0]); }
              if (n == 4) { xn[1] = xn[0] ^ 64; wn[1] = wn[0] ^ 64; pin(xn[1]); pin(wn[1]); }
              if (n == 6 && p) { --left; pins(left); }
              if (n >= 10 && n < 26) rd(p ^ 1, n - 10);
              sched();
            }
            lgk0();
          }
        } while (left);
        settle();
        if (q == 0) { *(LAS float*)(L + L_CS1 + (16 * w + c) * 4) = ac[0][0]; *(LAS float*)(L + L_CS1 + (64 + c) * 4) = ac[1][0]; }
#pragma unroll
        for (int t = 0; t < 2; ++t) {
          const float s = ((c & 2) ? ((c & 1) ? as[t][3] : as[t][2]) : ((c & 1) ? as[t][1] : as[t][0])) * (1.f / 640.f);
          const float ss = ((c & 2) ? ((c & 1) ? aq[t][3] : aq[t][2]) : ((c & 1) ? aq[t][1] : aq[t][0])) * (1.f / 640.f);
          const float r = __builtin_amdgcn_rsqf(fmaxf(ss - s * s, 0.f) + 1e-5f);
          if ((c >> 2) == q) { *(LAS float*)(L + L_MEAN + (32 * w + 16 * t + c) * 4) = -s; *(LAS float*)(L + L_RSTD + (32 * w + 16 * t + c) * 4) = k.mat ? -r : r; }
        }
        bar();
        tables();
      } else {
#define KS(s) (((s) + 12) % 20)
        unsigned wb[2][2] = {{L_D + rowrd, L_D + rowrd + 51200}, {L_D + (rowrd ^ 64), L_D + (rowrd ^ 64) + 51200}};
        unsigned xv[2] = {OFF_XT + XT_BUF + (8 * k.P1 + 2 * w) * 20480 + lane * 16, 0}; xv[1] = xv[0] + 20480;
        pin(wb[0][0]); pin(wb[0][1]); pin(wb[1][0]); pin(wb[1][1]); pin(xv[0]); pin(xv[1]);
        constexpr int R = AHEAD + 2;
        bf16x8 xa[R];
        auto req = [&](int l) { const int ks = KS(2 * (l >> 2) + (l & 1)); if (l < 40) xa[l % R] = ld16(rw, xv[(l >> 1) & 1] + (ks & 3) * 1024, (ks >> 2) * 4096); };
        auto rd = [&](int d, int n) { const int s = 2 * d + (n & 1); wf[s & 3][n >> 1] = lds16(L, wb[s & 1][KS(s) / 10] + ((KS(s) >> 1) % 5) * 10240 + (n >> 1) * 2048); };
        bar();
#pragma unroll
        for (int l = 0; l < AHEAD; ++l) { req(l); sched(); }
#pragma unroll
        for (int n = 0; n < 10; ++n) rd(0, n);
        lgk0();
#pragma unroll
        for (int d = 0; d < 10; ++d) {
          if (d == 8) barv();
          if (d == 9) { tables(); sched(); }
#pragma unroll
          for (int n = 0; n < 20; ++n) {
            const int m = n >> 1, t = m / 5, i = t ? 9 - m : m, e = (n ^ m) & 1;
            if (d == 0 && !(n & 1)) mma0(acc[t][i], xa[(2 * t + e) % R], wf[e][i]); else mma(acc[t][i], xa[(4 * d + 2 * t + e) % R], wf[(2 * d + e) & 3][i]);
            if (n % 5 == 4) req(4 * d + n / 5 + AHEAD);
            else if (d < 9 && n < 13) { const int j = n - n / 5; if (j < 10) rd(d + 1, j); }
            if (n == 18 && d < 9) lgk0();
            sched();
          }
        }
        settle();
      }
#pragma unroll
      for (int i = 0; i < 5; ++i) {
        const float cs = cs1[i];
        const f32x4 a = (mean[0] * cs + acc[0][i]) * rstd[0], bb = (mean[1] * cs + acc[1][i]) * rstd[1];
        st16(rw, swap_pair(pk(a[0], a[1]), pk(a[2], a[3]), pk(bb[0], bb[1]), pk(bb[2], bb[3])), vo + i * 1024);
      }
      if (layer == 0 && (k.P & 1) && w < 2) {
        const int j = (k.b >> 7) * 64 + (k.b & 63), g3 = (j * 2731) >> 16;
        const f32x4 t = *(LAS f32x4*)(L + L_CP + (w * 64 + lane) * 16);
        if (j < 120) __builtin_amdgcn_raw_buffer_store_b32(__float_as_uint((t[0] + t[1]) + (t[2] + t[3])), rw, lane * 4, OFF_CS + (g3 * 3072 + w * 1536 + 64 * (j - g3 * 24)) * 4, SC1);
      }
      drain_bar();
    }
    {
      PHASE_LANE();
      const int wm = w >> 1, wn = w & 1;
      f32x4 acc[4][5];
      bf16x8 fa[2][4], fl[2][2], fb[2][2][4];
#pragma unroll
      for (int t = 0; t < 4; ++t)
#pragma unroll
        for (int i = 0; i < 5; ++i) { acc[t][i] = (f32x4){0.f, 0.f, 0.f, 0.f}; pin(acc[t][i]); }
      unsigned wb = L_WR + wm * 8192 + rowrd, ab = L_D + wn * 5120 + lane * 16;
      unsigned ws_slot = 65536, ab_slot = 20480;
      unsigned ev[2], ev8[2], en = w == k.kq ? 0 : WS_END;
      ev[0] = OFF_EX + layer * EX_BUF + ((k.P * 16 + w * 4 + k.np) * 3 + ((k.kq - w) & 3) - 1) * 10240 + lane * 16; ev[1] = ev[0] + 5120;
      ev8[0] = ev[0] - lane * 8 + 2048; ev8[1] = ev8[0] + 5120;
      unsigned lo = L_D + 20480 + w * 10240 + lane * 16, lo8 = lo - lane * 8 + 2048;
      pin(lo); pin(lo8);
      pin(wb); pin(ab); pin(ev[0]); pin(ev[1]); pin(ev8[0]); pin(ev8[1]); pins(en);
      const rsrc_t re = mk(k.ws, en);
      bar();
#pragma unroll
      for (int n = 0; n < 8; ++n) fb[0][n & 1][n >> 1] = lds16(L, (wb ^ ((n & 1) * 64)) + 49152 + (n >> 1) * 2048);
      unsigned an = ab + 20480, w0 = wb + 65536, w1 = w0 ^ 64;
      pin(an); pin(w0); pin(w1);
      bar();
#pragma unroll
      for (int n = 0; n < 10; ++n) { if (n < 8) fa[n & 1][n >> 1] = lds16(L, ab + (n & 1) * 10240 + (n >> 1) * 1024); else fl[0][n & 1] = lds16(L, ab + (n & 1) * 10240 + 4096); }
      lgk0();
      int left = 7;
      auto tile = [&](int p, bool last) {
#pragma unroll
        for (int n = 0; n < 40; ++n) {
          const int m = n >> 1, i = m >> 2, t = (i & 1) ? 3 - (m & 3) : m & 3, kk = (n ^ m) & 1, e = n & 1;
          if (n == 16) barv();
          mma(acc[t][i], i < 4 ? fa[kk][i & 3] : fl[p][kk], fb[p][kk][t]);
          if (n < 8) fb[p ^ 1][e][m] = lds16(L, (e ? w1 : w0) + m * 2048);
          else if (n >= 16 && n < 20) fa[e][m - 8] = lds16(L, an + e * 10240 + (m - 8) * 1024);
          else if (m == 10) fl[p ^ 1][e] = lds16(L, an + e * 10240 + 4096);
          else if (m == 12 || m == 16) fa[e][m / 4 - 1] = lds16(L, an + e * 10240 + (m / 4 - 1) * 1024);
          else if (!last) {
            if (n == 8) { ws_slot = ws_slot == 81920 ? 0 : ws_slot + 16384; pins(ws_slot); }
            if (n == 9) { ab_slot = ab_slot == 40960 ? 0 : ab_slot + 20480; pins(ab_slot); }
            if (n == 10) { w0 = wb + ws_slot; pin(w0); }
            if (n == 11) { w1 = w0 ^ 64; pin(w1); }
            if (n == 34) { an = ab + ab_slot; pin(an); if (p) { --left; pins(left); } }
          }
          sched();
        }
        lgk0();
      };
#pragma unroll 1
      do { tile(0, false); tile(1, false); } while (left);
      tile(0, true);
      {
        unsigned o[10];
        auto send = [&](int t, int j) {
          if (j < 5) { o[2 * j] = pk(acc[t][j][0], acc[t][j][1]); o[2 * j + 1] = pk(acc[t][j][2], acc[t][j][3]); }
          if (j == 5) st16(re, (u32x4){o[0], o[1], o[2], o[3]}, ev[t >> 1] + (t & 1) * 2560);
          if (j == 6) { *(LAS u32x4*)(L + lo + t * 2560) = (u32x4){o[0], o[1], o[2], o[3]}; *(LAS u32x2*)(L + lo8 + t * 2560) = (u32x2){o[8], o[9]}; }
          if (j == 7) st16(re, (u32x4){o[4], o[5], o[6], o[7]}, ev[t >> 1] + (t & 1) * 2560 + 1024);
          if (j == 8) { __builtin_amdgcn_raw_buffer_store_b64((u32x2){o[8], o[9]}, re, ev8[t >> 1] + (t & 1) * 2560, 0, SC1); *(LAS u32x4*)(L + lo + t * 2560 + 1024) = (u32x4){o[4], o[5], o[6], o[7]}; }
        };
#pragma unroll
        for (int n = 0; n < 40; ++n) {
          const int t = n / 10, i = (n % 10) >> 1, kk = (n ^ i) & 1;
          mma(acc[t][i], i < 4 ? fa[kk][i & 3] : fl[1][kk], fb[1][kk][t]);
          if (t) send(t - 1, n % 10);
          sched();
        }
#pragma unroll
        for (int j = 0; j < 9; ++j) { send(3, j); sched(); }
      }
      drain_bar();
      if (layer == 0) {
        const rsrc_t rx = mk(x0, 2048 * 640 * 2);
#pragma unroll
        for (int j = 0; j < 3; ++j) {
          const int u = 64 * j + lane, r = (u * 205) >> 11;
          dma<0, 0>(rx, L + L_D + w * 3072 + j * 1024, r * 1120 + u * 16 + (j == 2 ? (lane & 32) << 25 : 0), 16 * tfo * 1280 + cto * 160);
        }
        __builtin_amdgcn_s_waitcnt(0);
        asm volatile("" ::: "memory");
#pragma unroll
        for (int i = 0; i < 5; ++i) { const u32x2 t = *(LAS u32x2*)(L + L_D + w * 3072 + c * 160 + 8 * q + 32 * i); res[2 * i] = t[0]; res[2 * i + 1] = t[1]; }
      }
      f32x4 v[5];
      {
        LAS char* t = L + L_D + 20480 + k.kq * 10240 + w * 2560;
        const u32x4 a = *(LAS u32x4*)(t + lane * 16), bb = *(LAS u32x4*)(t + 1024 + lane * 16); const u32x2 d = *(LAS u32x2*)(t + 2048 + lane * 8);
        v[0] = unpk(a[0], a[1]); v[1] = unpk(a[2], a[3]); v[2] = unpk(bb[0], bb[1]); v[3] = unpk(bb[2], bb[3]); v[4] = unpk(d[0], d[1]);
#pragma unroll
        for (int i = 0; i < 5; ++i) { v[i] += unpk(res[2 * i], res[2 * i + 1]); pin(v[i]); }
      }
      unsigned rv = OFF_EX + layer * EX_BUF + (k.P * 16 + k.kq * 4 + k.np) * 30720 + w * 2560 + lane * 16, rv8 = rv - lane * 8;
      pin(rv); pin(rv8);
      X_ADDR(2 * layer);
      bar();
      u32x4 e[3][2]; u32x2 e8[3];
#pragma unroll
      for (int j = 0; j < 3; ++j)
#pragma unroll
        for (int n = 0; n < 3; ++n) {
          if (j < 2) e[n][j] = __builtin_amdgcn_raw_buffer_load_b128(rw, rv + j * 1024, n * 10240, SC1);
          else e8[n] = __builtin_amdgcn_raw_buffer_load_b64(rw, rv8 + 2048, n * 10240, SC1);
          sched();
        }
      float s = 0.f, ss = 0.f;
#pragma unroll
      for (int j = 0; j < 3; ++j) {
#pragma unroll
        for (int n = 0; n < 3; ++n) {
          if (j < 2) { v[2 * j] += unpk(e[n][j][0], e[n][j][1]); v[2 * j + 1] += unpk(e[n][j][2], e[n][j][3]); }
          else v[4] += unpk(e8[n][0], e8[n][1]);
        }
        x_part(rw, vx, vx2, j, v, res, s, ss);
        sched();
      }
      x_stats(rw, vs, s, ss);
      drain_bar();
    }
    {
      PHASE_LANE();
      const int wm = w >> 1, wn = w & 1;
      f32x4 acc[4][6];
      bf16x8 fa[2][5], fl[2][2], fb[2][2][4];
#pragma unroll
      for (int t = 0; t < 4; ++t)
#pragma unroll
        for (int i = 0; i < 6; ++i) { acc[t][i] = (f32x4){0.f, 0.f, 0.f, 0.f}; pin(acc[t][i]); }
      unsigned wb = L_WR + wn * 6144 + lane * 16, xb = L_D + wm * 8192 + lane * 16;
      unsigned w_slot = 73728, x_slot = 16384;
      unsigned vo = OFF_HT + layer * HT_BUF + ((8 * k.P + 4 * wm + (q & 1)) * 48 + 3 * (4 * k.kq + k.np)) * 1024 + (6 * wn + (q >> 1)) * 256 + c * 16, vo2 = vo + 98304;
      unsigned ta = (64 * wm + c) * 4, tc = L_CS3 + (48 * wn + 4 * q) * 4;
      unsigned sa = L_D + 49152 + (2 * w + (lane >> 5)) * 1024 + (lane & 31) * 4, sw = (32 * w + 16 * (lane >> 5) + c) * 4;
      pin(wb); pin(xb); pin(vo); pin(vo2); pin(ta); pin(tc); pin(sa); pin(sw);
      bar();
#pragma unroll
      for (int n = 0; n < 12; ++n) { if (n < 10) fa[n & 1][n >> 1] = lds16(L, wb + 49152 + (n & 1) * 12288 + (n >> 1) * 1024); else fl[0][n & 1] = lds16(L, wb + 49152 + (n & 1) * 12288 + 5120); }
      unsigned wn3 = wb + 73728, xn = xb + 16384;
      pin(wn3); pin(xn);
      bar();
#pragma unroll
      for (int n = 0; n < 8; ++n) fb[0][n & 1][n >> 1] = lds16(L, xb + n * 1024);
      lgk0();
      int left = 4;
      auto mm = [&](int p, int n) { const int m = n >> 1, i = m >> 2, t = (i & 1) ? 3 - (m & 3) : m & 3, kk = (n ^ m) & 1; mma(acc[t][i], i < 5 ? fa[kk][i < 5 ? i : 0] : fl[p][kk], fb[p][kk][t]); };
      auto rd = [&](int p, int n) {
        const int m = n >> 1, e = n & 1;
        if (m == 8 || m == 9) fa[e][m - 8] = lds16(L, wn3 + e * 12288 + (m - 8) * 1024);
        else if (m == 10) fl[p ^ 1][e] = lds16(L, wn3 + e * 12288 + 5120);
        else if (m == 12 || m == 16 || m == 20) fa[e][m / 4 - 1] = lds16(L, wn3 + e * 12288 + (m / 4 - 1) * 1024);
        else if (m == 11) fb[p ^ 1][e][0] = lds16(L, xn + e * 1024);
        else if (m >= 13 && m <= 15) fb[p ^ 1][e][m - 12] = lds16(L, xn + e * 1024 + (m - 12) * 2048);
      };
#pragma unroll 1
      do {
#pragma unroll
        for (int p = 0; p < 2; ++p) {
#pragma unroll
          for (int n = 0; n < 48; ++n) {
            if (n == 16) barv();
            mm(p, n); rd(p, n);
            if (n == 34) { x_slot = x_slot == 32768 ? 0 : x_slot + 16384; pins(x_slot); }
            if (n == 35) { w_slot = w_slot == 73728 ? 0 : w_slot + 24576; pins(w_slot); }
            if (n == 42) { wn3 = wb + w_slot; xn = xb + x_slot; pin(wn3); pin(xn); }
            if (n == 43 && p) { --left; pins(left); }
            sched();
          }
          lgk0();
        }
      } while (left);
      float mean[4], rl2[4], sd[4];
      f32x4 cs[6];
      unsigned h[4][2];
      f32x4 o[4];
      auto corr1 = [&](int f, int n) { const int t = n >> 3, i = 2 * f + ((n >> 2) & 1), e = n & 3; if (n < 32) acc[t][i][e] = one(fmaf(cs[i][e], mean[t], acc[t][i][e])); };
      auto glu1 = [&](int n) {
        const int t = n / 15, j = n % 15;
        if (j < 2) { o[t][2 * j] = one(acc[t][0][2 * j] * rl2[t]); o[t][2 * j + 1] = one(acc[t][0][2 * j + 1] * rl2[t]); }
        else if (j < 6) o[t][j - 2] = __builtin_amdgcn_exp2f(o[t][j - 2]);
        else if (j < 8) { o[t][2 * j - 12] = one(fmaf(o[t][2 * j - 12], sd[t], sd[t])); o[t][2 * j - 11] = one(fmaf(o[t][2 * j - 11], sd[t], sd[t])); }
        else if (j < 12) o[t][j - 8] = __builtin_amdgcn_rcpf(o[t][j - 8]);
        else if (j < 14) { o[t][2 * j - 24] = one(o[t][2 * j - 24] * acc[t][1][2 * j - 24]); o[t][2 * j - 23] = one(o[t][2 * j - 23] * acc[t][1][2 * j - 23]); }
        else { h[t][0] = pk(o[t][0], o[t][1]); h[t][1] = pk(o[t][2], o[t][3]); }
      };
      auto glu = [&](int f, int t, int from) {
        if (from < 1) { o[t] = acc[t][2 * f] * rl2[t]; asm("" : "+v"(o[t])); }
        if (from < 2) {
#pragma unroll
          for (int r = 0; r < 4; ++r) o[t][r] = __builtin_amdgcn_exp2f(o[t][r]);
        }
        o[t] = o[t] * sd[t] + sd[t]; asm("" : "+v"(o[t]));
#pragma unroll
        for (int r = 0; r < 4; ++r) o[t][r] = __builtin_amdgcn_rcpf(o[t][r]);
        o[t] *= acc[t][2 * f + 1];
        h[t][0] = pk(o[t][0], o[t][1]); h[t][1] = pk(o[t][2], o[t][3]);
      };
      auto tab2 = [&](float (&d)[4], unsigned off, int j) { d[2 * j] = *(LAS float*)(L + ta + off + 128 * j); d[2 * j + 1] = *(LAS float*)(L + ta + off + 128 * j + 64); };
      float pv[8], s1 = 0.f, s2 = 0.f;
#pragma unroll
      for (int n = 0; n < 26; ++n) {
        if (n == 16) barv();
        mm(0, n); rd(0, n);
        if (n < 2) {
#pragma unroll
          for (int j = 0; j < 4; ++j) pv[4 * n + j] = *(LAS float*)(L + sa + 128 * (4 * n + j));
        }
        if (n == 2) { cs[0] = *(LAS f32x4*)(L + tc); cs[1] = *(LAS f32x4*)(L + tc + 384); }
        if (n == 6) { s1 = one(pv[0] + pv[1]); s2 = one(pv[2] + pv[3]); }
        if (n == 7 || n == 8) { s1 = one(s1 + pv[2 * n - 10]); s2 = one(s2 + pv[2 * n - 9]); }
        if (n == 9) s1 = one(s1 + s2);
        if (n == 10) { s2 = one(s1 * (1.f / 640.f)); s1 = one(s1 * (1.f / 640.f)); }
        if (n == 11) { const auto r = __builtin_amdgcn_permlane16_swap(__float_as_uint(s1), __float_as_uint(s2), false, false); s1 = one(__uint_as_float(r[0])); s2 = one(__uint_as_float(r[1])); }
        if (n == 12) { s2 = one(fmaf(-s1, s1, s2)); s1 = one(-s1); }
        if (n == 13) { *(LAS float*)(L + sw + L_MEAN) = s1; s2 = one(fmaxf(s2, 0.f)); }
        if (n == 14) s2 = one(s2 + 1e-5f);
        if (n == 15) s1 = __builtin_amdgcn_rsqf(s2);
        if (n == 16 || n == 17) tab2(mean, L_MEAN, n & 1);
        if (n == 18) s2 = one(s2 * s1);
        if (n == 19) s1 = one(s1 * -1.44269504089f);
        if (n == 20) *(LAS float*)(L + sw + L_STD) = s2;
        if (n == 21) *(LAS float*)(L + sw + L_RSTD) = s1;
        sched();
      }
#pragma unroll
      for (int n = 26; n < 48; ++n) {
        mm(0, n); rd(0, n);
        if (n < 34) corr1(0, n - 26);
        if (n >= 34 && n < 40) { corr1(0, 2 * n - 60); corr1(0, 2 * n - 59); }
        if (n == 40 || n == 41) corr1(0, n - 20);
        if (n == 42) { cs[2] = *(LAS f32x4*)(L + tc + 64); cs[3] = *(LAS f32x4*)(L + tc + 384 + 64); }
        if (n > 42) { corr1(0, 2 * n - 64); corr1(0, 2 * n - 63); }
        sched();
      }
      barv();
#pragma unroll
      for (int n = 0; n < 48; ++n) {
        mm(1, n);
        if (n < 16) { corr1(1, 2 * n); corr1(1, 2 * n + 1); }
        if (n == 12 || n == 13) tab2(rl2, L_RSTD, n & 1);
        if (n == 14 || n == 15) tab2(sd, L_STD, n & 1);
        if (n == 16 || n == 17) cs[n - 12] = *(LAS f32x4*)(L + tc + 128 + 384 * (n & 1));
        if (n >= 18) glu1(n - 18);
        sched();
      }
      st16(rw, swap_pair(h[0][0], h[0][1], h[1][0], h[1][1]), vo);
      sched();
      glu(0, 2, 0); glu(0, 3, 0);
      st16(rw, swap_pair(h[2][0], h[2][1], h[3][0], h[3][1]), vo2);
      sched();
#pragma unroll
      for (int f = 1; f < 3; ++f)
#pragma unroll
        for (int t = 0; t < 4; t += 2) {
          if (f == 2) { acc[t][4] += cs[4] * mean[t]; acc[t][5] += cs[5] * mean[t]; acc[t + 1][4] += cs[4] * mean[t + 1]; acc[t + 1][5] += cs[5] * mean[t + 1]; }
          glu(f, t, 0); glu(f, t + 1, 0);
          st16(rw, swap_pair(h[t][0], h[t][1], h[t + 1][0], h[t + 1][1]), (t ? vo2 : vo) + f * 512);
          sched();
        }
    }
    {
      PHASE_LANE();
      const int kh = w & 1;
      unsigned wb[3] = {L_W4 + kh * 5120 + lane * 16, 0, 0}; wb[1] = wb[0] + 51200; wb[2] = wb[0] + 102400;
      pin(wb[0]); pin(wb[1]); pin(wb[2]);
      bar();
      constexpr int R = AHEAD + 2;
      bf16x8 ha[R], wf[6][5];
      f32x4 acc[2][5];
      auto rdw = [&](int s, int i) { wf[s % 6][i] = lds16(L, wb[w4off(s) / 51200] + w4off(s) % 51200 + i * 1024); };
#pragma unroll
      for (int n = 0; n < 30; ++n) rdw(n / 5, n % 5);
      lgk0();
      drain_bar();
      sched();
      unsigned hv[2] = {OFF_HT + layer * HT_BUF + tfo * 49152 + kh * 1024 + lane * 16, OFF_HT + layer * HT_BUF + (tfo ^ 1) * 49152 + kh * 1024 + lane * 16};
      unsigned rd = L_RED + w * 5120 + lane * 16, wr = L_RED + (w ^ 1) * 5120 + lane * 16;
      pin(hv[0]); pin(hv[1]); pin(rd); pin(wr);
      X_ADDR(1);
      unsigned ow = L_W4 + 61440 + w * 3072 + c * 160 + 8 * q, orr = L_W4 + 61440 + w * 3072 + lane * 16, vo3[3];
#pragma unroll
      for (int j = 0; j < 3; ++j) { const int u = 64 * j + lane; vo3[j] = 16 * tfo * 1280 + cto * 160 + ((u * 205) >> 11) * 1120 + u * 16 + (j == 2 ? (lane & 32) << 25 : 0); pin(vo3[j]); }
      pin(ow); pin(orr);
#pragma unroll
      for (int i = 0; i < 5; ++i) { acc[0][i] = unpk(res[2 * i], res[2 * i + 1]); pin(acc[0][i]); }
      bar();
      auto req = [&](int l) { if (l < 48) ha[l % R] = ld16(rw, hv[1 - ((l >> 1) & 1)] + (l & 1) * 2048, (l >> 2) * 4096); };
#pragma unroll
      for (int l = 0; l < AHEAD; ++l) { req(l); sched(); }
      f32x4 pt[5];
#pragma unroll
      for (int d = 0; d < 12; ++d) {
        if (d == 4 || d == 9 || d == 10) barv();
#pragma unroll
        for (int n = 0; n < 20; ++n) {
          const int m = n >> 1, v = m / 5, i = v && d < 11 ? 9 - m : m % 5, e = (n ^ m) & 1;
          if (d == 11 && n == 17) barv();
          if (d == 0 && !v && !(n & 1)) mma0(acc[1][i], wf[e][i], ha[e]); else mma(acc[1 - v][i], wf[(2 * d + e) % 6][i], ha[(4 * d + 2 * v + e) % R]);
          if (n % 5 == 4) req(4 * d + n / 5 + AHEAD);
          else if (d >= 2 && d < 11 && n < 13) { const int j = n - n / 5; if (j < 10) rdw(2 * d + 2 + (j & 1), j >> 1); }
          if (n == 18 && d >= 2 && d < 11) lgk0();
          if (d == 11) {
            if (n >= 10 && n < 15) *(LAS f32x4*)(L + wr + (n - 10) * 1024) = acc[1][n - 10];
            if (n >= 17) { pt[2 * n - 34] = *(LAS f32x4*)(L + rd + (2 * n - 34) * 1024); if (n < 19) pt[2 * n - 33] = *(LAS f32x4*)(L + rd + (2 * n - 33) * 1024); }
          }
          sched();
        }
      }
#pragma unroll
      for (int i = 0; i < 5; ++i) { acc[0][i] += pt[i]; pin(acc[0][i]); sched(); }
      if (layer == 1) {
        const rsrc_t ro = mk(out, 2048 * 640 * 2);
#pragma unroll
        for (int i = 0; i < 5; ++i) *(LAS u32x2*)(L + ow + 32 * i) = (u32x2){pk(acc[0][i][0], acc[0][i][1]), pk(acc[0][i][2], acc[0][i][3])};
        asm volatile("" ::: "memory");
#pragma unroll
        for (int j = 0; j < 3; ++j) st16(ro, *(LAS u32x4*)(L + orr + j * 1024), vo3[j]);
      } else {
        float s = 0.f, ss = 0.f;
#pragma unroll
        for (int j = 0; j < 3; ++j) x_part(rw, vx, vx2, j, acc[0], res, s, ss);
        x_stats(rw, vs, s, ss);
        bar();
        drain_bar();
      }
    }
  }
}

DI void stats_load(const Ctx& k, rsrc_t rw, int xb, int tf0, u32x2 (&sv)[8]) {
#pragma unroll
  for (int j = 0; j < 8; ++j) sv[j] = __builtin_amdgcn_raw_buffer_load_b64(rw, (k.lane >> 4) * 1024 + (k.lane & 15) * 8 + j * 128, OFF_ST + xb * ST_BUF + tf0 * 1024, SC1);
}
DI void stats_sum(u32x2 (&sv)[8], float (&s)[2]) {
#pragma unroll
  for (int j = 0; j < 8; ++j) pin(sv[j]);
#pragma unroll
  for (int e = 0; e < 2; ++e) {
    s[e] = __uint_as_float(sv[0][e]);
#pragma unroll
    for (int j = 1; j < 8; ++j) s[e] = one(s[e] + __uint_as_float(sv[j][e]));
    s[e] *= 1.f / 640.f; pin(s[e]);
  }
}
DI void stats_table(const Ctx& k, int a, float (&s)[2], float scale) {
  pin(s[0]); pin(s[1]);
  float r[2];
#pragma unroll
  for (int e = 0; e < 2; ++e) {
    const float ss = __builtin_bit_cast(float, __builtin_amdgcn_update_dpp(0, __builtin_bit_cast(int, s[e]), 0x128, 0xf, 0xf, true));
    const float v = one(fmaxf(one(fmaf(-s[e], s[e], ss)), 0.f) + 1e-5f), q = __builtin_amdgcn_rsqf(v);
    r[e] = one(q * scale);
  }
  if (!(k.lane & 8)) {
    const unsigned t = (64 * a + (k.lane & 48) + 2 * (k.lane & 7)) * 4;
    *(LAS f32x2*)(k.lds + L_MEAN + t) = (f32x2){one(-s[0]), one(-s[1])};
    *(LAS f32x2*)(k.lds + L_RSTD + t) = (f32x2){r[0], r[1]};
  }
}

DI void loader(const Ctx& k0, const char* x0, const char* wsp, const char* wdp, int a4) {
  Ctx k = k0;
  const bool isW = a4 < 2;
  LAS char* const L = k.lds;
  const rsrc_t rw = mk(k.ws, WS_END);
  const rsrc_t rs = mk(wsp, 2048 * 2048 * 2);
  auto fresh = [&]() { asm volatile("" : "+v"(k.lane), "+s"(k.P), "+s"(k.kq), "+s"(k.np), "+s"(k.P1), "+s"(k.ct1), "+s"(k.mat), "+s"(a4)); };
#define lane k.lane
#define a (a4 & 1)
#define me (k.P * 16 + k.kq * 4 + k.np)
#define n3 (4 * k.kq + k.np)
#define sw (((lane & 7) ^ (4 * a + (lane >> 4))) * 16)
#define vo2 ((lane >> 3) * 4096 + sw)
#define so2 ((128 * k.P + 8 * a) * 4096 + (k.kq & 1) * 2048)
#define so3 (OFF_W3 + n3 * 20 * 12288 + a * 12288)
#define so4 (OFF_W4 + (2 * k.np + (k.kq & 1)) * 48 * 5120)
  bf16x8 k1[25], k4[3][8];
  u64 tag = 0;
  int rot = 2 * k.np;
  auto ktile = [&](int t) { const int r = (t + rot) & 7; return 4 * (r >> 1) + 2 * (t >> 3) + (r & 1); };
  auto w2 = [&](int t, int slot) {
#pragma unroll
    for (int j = 0; j < 8; ++j) dma<0, 0>(rs, L + L_WR + slot * 16384 + (2 * j + a) * 1024, vo2, so2 + j * 16 * 4096 + ktile(t) * 128);
  };
  auto w2n = [&](int t0, int t1) {
#pragma unroll 1
    for (int t = t0; t < t1; ++t) w2(t, t < 3 ? t + 3 : t - 3);
  };
  auto w3 = [&](int t, int slot) { dma_lin<SC1, 12>(rw, L + L_WR + slot * 24576 + a * 12288, lane * 16, so3 + t * 24576); };
  auto w4 = [&](int ch) { dma_lin<SC1, 15>(rw, L + L_W4 + w4slot(ch) * 30720 + a * 15360, lane * 16, so4 + ch * 30720 + a * 15360); };
  auto push = [&](int ev, int wg0, int wgstep, int n, int slot) {
    if (a4 == 2) {
      unsigned dst = lane < n ? OFF_MB + ev * MB_EV + (wg0 + lane * wgstep) * 128 + slot * 8 : 0x7fffffff;
      pin(dst); pin(tag);
      bar();
      __builtin_amdgcn_raw_buffer_store_b64(__builtin_bit_cast(u32x2, tag), rw, dst, 0, SC1);
    } else bar();
    fresh();
  };
  unsigned budget = SPIN_CAP;
  auto wait_mb = [&](int ev, int wg, int n) {
    GAS u64* src = (GAS u64*)(k.ws + OFF_MB + ev * MB_EV + wg * 128) + (lane & (n - 1));
#pragma unroll 1
    for (; budget; budget = uni(budget - 1)) {
      const u64 v = __hip_atomic_load(src, __ATOMIC_RELAXED, __HIP_MEMORY_SCOPE_AGENT);
      if (__all(v == tag)) break;
      __builtin_amdgcn_s_sleep(1);
    }
  };
  auto poll = [&](int ev, int wg, int n) {
    if (a4 == 2) wait_mb(ev, wg, n);
    drain_bar();
  };
  auto get_tag = [&]() { const char* e = k.ws; pins(e); tag = *(const GAS u64*)e; };
  auto mix_tag = [&]() { pin(tag); tag = tag * 6364136223846793005ull + 1442695040888963407ull; };
  if (!isW) __builtin_amdgcn_s_setprio(1);
  {
    const rsrc_t r1 = isW ? mk(wdp, 640 * 640 * 2) : mk(x0, 2048 * 640 * 2);
    const unsigned v1 = (lane >> 3) * 1280 + sw, s1 = ((isW ? 80 * k.ct1 : 128 * k.P1) + 8 * a) * 1280;
    int slot = 2, wt = rot1(k);
    if ((k.P & 1) && !isW) __builtin_amdgcn_s_sleep(LAG);
#pragma unroll 1
    for (int t = 0; t < 10; ++t) {
      if (isW) {
        if (t >= 3) { waitv<10>(); bar(); }
#pragma unroll
        for (int j = 0; j < 5; ++j) dma<0, 0>(r1, L + L_D + wt * 10240 + (2 * j + a) * 1024, v1, s1 + 16 * j * 1280 + wt * 128);
      } else {
        if (t >= 2) { waitv<8>(); bar(); }
#pragma unroll
        for (int j = 0; j < 8; ++j) dma<0, 0>(r1, L + L_XR + slot * 16384 + (2 * j + a) * 1024, v1, s1 + 16 * j * 1280 + wt * 128);
      }
      slot = slot == 2 ? 0 : slot + 1; wt = wt == 9 ? 0 : wt + 1;
    }
    if (isW) {
      waitv<10>(); bar();
      waitv<5>(); bar(); w2(0, 3);
      waitv<8>(); bar(); w2(1, 4);
      bar();
    } else {
      get_tag();
      waitv<8>(); bar(); waitv<0>();
      mix_tag(); pin(tag);
      bar();
      if (!(k.P & 1)) {
#pragma unroll
        for (int j = 0; j < 2; ++j) touch<0>(rs, L + L_JUNK, (lane >> 1) * 4096 + (lane & 1) * 128, (128 * k.P + 64 * a + 32 * j) * 4096 + (k.kq & 1) * 2048 + ktile(8) * 128);
      }
      bar();
    }
  }
  {
    unsigned kb[2] = {L_D + lane * 16 + ((a4 >> 1) * 10 + a) * 1024, 0}; kb[1] = kb[0] + 61440; pin(kb[0]); pin(kb[1]);
    bar();
#pragma unroll
    for (int j = 0; j < 25; ++j) k1[j] = lds16(L, kb[j / 15] + ((j / 5) % 3) * 20480 + (j % 5) * 2048);
    if (isW) w2(2, 5);
  }
#pragma unroll 1
  for (int layer = 0; layer < 2; ++layer) {
    const int ev0 = layer * EV_PER_LAYER;
    if (layer) {
      poll(ev0 + EV_X, k.P1 * 16 + k.mat * 8 + k.ct1, 16);
      if (!isW) { u32x2 sv[8]; float sm[2]; stats_load(k, rw, 1, 8 * k.P1 + 4 * a, sv); stats_sum(sv, sm); stats_table(k, a, sm, k.mat ? -1.f : 1.f); }
      bar();
      if (isW) w2n(3, 6);
    }
    push(ev0 + EV_AB, (2 * k.mat + (k.P1 >> 3)) * 4 + (k.ct1 >> 1), 16, 16, 8 * (k.P1 & 1) + (k.P1 & 6) + (k.ct1 & 1));
    GAS u64* rtf = (GAS u64*)(k.ws + OFF_MB + EV_RT * MB_EV);
    if (layer == 0 && a4 == 3 && lane == 0) __hip_atomic_store(rtf + k.b, tag, __ATOMIC_RELAXED, __HIP_MEMORY_SCOPE_AGENT);
    if (isW && layer == 0) { get_tag(); w2n(3, 6); mix_tag(); }
    const unsigned soab = OFF_AB + layer * AB_BUF + (((k.kq >> 1) * 4 + k.np) * 64 + 32 * (k.kq & 1)) * 10240;
    unsigned l16 = lane * 16, so0 = soab + a * 10240 + ktile(0) * 20480, la0 = L_D + a * 10240;
    so0 = __builtin_amdgcn_readfirstlane(so0); la0 = __builtin_amdgcn_readfirstlane(la0);
    pin(l16); pins(so0); pins(la0);
    if (a4 == 3 && (k.P & 1) && layer == 0) __builtin_amdgcn_s_sleep(LAG2);
    poll(ev0 + EV_AB, me, 8);
    if (!isW) dma_lin<SC1, 10>(rw, L + la0, l16, so0);
    if (isW) {
      const unsigned sb2 = uni(so2), lb2 = uni(L_WR + a * 1024);
      unsigned v2 = vo2, so = uni(sb2 + ktile(6) * 128), la = uni(lb2 + 49152), t = 7;
      pin(v2);
#pragma unroll 1
      do {
        waitv<32>(); bar();
#pragma unroll
        for (int j = 0; j < 2; ++j) dma<0, 0>(rs, L + la + j * 2048, v2, so + j * 16 * 4096);
        sched();
        const unsigned sn = uni(sb2 + ktile(t) * 128), ln = uni(la == lb2 + 81920 ? lb2 : la + 16384);
        t = uni(t + 1);
        sched();
#pragma unroll
        for (int j = 2; j < 8; ++j) dma<0, 0>(rs, L + la + j * 2048, v2, so + j * 16 * 4096);
        so = sn; la = ln;
      } while (t != 17);
      const bool chk = layer == 0 && a == 0;
      u64 f[4];
      if (chk) {
#pragma unroll
        for (int j = 0; j < 4; ++j) f[j] = __hip_atomic_load(rtf + lane + 64 * j, __ATOMIC_RELAXED, __HIP_MEMORY_SCOPE_AGENT);
      }
      waitv<32>(); bar();
      if (chk) {
#pragma unroll
        for (int j = 0; j < 4; ++j) pin(f[j]);
#pragma unroll 1
        for (unsigned spins = 0; spins < SPIN_CAP && !__all(f[0] == tag && f[1] == tag && f[2] == tag && f[3] == tag); ++spins) {
#pragma unroll
          for (int j = 0; j < 4; ++j) f[j] = __hip_atomic_load(rtf + lane + 64 * j, __ATOMIC_RELAXED, __HIP_MEMORY_SCOPE_AGENT);
        }
      }
      waitv<0>(); bar();
      w3(3, 1); bar(); bar(); w3(0, 2); bar();
      w3(1, 3); bar(); w3(2, 0);
    } else {
      const unsigned sba = uni(soab + a * 10240);
      dma_lin<SC1, 10>(rw, L + la0 + 20480, l16, sba + ktile(1) * 20480);
      unsigned so = uni(sba + ktile(2) * 20480), la = uni(la0 + 40960), t = 3;
      auto step = [&]() {
        bar();
        dma_lin<SC1, 4>(rw, L + la, l16, so);
        sched();
        const unsigned sn = uni(sba + ktile(t) * 20480), ln = uni(la == la0 + 40960 ? la0 : la + 20480);
        t = uni(t + 1);
        sched();
        dma_lin<SC1, 6>(rw, L + la + 4096, l16, so + 4096);
        so = sn; la = ln;
      };
#pragma unroll 1
      do { waitv<10>(); step(); } while (t != 8);
      unsigned ja = L_JUNK + 256 + layer * 256 + a * 128;
      if (lane < 8) dma<SC1, 0>(rw, L + ja, l16, OFF_MB + (ev0 + EV_AB) * MB_EV + me * 128);
      ja += (lane & 15) * 8; pin(ja);
      waitv<11>(); step();
      waitv<10>();
      if (!__all(*(LAS u64*)(L + ja) == tag)) wait_mb(ev0 + EV_AB, me, 16);
      step();
#pragma unroll 1
      do { waitv<10>(); step(); } while (t != 17);
      waitv<10>(); bar();
      waitv<0>(); bar();
    }
    auto k4load = [&](int n) {
#pragma unroll
      for (int j = 0; j < 8; ++j) k4[n][j] = ld16(rw, lane * 16 + a4 * 1024, so4 + (5 + n) * 30720 + j * 4096);
    };
    push(ev0 + EV_EX, k.P * 16 + k.np, 4, 4, k.kq);
    if (layer == 0) k4load(0);
    poll(ev0 + EV_EX, me, 4);
    push(ev0 + EV_X1, k.P * 16, 1, 16, n3);
    if (layer == 0) {
      u32x2 cv[5];
      if (isW) {
#pragma unroll
        for (int j = 0; j < 5; ++j) cv[j] = __builtin_amdgcn_raw_buffer_load_b64(rw, lane * 8, OFF_CS + (j * 3072 + a * 1536 + 96 * n3) * 4, SC1);
      }
      k4load(1);
      if (isW) {
        sched();
        f32x2 s = {0.f, 0.f};
#pragma unroll
        for (int j = 0; j < 5; ++j) s += __builtin_bit_cast(f32x2, cv[j]);
        if (lane < 48) *(LAS f32x2*)(L + L_CS3 + (96 * a + 2 * lane) * 4) = s;
      }
    }
    l16 = lane * 16; so0 = OFF_XT + 2 * layer * XT_BUF + (8 * k.P + 4 * a) * 20480; la0 = L_D + a * 8192;
    so0 = __builtin_amdgcn_readfirstlane(so0); la0 = __builtin_amdgcn_readfirstlane(la0);
    pin(l16); pins(so0); pins(la0);
    poll(ev0 + EV_X1, me, 16);
    if (!isW) {
#pragma unroll
      for (int j = 0; j < 4; ++j) dma_lin<SC1, 2>(rw, L + la0 + j * 2048, l16, so0 + j * 20480);
    }
    if (isW) {
      bar();
      __builtin_amdgcn_s_sleep(3);
      dma_lin<SC1, 4>(rw, L + L_D + 49152 + a * 4096, lane * 16, OFF_ST + 2 * layer * ST_BUF + (8 * k.P + 4 * a) * 1024);
      const unsigned lb3 = uni(L_WR + a * 12288), se = uni(so3 + 10 * 24576);
      unsigned so = uni(so3 + 4 * 24576), la = uni(lb3 + 49152);
#pragma unroll 1
      do {
        waitv<24>(); bar();
        dma_lin<SC1, 4>(rw, L + la, l16, so);
        sched();
        const unsigned sn = uni(so + 24576), ln = uni(la == lb3 + 73728 ? lb3 : la + 24576);
        sched();
        dma_lin<SC1, 8>(rw, L + la + 4096, l16, so + 4096);
        so = sn; la = ln;
      } while (so != se);
      waitv<24>(); bar();
      waitv<12>(); bar(); waitv<0>(); bar();
      bar();
      w4(2); w4(3); w4(4);
    } else {
#pragma unroll
      for (int j = 0; j < 4; ++j) dma_lin<SC1, 2>(rw, L + la0 + 16384 + j * 2048, l16, so0 + 2048 + j * 20480);
      unsigned so = uni(so0 + 4096), la = uni(la0 + 32768);
#pragma unroll 1
      do {
        waitv<8>(); bar();
        dma_lin<SC1, 2>(rw, L + la, l16, so);
        sched();
        const unsigned sn = uni(so + 2048), ln = uni(la == la0 + 32768 ? la0 : la + 16384);
        sched();
#pragma unroll
        for (int j = 1; j < 4; ++j) dma_lin<SC1, 2>(rw, L + la + j * 2048, l16, so + j * 20480);
        so = sn; la = ln;
      } while (so != so0 + 20480);
      waitv<8>(); bar();

      w4(0);
      waitv<15>(); bar();

      w4(1);
      bar();

      waitv<0>();
    }
    bar();
    push(ev0 + EV_H, k.P * 16, 1, 16, n3);
    if (layer == 0) k4load(2);
    auto k4put = [&](int n, bool paced) {
      unsigned kb = L_W4 + w4slot(5 + n) * 30720 + a4 * 1024 + lane * 16; pin(kb);
#pragma unroll
      for (int j = 0; j < 8; ++j) if (j < 7 || isW) { *(LAS bf16x8*)(L + kb + j * 4096) = k4[n][j]; if (paced) __builtin_amdgcn_s_sleep(2); }
    };
    k4put(0, false); k4put(1, false);
    poll(ev0 + EV_H, me, 16);
    bar();
    k4put(2, true);
    bar(); bar();
    rot = 0;
    if (isW && layer == 0) w2n(0, 2);
    bar();
    if (layer == 0) {
      if (isW) w2n(2, 3);
      unsigned kb[2] = {L_D + lane * 16 + ((a4 >> 1) * 10 + a) * 1024, 0}; kb[1] = kb[0] + 61440; pin(kb[0]); pin(kb[1]);
      bar();
#pragma unroll
      for (int j = 0; j < 7; ++j) *(LAS bf16x8*)(L + kb[j / 15] + ((j / 5) % 3) * 20480 + (j % 5) * 2048) = k1[j];
      push(EV_PER_LAYER + EV_X, k.P * 16, 1, 16, n3);
#pragma unroll
      for (int j = 7; j < 25; ++j) *(LAS bf16x8*)(L + kb[j / 15] + ((j / 5) % 3) * 20480 + (j % 5) * 2048) = k1[j];
    }
  }
  if (k.b == 0 && a4 == 2 && lane == 0) *(GAS u64*)k.ws = tag;
#undef lane
#undef a
#undef me
#undef n3
#undef sw
#undef vo2
#undef so2
#undef so3
#undef so4
}

__global__ void __launch_bounds__(512, 2) fnet_glu_kernel(const char* x0, const char* wsr, const char* wsi, const char* wdr, const char* wdi, const char* wg, const char* wu, const char* wdn, char* out, char* ws) {
  __shared__ __attribute__((aligned(16))) char smem[163840];
  Ctx k;
  k.lds = (LAS char*)smem; k.lane = threadIdx.x & 63; k.b = blockIdx.x;
  const int w = __builtin_amdgcn_readfirstlane(threadIdx.x >> 6);
  if (w < 4 || w == 7) {
    const char* pc; asm volatile("s_getpc_b64 %0" : "=s"(pc));
    touch<0>(mk(pc, CODE_BYTES), k.lds + L_JUNK, (w < 4 ? threadIdx.x : threadIdx.x - 192) * 128, 0);
  }
  asm volatile("" : "+s"(x0), "+s"(wsr), "+s"(wsi), "+s"(wdr), "+s"(wdi), "+s"(wg), "+s"(wu), "+s"(wdn), "+s"(out), "+s"(ws));
  k.ws = ws;
  const int x = k.b & 7, i = k.b >> 3;
  k.kq = x >> 1; k.P = 8 * (x & 1) + 4 * (i >> 4) + 2 * ((i >> 2) & 1) + ((i >> 3) & 1); k.np = i & 3;
  k.P1 = 4 * (x >> 1) + (i >> 3); k.mat = x & 1; k.ct1 = i & 7;
  if (w < 4) consumer(k, x0, wg, wu, wdn, out, w);
  else loader(k, x0, (x >> 2) ? wsi : wsr, (x & 1) ? wdi : wdr, w - 4);
}
}

extern "C" void kernel_launch(void* const* d_in, const int* in_sizes, int n_in, void* d_out, int out_size, void* d_ws, size_t ws_size, hipStream_t stream) {
  fnet_glu_kernel<<<256, 512, 0, stream>>>((const char*)d_in[0], (const char*)d_in[1], (const char*)d_in[2], (const char*)d_in[3], (const char*)d_in[4],
                                           (const char*)d_in[6], (const char*)d_in[7], (const char*)d_in[8], (char*)d_out, (char*)d_ws);
}
```

```cpp
#include <hip/hip_runtime.h>

typedef unsigned long long u64;
typedef short bf16x8 __attribute__((ext_vector_type(8)));
typedef short s16x4 __attribute__((ext_vector_type(4)));
typedef float f32x4 __attribute__((ext_vector_type(4)));
typedef float f32x2 __attribute__((ext_vector_type(2)));
typedef unsigned u32x4 __attribute__((ext_vector_type(4)));
typedef unsigned u32x2 __attribute__((ext_vector_type(2)));
typedef __bf16 bf2 __attribute__((ext_vector_type(2)));
#define LAS __attribute__((address_space(3)))
#define GAS __attribute__((address_space(1)))
#define DI __device__ __forceinline__
typedef __amdgpu_buffer_rsrc_t rsrc_t;

#define STAMPS 1
#define FL 1
#define FINE 0

namespace {
constexpr int AHEAD = 10;
constexpr int LAG = 12, LAG2 = 48;
constexpr unsigned CODE_BYTES = 34944;
constexpr unsigned OFF_MB = 4096, MB_EV = 256 * 128;
constexpr int EV_X = 0, EV_AB = 1, EV_EX = 2, EV_X1 = 3, EV_H = 4, EV_PER_LAYER = 5, EV_RT = 0;
constexpr unsigned OFF_CS = 0x60000;
constexpr unsigned OFF_ST = 0x80000, ST_BUF = 8 * 2048 * 8;
constexpr unsigned OFF_XT = 0x100000, XT_BUF = 2048 * 640 * 2;
constexpr unsigned OFF_AB = OFF_XT + 3 * XT_BUF, AB_BUF = 2048 * 1280 * 2;
constexpr unsigned OFF_HT = OFF_AB + 2 * AB_BUF, HT_BUF = 2048 * 1536 * 2;
constexpr unsigned OFF_EX = OFF_HT + 2 * HT_BUF, EX_BUF = 256 * 12 * 2560;
constexpr unsigned OFF_W3 = OFF_EX + 2 * EX_BUF;
constexpr unsigned OFF_W4 = OFF_W3 + 16 * 20 * 12288;
constexpr unsigned WS_END = OFF_W4 + 8 * 48 * 5120;
constexpr unsigned OFF_STAMP = 0x6000000;
static_assert(OFF_MB + 10 * MB_EV <= OFF_CS && OFF_CS + 5 * 3072 * 4 <= OFF_ST && OFF_ST + 3 * ST_BUF <= OFF_XT && WS_END <= 0x4000000, "ws map");
constexpr unsigned L_MEAN = 0, L_RSTD = 512, L_CS1 = 1024, L_STD = 1536, L_CS3 = 2304, L_JUNK = 3072;
constexpr unsigned L_D = 4096;
constexpr unsigned L_WR = L_D + 61440, L_W4 = L_D + 6144, L_CP = L_D + 102400, L_XR = L_D + 110592, L_RED = L_W4 + 30720;
constexpr int w4slot(int ch) { return ch == 4 ? 4 : ((ch < 5 ? ch : ch - 5) + 2) & 3; }
constexpr int w4off(int s) { return w4slot(s / 3) * 30720 + (s % 3) * 10240; }
constexpr int SC1 = 16;
constexpr unsigned SPIN_CAP = 1u << 21;

DI rsrc_t mk(const void* p, unsigned bytes) { return __builtin_amdgcn_make_buffer_rsrc((void*)p, 0, bytes, 0x00020000); }
template <int N> DI void waitv() { asm volatile("s_waitcnt vmcnt(%0)" ::"n"(N) : "memory"); }
DI void bar() { asm volatile("s_waitcnt lgkmcnt(0)\n\ts_barrier" ::: "memory"); }
DI void lgk0() { __builtin_amdgcn_sched_barrier(0); __builtin_amdgcn_s_waitcnt(0xC07F); __builtin_amdgcn_sched_barrier(0); }
DI void barv() { lgk0(); asm volatile("s_barrier" ::: "memory"); __builtin_amdgcn_sched_barrier(0); }
DI void drain_bar() {
  asm volatile("s_waitcnt vmcnt(0) lgkmcnt(0)\n\ts_barrier" ::: "memory");
  __builtin_amdgcn_s_waitcnt(0);
  asm volatile("" ::: "memory");
}
DI void sched() { __builtin_amdgcn_sched_barrier(0); }
template <class T> DI void pin(T& x) { asm volatile("" : "+v"(x)); }
template <class T> DI void pins(T& x) { asm volatile("" : "+s"(x)); }
template <int AUX, int IMM> DI void dma(rsrc_t r, LAS char* l, unsigned vo, unsigned so) { __builtin_amdgcn_raw_ptr_buffer_load_lds(r, (LAS void*)l, 16, vo, so, IMM, AUX); }
template <int AUX> DI void touch(rsrc_t r, LAS char* l, unsigned vo, unsigned so) { __builtin_amdgcn_raw_ptr_buffer_load_lds(r, (LAS void*)l, 4, vo, so, 0, AUX); }
template <int AUX, int N> DI void dma_lin(rsrc_t r, LAS char* l, unsigned vo, unsigned so) {
#pragma unroll
  for (int j = 0; j < N; j += 4) {
    dma<AUX, 0>(r, l + j * 1024, vo, so + j * 1024);
    if (j + 1 < N) dma<AUX, 1024>(r, l + j * 1024, vo, so + j * 1024);
    if (j + 2 < N) dma<AUX, 2048>(r, l + j * 1024, vo, so + j * 1024);
    if (j + 3 < N) dma<AUX, 3072>(r, l + j * 1024, vo, so + j * 1024);
  }
}
DI unsigned uni(unsigned x) { x = __builtin_amdgcn_readfirstlane(x); asm volatile("" : "+s"(x)); return x; }
DI bf16x8 ld16(rsrc_t r, unsigned vo, unsigned so) { return __builtin_bit_cast(bf16x8, __builtin_amdgcn_raw_buffer_load_b128(r, vo, so, SC1)); }
DI void st16(rsrc_t r, u32x4 v, unsigned vo) { __builtin_amdgcn_raw_buffer_store_b128(v, r, vo, 0, SC1); }
DI bf16x8 lds16(LAS char* l, unsigned a) { return *(LAS bf16x8*)(l + a); }
DI unsigned pk(float lo, float hi) { f32x2 v = {lo, hi}; return __builtin_bit_cast(unsigned, __builtin_convertvector(v, bf2)); }
DI float blo(unsigned u) { return __uint_as_float(u << 16); }
DI float bhi(unsigned u) { return __uint_as_float(u & 0xffff0000u); }
DI f32x4 unpk(unsigned a, unsigned b) { return (f32x4){blo(a), bhi(a), blo(b), bhi(b)}; }
DI f32x4 mfma(bf16x8 a, bf16x8 b, f32x4 c) { return __builtin_amdgcn_mfma_f32_16x16x32_bf16(a, b, c, 0, 0, 0); }
DI void mma(f32x4& c, const bf16x8& a, const bf16x8& b) { asm volatile("v_mfma_f32_16x16x32_bf16 %0, %1, %2, %0" : "+v"(c) : "v"(a), "v"(b)); __builtin_amdgcn_sched_barrier(0); }
DI void mma0(f32x4& c, const bf16x8& a, const bf16x8& b) { asm volatile("v_mfma_f32_16x16x32_bf16 %0, %1, %2, 0" : "=&v"(c) : "v"(a), "v"(b)); __builtin_amdgcn_sched_barrier(0); }
DI void settle() { asm volatile("s_nop 7"); __builtin_amdgcn_sched_barrier(0); }
DI float dot2(unsigned a, unsigned b, float s) { return __builtin_amdgcn_fdot2_f32_bf16(__builtin_bit_cast(bf2, a), __builtin_bit_cast(bf2, b), s, false); }
DI float one(float v) { asm("" : "+v"(v)); return v; }
DI u32x4 swap_pair(unsigned a0, unsigned a1, unsigned b0, unsigned b1) {
  const auto r0 = __builtin_amdgcn_permlane16_swap(a0, b0, false, false);
  const auto r1 = __builtin_amdgcn_permlane16_swap(a1, b1, false, false);
  return (u32x4){r0[0], r1[0], r0[1], r1[1]};
}

struct Ctx {
  char* ws; LAS char* lds; int lane, b; unsigned top;
  int kq, P, np;
  int P1, mat, ct1;
};
struct St { unsigned t, c; };
#define STAMP_DECL St st_ = {0u, 0u}
#define stamp(k, w, id) do { sched(); const unsigned t_ = (unsigned)__builtin_amdgcn_s_memrealtime(), c_ = (unsigned)__builtin_amdgcn_s_memtime(); const bool m_ = (int)(threadIdx.x & 63) == (id); st_.t = m_ ? t_ : st_.t; st_.c = m_ ? c_ : st_.c; pin(st_.t); pin(st_.c); sched(); } while (0)
#define STAMP_OUT(k, w) do { if ((w) == 4 && k.b == 0) { unsigned d_ = 0; d_ = d_ > 20000u ? 20000u : d_; const unsigned t0_ = (unsigned)__builtin_amdgcn_s_memrealtime(); while ((unsigned)__builtin_amdgcn_s_memrealtime() - t0_ < 300u + d_ * 30u) __builtin_amdgcn_s_sleep(8); } } while (0)

DI u32x4 x_pack(int j, const f32x4 (&v)[5], unsigned (&res)[10]) {
  const int i1 = j < 2 ? 2 * j + 1 : 4;
#pragma unroll
  for (int i = 2 * j; i <= i1; ++i) { res[2 * i] = pk(v[i][0], v[i][1]); res[2 * i + 1] = pk(v[i][2], v[i][3]); }
  return swap_pair(res[4 * j], res[4 * j + 1], res[2 * i1], res[2 * i1 + 1]);
}
DI void x_part(rsrc_t rw, unsigned vx, unsigned vx2, int j, const f32x4 (&v)[5], unsigned (&res)[10], float& s, float& ss) {
  const u32x4 o = x_pack(j, v, res);
  st16(rw, o, j < 2 ? vx + j * 1024 : vx2);
#pragma unroll
  for (int e = 4 * j; e < (j < 2 ? 4 * j + 4 : 10); ++e) { s = dot2(res[e], 0x3f803f80u, s); ss = dot2(res[e], res[e], ss); }
  pin(s); pin(ss);
}
DI void x_stats(rsrc_t rw, unsigned vs, float s, float ss) {
  const auto r = __builtin_amdgcn_permlane16_swap(__float_as_uint(s), __float_as_uint(ss), false, false);
  const unsigned t = __float_as_uint(__uint_as_float(r[0]) + __uint_as_float(r[1]));
  const auto r2 = __builtin_amdgcn_permlane32_swap(t, t, false, false);
  const float u = __uint_as_float(r2[0]) + __uint_as_float(r2[1]);
  __builtin_amdgcn_raw_buffer_store_b32(__float_as_uint(u), rw, vs, 0, SC1);
}

DI int rot1(const Ctx& k) { const int r = ((k.ct1 * 5) >> 2) + (((k.P1 & 3) * 5) >> 1); return r >= 10 ? r - 10 : r; }

#define PHASE_LANE() Ctx k = k0; int w = w0; asm volatile("" : "+v"(k.lane), "+s"(w), "+s"(k.P), "+s"(k.kq), "+s"(k.np), "+s"(k.P1), "+s"(k.ct1), "+s"(k.mat)); \
  const int tfo = 8 * k.P + 4 * (k.kq >> 1) + w, cto = 2 * k.np + (k.kq & 1); (void)tfo; (void)cto; \
  const int lane = k.lane, q = lane >> 4, c = lane & 15, qs = 2 * (q & 1) + (q >> 1); (void)qs; \
  const unsigned rowrd = (c >> 3) * 1024 + (c & 7) * 128 + ((q ^ (c >> 1)) * 16); (void)rowrd
#define X_ADDR(xb) unsigned vx = OFF_XT + (xb) * XT_BUF + tfo * 20480 + (10 * cto + qs) * 256 + c * 16, vx2 = vx + 2048 + ((lane & 16) << 26), \
  vs = OFF_ST + (xb) * ST_BUF + tfo * 1024 + cto * 128 + lane * 4 + ((lane & 32) << 25); pin(vx); pin(vx2); pin(vs)

DI void consumer(const Ctx& k0, const char* x0, const char* wg, const char* wu, const char* wdn, char* out, const int w0) {
  LAS char* const L = k0.lds;
  const rsrc_t rw = mk(k0.ws, WS_END);
  unsigned res[10];
  STAMP_DECL;
  __builtin_amdgcn_s_setprio(2);
  stamp(k0, w0, 0);
  auto retile = [&](const Ctx& k0, const int w) {
    int e = ((k0.b >> 7) * 64 + (k0.b & 63)) * 4 + w;
    pins(e);
    const int lane = k0.lane;
    const int g3 = ((e >> 2) * 2731) >> 16, ks3 = 4 * g3 + w, st3 = (e >> 2) - g3 * 24, ks4 = (e * 6554) >> 16, st4 = e - ks4 * 10;
    LAS char* t[3];
#pragma unroll
    for (int m = 0; m < 3; ++m) { const int u = rot1(k0) + (m < 2 ? 4 + w : 8 + (w >> 1)); t[m] = L + L_D + (u >= 10 ? u - 10 : u) * 10240 + (m < 2 ? m : w & 1) * 4096; }
    __builtin_amdgcn_s_sleep(LAG);
#pragma unroll
    for (int m = 0; m < 3; ++m) {
      const unsigned stride = m < 2 ? 3072 : 1280;
      const rsrc_t rr = mk(m == 0 ? wg : (m == 1 ? wu : wdn), e < 480 ? 640 * 1536 * 2 : 0);
#pragma unroll
      for (int j = 0; j < 4; ++j) dma<0, 0>(rr, t[m] + j * 1024, (lane >> 3) * stride + (lane & 7) * 16, (32 * (m < 2 ? ks3 : ks4) + 8 * j) * stride + 128 * (m < 2 ? st3 : st4));
    }
    const rsrc_t ro = mk(k0.ws, e < 480 ? WS_END : 0);
    const bf16x8 ones = {0x3f80, 0x3f80, 0x3f80, 0x3f80, 0x3f80, 0x3f80, 0x3f80, 0x3f80};
    const unsigned tr = (lane >> 4) * 1024 + ((lane >> 2) & 3) * 128 + (lane & 3) * 8;
    __builtin_amdgcn_s_waitcnt(0);
    asm volatile("" ::: "memory");
    u32x4 f[3][4];
#pragma unroll
    for (int m = 0; m < 3; ++m)
#pragma unroll
      for (int n = 0; n < 4; ++n) {
        const u32x2 lo = __builtin_bit_cast(u32x2, __builtin_amdgcn_ds_read_tr16_b64_v4i16((LAS s16x4*)(t[m] + tr + 32 * n)));
        const u32x2 hi = __builtin_bit_cast(u32x2, __builtin_amdgcn_ds_read_tr16_b64_v4i16((LAS s16x4*)(t[m] + tr + 32 * n + 512)));
        f[m][n] = (u32x4){lo[0], lo[1], hi[0], hi[1]};
      }
    stamp(k0, w, 61);
#pragma unroll
    for (int m = 0; m < 3; ++m) {
      f32x4 cs[4];
#pragma unroll
      for (int n = 0; n < 4; ++n) {
        const int nf = 4 * (m < 2 ? st3 : st4) + n;
        st16(ro, f[m][n], lane * 16 + (m < 2 ? OFF_W3 + m * 1024 + ks3 * 12288 + ((nf * 171) >> 10) * 19 * 12288 + nf * 2048 : OFF_W4 + ks4 * 5120 + ((nf * 205) >> 10) * 47 * 5120 + nf * 1024));
        if (m < 2) cs[n] = mfma(ones, __builtin_bit_cast(bf16x8, f[m][n]), (f32x4){0.f, 0.f, 0.f, 0.f});
      }
      if (m < 2) *(LAS float*)(L + L_CP + (m * 64 + lane) * 16 + w * 4) = (lane & 32) ? ((lane & 16) ? cs[3][0] : cs[2][0]) : ((lane & 16) ? cs[1][0] : cs[0][0]);
    }
    stamp(k0, w, 62);
  };
#pragma unroll 1
  for (int layer = 0; layer < 2; ++layer) {
    const int sb = 3 + layer * 24;
    {
      PHASE_LANE();
      unsigned vo = OFF_AB + layer * AB_BUF + ((k.mat * 4 + (k.ct1 >> 1)) * 64 + 4 * k.P1 + w) * 10240 + 5 * (k.ct1 & 1) * 1024 + (qs * 16 + c) * 16;
      unsigned ta = (32 * w + 4 * q) * 4, tc = c * 4;
      pin(vo); pin(ta); pin(tc);
      f32x4 acc[2][5], mean[2], rstd[2]; float cs1[5];
      bf16x8 wf[4][5];
      auto tables = [&]() {
#pragma unroll
        for (int i = 0; i < 5; ++i) cs1[i] = *(LAS float*)(L + L_CS1 + tc + i * 64);
#pragma unroll
        for (int t = 0; t < 2; ++t) { mean[t] = *(LAS f32x4*)(L + ta + L_MEAN + 64 * t); rstd[t] = *(LAS f32x4*)(L + ta + L_RSTD + 64 * t); }
      };
#pragma unroll
      for (int i = 0; i < 5; ++i) { acc[0][i] = (f32x4){0.f, 0.f, 0.f, 0.f}; acc[1][i] = (f32x4){0.f, 0.f, 0.f, 0.f}; }
      if (layer == 0) {
        bf16x8 xf[4][2], cf[4], ones = {0x3f80, 0x3f80, 0x3f80, 0x3f80, 0x3f80, 0x3f80, 0x3f80, 0x3f80};
        f32x4 as[2], aq[2], ac[2];
        unsigned wt = rot1(k) * 10240, wa0 = L_D + rowrd;
        pin(wa0);
        unsigned wa = wa0 + wt, xr = L_XR + w * 4096 + rowrd, x_slot = 32768;
#pragma unroll
        for (int i = 0; i < 5; ++i) { pin(acc[0][i]); pin(acc[1][i]); }
#pragma unroll
        for (int t = 0; t < 2; ++t) { as[t] = (f32x4){0.f, 0.f, 0.f, 0.f}; aq[t] = as[t]; ac[t] = as[t]; pin(as[t]); pin(aq[t]); pin(ac[t]); }
        pin(wa); pin(xr); pin(ones);
        if (k.P & 1) retile(k, w);
        bar();
        stamp(k, w, sb + 0);
        unsigned xn[2] = {xr + 32768, (xr + 32768) ^ 64}, wn[2] = {wa, wa ^ 64};
        const unsigned wo = w * 2048;
        auto rd = [&](int p, int n) {
          const int e = n & 1, m = n >> 1;
          if (m < 2) xf[2 * p + e][m] = lds16(L, xn[e] + m * 2048);
          else if (m < 7) wf[2 * p + e][m - 2] = lds16(L, wn[e] + (m - 2) * 2048);
          else cf[2 * p + e] = lds16(L, wn[e] + wo);
        };
#pragma unroll
        for (int n = 0; n < 16; ++n) rd(0, n);
        lgk0();
        int left = 5;
#pragma unroll 1
        do {
#pragma unroll
          for (int p = 0; p < 2; ++p) {
#pragma unroll
            for (int n = 0; n < 32; ++n) {
              const int m = n >> 1, h = 2 * p + ((n ^ m) & 1);
              if (n == 10) barv();
              if (m < 10) { const int t = m / 5, i = t ? 9 - m : m; mma(acc[t][i], xf[h][t], wf[h][i]); }
              else if (m == 10) mma(aq[1], xf[h][1], xf[h][1]);
              else if (m == 11) mma(as[1], xf[h][1], ones);
              else if (m == 12) mma(as[0], xf[h][0], ones);
              else if (m == 13) mma(aq[0], xf[h][0], xf[h][0]);
              else if (m == 14) mma(ac[0], ones, cf[h]);
              else mma(ac[1], ones, wf[h][4]);
              if (n == 0) { x_slot = x_slot == 32768 ? 0 : x_slot + 16384; pins(x_slot); }
              if (n == 1) { wt = wt == 92160 ? 0 : wt + 10240; pins(wt); }
              if (n == 2) { xn[0] = xr + x_slot; wn[0] = wa0 + wt; pin(xn[0]); pin(wn[0]); }
              if (n == 4) { xn[1] = xn[0] ^ 64; wn[1] = wn[0] ^ 64; pin(xn[1]); pin(wn[1]); }
              if (n == 6 && p) { --left; pins(left); }
              if (n >= 10 && n < 26) rd(p ^ 1, n - 10);
              sched();
            }
            lgk0();
          }
        } while (left);
        settle();
        stamp(k, w, 50);
        if (q == 0) { *(LAS float*)(L + L_CS1 + (16 * w + c) * 4) = ac[0][0]; *(LAS float*)(L + L_CS1 + (64 + c) * 4) = ac[1][0]; }
#pragma unroll
        for (int t = 0; t < 2; ++t) {
          const float s = ((c & 2) ? ((c & 1) ? as[t][3] : as[t][2]) : ((c & 1) ? as[t][1] : as[t][0])) * (1.f / 640.f);
          const float ss = ((c & 2) ? ((c & 1) ? aq[t][3] : aq[t][2]) : ((c & 1) ? aq[t][1] : aq[t][0])) * (1.f / 640.f);
          const float r = __builtin_amdgcn_rsqf(fmaxf(ss - s * s, 0.f) + 1e-5f);
          if ((c >> 2) == q) { *(LAS float*)(L + L_MEAN + (32 * w + 16 * t + c) * 4) = -s; *(LAS float*)(L + L_RSTD + (32 * w + 16 * t + c) * 4) = k.mat ? -r : r; }
        }
        bar();
        tables();
      } else {
#define KS(s) (((s) + 12) % 20)
        unsigned wb[2][2] = {{L_D + rowrd, L_D + rowrd + 51200}, {L_D + (rowrd ^ 64), L_D + (rowrd ^ 64) + 51200}};
        unsigned xv[2] = {OFF_XT + XT_BUF + (8 * k.P1 + 2 * w) * 20480 + lane * 16, 0}; xv[1] = xv[0] + 20480;
        pin(wb[0][0]); pin(wb[0][1]); pin(wb[1][0]); pin(wb[1][1]); pin(xv[0]); pin(xv[1]);
        constexpr int R = AHEAD + 2;
        bf16x8 xa[R];
        auto req = [&](int l) { const int ks = KS(2 * (l >> 2) + (l & 1)); if (l < 40) xa[l % R] = ld16(rw, xv[(l >> 1) & 1] + (ks & 3) * 1024, (ks >> 2) * 4096); };
        auto rd = [&](int d, int n) { const int s = 2 * d + (n & 1); wf[s & 3][n >> 1] = lds16(L, wb[s & 1][KS(s) / 10] + ((KS(s) >> 1) % 5) * 10240 + (n >> 1) * 2048); };
        bar();
        stamp(k, w, sb + 0);
#pragma unroll
        for (int l = 0; l < AHEAD; ++l) { req(l); sched(); }
#pragma unroll
        for (int n = 0; n < 10; ++n) rd(0, n);
        lgk0();
#pragma unroll
        for (int d = 0; d < 10; ++d) {
          if (d == 8) barv();
          if (d == 9) { tables(); sched(); }
#pragma unroll
          for (int n = 0; n < 20; ++n) {
            const int m = n >> 1, t = m / 5, i = t ? 9 - m : m, e = (n ^ m) & 1;
            if (d == 0 && !(n & 1)) mma0(acc[t][i], xa[(2 * t + e) % R], wf[e][i]); else mma(acc[t][i], xa[(4 * d + 2 * t + e) % R], wf[(2 * d + e) & 3][i]);
            if (n % 5 == 4) req(4 * d + n / 5 + AHEAD);
            else if (d < 9 && n < 13) { const int j = n - n / 5; if (j < 10) rd(d + 1, j); }
            if (n == 18 && d < 9) lgk0();
            sched();
          }
        }
        settle();
      }
      stamp(k, w, sb + 1);
#pragma unroll
      for (int i = 0; i < 5; ++i) {
        const float cs = cs1[i];
        const f32x4 a = (mean[0] * cs + acc[0][i]) * rstd[0], bb = (mean[1] * cs + acc[1][i]) * rstd[1];
        st16(rw, swap_pair(pk(a[0], a[1]), pk(a[2], a[3]), pk(bb[0], bb[1]), pk(bb[2], bb[3])), vo + i * 1024);
      }
      if (layer == 0 && (k.P & 1) && w < 2) {
        const int j = (k.b >> 7) * 64 + (k.b & 63), g3 = (j * 2731) >> 16;
        const f32x4 t = *(LAS f32x4*)(L + L_CP + (w * 64 + lane) * 16);
        if (j < 120) __builtin_amdgcn_raw_buffer_store_b32(__float_as_uint((t[0] + t[1]) + (t[2] + t[3])), rw, lane * 4, OFF_CS + (g3 * 3072 + w * 1536 + 64 * (j - g3 * 24)) * 4, SC1);
      }
      stamp(k, w, sb + 2);
      drain_bar();
      stamp(k, w, sb + 3);
    }
    {
      PHASE_LANE();
      const int wm = w >> 1, wn = w & 1;
      f32x4 acc[4][5];
      bf16x8 fa[2][4], fl[2][2], fb[2][2][4];
#pragma unroll
      for (int t = 0; t < 4; ++t)
#pragma unroll
        for (int i = 0; i < 5; ++i) { acc[t][i] = (f32x4){0.f, 0.f, 0.f, 0.f}; pin(acc[t][i]); }
      unsigned wb = L_WR + wm * 8192 + rowrd, ab = L_D + wn * 5120 + lane * 16;
      unsigned ws_slot = 65536, ab_slot = 20480;
      unsigned ev[2], ev8[2], en = w == k.kq ? 0 : WS_END;
      ev[0] = OFF_EX + layer * EX_BUF + ((k.P * 16 + w * 4 + k.np) * 3 + ((k.kq - w) & 3) - 1) * 10240 + lane * 16; ev[1] = ev[0] + 5120;
      ev8[0] = ev[0] - lane * 8 + 2048; ev8[1] = ev8[0] + 5120;
      unsigned lo = L_D + 20480 + w * 10240 + lane * 16, lo8 = lo - lane * 8 + 2048;
      pin(lo); pin(lo8);
      pin(wb); pin(ab); pin(ev[0]); pin(ev[1]); pin(ev8[0]); pin(ev8[1]); pins(en);
      const rsrc_t re = mk(k.ws, en);
      bar();
#pragma unroll
      for (int n = 0; n < 8; ++n) fb[0][n & 1][n >> 1] = lds16(L, (wb ^ ((n & 1) * 64)) + 49152 + (n >> 1) * 2048);
      unsigned an = ab + 20480, w0 = wb + 65536, w1 = w0 ^ 64;
      pin(an); pin(w0); pin(w1);
      bar();
      stamp(k, w, sb + 4);
#pragma unroll
      for (int n = 0; n < 10; ++n) { if (n < 8) fa[n & 1][n >> 1] = lds16(L, ab + (n & 1) * 10240 + (n >> 1) * 1024); else fl[0][n & 1] = lds16(L, ab + (n & 1) * 10240 + 4096); }
      lgk0();
      int left = 7;
      auto tile = [&](int p, bool last) {
#pragma unroll
        for (int n = 0; n < 40; ++n) {
          const int m = n >> 1, i = m >> 2, t = (i & 1) ? 3 - (m & 3) : m & 3, kk = (n ^ m) & 1, e = n & 1;
          if (n == 16) barv();
          mma(acc[t][i], i < 4 ? fa[kk][i & 3] : fl[p][kk], fb[p][kk][t]);
          if (n < 8) fb[p ^ 1][e][m] = lds16(L, (e ? w1 : w0) + m * 2048);
          else if (n >= 16 && n < 20) fa[e][m - 8] = lds16(L, an + e * 10240 + (m - 8) * 1024);
          else if (m == 10) fl[p ^ 1][e] = lds16(L, an + e * 10240 + 4096);
          else if (m == 12 || m == 16) fa[e][m / 4 - 1] = lds16(L, an + e * 10240 + (m / 4 - 1) * 1024);
          else if (!last) {
            if (n == 8) { ws_slot = ws_slot == 81920 ? 0 : ws_slot + 16384; pins(ws_slot); }
            if (n == 9) { ab_slot = ab_slot == 40960 ? 0 : ab_slot + 20480; pins(ab_slot); }
            if (n == 10) { w0 = wb + ws_slot; pin(w0); }
            if (n == 11) { w1 = w0 ^ 64; pin(w1); }
            if (n == 34) { an = ab + ab_slot; pin(an); if (p) { --left; pins(left); } }
          }
          sched();
        }
        lgk0();
      };
#pragma unroll 1
      do { tile(0, false); tile(1, false); } while (left);
      tile(0, true);
      {
        unsigned o[10];
        auto send = [&](int t, int j) {
          if (j < 5) { o[2 * j] = pk(acc[t][j][0], acc[t][j][1]); o[2 * j + 1] = pk(acc[t][j][2], acc[t][j][3]); }
          if (j == 5) st16(re, (u32x4){o[0], o[1], o[2], o[3]}, ev[t >> 1] + (t & 1) * 2560);
          if (j == 6) { *(LAS u32x4*)(L + lo + t * 2560) = (u32x4){o[0], o[1], o[2], o[3]}; *(LAS u32x2*)(L + lo8 + t * 2560) = (u32x2){o[8], o[9]}; }
          if (j == 7) st16(re, (u32x4){o[4], o[5], o[6], o[7]}, ev[t >> 1] + (t & 1) * 2560 + 1024);
          if (j == 8) { __builtin_amdgcn_raw_buffer_store_b64((u32x2){o[8], o[9]}, re, ev8[t >> 1] + (t & 1) * 2560, 0, SC1); *(LAS u32x4*)(L + lo + t * 2560 + 1024) = (u32x4){o[4], o[5], o[6], o[7]}; }
        };
#pragma unroll
        for (int n = 0; n < 40; ++n) {
          const int t = n / 10, i = (n % 10) >> 1, kk = (n ^ i) & 1;
          mma(acc[t][i], i < 4 ? fa[kk][i & 3] : fl[1][kk], fb[1][kk][t]);
          if (t) send(t - 1, n % 10);
          sched();
        }
        stamp(k, w, sb + 5);
#pragma unroll
        for (int j = 0; j < 9; ++j) { send(3, j); sched(); }
      }
      stamp(k, w, sb + 6);
      drain_bar();
      stamp(k, w, sb + 7);
      if (layer == 0) {
        const rsrc_t rx = mk(x0, 2048 * 640 * 2);
#pragma unroll
        for (int j = 0; j < 3; ++j) {
          const int u = 64 * j + lane, r = (u * 205) >> 11;
          dma<0, 0>(rx, L + L_D + w * 3072 + j * 1024, r * 1120 + u * 16 + (j == 2 ? (lane & 32) << 25 : 0), 16 * tfo * 1280 + cto * 160);
        }
        __builtin_amdgcn_s_waitcnt(0);
        asm volatile("" ::: "memory");
#pragma unroll
        for (int i = 0; i < 5; ++i) { const u32x2 t = *(LAS u32x2*)(L + L_D + w * 3072 + c * 160 + 8 * q + 32 * i); res[2 * i] = t[0]; res[2 * i + 1] = t[1]; }
      }
      f32x4 v[5];
      {
        LAS char* t = L + L_D + 20480 + k.kq * 10240 + w * 2560;
        const u32x4 a = *(LAS u32x4*)(t + lane * 16), bb = *(LAS u32x4*)(t + 1024 + lane * 16); const u32x2 d = *(LAS u32x2*)(t + 2048 + lane * 8);
        v[0] = unpk(a[0], a[1]); v[1] = unpk(a[2], a[3]); v[2] = unpk(bb[0], bb[1]); v[3] = unpk(bb[2], bb[3]); v[4] = unpk(d[0], d[1]);
#pragma unroll
        for (int i = 0; i < 5; ++i) { v[i] += unpk(res[2 * i], res[2 * i + 1]); pin(v[i]); }
      }
      unsigned rv = OFF_EX + layer * EX_BUF + (k.P * 16 + k.kq * 4 + k.np) * 30720 + w * 2560 + lane * 16, rv8 = rv - lane * 8;
      pin(rv); pin(rv8);
      X_ADDR(2 * layer);
      bar();
      stamp(k, w, sb + 8);
      u32x4 e[3][2]; u32x2 e8[3];
#pragma unroll
      for (int j = 0; j < 3; ++j)
#pragma unroll
        for (int n = 0; n < 3; ++n) {
          if (j < 2) e[n][j] = __builtin_amdgcn_raw_buffer_load_b128(rw, rv + j * 1024, n * 10240, SC1);
          else e8[n] = __builtin_amdgcn_raw_buffer_load_b64(rw, rv8 + 2048, n * 10240, SC1);
          sched();
        }
      float s = 0.f, ss = 0.f;
#pragma unroll
      for (int j = 0; j < 3; ++j) {
#pragma unroll
        for (int n = 0; n < 3; ++n) {
          if (j < 2) { v[2 * j] += unpk(e[n][j][0], e[n][j][1]); v[2 * j + 1] += unpk(e[n][j][2], e[n][j][3]); }
          else v[4] += unpk(e8[n][0], e8[n][1]);
        }
        x_part(rw, vx, vx2, j, v, res, s, ss);
        sched();
      }
      x_stats(rw, vs, s, ss);
      stamp(k, w, sb + 9);
      drain_bar();
      stamp(k, w, sb + 10);
    }
    {
      PHASE_LANE();
      const int wm = w >> 1, wn = w & 1;
      f32x4 acc[4][6];
      bf16x8 fa[2][5], fl[2][2], fb[2][2][4];
#pragma unroll
      for (int t = 0; t < 4; ++t)
#pragma unroll
        for (int i = 0; i < 6; ++i) { acc[t][i] = (f32x4){0.f, 0.f, 0.f, 0.f}; pin(acc[t][i]); }
      unsigned wb = L_WR + wn * 6144 + lane * 16, xb = L_D + wm * 8192 + lane * 16;
      unsigned w_slot = 73728, x_slot = 16384;
      unsigned vo = OFF_HT + layer * HT_BUF + ((8 * k.P + 4 * wm + (q & 1)) * 48 + 3 * (4 * k.kq + k.np)) * 1024 + (6 * wn + (q >> 1)) * 256 + c * 16, vo2 = vo + 98304;
      unsigned ta = (64 * wm + c) * 4, tc = L_CS3 + (48 * wn + 4 * q) * 4;
      unsigned sa = L_D + 49152 + (2 * w + (lane >> 5)) * 1024 + (lane & 31) * 4, sw = (32 * w + 16 * (lane >> 5) + c) * 4;
      pin(wb); pin(xb); pin(vo); pin(vo2); pin(ta); pin(tc); pin(sa); pin(sw);
      bar();
#pragma unroll
      for (int n = 0; n < 12; ++n) { if (n < 10) fa[n & 1][n >> 1] = lds16(L, wb + 49152 + (n & 1) * 12288 + (n >> 1) * 1024); else fl[0][n & 1] = lds16(L, wb + 49152 + (n & 1) * 12288 + 5120); }
      unsigned wn3 = wb + 73728, xn = xb + 16384;
      pin(wn3); pin(xn);
      bar();
      stamp(k, w, sb + 11);
#pragma unroll
      for (int n = 0; n < 8; ++n) fb[0][n & 1][n >> 1] = lds16(L, xb + n * 1024);
      lgk0();
      int left = 4;
      auto mm = [&](int p, int n) { const int m = n >> 1, i = m >> 2, t = (i & 1) ? 3 - (m & 3) : m & 3, kk = (n ^ m) & 1; mma(acc[t][i], i < 5 ? fa[kk][i < 5 ? i : 0] : fl[p][kk], fb[p][kk][t]); };
      auto rd = [&](int p, int n) {
        const int m = n >> 1, e = n & 1;
        if (m == 8 || m == 9) fa[e][m - 8] = lds16(L, wn3 + e * 12288 + (m - 8) * 1024);
        else if (m == 10) fl[p ^ 1][e] = lds16(L, wn3 + e * 12288 + 5120);
        else if (m == 12 || m == 16 || m == 20) fa[e][m / 4 - 1] = lds16(L, wn3 + e * 12288 + (m / 4 - 1) * 1024);
        else if (m == 11) fb[p ^ 1][e][0] = lds16(L, xn + e * 1024);
        else if (m >= 13 && m <= 15) fb[p ^ 1][e][m - 12] = lds16(L, xn + e * 1024 + (m - 12) * 2048);
      };
#pragma unroll 1
      do {
#pragma unroll
        for (int p = 0; p < 2; ++p) {
#pragma unroll
          for (int n = 0; n < 48; ++n) {
            if (n == 16) barv();
            mm(p, n); rd(p, n);
            if (n == 34) { x_slot = x_slot == 32768 ? 0 : x_slot + 16384; pins(x_slot); }
            if (n == 35) { w_slot = w_slot == 73728 ? 0 : w_slot + 24576; pins(w_slot); }
            if (n == 42) { wn3 = wb + w_slot; xn = xb + x_slot; pin(wn3); pin(xn); }
            if (n == 43 && p) { --left; pins(left); }
            sched();
          }
          lgk0();
        }
      } while (left);
      float mean[4], rl2[4], sd[4];
      f32x4 cs[6];
      unsigned h[4][2];
      f32x4 o[4];
      auto corr1 = [&](int f, int n) { const int t = n >> 3, i = 2 * f + ((n >> 2) & 1), e = n & 3; if (n < 32) acc[t][i][e] = one(fmaf(cs[i][e], mean[t], acc[t][i][e])); };
      auto glu1 = [&](int n) {
        const int t = n / 15, j = n % 15;
        if (j < 2) { o[t][2 * j] = one(acc[t][0][2 * j] * rl2[t]); o[t][2 * j + 1] = one(acc[t][0][2 * j + 1] * rl2[t]); }
        else if (j < 6) o[t][j - 2] = __builtin_amdgcn_exp2f(o[t][j - 2]);
        else if (j < 8) { o[t][2 * j - 12] = one(fmaf(o[t][2 * j - 12], sd[t], sd[t])); o[t][2 * j - 11] = one(fmaf(o[t][2 * j - 11], sd[t], sd[t])); }
        else if (j < 12) o[t][j - 8] = __builtin_amdgcn_rcpf(o[t][j - 8]);
        else if (j < 14) { o[t][2 * j - 24] = one(o[t][2 * j - 24] * acc[t][1][2 * j - 24]); o[t][2 * j - 23] = one(o[t][2 * j - 23] * acc[t][1][2 * j - 23]); }
        else { h[t][0] = pk(o[t][0], o[t][1]); h[t][1] = pk(o[t][2], o[t][3]); }
      };
      auto glu = [&](int f, int t, int from) {
        if (from < 1) { o[t] = acc[t][2 * f] * rl2[t]; asm("" : "+v"(o[t])); }
        if (from < 2) {
#pragma unroll
          for (int r = 0; r < 4; ++r) o[t][r] = __builtin_amdgcn_exp2f(o[t][r]);
        }
        o[t] = o[t] * sd[t] + sd[t]; asm("" : "+v"(o[t]));
#pragma unroll
        for (int r = 0; r < 4; ++r) o[t][r] = __builtin_amdgcn_rcpf(o[t][r]);
        o[t] *= acc[t][2 * f + 1];
        h[t][0] = pk(o[t][0], o[t][1]); h[t][1] = pk(o[t][2], o[t][3]);
      };
      auto tab2 = [&](float (&d)[4], unsigned off, int j) { d[2 * j] = *(LAS float*)(L + ta + off + 128 * j); d[2 * j + 1] = *(LAS float*)(L + ta + off + 128 * j + 64); };
      float pv[8], s1 = 0.f, s2 = 0.f;
#pragma unroll
      for (int n = 0; n < 26; ++n) {
        if (n == 16) barv();
        mm(0, n); rd(0, n);
        if (n < 2) {
#pragma unroll
          for (int j = 0; j < 4; ++j) pv[4 * n + j] = *(LAS float*)(L + sa + 128 * (4 * n + j));
        }
        if (n == 2) { cs[0] = *(LAS f32x4*)(L + tc); cs[1] = *(LAS f32x4*)(L + tc + 384); }
        if (n == 6) { s1 = one(pv[0] + pv[1]); s2 = one(pv[2] + pv[3]); }
        if (n == 7 || n == 8) { s1 = one(s1 + pv[2 * n - 10]); s2 = one(s2 + pv[2 * n - 9]); }
        if (n == 9) s1 = one(s1 + s2);
        if (n == 10) { s2 = one(s1 * (1.f / 640.f)); s1 = one(s1 * (1.f / 640.f)); }
        if (n == 11) { const auto r = __builtin_amdgcn_permlane16_swap(__float_as_uint(s1), __float_as_uint(s2), false, false); s1 = one(__uint_as_float(r[0])); s2 = one(__uint_as_float(r[1])); }
        if (n == 12) { s2 = one(fmaf(-s1, s1, s2)); s1 = one(-s1); }
        if (n == 13) { *(LAS float*)(L + sw + L_MEAN) = s1; s2 = one(fmaxf(s2, 0.f)); }
        if (n == 14) s2 = one(s2 + 1e-5f);
        if (n == 15) s1 = __builtin_amdgcn_rsqf(s2);
        if (n == 16 || n == 17) tab2(mean, L_MEAN, n & 1);
        if (n == 18) s2 = one(s2 * s1);
        if (n == 19) s1 = one(s1 * -1.44269504089f);
        if (n == 20) *(LAS float*)(L + sw + L_STD) = s2;
        if (n == 21) *(LAS float*)(L + sw + L_RSTD) = s1;
        sched();
      }
#pragma unroll
      for (int n = 26; n < 48; ++n) {
        mm(0, n); rd(0, n);
        if (n < 34) corr1(0, n - 26);
        if (n >= 34 && n < 40) { corr1(0, 2 * n - 60); corr1(0, 2 * n - 59); }
        if (n == 40 || n == 41) corr1(0, n - 20);
        if (n == 42) { cs[2] = *(LAS f32x4*)(L + tc + 64); cs[3] = *(LAS f32x4*)(L + tc + 384 + 64); }
        if (n > 42) { corr1(0, 2 * n - 64); corr1(0, 2 * n - 63); }
        sched();
      }
      barv();
#pragma unroll
      for (int n = 0; n < 48; ++n) {
        mm(1, n);
        if (n < 16) { corr1(1, 2 * n); corr1(1, 2 * n + 1); }
        if (n == 12 || n == 13) tab2(rl2, L_RSTD, n & 1);
        if (n == 14 || n == 15) tab2(sd, L_STD, n & 1);
        if (n == 16 || n == 17) cs[n - 12] = *(LAS f32x4*)(L + tc + 128 + 384 * (n & 1));
        if (n >= 18) glu1(n - 18);
        sched();
      }
      stamp(k, w, sb + 12);
      st16(rw, swap_pair(h[0][0], h[0][1], h[1][0], h[1][1]), vo);
      sched();
      glu(0, 2, 0); glu(0, 3, 0);
      st16(rw, swap_pair(h[2][0], h[2][1], h[3][0], h[3][1]), vo2);
      sched();
#pragma unroll
      for (int f = 1; f < 3; ++f)
#pragma unroll
        for (int t = 0; t < 4; t += 2) {
          if (f == 2) { acc[t][4] += cs[4] * mean[t]; acc[t][5] += cs[5] * mean[t]; acc[t + 1][4] += cs[4] * mean[t + 1]; acc[t + 1][5] += cs[5] * mean[t + 1]; }
          glu(f, t, 0); glu(f, t + 1, 0);
          st16(rw, swap_pair(h[t][0], h[t][1], h[t + 1][0], h[t + 1][1]), (t ? vo2 : vo) + f * 512);
          sched();
        }
      stamp(k, w, sb + 13);
    }
    {
      PHASE_LANE();
      const int kh = w & 1;
      unsigned wb[3] = {L_W4 + kh * 5120 + lane * 16, 0, 0}; wb[1] = wb[0] + 51200; wb[2] = wb[0] + 102400;
      pin(wb[0]); pin(wb[1]); pin(wb[2]);
      bar();
      constexpr int R = AHEAD + 2;
      bf16x8 ha[R], wf[6][5];
      f32x4 acc[2][5];
      auto rdw = [&](int s, int i) { wf[s % 6][i] = lds16(L, wb[w4off(s) / 51200] + w4off(s) % 51200 + i * 1024); };
#pragma unroll
      for (int n = 0; n < 30; ++n) rdw(n / 5, n % 5);
      lgk0();
      drain_bar();
      stamp(k, w, sb + 14);
      sched();
      unsigned hv[2] = {OFF_HT + layer * HT_BUF + tfo * 49152 + kh * 1024 + lane * 16, OFF_HT + layer * HT_BUF + (tfo ^ 1) * 49152 + kh * 1024 + lane * 16};
      unsigned rd = L_RED + w * 5120 + lane * 16, wr = L_RED + (w ^ 1) * 5120 + lane * 16;
      pin(hv[0]); pin(hv[1]); pin(rd); pin(wr);
      X_ADDR(1);
      unsigned ow = L_W4 + 61440 + w * 3072 + c * 160 + 8 * q, orr = L_W4 + 61440 + w * 3072 + lane * 16, vo3[3];
#pragma unroll
      for (int j = 0; j < 3; ++j) { const int u = 64 * j + lane; vo3[j] = 16 * tfo * 1280 + cto * 160 + ((u * 205) >> 11) * 1120 + u * 16 + (j == 2 ? (lane & 32) << 25 : 0); pin(vo3[j]); }
      pin(ow); pin(orr);
#pragma unroll
      for (int i = 0; i < 5; ++i) { acc[0][i] = unpk(res[2 * i], res[2 * i + 1]); pin(acc[0][i]); }
      bar();
      stamp(k, w, sb + 15);
      auto req = [&](int l) { if (l < 48) ha[l % R] = ld16(rw, hv[1 - ((l >> 1) & 1)] + (l & 1) * 2048, (l >> 2) * 4096); };
#pragma unroll
      for (int l = 0; l < AHEAD; ++l) { req(l); sched(); }
      f32x4 pt[5];
#pragma unroll
      for (int d = 0; d < 12; ++d) {
        if (d == 4 || d == 9 || d == 10) barv();
#pragma unroll
        for (int n = 0; n < 20; ++n) {
          const int m = n >> 1, v = m / 5, i = v && d < 11 ? 9 - m : m % 5, e = (n ^ m) & 1;
          if (d == 11 && n == 17) barv();
          if (d == 0 && !v && !(n & 1)) mma0(acc[1][i], wf[e][i], ha[e]); else mma(acc[1 - v][i], wf[(2 * d + e) % 6][i], ha[(4 * d + 2 * v + e) % R]);
          if (n % 5 == 4) req(4 * d + n / 5 + AHEAD);
          else if (d >= 2 && d < 11 && n < 13) { const int j = n - n / 5; if (j < 10) rdw(2 * d + 2 + (j & 1), j >> 1); }
          if (n == 18 && d >= 2 && d < 11) lgk0();
          if (d == 11) {
            if (n >= 10 && n < 15) *(LAS f32x4*)(L + wr + (n - 10) * 1024) = acc[1][n - 10];
            if (n >= 17) { pt[2 * n - 34] = *(LAS f32x4*)(L + rd + (2 * n - 34) * 1024); if (n < 19) pt[2 * n - 33] = *(LAS f32x4*)(L + rd + (2 * n - 33) * 1024); }
          }
          sched();
        }
      }
      stamp(k, w, sb + 16);
#pragma unroll
      for (int i = 0; i < 5; ++i) { acc[0][i] += pt[i]; pin(acc[0][i]); sched(); }
      if (layer == 1) {
        const rsrc_t ro = mk(out, 2048 * 640 * 2);
#pragma unroll
        for (int i = 0; i < 5; ++i) *(LAS u32x2*)(L + ow + 32 * i) = (u32x2){pk(acc[0][i][0], acc[0][i][1]), pk(acc[0][i][2], acc[0][i][3])};
        asm volatile("" ::: "memory");
#pragma unroll
        for (int j = 0; j < 3; ++j) st16(ro, *(LAS u32x4*)(L + orr + j * 1024), vo3[j]);
        stamp(k, w, sb + 17);
      } else {
        float s = 0.f, ss = 0.f;
#pragma unroll
        for (int j = 0; j < 3; ++j) x_part(rw, vx, vx2, j, acc[0], res, s, ss);
        x_stats(rw, vs, s, ss);
        bar();
        stamp(k, w, sb + 17);
        drain_bar();
      }
      stamp(k, w, sb + 18);
    }
  }
  STAMP_OUT(k0, w0);
}

DI void stats_load(const Ctx& k, rsrc_t rw, int xb, int tf0, u32x2 (&sv)[8]) {
#pragma unroll
  for (int j = 0; j < 8; ++j) sv[j] = __builtin_amdgcn_raw_buffer_load_b64(rw, (k.lane >> 4) * 1024 + (k.lane & 15) * 8 + j * 128, OFF_ST + xb * ST_BUF + tf0 * 1024, SC1);
}
DI void stats_sum(u32x2 (&sv)[8], float (&s)[2]) {
#pragma unroll
  for (int j = 0; j < 8; ++j) pin(sv[j]);
#pragma unroll
  for (int e = 0; e < 2; ++e) {
    s[e] = __uint_as_float(sv[0][e]);
#pragma unroll
    for (int j = 1; j < 8; ++j) s[e] = one(s[e] + __uint_as_float(sv[j][e]));
    s[e] *= 1.f / 640.f; pin(s[e]);
  }
}
DI void stats_table(const Ctx& k, int a, float (&s)[2], float scale) {
  pin(s[0]); pin(s[1]);
  float r[2];
#pragma unroll
  for (int e = 0; e < 2; ++e) {
    const float ss = __builtin_bit_cast(float, __builtin_amdgcn_update_dpp(0, __builtin_bit_cast(int, s[e]), 0x128, 0xf, 0xf, true));
    const float v = one(fmaxf(one(fmaf(-s[e], s[e], ss)), 0.f) + 1e-5f), q = __builtin_amdgcn_rsqf(v);
    r[e] = one(q * scale);
  }
  if (!(k.lane & 8)) {
    const unsigned t = (64 * a + (k.lane & 48) + 2 * (k.lane & 7)) * 4;
    *(LAS f32x2*)(k.lds + L_MEAN + t) = (f32x2){one(-s[0]), one(-s[1])};
    *(LAS f32x2*)(k.lds + L_RSTD + t) = (f32x2){r[0], r[1]};
  }
}

DI void loader(const Ctx& k0, const char* x0, const char* wsp, const char* wdp, int a4) {
  Ctx k = k0;
  const bool isW = a4 < 2;
  LAS char* const L = k.lds;
  const rsrc_t rw = mk(k.ws, WS_END);
  const rsrc_t rs = mk(wsp, 2048 * 2048 * 2);
  auto fresh = [&]() { asm volatile("" : "+v"(k.lane), "+s"(k.P), "+s"(k.kq), "+s"(k.np), "+s"(k.P1), "+s"(k.ct1), "+s"(k.mat), "+s"(a4)); };
#define lane k.lane
#define a (a4 & 1)
#define me (k.P * 16 + k.kq * 4 + k.np)
#define n3 (4 * k.kq + k.np)
#define sw (((lane & 7) ^ (4 * a + (lane >> 4))) * 16)
#define vo2 ((lane >> 3) * 4096 + sw)
#define so2 ((128 * k.P + 8 * a) * 4096 + (k.kq & 1) * 2048)
#define so3 (OFF_W3 + n3 * 20 * 12288 + a * 12288)
#define so4 (OFF_W4 + (2 * k.np + (k.kq & 1)) * 48 * 5120)
  bf16x8 k1[25], k4[3][8];
  u64 tag = 0;
  int rot = 2 * k.np;
  auto ktile = [&](int t) { const int r = (t + rot) & 7; return 4 * (r >> 1) + 2 * (t >> 3) + (r & 1); };
  STAMP_DECL;
  auto w2 = [&](int t, int slot) {
#pragma unroll
    for (int j = 0; j < 8; ++j) dma<0, 0>(rs, L + L_WR + slot * 16384 + (2 * j + a) * 1024, vo2, so2 + j * 16 * 4096 + ktile(t) * 128);
  };
  auto w2n = [&](int t0, int t1) {
#pragma unroll 1
    for (int t = t0; t < t1; ++t) w2(t, t < 3 ? t + 3 : t - 3);
  };
  auto w3 = [&](int t, int slot) { dma_lin<SC1, 12>(rw, L + L_WR + slot * 24576 + a * 12288, lane * 16, so3 + t * 24576); };
  auto w4 = [&](int ch) { dma_lin<SC1, 15>(rw, L + L_W4 + w4slot(ch) * 30720 + a * 15360, lane * 16, so4 + ch * 30720 + a * 15360); };
  auto push = [&](int ev, int wg0, int wgstep, int n, int slot) {
    if (a4 == 2) {
      unsigned dst = lane < n ? OFF_MB + ev * MB_EV + (wg0 + lane * wgstep) * 128 + slot * 8 : 0x7fffffff;
      pin(dst); pin(tag);
      bar();
      __builtin_amdgcn_raw_buffer_store_b64(__builtin_bit_cast(u32x2, tag), rw, dst, 0, SC1);
    } else bar();
    fresh();
  };
  unsigned budget = SPIN_CAP;
  auto wait_mb = [&](int ev, int wg, int n) {
    GAS u64* src = (GAS u64*)(k.ws + OFF_MB + ev * MB_EV + wg * 128) + (lane & (n - 1));
#pragma unroll 1
    for (; budget; budget = uni(budget - 1)) {
      const u64 v = __hip_atomic_load(src, __ATOMIC_RELAXED, __HIP_MEMORY_SCOPE_AGENT);
      if (__all(v == tag)) break;
      __builtin_amdgcn_s_sleep(1);
    }
  };
  auto poll = [&](int ev, int wg, int n) {
    if (a4 == 2) wait_mb(ev, wg, n);
    drain_bar();
  };

  auto get_tag = [&]() { const char* e = k.ws; pins(e); tag = *(const GAS u64*)e; };
  auto mix_tag = [&]() { pin(tag); tag = tag * 6364136223846793005ull + 1442695040888963407ull; };
  if (!isW) __builtin_amdgcn_s_setprio(1);
  {
    const rsrc_t r1 = isW ? mk(wdp, 640 * 640 * 2) : mk(x0, 2048 * 640 * 2);
    const unsigned v1 = (lane >> 3) * 1280 + sw, s1 = ((isW ? 80 * k.ct1 : 128 * k.P1) + 8 * a) * 1280;
    int slot = 2, wt = rot1(k);
    if ((k.P & 1) && !isW) __builtin_amdgcn_s_sleep(LAG);
    stamp(k, 4 + a4, 40);
#pragma unroll 1
    for (int t = 0; t < 10; ++t) {
      if (isW) {
        if (t >= 3) { waitv<10>(); bar(); }
#pragma unroll
        for (int j = 0; j < 5; ++j) dma<0, 0>(r1, L + L_D + wt * 10240 + (2 * j + a) * 1024, v1, s1 + 16 * j * 1280 + wt * 128);
      } else {
        if (t >= 2) { waitv<8>(); bar(); }
#pragma unroll
        for (int j = 0; j < 8; ++j) dma<0, 0>(r1, L + L_XR + slot * 16384 + (2 * j + a) * 1024, v1, s1 + 16 * j * 1280 + wt * 128);
      }
      slot = slot == 2 ? 0 : slot + 1; wt = wt == 9 ? 0 : wt + 1;
    }
    if (isW) {
      waitv<10>(); bar();
      waitv<5>(); bar(); w2(0, 3);
      waitv<8>(); bar(); w2(1, 4);
      bar();
    } else {
      get_tag();
      waitv<8>(); bar(); waitv<0>();
      mix_tag(); pin(tag);
      bar();
      if (!(k.P & 1)) {
#pragma unroll
        for (int j = 0; j < 2; ++j) touch<0>(rs, L + L_JUNK, (lane >> 1) * 4096 + (lane & 1) * 128, (128 * k.P + 64 * a + 32 * j) * 4096 + (k.kq & 1) * 2048 + ktile(8) * 128);
      }
      bar();
    }
  }
  {
    unsigned kb[2] = {L_D + lane * 16 + ((a4 >> 1) * 10 + a) * 1024, 0}; kb[1] = kb[0] + 61440; pin(kb[0]); pin(kb[1]);
    bar();
#pragma unroll
    for (int j = 0; j < 25; ++j) k1[j] = lds16(L, kb[j / 15] + ((j / 5) % 3) * 20480 + (j % 5) * 2048);
    if (isW) w2(2, 5);
    stamp(k, 4 + a4, 51);
  }
#pragma unroll 1
  for (int layer = 0; layer < 2; ++layer) {
    const int ev0 = layer * EV_PER_LAYER, sb = 3 + layer * 24;
    if (layer) {
      stamp(k, 4 + a4, sb + 0);
      poll(ev0 + EV_X, k.P1 * 16 + k.mat * 8 + k.ct1, 16);
      if (!isW) { u32x2 sv[8]; float sm[2]; stats_load(k, rw, 1, 8 * k.P1 + 4 * a, sv); stats_sum(sv, sm); stats_table(k, a, sm, k.mat ? -1.f : 1.f); }
      bar();
      if (isW) w2n(3, 6);
    }
    push(ev0 + EV_AB, (2 * k.mat + (k.P1 >> 3)) * 4 + (k.ct1 >> 1), 16, 16, 8 * (k.P1 & 1) + (k.P1 & 6) + (k.ct1 & 1));
    GAS u64* rtf = (GAS u64*)(k.ws + OFF_MB + EV_RT * MB_EV);
    if (layer == 0 && a4 == 3 && lane == 0) __hip_atomic_store(rtf + k.b, tag, __ATOMIC_RELAXED, __HIP_MEMORY_SCOPE_AGENT);
    if (isW && layer == 0) { get_tag(); w2n(3, 6); mix_tag(); }
    stamp(k, 4 + a4, sb + 4);
    const unsigned soab = OFF_AB + layer * AB_BUF + (((k.kq >> 1) * 4 + k.np) * 64 + 32 * (k.kq & 1)) * 10240;
    unsigned l16 = lane * 16, so0 = soab + a * 10240 + ktile(0) * 20480, la0 = L_D + a * 10240;
    so0 = __builtin_amdgcn_readfirstlane(so0); la0 = __builtin_amdgcn_readfirstlane(la0);
    pin(l16); pins(so0); pins(la0);
    if (a4 == 3 && (k.P & 1) && layer == 0) __builtin_amdgcn_s_sleep(LAG2);
    poll(ev0 + EV_AB, me, 8);
    if (!isW) dma_lin<SC1, 10>(rw, L + la0, l16, so0);
    if (isW) {
      const unsigned sb2 = uni(so2), lb2 = uni(L_WR + a * 1024);
      unsigned v2 = vo2, so = uni(sb2 + ktile(6) * 128), la = uni(lb2 + 49152), t = 7;
      pin(v2);
#pragma unroll 1
      do {
        waitv<32>(); bar();
#pragma unroll
        for (int j = 0; j < 2; ++j) dma<0, 0>(rs, L + la + j * 2048, v2, so + j * 16 * 4096);
        sched();
        const unsigned sn = uni(sb2 + ktile(t) * 128), ln = uni(la == lb2 + 81920 ? lb2 : la + 16384);
        t = uni(t + 1);
        sched();
#pragma unroll
        for (int j = 2; j < 8; ++j) dma<0, 0>(rs, L + la + j * 2048, v2, so + j * 16 * 4096);
        so = sn; la = ln;
      } while (t != 17);
      const bool chk = layer == 0 && a == 0;
      u64 f[4];
      if (chk) {
#pragma unroll
        for (int j = 0; j < 4; ++j) f[j] = __hip_atomic_load(rtf + lane + 64 * j, __ATOMIC_RELAXED, __HIP_MEMORY_SCOPE_AGENT);
      }
      waitv<32>(); bar();
      if (chk) {
#pragma unroll
        for (int j = 0; j < 4; ++j) pin(f[j]);
#pragma unroll 1
        for (unsigned spins = 0; spins < SPIN_CAP && !__all(f[0] == tag && f[1] == tag && f[2] == tag && f[3] == tag); ++spins) {
#pragma unroll
          for (int j = 0; j < 4; ++j) f[j] = __hip_atomic_load(rtf + lane + 64 * j, __ATOMIC_RELAXED, __HIP_MEMORY_SCOPE_AGENT);
        }
      }
      waitv<0>(); bar();
      w3(3, 1); bar(); bar(); w3(0, 2); bar();
      w3(1, 3); bar(); w3(2, 0);
    } else {
      stamp(k, 4 + a4, sb + 3);
      const unsigned sba = uni(soab + a * 10240);
      dma_lin<SC1, 10>(rw, L + la0 + 20480, l16, sba + ktile(1) * 20480);
      unsigned so = uni(sba + ktile(2) * 20480), la = uni(la0 + 40960), t = 3;
      auto step = [&]() {
        bar();
        if (FINE == 2 && layer == FL) stamp(k, 4 + a4, 43 + t);
        dma_lin<SC1, 4>(rw, L + la, l16, so);
        sched();
        const unsigned sn = uni(sba + ktile(t) * 20480), ln = uni(la == la0 + 40960 ? la0 : la + 20480);
        t = uni(t + 1);
        sched();
        dma_lin<SC1, 6>(rw, L + la + 4096, l16, so + 4096);
        so = sn; la = ln;
      };
#pragma unroll 1
      do { waitv<10>(); step(); } while (t != 8);
      unsigned ja = L_JUNK + 256 + layer * 256 + a * 128;
      if (lane < 8) dma<SC1, 0>(rw, L + ja, l16, OFF_MB + (ev0 + EV_AB) * MB_EV + me * 128);
      ja += (lane & 15) * 8; pin(ja);
      waitv<11>(); step();
      waitv<10>();
      if (!__all(*(LAS u64*)(L + ja) == tag)) wait_mb(ev0 + EV_AB, me, 16);
      step();
#pragma unroll 1
      do { waitv<10>(); step(); } while (t != 17);
      waitv<10>(); bar(); if (FINE == 2 && layer == FL) stamp(k, 4 + a4, 60);
      waitv<0>(); bar(); if (FINE == 2 && layer == FL) stamp(k, 4 + a4, 61);

    }
    auto k4load = [&](int n) {
#pragma unroll
      for (int j = 0; j < 8; ++j) k4[n][j] = ld16(rw, lane * 16 + a4 * 1024, so4 + (5 + n) * 30720 + j * 4096);
    };
    push(ev0 + EV_EX, k.P * 16 + k.np, 4, 4, k.kq);
    if (layer == 0) k4load(0);
    poll(ev0 + EV_EX, me, 4);
    push(ev0 + EV_X1, k.P * 16, 1, 16, n3);

    if (layer == 0) {
      u32x2 cv[5];
      if (isW) {
#pragma unroll
        for (int j = 0; j < 5; ++j) cv[j] = __builtin_amdgcn_raw_buffer_load_b64(rw, lane * 8, OFF_CS + (j * 3072 + a * 1536 + 96 * n3) * 4, SC1);
      }
      k4load(1);
      if (isW) {
        sched();
        f32x2 s = {0.f, 0.f};
#pragma unroll
        for (int j = 0; j < 5; ++j) s += __builtin_bit_cast(f32x2, cv[j]);
        if (lane < 48) *(LAS f32x2*)(L + L_CS3 + (96 * a + 2 * lane) * 4) = s;
      }
    }
    stamp(k, 4 + a4, sb + 11);
    l16 = lane * 16; so0 = OFF_XT + 2 * layer * XT_BUF + (8 * k.P + 4 * a) * 20480; la0 = L_D + a * 8192;
    so0 = __builtin_amdgcn_readfirstlane(so0); la0 = __builtin_amdgcn_readfirstlane(la0);
    pin(l16); pins(so0); pins(la0);
    poll(ev0 + EV_X1, me, 16);
    if (!isW) {
#pragma unroll
      for (int j = 0; j < 4; ++j) dma_lin<SC1, 2>(rw, L + la0 + j * 2048, l16, so0 + j * 20480);
    }
    if (isW) {
      bar();
      __builtin_amdgcn_s_sleep(3);
      dma_lin<SC1, 4>(rw, L + L_D + 49152 + a * 4096, lane * 16, OFF_ST + 2 * layer * ST_BUF + (8 * k.P + 4 * a) * 1024);
      const unsigned lb3 = uni(L_WR + a * 12288), se = uni(so3 + 10 * 24576);
      unsigned so = uni(so3 + 4 * 24576), la = uni(lb3 + 49152);
#pragma unroll 1
      do {
        waitv<24>(); bar();
        dma_lin<SC1, 4>(rw, L + la, l16, so);
        sched();
        const unsigned sn = uni(so + 24576), ln = uni(la == lb3 + 73728 ? lb3 : la + 24576);
        sched();
        dma_lin<SC1, 8>(rw, L + la + 4096, l16, so + 4096);
        so = sn; la = ln;
      } while (so != se);
      waitv<24>(); bar();
      waitv<12>(); bar(); waitv<0>(); bar();
      bar();
      w4(2); w4(3); w4(4);
      stamp(k, 4 + a4, sb + 15);
    } else {
      stamp(k, 4 + a4, sb + 10);
#pragma unroll
      for (int j = 0; j < 4; ++j) dma_lin<SC1, 2>(rw, L + la0 + 16384 + j * 2048, l16, so0 + 2048 + j * 20480);
      unsigned so = uni(so0 + 4096), la = uni(la0 + 32768);
#pragma unroll 1
      do {
        waitv<8>(); bar();
        if (FINE == 3 && layer == FL) stamp(k, 4 + a4, 44 + ((so - so0) >> 11));
        dma_lin<SC1, 2>(rw, L + la, l16, so);
        sched();
        const unsigned sn = uni(so + 2048), ln = uni(la == la0 + 32768 ? la0 : la + 16384);
        sched();
#pragma unroll
        for (int j = 1; j < 4; ++j) dma_lin<SC1, 2>(rw, L + la + j * 2048, l16, so + j * 20480);
        so = sn; la = ln;
      } while (so != so0 + 20480);
      waitv<8>(); bar();
      if (FINE == 3 && layer == FL) stamp(k, 4 + a4, 54);
      w4(0);
      waitv<15>(); bar();
      if (FINE == 3 && layer == FL) stamp(k, 4 + a4, 55);
      w4(1);
      bar();
      if (FINE == 3 && layer == FL) stamp(k, 4 + a4, 56);
      waitv<0>();
    }
    bar();
    push(ev0 + EV_H, k.P * 16, 1, 16, n3);
    if (layer == 0) k4load(2);
    auto k4put = [&](int n, bool paced) {
      unsigned kb = L_W4 + w4slot(5 + n) * 30720 + a4 * 1024 + lane * 16; pin(kb);
#pragma unroll
      for (int j = 0; j < 8; ++j) if (j < 7 || isW) { *(LAS bf16x8*)(L + kb + j * 4096) = k4[n][j]; if (paced) __builtin_amdgcn_s_sleep(2); }
    };
    k4put(0, false); k4put(1, false);
    poll(ev0 + EV_H, me, 16);
    bar();
    k4put(2, true);
    bar(); bar();
    rot = 0;
    if (isW && layer == 0) w2n(0, 2);
    bar();
    if (layer == 0) {
      if (isW) w2n(2, 3);
      unsigned kb[2] = {L_D + lane * 16 + ((a4 >> 1) * 10 + a) * 1024, 0}; kb[1] = kb[0] + 61440; pin(kb[0]); pin(kb[1]);
      bar();
#pragma unroll
      for (int j = 0; j < 7; ++j) *(LAS bf16x8*)(L + kb[j / 15] + ((j / 5) % 3) * 20480 + (j % 5) * 2048) = k1[j];
      push(EV_PER_LAYER + EV_X, k.P * 16, 1, 16, n3);
#pragma unroll
      for (int j = 7; j < 25; ++j) *(LAS bf16x8*)(L + kb[j / 15] + ((j / 5) % 3) * 20480 + (j % 5) * 2048) = k1[j];
    }
  }
#undef lane
  STAMP_OUT(k, 4 + a4);
#define lane k.lane
  if (k.b == 0 && a4 == 2 && lane == 0) *(GAS u64*)k.ws = tag;
#undef lane
#undef a
#undef me
#undef n3
#undef sw
#undef vo2
#undef so2
#undef so3
#undef so4
}

__global__ void __launch_bounds__(512, 2) fnet_glu_kernel(const char* x0, const char* wsr, const char* wsi, const char* wdr, const char* wdi, const char* wg, const char* wu, const char* wdn, char* out, char* ws) {
  __shared__ __attribute__((aligned(16))) char smem[163840];
  Ctx k; k.top = (unsigned)__builtin_amdgcn_s_memrealtime(); sched();
  k.lds = (LAS char*)smem; k.lane = threadIdx.x & 63; k.b = blockIdx.x;
  const int w = __builtin_amdgcn_readfirstlane(threadIdx.x >> 6);
  if (w < 4 || w == 7) {
    const char* pc; asm volatile("s_getpc_b64 %0" : "=s"(pc));
    touch<0>(mk(pc, CODE_BYTES), k.lds + L_JUNK, (w < 4 ? threadIdx.x : threadIdx.x - 192) * 128, 0);
  }
  asm volatile("" : "+s"(x0), "+s"(wsr), "+s"(wsi), "+s"(wdr), "+s"(wdi), "+s"(wg), "+s"(wu), "+s"(wdn), "+s"(out), "+s"(ws));
  k.ws = ws;
  const int x = k.b & 7, i = k.b >> 3;
  k.kq = x >> 1; k.P = 8 * (x & 1) + 4 * (i >> 4) + 2 * ((i >> 2) & 1) + ((i >> 3) & 1); k.np = i & 3;
  k.P1 = 4 * (x >> 1) + (i >> 3); k.mat = x & 1; k.ct1 = i & 7;
  if (w < 4) consumer(k, x0, wg, wu, wdn, out, w);
  else loader(k, x0, (x >> 2) ? wsi : wsr, (x & 1) ? wdi : wdr, w - 4);
}
}

extern "C" void kernel_launch(void* const* d_in, const int* in_sizes, int n_in, void* d_out, int out_size, void* d_ws, size_t ws_size, hipStream_t stream) {
  fnet_glu_kernel<<<256, 512, 0, stream>>>((const char*)d_in[0], (const char*)d_in[1], (const char*)d_in[2], (const char*)d_in[3], (const char*)d_in[4],
                                           (const char*)d_in[6], (const char*)d_in[7], (const char*)d_in[8], (char*)d_out, (char*)d_ws);
}
```

```cpp
#include <hip/hip_runtime.h>

typedef unsigned long long u64;
typedef short bf16x8 __attribute__((ext_vector_type(8)));
typedef short s16x4 __attribute__((ext_vector_type(4)));
typedef float f32x4 __attribute__((ext_vector_type(4)));
typedef float f32x2 __attribute__((ext_vector_type(2)));
typedef unsigned u32x4 __attribute__((ext_vector_type(4)));
typedef unsigned u32x2 __attribute__((ext_vector_type(2)));
typedef __bf16 bf2 __attribute__((ext_vector_type(2)));
#define LAS __attribute__((address_space(3)))
#define GAS __attribute__((address_space(1)))
#define DI __device__ __forceinline__
typedef __amdgpu_buffer_rsrc_t rsrc_t;

#define STAMPS 0
#define FL 1
#define FINE 0

namespace {
constexpr int AHEAD = 10;
constexpr int LAG = 12, LAG2 = 48;
constexpr unsigned CODE_BYTES = 34944;
constexpr unsigned OFF_MB = 4096, MB_EV = 256 * 128;
constexpr int EV_X = 0, EV_AB = 1, EV_EX = 2, EV_X1 = 3, EV_H = 4, EV_PER_LAYER = 5, EV_RT = 0;
constexpr unsigned OFF_CS = 0x60000;
constexpr unsigned OFF_ST = 0x80000, ST_BUF = 8 * 2048 * 8;
constexpr unsigned OFF_XT = 0x100000, XT_BUF = 2048 * 640 * 2;
constexpr unsigned OFF_AB = OFF_XT + 3 * XT_BUF, AB_BUF = 2048 * 1280 * 2;
constexpr unsigned OFF_HT = OFF_AB + 2 * AB_BUF, HT_BUF = 2048 * 1536 * 2;
constexpr unsigned OFF_EX = OFF_HT + 2 * HT_BUF, EX_BUF = 256 * 12 * 2560;
constexpr unsigned OFF_W3 = OFF_EX + 2 * EX_BUF;
constexpr unsigned OFF_W4 = OFF_W3 + 16 * 20 * 12288;
constexpr unsigned WS_END = OFF_W4 + 8 * 48 * 5120;
constexpr unsigned OFF_STAMP = 0x6000000;
static_assert(OFF_MB + 10 * MB_EV <= OFF_CS && OFF_CS + 5 * 3072 * 4 <= OFF_ST && OFF_ST + 3 * ST_BUF <= OFF_XT && WS_END <= 0x4000000, "ws map");
constexpr unsigned L_MEAN = 0, L_RSTD = 512, L_CS1 = 1024, L_STD = 1536, L_CS3 = 2304, L_JUNK = 3072;
constexpr unsigned L_D = 4096;
constexpr unsigned L_WR = L_D + 61440, L_W4 = L_D + 6144, L_CP = L_D + 102400, L_XR = L_D + 110592, L_RED = L_W4 + 30720;
constexpr int w4slot(int ch) { return ch == 4 ? 4 : ((ch < 5 ? ch : ch - 5) + 2) & 3; }
constexpr int w4off(int s) { return w4slot(s / 3) * 30720 + (s % 3) * 10240; }
constexpr int SC1 = 16;
constexpr unsigned SPIN_CAP = 1u << 21;

DI rsrc_t mk(const void* p, unsigned bytes) { return __builtin_amdgcn_make_buffer_rsrc((void*)p, 0, bytes, 0x00020000); }
template <int N> DI void waitv() { asm volatile("s_waitcnt vmcnt(%0)" ::"n"(N) : "memory"); }
DI void bar() { asm volatile("s_waitcnt lgkmcnt(0)\n\ts_barrier" ::: "memory"); }
DI void lgk0() { __builtin_amdgcn_sched_barrier(0); __builtin_amdgcn_s_waitcnt(0xC07F); __builtin_amdgcn_sched_barrier(0); }
DI void barv() { lgk0(); asm volatile("s_barrier" ::: "memory"); __builtin_amdgcn_sched_barrier(0); }
DI void drain_bar() {
  asm volatile("s_waitcnt vmcnt(0) lgkmcnt(0)\n\ts_barrier" ::: "memory");
  __builtin_amdgcn_s_waitcnt(0);
  asm volatile("" ::: "memory");
}
DI void sched() { __builtin_amdgcn_sched_barrier(0); }
template <class T> DI void pin(T& x) { asm volatile("" : "+v"(x)); }
template <class T> DI void pins(T& x) { asm volatile("" : "+s"(x)); }
template <int AUX, int IMM> DI void dma(rsrc_t r, LAS char* l, unsigned vo, unsigned so) { __builtin_amdgcn_raw_ptr_buffer_load_lds(r, (LAS void*)l, 16, vo, so, IMM, AUX); }
template <int AUX> DI void touch(rsrc_t r, LAS char* l, unsigned vo, unsigned so) { __builtin_amdgcn_raw_ptr_buffer_load_lds(r, (LAS void*)l, 4, vo, so, 0, AUX); }
template <int AUX, int N> DI void dma_lin(rsrc_t r, LAS char* l, unsigned vo, unsigned so) {
#pragma unroll
  for (int j = 0; j < N; j += 4) {
    dma<AUX, 0>(r, l + j * 1024, vo, so + j * 1024);
    if (j + 1 < N) dma<AUX, 1024>(r, l + j * 1024, vo, so + j * 1024);
    if (j + 2 < N) dma<AUX, 2048>(r, l + j * 1024, vo, so + j * 1024);
    if (j + 3 < N) dma<AUX, 3072>(r, l + j * 1024, vo, so + j * 1024);
  }
}
DI unsigned uni(unsigned x) { x = __builtin_amdgcn_readfirstlane(x); asm volatile("" : "+s"(x)); return x; }
DI bf16x8 ld16(rsrc_t r, unsigned vo, unsigned so) { return __builtin_bit_cast(bf16x8, __builtin_amdgcn_raw_buffer_load_b128(r, vo, so, SC1)); }
DI void st16(rsrc_t r, u32x4 v, unsigned vo) { __builtin_amdgcn_raw_buffer_store_b128(v, r, vo, 0, SC1); }
DI bf16x8 lds16(LAS char* l, unsigned a) { return *(LAS bf16x8*)(l + a); }
DI unsigned pk(float lo, float hi) { f32x2 v = {lo, hi}; return __builtin_bit_cast(unsigned, __builtin_convertvector(v, bf2)); }
DI float blo(unsigned u) { return __uint_as_float(u << 16); }
DI float bhi(unsigned u) { return __uint_as_float(u & 0xffff0000u); }
DI f32x4 unpk(unsigned a, unsigned b) { return (f32x4){blo(a), bhi(a), blo(b), bhi(b)}; }
DI f32x4 mfma(bf16x8 a, bf16x8 b, f32x4 c) { return __builtin_amdgcn_mfma_f32_16x16x32_bf16(a, b, c, 0, 0, 0); }
DI void mma(f32x4& c, const bf16x8& a, const bf16x8& b) { asm volatile("v_mfma_f32_16x16x32_bf16 %0, %1, %2, %0" : "+v"(c) : "v"(a), "v"(b)); __builtin_amdgcn_sched_barrier(0); }
DI void mma0(f32x4& c, const bf16x8& a, const bf16x8& b) { asm volatile("v_mfma_f32_16x16x32_bf16 %0, %1, %2, 0" : "=&v"(c) : "v"(a), "v"(b)); __builtin_amdgcn_sched_barrier(0); }
DI void settle() { asm volatile("s_nop 7"); __builtin_amdgcn_sched_barrier(0); }
DI float dot2(unsigned a, unsigned b, float s) { return __builtin_amdgcn_fdot2_f32_bf16(__builtin_bit_cast(bf2, a), __builtin_bit_cast(bf2, b), s, false); }
DI float one(float v) { asm("" : "+v"(v)); return v; }
DI u32x4 swap_pair(unsigned a0, unsigned a1, unsigned b0, unsigned b1) {
  const auto r0 = __builtin_amdgcn_permlane16_swap(a0, b0, false, false);
  const auto r1 = __builtin_amdgcn_permlane16_swap(a1, b1, false, false);
  return (u32x4){r0[0], r1[0], r0[1], r1[1]};
}

struct Ctx {
  char* ws; LAS char* lds; int lane, b;
  int kq, P, np;
  int P1, mat, ct1;
};
#define STAMP_DECL
#define stamp(k, w, id) do { (void)(id); } while (0)
#define STAMP_OUT(k, w)

DI u32x4 x_pack(int j, const f32x4 (&v)[5], unsigned (&res)[10]) {
  const int i1 = j < 2 ? 2 * j + 1 : 4;
#pragma unroll
  for (int i = 2 * j; i <= i1; ++i) { res[2 * i] = pk(v[i][0], v[i][1]); res[2 * i + 1] = pk(v[i][2], v[i][3]); }
  return swap_pair(res[4 * j], res[4 * j + 1], res[2 * i1], res[2 * i1 + 1]);
}
DI void x_part(rsrc_t rw, unsigned vx, unsigned vx2, int j, const f32x4 (&v)[5], unsigned (&res)[10], float& s, float& ss) {
  const u32x4 o = x_pack(j, v, res);
  st16(rw, o, j < 2 ? vx + j * 1024 : vx2);
#pragma unroll
  for (int e = 4 * j; e < (j < 2 ? 4 * j + 4 : 10); ++e) { s = dot2(res[e], 0x3f803f80u, s); ss = dot2(res[e], res[e], ss); }
  pin(s); pin(ss);
}
DI void x_stats(rsrc_t rw, unsigned vs, float s, float ss) {
  const auto r = __builtin_amdgcn_permlane16_swap(__float_as_uint(s), __float_as_uint(ss), false, false);
  const unsigned t = __float_as_uint(__uint_as_float(r[0]) + __uint_as_float(r[1]));
  const auto r2 = __builtin_amdgcn_permlane32_swap(t, t, false, false);
  const float u = __uint_as_float(r2[0]) + __uint_as_float(r2[1]);
  __builtin_amdgcn_raw_buffer_store_b32(__float_as_uint(u), rw, vs, 0, SC1);
}

DI int rot1(const Ctx& k) { const int r = ((k.ct1 * 5) >> 2) + (((k.P1 & 3) * 5) >> 1); return r >= 10 ? r - 10 : r; }

#define PHASE_LANE() Ctx k = k0; int w = w0; asm volatile("" : "+v"(k.lane), "+s"(w), "+s"(k.P), "+s"(k.kq), "+s"(k.np), "+s"(k.P1), "+s"(k.ct1), "+s"(k.mat)); \
  const int tfo = 8 * k.P + 4 * (k.kq >> 1) + w, cto = 2 * k.np + (k.kq & 1); (void)tfo; (void)cto; \
  const int lane = k.lane, q = lane >> 4, c = lane & 15, qs = 2 * (q & 1) + (q >> 1); (void)qs; \
  const unsigned rowrd = (c >> 3) * 1024 + (c & 7) * 128 + ((q ^ (c >> 1)) * 16); (void)rowrd
#define X_ADDR(xb) unsigned vx = OFF_XT + (xb) * XT_BUF + tfo * 20480 + (10 * cto + qs) * 256 + c * 16, vx2 = vx + 2048 + ((lane & 16) << 26), \
  vs = OFF_ST + (xb) * ST_BUF + tfo * 1024 + cto * 128 + lane * 4 + ((lane & 32) << 25); pin(vx); pin(vx2); pin(vs)

DI void consumer(const Ctx& k0, const char* x0, const char* wg, const char* wu, const char* wdn, char* out, const int w0) {
  LAS char* const L = k0.lds;
  const rsrc_t rw = mk(k0.ws, WS_END);
  unsigned res[10];
  STAMP_DECL;
  __builtin_amdgcn_s_setprio(2);
  stamp(k0, w0, 0);
  auto retile = [&](const Ctx& k0, const int w) {
    int e = ((k0.b >> 7) * 64 + (k0.b & 63)) * 4 + w;
    pins(e);
    const int lane = k0.lane;
    const int g3 = ((e >> 2) * 2731) >> 16, ks3 = 4 * g3 + w, st3 = (e >> 2) - g3 * 24, ks4 = (e * 6554) >> 16, st4 = e - ks4 * 10;
    LAS char* t[3];
#pragma unroll
    for (int m = 0; m < 3; ++m) { const int u = rot1(k0) + (m < 2 ? 4 + w : 8 + (w >> 1)); t[m] = L + L_D + (u >= 10 ? u - 10 : u) * 10240 + (m < 2 ? m : w & 1) * 4096; }
    __builtin_amdgcn_s_sleep(LAG);
#pragma unroll
    for (int m = 0; m < 3; ++m) {
      const unsigned stride = m < 2 ? 3072 : 1280;
      const rsrc_t rr = mk(m == 0 ? wg : (m == 1 ? wu : wdn), e < 480 ? 640 * 1536 * 2 : 0);
#pragma unroll
      for (int j = 0; j < 4; ++j) dma<0, 0>(rr, t[m] + j * 1024, (lane >> 3) * stride + (lane & 7) * 16, (32 * (m < 2 ? ks3 : ks4) + 8 * j) * stride + 128 * (m < 2 ? st3 : st4));
    }
    const rsrc_t ro = mk(k0.ws, e < 480 ? WS_END : 0);
    const bf16x8 ones = {0x3f80, 0x3f80, 0x3f80, 0x3f80, 0x3f80, 0x3f80, 0x3f80, 0x3f80};
    const unsigned tr = (lane >> 4) * 1024 + ((lane >> 2) & 3) * 128 + (lane & 3) * 8;
    __builtin_amdgcn_s_waitcnt(0);
    asm volatile("" ::: "memory");
    u32x4 f[3][4];
#pragma unroll
    for (int m = 0; m < 3; ++m)
#pragma unroll
      for (int n = 0; n < 4; ++n) {
        const u32x2 lo = __builtin_bit_cast(u32x2, __builtin_amdgcn_ds_read_tr16_b64_v4i16((LAS s16x4*)(t[m] + tr + 32 * n)));
        const u32x2 hi = __builtin_bit_cast(u32x2, __builtin_amdgcn_ds_read_tr16_b64_v4i16((LAS s16x4*)(t[m] + tr + 32 * n + 512)));
        f[m][n] = (u32x4){lo[0], lo[1], hi[0], hi[1]};
      }
    stamp(k0, w, 61);
#pragma unroll
    for (int m = 0; m < 3; ++m) {
      f32x4 cs[4];
#pragma unroll
      for (int n = 0; n < 4; ++n) {
        const int nf = 4 * (m < 2 ? st3 : st4) + n;
        st16(ro, f[m][n], lane * 16 + (m < 2 ? OFF_W3 + m * 1024 + ks3 * 12288 + ((nf * 171) >> 10) * 19 * 12288 + nf * 2048 : OFF_W4 + ks4 * 5120 + ((nf * 205) >> 10) * 47 * 5120 + nf * 1024));
        if (m < 2) cs[n] = mfma(ones, __builtin_bit_cast(bf16x8, f[m][n]), (f32x4){0.f, 0.f, 0.f, 0.f});
      }
      if (m < 2) *(LAS float*)(L + L_CP + (m * 64 + lane) * 16 + w * 4) = (lane & 32) ? ((lane & 16) ? cs[3][0] : cs[2][0]) : ((lane & 16) ? cs[1][0] : cs[0][0]);
    }
    stamp(k0, w, 62);
  };
#pragma unroll 1
  for (int layer = 0; layer < 2; ++layer) {
    const int sb = 3 + layer * 24;
    {
      PHASE_LANE();
      unsigned vo = OFF_AB + layer * AB_BUF + ((k.mat * 4 + (k.ct1 >> 1)) * 64 + 4 * k.P1 + w) * 10240 + 5 * (k.ct1 & 1) * 1024 + (qs * 16 + c) * 16;
      unsigned ta = (32 * w + 4 * q) * 4, tc = c * 4;
      pin(vo); pin(ta); pin(tc);
      f32x4 acc[2][5], mean[2], rstd[2]; float cs1[5];
      bf16x8 wf[4][5];
      auto tables = [&]() {
#pragma unroll
        for (int i = 0; i < 5; ++i) cs1[i] = *(LAS float*)(L + L_CS1 + tc + i * 64);
#pragma unroll
        for (int t = 0; t < 2; ++t) { mean[t] = *(LAS f32x4*)(L + ta + L_MEAN + 64 * t); rstd[t] = *(LAS f32x4*)(L + ta + L_RSTD + 64 * t); }
      };
#pragma unroll
      for (int i = 0; i < 5; ++i) { acc[0][i] = (f32x4){0.f, 0.f, 0.f, 0.f}; acc[1][i] = (f32x4){0.f, 0.f, 0.f, 0.f}; }
      if (layer == 0) {
        bf16x8 xf[4][2], cf[4], ones = {0x3f80, 0x3f80, 0x3f80, 0x3f80, 0x3f80, 0x3f80, 0x3f80, 0x3f80};
        f32x4 as[2], aq[2], ac[2];
        unsigned wt = rot1(k) * 10240, wa0 = L_D + rowrd;
        pin(wa0);
        unsigned wa = wa0 + wt, xr = L_XR + w * 4096 + rowrd, x_slot = 32768;
#pragma unroll
        for (int i = 0; i < 5; ++i) { pin(acc[0][i]); pin(acc[1][i]); }
#pragma unroll
        for (int t = 0; t < 2; ++t) { as[t] = (f32x4){0.f, 0.f, 0.f, 0.f}; aq[t] = as[t]; ac[t] = as[t]; pin(as[t]); pin(aq[t]); pin(ac[t]); }
        pin(wa); pin(xr); pin(ones);
        if (k.P & 1) retile(k, w);
        bar();
        stamp(k, w, sb + 0);
        unsigned xn[2] = {xr + 32768, (xr + 32768) ^ 64}, wn[2] = {wa, wa ^ 64};
        const unsigned wo = w * 2048;
        auto rd = [&](int p, int n) {
          const int e = n & 1, m = n >> 1;
          if (m < 2) xf[2 * p + e][m] = lds16(L, xn[e] + m * 2048);
          else if (m < 7) wf[2 * p + e][m - 2] = lds16(L, wn[e] + (m - 2) * 2048);
          else cf[2 * p + e] = lds16(L, wn[e] + wo);
        };
#pragma unroll
        for (int n = 0; n < 16; ++n) rd(0, n);
        lgk0();
        int left = 5;
#pragma unroll 1
        do {
#pragma unroll
          for (int p = 0; p < 2; ++p) {
#pragma unroll
            for (int n = 0; n < 32; ++n) {
              const int m = n >> 1, h = 2 * p + ((n ^ m) & 1);
              if (n == 10) barv();
              if (m < 10) { const int t = m / 5, i = t ? 9 - m : m; mma(acc[t][i], xf[h][t], wf[h][i]); }
              else if (m == 10) mma(aq[1], xf[h][1], xf[h][1]);
              else if (m == 11) mma(as[1], xf[h][1], ones);
              else if (m == 12) mma(as[0], xf[h][0], ones);
              else if (m == 13) mma(aq[0], xf[h][0], xf[h][0]);
              else if (m == 14) mma(ac[0], ones, cf[h]);
              else mma(ac[1], ones, wf[h][4]);
              if (n == 0) { x_slot = x_slot == 32768 ? 0 : x_slot + 16384; pins(x_slot); }
              if (n == 1) { wt = wt == 92160 ? 0 : wt + 10240; pins(wt); }
              if (n == 2) { xn[0] = xr + x_slot; wn[0] = wa0 + wt; pin(xn[0]); pin(wn[0]); }
              if (n == 4) { xn[1] = xn[0] ^ 64; wn[1] = wn[0] ^ 64; pin(xn[1]); pin(wn[1]); }
              if (n == 6 && p) { --left; pins(left); }
              if (n >= 10 && n < 26) rd(p ^ 1, n - 10);
              sched();
            }
            lgk0();
          }
        } while (left);
        settle();
        stamp(k, w, 50);
        if (q == 0) { *(LAS float*)(L + L_CS1 + (16 * w + c) * 4) = ac[0][0]; *(LAS float*)(L + L_CS1 + (64 + c) * 4) = ac[1][0]; }
#pragma unroll
        for (int t = 0; t < 2; ++t) {
          const float s = ((c & 2) ? ((c & 1) ? as[t][3] : as[t][2]) : ((c & 1) ? as[t][1] : as[t][0])) * (1.f / 640.f);
          const float ss = ((c & 2) ? ((c & 1) ? aq[t][3] : aq[t][2]) : ((c & 1) ? aq[t][1] : aq[t][0])) * (1.f / 640.f);
          const float r = __builtin_amdgcn_rsqf(fmaxf(ss - s * s, 0.f) + 1e-5f);
          if ((c >> 2) == q) { *(LAS float*)(L + L_MEAN + (32 * w + 16 * t + c) * 4) = -s; *(LAS float*)(L + L_RSTD + (32 * w + 16 * t + c) * 4) = k.mat ? -r : r; }
        }
        bar();
        tables();
      } else {
#define KS(s) (((s) + 12) % 20)
        unsigned wb[2][2] = {{L_D + rowrd, L_D + rowrd + 51200}, {L_D + (rowrd ^ 64), L_D + (rowrd ^ 64) + 51200}};
        unsigned xv[2] = {OFF_XT + XT_BUF + (8 * k.P1 + 2 * w) * 20480 + lane * 16, 0}; xv[1] = xv[0] + 20480;
        pin(wb[0][0]); pin(wb[0][1]); pin(wb[1][0]); pin(wb[1][1]); pin(xv[0]); pin(xv[1]);
        constexpr int R = AHEAD + 2;
        bf16x8 xa[R];
        auto req = [&](int l) { const int ks = KS(2 * (l >> 2) + (l & 1)); if (l < 40) xa[l % R] = ld16(rw, xv[(l >> 1) & 1] + (ks & 3) * 1024, (ks >> 2) * 4096); };
        auto rd = [&](int d, int n) { const int s = 2 * d + (n & 1); wf[s & 3][n >> 1] = lds16(L, wb[s & 1][KS(s) / 10] + ((KS(s) >> 1) % 5) * 10240 + (n >> 1) * 2048); };
        bar();
        stamp(k, w, sb + 0);
#pragma unroll
        for (int l = 0; l < AHEAD; ++l) { req(l); sched(); }
#pragma unroll
        for (int n = 0; n < 10; ++n) rd(0, n);
        lgk0();
#pragma unroll
        for (int d = 0; d < 10; ++d) {
          if (d == 8) barv();
          if (d == 9) { tables(); sched(); }
#pragma unroll
          for (int n = 0; n < 20; ++n) {
            const int m = n >> 1, t = m / 5, i = t ? 9 - m : m, e = (n ^ m) & 1;
            if (d == 0 && !(n & 1)) mma0(acc[t][i], xa[(2 * t + e) % R], wf[e][i]); else mma(acc[t][i], xa[(4 * d + 2 * t + e) % R], wf[(2 * d + e) & 3][i]);
            if (n % 5 == 4) req(4 * d + n / 5 + AHEAD);
            else if (d < 9 && n < 13) { const int j = n - n / 5; if (j < 10) rd(d + 1, j); }
            if (n == 18 && d < 9) lgk0();
            sched();
          }
        }
        settle();
      }
      stamp(k, w, sb + 1);
#pragma unroll
      for (int i = 0; i < 5; ++i) {
        const float cs = cs1[i];
        const f32x4 a = (mean[0] * cs + acc[0][i]) * rstd[0], bb = (mean[1] * cs + acc[1][i]) * rstd[1];
        st16(rw, swap_pair(pk(a[0], a[1]), pk(a[2], a[3]), pk(bb[0], bb[1]), pk(bb[2], bb[3])), vo + i * 1024);
      }
      if (layer == 0 && (k.P & 1) && w < 2) {
        const int j = (k.b >> 7) * 64 + (k.b & 63), g3 = (j * 2731) >> 16;
        const f32x4 t = *(LAS f32x4*)(L + L_CP + (w * 64 + lane) * 16);
        if (j < 120) __builtin_amdgcn_raw_buffer_store_b32(__float_as_uint((t[0] + t[1]) + (t[2] + t[3])), rw, lane * 4, OFF_CS + (g3 * 3072 + w * 1536 + 64 * (j - g3 * 24)) * 4, SC1);
      }
      stamp(k, w, sb + 2);
      drain_bar();
      stamp(k, w, sb + 3);
    }
    {
      PHASE_LANE();
      const int wm = w >> 1, wn = w & 1;
      f32x4 acc[4][5];
      bf16x8 fa[2][4], fl[2][2], fb[2][2][4];
#pragma unroll
      for (int t = 0; t < 4; ++t)
#pragma unroll
        for (int i = 0; i < 5; ++i) { acc[t][i] = (f32x4){0.f, 0.f, 0.f, 0.f}; pin(acc[t][i]); }
      unsigned wb = L_WR + wm * 8192 + rowrd, ab = L_D + wn * 5120 + lane * 16;
      unsigned ws_slot = 65536, ab_slot = 20480;
      unsigned ev[2], ev8[2], en = w == k.kq ? 0 : WS_END;
      ev[0] = OFF_EX + layer * EX_BUF + ((k.P * 16 + w * 4 + k.np) * 3 + ((k.kq - w) & 3) - 1) * 10240 + lane * 16; ev[1] = ev[0] + 5120;
      ev8[0] = ev[0] - lane * 8 + 2048; ev8[1] = ev8[0] + 5120;
      unsigned lo = L_D + 20480 + w * 10240 + lane * 16, lo8 = lo - lane * 8 + 2048;
      pin(lo); pin(lo8);
      pin(wb); pin(ab); pin(ev[0]); pin(ev[1]); pin(ev8[0]); pin(ev8[1]); pins(en);
      const rsrc_t re = mk(k.ws, en);
      bar();
#pragma unroll
      for (int n = 0; n < 8; ++n) fb[0][n & 1][n >> 1] = lds16(L, (wb ^ ((n & 1) * 64)) + 49152 + (n >> 1) * 2048);
      unsigned an = ab + 20480, w0 = wb + 65536, w1 = w0 ^ 64;
      pin(an); pin(w0); pin(w1);
      bar();
      stamp(k, w, sb + 4);
#pragma unroll
      for (int n = 0; n < 10; ++n) { if (n < 8) fa[n & 1][n >> 1] = lds16(L, ab + (n & 1) * 10240 + (n >> 1) * 1024); else fl[0][n & 1] = lds16(L, ab + (n & 1) * 10240 + 4096); }
      lgk0();
      int left = 7;
      auto tile = [&](int p, bool last) {
#pragma unroll
        for (int n = 0; n < 40; ++n) {
          const int m = n >> 1, i = m >> 2, t = (i & 1) ? 3 - (m & 3) : m & 3, kk = (n ^ m) & 1, e = n & 1;
          if (n == 16) barv();
          mma(acc[t][i], i < 4 ? fa[kk][i & 3] : fl[p][kk], fb[p][kk][t]);
          if (n < 8) fb[p ^ 1][e][m] = lds16(L, (e ? w1 : w0) + m * 2048);
          else if (n >= 16 && n < 20) fa[e][m - 8] = lds16(L, an + e * 10240 + (m - 8) * 1024);
          else if (m == 10) fl[p ^ 1][e] = lds16(L, an + e * 10240 + 4096);
          else if (m == 12 || m == 16) fa[e][m / 4 - 1] = lds16(L, an + e * 10240 + (m / 4 - 1) * 1024);
          else if (!last) {
            if (n == 8) { ws_slot = ws_slot == 81920 ? 0 : ws_slot + 16384; pins(ws_slot); }
            if (n == 9) { ab_slot = ab_slot == 40960 ? 0 : ab_slot + 20480; pins(ab_slot); }
            if (n == 10) { w0 = wb + ws_slot; pin(w0); }
            if (n == 11) { w1 = w0 ^ 64; pin(w1); }
            if (n == 34) { an = ab + ab_slot; pin(an); if (p) { --left; pins(left); } }
          }
          sched();
        }
        lgk0();
      };
#pragma unroll 1
      do { tile(0, false); tile(1, false); } while (left);
      tile(0, true);
      {
        unsigned o[10];
        auto send = [&](int t, int j) {
          if (j < 5) { o[2 * j] = pk(acc[t][j][0], acc[t][j][1]); o[2 * j + 1] = pk(acc[t][j][2], acc[t][j][3]); }
          if (j == 5) st16(re, (u32x4){o[0], o[1], o[2], o[3]}, ev[t >> 1] + (t & 1) * 2560);
          if (j == 6) { *(LAS u32x4*)(L + lo + t * 2560) = (u32x4){o[0], o[1], o[2], o[3]}; *(LAS u32x2*)(L + lo8 + t * 2560) = (u32x2){o[8], o[9]}; }
          if (j == 7) st16(re, (u32x4){o[4], o[5], o[6], o[7]}, ev[t >> 1] + (t & 1) * 2560 + 1024);
          if (j == 8) { __builtin_amdgcn_raw_buffer_store_b64((u32x2){o[8], o[9]}, re, ev8[t >> 1] + (t & 1) * 2560, 0, SC1); *(LAS u32x4*)(L + lo + t * 2560 + 1024) = (u32x4){o[4], o[5], o[6], o[7]}; }
        };
#pragma unroll
        for (int n = 0; n < 40; ++n) {
          const int t = n / 10, i = (n % 10) >> 1, kk = (n ^ i) & 1;
          mma(acc[t][i], i < 4 ? fa[kk][i & 3] : fl[1][kk], fb[1][kk][t]);
          if (t) send(t - 1, n % 10);
          sched();
        }
        stamp(k, w, sb + 5);
#pragma unroll
        for (int j = 0; j < 9; ++j) { send(3, j); sched(); }
      }
      stamp(k, w, sb + 6);
      drain_bar();
      stamp(k, w, sb + 7);
      if (layer == 0) {
        const rsrc_t rx = mk(x0, 2048 * 640 * 2);
#pragma unroll
        for (int j = 0; j < 3; ++j) {
          const int u = 64 * j + lane, r = (u * 205) >> 11;
          dma<0, 0>(rx, L + L_D + w * 3072 + j * 1024, r * 1120 + u * 16 + (j == 2 ? (lane & 32) << 25 : 0), 16 * tfo * 1280 + cto * 160);
        }
        __builtin_amdgcn_s_waitcnt(0);
        asm volatile("" ::: "memory");
#pragma unroll
        for (int i = 0; i < 5; ++i) { const u32x2 t = *(LAS u32x2*)(L + L_D + w * 3072 + c * 160 + 8 * q + 32 * i); res[2 * i] = t[0]; res[2 * i + 1] = t[1]; }
      }
      f32x4 v[5];
      {
        LAS char* t = L + L_D + 20480 + k.kq * 10240 + w * 2560;
        const u32x4 a = *(LAS u32x4*)(t + lane * 16), bb = *(LAS u32x4*)(t + 1024 + lane * 16); const u32x2 d = *(LAS u32x2*)(t + 2048 + lane * 8);
        v[0] = unpk(a[0], a[1]); v[1] = unpk(a[2], a[3]); v[2] = unpk(bb[0], bb[1]); v[3] = unpk(bb[2], bb[3]); v[4] = unpk(d[0], d[1]);
#pragma unroll
        for (int i = 0; i < 5; ++i) { v[i] += unpk(res[2 * i], res[2 * i + 1]); pin(v[i]); }
      }
      unsigned rv = OFF_EX + layer * EX_BUF + (k.P * 16 + k.kq * 4 + k.np) * 30720 + w * 2560 + lane * 16, rv8 = rv - lane * 8;
      pin(rv); pin(rv8);
      X_ADDR(2 * layer);
      bar();
      stamp(k, w, sb + 8);
      u32x4 e[3][2]; u32x2 e8[3];
#pragma unroll
      for (int j = 0; j < 3; ++j)
#pragma unroll
        for (int n = 0; n < 3; ++n) {
          if (j < 2) e[n][j] = __builtin_amdgcn_raw_buffer_load_b128(rw, rv + j * 1024, n * 10240, SC1);
          else e8[n] = __builtin_amdgcn_raw_buffer_load_b64(rw, rv8 + 2048, n * 10240, SC1);
          sched();
        }
      float s = 0.f, ss = 0.f;
#pragma unroll
      for (int j = 0; j < 3; ++j) {
#pragma unroll
        for (int n = 0; n < 3; ++n) {
          if (j < 2) { v[2 * j] += unpk(e[n][j][0], e[n][j][1]); v[2 * j + 1] += unpk(e[n][j][2], e[n][j][3]); }
          else v[4] += unpk(e8[n][0], e8[n][1]);
        }
        x_part(rw, vx, vx2, j, v, res, s, ss);
        sched();
      }
      x_stats(rw, vs, s, ss);
      stamp(k, w, sb + 9);
      drain_bar();
      stamp(k, w, sb + 10);
    }
    {
      PHASE_LANE();
      const int wm = w >> 1, wn = w & 1;
      f32x4 acc[4][6];
      bf16x8 fa[2][5], fl[2][2], fb[2][2][4];
#pragma unroll
      for (int t = 0; t < 4; ++t)
#pragma unroll
        for (int i = 0; i < 6; ++i) { acc[t][i] = (f32x4){0.f, 0.f, 0.f, 0.f}; pin(acc[t][i]); }
      unsigned wb = L_WR + wn * 6144 + lane * 16, xb = L_D + wm * 8192 + lane * 16;
      unsigned w_slot = 73728, x_slot = 16384;
      unsigned vo = OFF_HT + layer * HT_BUF + ((8 * k.P + 4 * wm + (q & 1)) * 48 + 3 * (4 * k.kq + k.np)) * 1024 + (6 * wn + (q >> 1)) * 256 + c * 16, vo2 = vo + 98304;
      unsigned ta = (64 * wm + c) * 4, tc = L_CS3 + (48 * wn + 4 * q) * 4;
      unsigned sa = L_D + 49152 + (2 * w + (lane >> 5)) * 1024 + (lane & 31) * 4, sw = (32 * w + 16 * (lane >> 5) + c) * 4;
      pin(wb); pin(xb); pin(vo); pin(vo2); pin(ta); pin(tc); pin(sa); pin(sw);
      bar();
#pragma unroll
      for (int n = 0; n < 12; ++n) { if (n < 10) fa[n & 1][n >> 1] = lds16(L, wb + 49152 + (n & 1) * 12288 + (n >> 1) * 1024); else fl[0][n & 1] = lds16(L, wb + 49152 + (n & 1) * 12288 + 5120); }
      unsigned wn3 = wb + 73728, xn = xb + 16384;
      pin(wn3); pin(xn);
      bar();
      stamp(k, w, sb + 11);
#pragma unroll
      for (int n = 0; n < 8; ++n) fb[0][n & 1][n >> 1] = lds16(L, xb + n * 1024);
      lgk0();
      int left = 4;
      auto mm = [&](int p, int n) { const int m = n >> 1, i = m >> 2, t = (i & 1) ? 3 - (m & 3) : m & 3, kk = (n ^ m) & 1; mma(acc[t][i], i < 5 ? fa[kk][i < 5 ? i : 0] : fl[p][kk], fb[p][kk][t]); };
      auto rd = [&](int p, int n) {
        const int m = n >> 1, e = n & 1;
        if (m == 8 || m == 9) fa[e][m - 8] = lds16(L, wn3 + e * 12288 + (m - 8) * 1024);
        else if (m == 10) fl[p ^ 1][e] = lds16(L, wn3 + e * 12288 + 5120);
        else if (m == 12 || m == 16 || m == 20) fa[e][m / 4 - 1] = lds16(L, wn3 + e * 12288 + (m / 4 - 1) * 1024);
        else if (m == 11) fb[p ^ 1][e][0] = lds16(L, xn + e * 1024);
        else if (m >= 13 && m <= 15) fb[p ^ 1][e][m - 12] = lds16(L, xn + e * 1024 + (m - 12) * 2048);
      };
#pragma unroll 1
      do {
#pragma unroll
        for (int p = 0; p < 2; ++p) {
#pragma unroll
          for (int n = 0; n < 48; ++n) {
            if (n == 16) barv();
            mm(p, n); rd(p, n);
            if (n == 34) { x_slot = x_slot == 32768 ? 0 : x_slot + 16384; pins(x_slot); }
            if (n == 35) { w_slot = w_slot == 73728 ? 0 : w_slot + 24576; pins(w_slot); }
            if (n == 42) { wn3 = wb + w_slot; xn = xb + x_slot; pin(wn3); pin(xn); }
            if (n == 43 && p) { --left; pins(left); }
            sched();
          }
          lgk0();
        }
      } while (left);
      float mean[4], rl2[4], sd[4];
      f32x4 cs[6];
      unsigned h[4][2];
      f32x4 o[4];
      auto corr1 = [&](int f, int n) { const int t = n >> 3, i = 2 * f + ((n >> 2) & 1), e = n & 3; if (n < 32) acc[t][i][e] = one(fmaf(cs[i][e], mean[t], acc[t][i][e])); };
      auto glu1 = [&](int n) {
        const int t = n / 15, j = n % 15;
        if (j < 2) { o[t][2 * j] = one(acc[t][0][2 * j] * rl2[t]); o[t][2 * j + 1] = one(acc[t][0][2 * j + 1] * rl2[t]); }
        else if (j < 6) o[t][j - 2] = __builtin_amdgcn_exp2f(o[t][j - 2]);
        else if (j < 8) { o[t][2 * j - 12] = one(fmaf(o[t][2 * j - 12], sd[t], sd[t])); o[t][2 * j - 11] = one(fmaf(o[t][2 * j - 11], sd[t], sd[t])); }
        else if (j < 12) o[t][j - 8] = __builtin_amdgcn_rcpf(o[t][j - 8]);
        else if (j < 14) { o[t][2 * j - 24] = one(o[t][2 * j - 24] * acc[t][1][2 * j - 24]); o[t][2 * j - 23] = one(o[t][2 * j - 23] * acc[t][1][2 * j - 23]); }
        else { h[t][0] = pk(o[t][0], o[t][1]); h[t][1] = pk(o[t][2], o[t][3]); }
      };
      auto glu = [&](int f, int t, int from) {
        if (from < 1) { o[t] = acc[t][2 * f] * rl2[t]; asm("" : "+v"(o[t])); }
        if (from < 2) {
#pragma unroll
          for (int r = 0; r < 4; ++r) o[t][r] = __builtin_amdgcn_exp2f(o[t][r]);
        }
        o[t] = o[t] * sd[t] + sd[t]; asm("" : "+v"(o[t]));
#pragma unroll
        for (int r = 0; r < 4; ++r) o[t][r] = __builtin_amdgcn_rcpf(o[t][r]);
        o[t] *= acc[t][2 * f + 1];
        h[t][0] = pk(o[t][0], o[t][1]); h[t][1] = pk(o[t][2], o[t][3]);
      };
      auto tab2 = [&](float (&d)[4], unsigned off, int j) { d[2 * j] = *(LAS float*)(L + ta + off + 128 * j); d[2 * j + 1] = *(LAS float*)(L + ta + off + 128 * j + 64); };
      float pv[8], s1 = 0.f, s2 = 0.f;
#pragma unroll
      for (int n = 0; n < 26; ++n) {
        if (n == 16) barv();
        mm(0, n); rd(0, n);
        if (n < 2) {
#pragma unroll
          for (int j = 0; j < 4; ++j) pv[4 * n + j] = *(LAS float*)(L + sa + 128 * (4 * n + j));
        }
        if (n == 2) { cs[0] = *(LAS f32x4*)(L + tc); cs[1] = *(LAS f32x4*)(L + tc + 384); }
        if (n == 6) { s1 = one(pv[0] + pv[1]); s2 = one(pv[2] + pv[3]); }
        if (n == 7 || n == 8) { s1 = one(s1 + pv[2 * n - 10]); s2 = one(s2 + pv[2 * n - 9]); }
        if (n == 9) s1 = one(s1 + s2);
        if (n == 10) { s2 = one(s1 * (1.f / 640.f)); s1 = one(s1 * (1.f / 640.f)); }
        if (n == 11) { const auto r = __builtin_amdgcn_permlane16_swap(__float_as_uint(s1), __float_as_uint(s2), false, false); s1 = one(__uint_as_float(r[0])); s2 = one(__uint_as_float(r[1])); }
        if (n == 12) { s2 = one(fmaf(-s1, s1, s2)); s1 = one(-s1); }
        if (n == 13) { *(LAS float*)(L + sw + L_MEAN) = s1; s2 = one(fmaxf(s2, 0.f)); }
        if (n == 14) s2 = one(s2 + 1e-5f);
        if (n == 15) s1 = __builtin_amdgcn_rsqf(s2);
        if (n == 16 || n == 17) tab2(mean, L_MEAN, n & 1);
        if (n == 18) s2 = one(s2 * s1);
        if (n == 19) s1 = one(s1 * -1.44269504089f);
        if (n == 20) *(LAS float*)(L + sw + L_STD) = s2;
        if (n == 21) *(LAS float*)(L + sw + L_RSTD) = s1;
        sched();
      }
#pragma unroll
      for (int n = 26; n < 48; ++n) {
        mm(0, n); rd(0, n);
        if (n < 34) corr1(0, n - 26);
        if (n >= 34 && n < 40) { corr1(0, 2 * n - 60); corr1(0, 2 * n - 59); }
        if (n == 40 || n == 41) corr1(0, n - 20);
        if (n == 42) { cs[2] = *(LAS f32x4*)(L + tc + 64); cs[3] = *(LAS f32x4*)(L + tc + 384 + 64); }
        if (n > 42) { corr1(0, 2 * n - 64); corr1(0, 2 * n - 63); }
        sched();
      }
      barv();
#pragma unroll
      for (int n = 0; n < 48; ++n) {
        mm(1, n);
        if (n < 16) { corr1(1, 2 * n); corr1(1, 2 * n + 1); }
        if (n == 12 || n == 13) tab2(rl2, L_RSTD, n & 1);
        if (n == 14 || n == 15) tab2(sd, L_STD, n & 1);
        if (n == 16 || n == 17) cs[n - 12] = *(LAS f32x4*)(L + tc + 128 + 384 * (n & 1));
        if (n >= 18) glu1(n - 18);
        sched();
      }
      stamp(k, w, sb + 12);
      st16(rw, swap_pair(h[0][0], h[0][1], h[1][0], h[1][1]), vo);
      sched();
      glu(0, 2, 0); glu(0, 3, 0);
      st16(rw, swap_pair(h[2][0], h[2][1], h[3][0], h[3][1]), vo2);
      sched();
#pragma unroll
      for (int f = 1; f < 3; ++f)
#pragma unroll
        for (int t = 0; t < 4; t += 2) {
          if (f == 2) { acc[t][4] += cs[4] * mean[t]; acc[t][5] += cs[5] * mean[t]; acc[t + 1][4] += cs[4] * mean[t + 1]; acc[t + 1][5] += cs[5] * mean[t + 1]; }
          glu(f, t, 0); glu(f, t + 1, 0);
          st16(rw, swap_pair(h[t][0], h[t][1], h[t + 1][0], h[t + 1][1]), (t ? vo2 : vo) + f * 512);
          sched();
        }
      stamp(k, w, sb + 13);
    }
    {
      PHASE_LANE();
      const int kh = w & 1;
      unsigned wb[3] = {L_W4 + kh * 5120 + lane * 16, 0, 0}; wb[1] = wb[0] + 51200; wb[2] = wb[0] + 102400;
      pin(wb[0]); pin(wb[1]); pin(wb[2]);
      bar();
      constexpr int R = AHEAD + 2;
      bf16x8 ha[R], wf[6][5];
      f32x4 acc[2][5];
      auto rdw = [&](int s, int i) { wf[s % 6][i] = lds16(L, wb[w4off(s) / 51200] + w4off(s) % 51200 + i * 1024); };
#pragma unroll
      for (int n = 0; n < 30; ++n) rdw(n / 5, n % 5);
      lgk0();
      drain_bar();
      stamp(k, w, sb + 14);
      sched();
      unsigned hv[2] = {OFF_HT + layer * HT_BUF + tfo * 49152 + kh * 1024 + lane * 16, OFF_HT + layer * HT_BUF + (tfo ^ 1) * 49152 + kh * 1024 + lane * 16};
      unsigned rd = L_RED + w * 5120 + lane * 16, wr = L_RED + (w ^ 1) * 5120 + lane * 16;
      pin(hv[0]); pin(hv[1]); pin(rd); pin(wr);
      X_ADDR(1);
      unsigned ow = L_W4 + 61440 + w * 3072 + c * 160 + 8 * q, orr = L_W4 + 61440 + w * 3072 + lane * 16, vo3[3];
#pragma unroll
      for (int j = 0; j < 3; ++j) { const int u = 64 * j + lane; vo3[j] = 16 * tfo * 1280 + cto * 160 + ((u * 205) >> 11) * 1120 + u * 16 + (j == 2 ? (lane & 32) << 25 : 0); pin(vo3[j]); }
      pin(ow); pin(orr);
#pragma unroll
      for (int i = 0; i < 5; ++i) { acc[0][i] = unpk(res[2 * i], res[2 * i + 1]); pin(acc[0][i]); }
      bar();
      stamp(k, w, sb + 15);
      auto req = [&](int l) { if (l < 48) ha[l % R] = ld16(rw, hv[1 - ((l >> 1) & 1)] + (l & 1) * 2048, (l >> 2) * 4096); };
#pragma unroll
      for (int l = 0; l < AHEAD; ++l) { req(l); sched(); }
      f32x4 pt[5];
#pragma unroll
      for (int d = 0; d < 12; ++d) {
        if (d == 4 || d == 9 || d == 10) barv();
#pragma unroll
        for (int n = 0; n < 20; ++n) {
          const int m = n >> 1, v = m / 5, i = v && d < 11 ? 9 - m : m % 5, e = (n ^ m) & 1;
          if (d == 11 && n == 17) barv();
          if (d == 0 && !v && !(n & 1)) mma0(acc[1][i], wf[e][i], ha[e]); else mma(acc[1 - v][i], wf[(2 * d + e) % 6][i], ha[(4 * d + 2 * v + e) % R]);
          if (n % 5 == 4) req(4 * d + n / 5 + AHEAD);
          else if (d >= 2 && d < 11 && n < 13) { const int j = n - n / 5; if (j < 10) rdw(2 * d + 2 + (j & 1), j >> 1); }
          if (n == 18 && d >= 2 && d < 11) lgk0();
          if (d == 11) {
            if (n >= 10 && n < 15) *(LAS f32x4*)(L + wr + (n - 10) * 1024) = acc[1][n - 10];
            if (n >= 17) { pt[2 * n - 34] = *(LAS f32x4*)(L + rd + (2 * n - 34) * 1024); if (n < 19) pt[2 * n - 33] = *(LAS f32x4*)(L + rd + (2 * n - 33) * 1024); }
          }
          sched();
        }
      }
      stamp(k, w, sb + 16);
#pragma unroll
      for (int i = 0; i < 5; ++i) { acc[0][i] += pt[i]; pin(acc[0][i]); sched(); }
      if (layer == 1) {
        const rsrc_t ro = mk(out, 2048 * 640 * 2);
#pragma unroll
        for (int i = 0; i < 5; ++i) *(LAS u32x2*)(L + ow + 32 * i) = (u32x2){pk(acc[0][i][0], acc[0][i][1]), pk(acc[0][i][2], acc[0][i][3])};
        asm volatile("" ::: "memory");
#pragma unroll
        for (int j = 0; j < 3; ++j) st16(ro, *(LAS u32x4*)(L + orr + j * 1024), vo3[j]);
        stamp(k, w, sb + 17);
      } else {
        float s = 0.f, ss = 0.f;
#pragma unroll
        for (int j = 0; j < 3; ++j) x_part(rw, vx, vx2, j, acc[0], res, s, ss);
        x_stats(rw, vs, s, ss);
        bar();
        stamp(k, w, sb + 17);
        drain_bar();
      }
      stamp(k, w, sb + 18);
    }
  }
  STAMP_OUT(k0, w0);
}

DI void stats_load(const Ctx& k, rsrc_t rw, int xb, int tf0, u32x2 (&sv)[8]) {
#pragma unroll
  for (int j = 0; j < 8; ++j) sv[j] = __builtin_amdgcn_raw_buffer_load_b64(rw, (k.lane >> 4) * 1024 + (k.lane & 15) * 8 + j * 128, OFF_ST + xb * ST_BUF + tf0 * 1024, SC1);
}
DI void stats_sum(u32x2 (&sv)[8], float (&s)[2]) {
#pragma unroll
  for (int j = 0; j < 8; ++j) pin(sv[j]);
#pragma unroll
  for (int e = 0; e < 2; ++e) {
    s[e] = __uint_as_float(sv[0][e]);
#pragma unroll
    for (int j = 1; j < 8; ++j) s[e] = one(s[e] + __uint_as_float(sv[j][e]));
    s[e] *= 1.f / 640.f; pin(s[e]);
  }
}
DI void stats_table(const Ctx& k, int a, float (&s)[2], float scale) {
  pin(s[0]); pin(s[1]);
  float r[2];
#pragma unroll
  for (int e = 0; e < 2; ++e) {
    const float ss = __builtin_bit_cast(float, __builtin_amdgcn_update_dpp(0, __builtin_bit_cast(int, s[e]), 0x128, 0xf, 0xf, true));
    const float v = one(fmaxf(one(fmaf(-s[e], s[e], ss)), 0.f) + 1e-5f), q = __builtin_amdgcn_rsqf(v);
    r[e] = one(q * scale);
  }
  if (!(k.lane & 8)) {
    const unsigned t = (64 * a + (k.lane & 48) + 2 * (k.lane & 7)) * 4;
    *(LAS f32x2*)(k.lds + L_MEAN + t) = (f32x2){one(-s[0]), one(-s[1])};
    *(LAS f32x2*)(k.lds + L_RSTD + t) = (f32x2){r[0], r[1]};
  }
}

DI void loader(const Ctx& k0, const char* x0, const char* wsp, const char* wdp, int a4) {
  Ctx k = k0;
  const bool isW = a4 < 2;
  LAS char* const L = k.lds;
  const rsrc_t rw = mk(k.ws, WS_END);
  const rsrc_t rs = mk(wsp, 2048 * 2048 * 2);
  auto fresh = [&]() { asm volatile("" : "+v"(k.lane), "+s"(k.P), "+s"(k.kq), "+s"(k.np), "+s"(k.P1), "+s"(k.ct1), "+s"(k.mat), "+s"(a4)); };
#define lane k.lane
#define a (a4 & 1)
#define me (k.P * 16 + k.kq * 4 + k.np)
#define n3 (4 * k.kq + k.np)
#define sw (((lane & 7) ^ (4 * a + (lane >> 4))) * 16)
#define vo2 ((lane >> 3) * 4096 + sw)
#define so2 ((128 * k.P + 8 * a) * 4096 + (k.kq & 1) * 2048)
#define so3 (OFF_W3 + n3 * 20 * 12288 + a * 12288)
#define so4 (OFF_W4 + (2 * k.np + (k.kq & 1)) * 48 * 5120)
  bf16x8 k1[25], k4[3][8];
  u64 tag = 0;
  int rot = 2 * k.np;
  auto ktile = [&](int t) { const int r = (t + rot) & 7; return 4 * (r >> 1) + 2 * (t >> 3) + (r & 1); };
  STAMP_DECL;
  auto w2 = [&](int t, int slot) {
#pragma unroll
    for (int j = 0; j < 8; ++j) dma<0, 0>(rs, L + L_WR + slot * 16384 + (2 * j + a) * 1024, vo2, so2 + j * 16 * 4096 + ktile(t) * 128);
  };
  auto w2n = [&](int t0, int t1) {
#pragma unroll 1
    for (int t = t0; t < t1; ++t) w2(t, t < 3 ? t + 3 : t - 3);
  };
  auto w3 = [&](int t, int slot) { dma_lin<SC1, 12>(rw, L + L_WR + slot * 24576 + a * 12288, lane * 16, so3 + t * 24576); };
  auto w4 = [&](int ch) { dma_lin<SC1, 15>(rw, L + L_W4 + w4slot(ch) * 30720 + a * 15360, lane * 16, so4 + ch * 30720 + a * 15360); };
  auto push = [&](int ev, int wg0, int wgstep, int n, int slot) {
    if (a4 == 2) {
      unsigned dst = lane < n ? OFF_MB + ev * MB_EV + (wg0 + lane * wgstep) * 128 + slot * 8 : 0x7fffffff;
      pin(dst); pin(tag);
      bar();
      __builtin_amdgcn_raw_buffer_store_b64(__builtin_bit_cast(u32x2, tag), rw, dst, 0, SC1);
    } else bar();
    fresh();
  };
  unsigned budget = SPIN_CAP;
  auto wait_mb = [&](int ev, int wg, int n) {
    GAS u64* src = (GAS u64*)(k.ws + OFF_MB + ev * MB_EV + wg * 128) + (lane & (n - 1));
#pragma unroll 1
    for (; budget; budget = uni(budget - 1)) {
      const u64 v = __hip_atomic_load(src, __ATOMIC_RELAXED, __HIP_MEMORY_SCOPE_AGENT);
      if (__all(v == tag)) break;
      __builtin_amdgcn_s_sleep(1);
    }
  };
  auto poll = [&](int ev, int wg, int n) {
    if (a4 == 2) wait_mb(ev, wg, n);
    drain_bar();
  };

  auto get_tag = [&]() { const char* e = k.ws; pins(e); tag = *(const GAS u64*)e; };
  auto mix_tag = [&]() { pin(tag); tag = tag * 6364136223846793005ull + 1442695040888963407ull; };
  if (!isW) __builtin_amdgcn_s_setprio(1);
  {
    const rsrc_t r1 = isW ? mk(wdp, 640 * 640 * 2) : mk(x0, 2048 * 640 * 2);
    const unsigned v1 = (lane >> 3) * 1280 + sw, s1 = ((isW ? 80 * k.ct1 : 128 * k.P1) + 8 * a) * 1280;
    int slot = 2, wt = rot1(k);
    if ((k.P & 1) && !isW) __builtin_amdgcn_s_sleep(LAG);
#pragma unroll 1
    for (int t = 0; t < 10; ++t) {
      if (isW) {
        if (t >= 3) { waitv<10>(); bar(); }
#pragma unroll
        for (int j = 0; j < 5; ++j) dma<0, 0>(r1, L + L_D + wt * 10240 + (2 * j + a) * 1024, v1, s1 + 16 * j * 1280 + wt * 128);
      } else {
        if (t >= 2) { waitv<8>(); bar(); }
#pragma unroll
        for (int j = 0; j < 8; ++j) dma<0, 0>(r1, L + L_XR + slot * 16384 + (2 * j + a) * 1024, v1, s1 + 16 * j * 1280 + wt * 128);
      }
      slot = slot == 2 ? 0 : slot + 1; wt = wt == 9 ? 0 : wt + 1;
    }
    if (isW) {
      waitv<10>(); bar();
      waitv<5>(); bar(); w2(0, 3);
      waitv<8>(); bar(); w2(1, 4);
      bar();
    } else {
      get_tag();
      waitv<8>(); bar(); waitv<0>();
      mix_tag(); pin(tag);
      bar();
      if (!(k.P & 1)) {
#pragma unroll
        for (int j = 0; j < 2; ++j) touch<0>(rs, L + L_JUNK, (lane >> 1) * 4096 + (lane & 1) * 128, (128 * k.P + 64 * a + 32 * j) * 4096 + (k.kq & 1) * 2048 + ktile(8) * 128);
      }
      bar();
    }
  }
  {
    unsigned kb[2] = {L_D + lane * 16 + ((a4 >> 1) * 10 + a) * 1024, 0}; kb[1] = kb[0] + 61440; pin(kb[0]); pin(kb[1]);
    bar();
#pragma unroll
    for (int j = 0; j < 25; ++j) k1[j] = lds16(L, kb[j / 15] + ((j / 5) % 3) * 20480 + (j % 5) * 2048);
    if (isW) w2(2, 5);
    stamp(k, 4 + a4, 51);
  }
#pragma unroll 1
  for (int layer = 0; layer < 2; ++layer) {
    const int ev0 = layer * EV_PER_LAYER, sb = 3 + layer * 24;
    if (layer) {
      stamp(k, 4 + a4, sb + 0);
      poll(ev0 + EV_X, k.P1 * 16 + k.mat * 8 + k.ct1, 16);
      if (!isW) { u32x2 sv[8]; float sm[2]; stats_load(k, rw, 1, 8 * k.P1 + 4 * a, sv); stats_sum(sv, sm); stats_table(k, a, sm, k.mat ? -1.f : 1.f); }
      bar();
      if (isW) w2n(3, 6);
    }
    push(ev0 + EV_AB, (2 * k.mat + (k.P1 >> 3)) * 4 + (k.ct1 >> 1), 16, 16, 8 * (k.P1 & 1) + (k.P1 & 6) + (k.ct1 & 1));
    GAS u64* rtf = (GAS u64*)(k.ws + OFF_MB + EV_RT * MB_EV);
    if (layer == 0 && a4 == 3 && lane == 0) __hip_atomic_store(rtf + k.b, tag, __ATOMIC_RELAXED, __HIP_MEMORY_SCOPE_AGENT);
    if (isW && layer == 0) { get_tag(); w2n(3, 6); mix_tag(); }
    stamp(k, 4 + a4, sb + 4);
    const unsigned soab = OFF_AB + layer * AB_BUF + (((k.kq >> 1) * 4 + k.np) * 64 + 32 * (k.kq & 1)) * 10240;
    unsigned l16 = lane * 16, so0 = soab + a * 10240 + ktile(0) * 20480, la0 = L_D + a * 10240;
    so0 = __builtin_amdgcn_readfirstlane(so0); la0 = __builtin_amdgcn_readfirstlane(la0);
    pin(l16); pins(so0); pins(la0);
    if (a4 == 3 && (k.P & 1) && layer == 0) __builtin_amdgcn_s_sleep(LAG2);
    poll(ev0 + EV_AB, me, 8);
    if (!isW) dma_lin<SC1, 10>(rw, L + la0, l16, so0);
    if (isW) {
      const unsigned sb2 = uni(so2), lb2 = uni(L_WR + a * 1024);
      unsigned v2 = vo2, so = uni(sb2 + ktile(6) * 128), la = uni(lb2 + 49152), t = 7;
      pin(v2);
#pragma unroll 1
      do {
        waitv<32>(); bar();
#pragma unroll
        for (int j = 0; j < 2; ++j) dma<0, 0>(rs, L + la + j * 2048, v2, so + j * 16 * 4096);
        sched();
        const unsigned sn = uni(sb2 + ktile(t) * 128), ln = uni(la == lb2 + 81920 ? lb2 : la + 16384);
        t = uni(t + 1);
        sched();
#pragma unroll
        for (int j = 2; j < 8; ++j) dma<0, 0>(rs, L + la + j * 2048, v2, so + j * 16 * 4096);
        so = sn; la = ln;
      } while (t != 17);
      const bool chk = layer == 0 && a == 0;
      u64 f[4];
      if (chk) {
#pragma unroll
        for (int j = 0; j < 4; ++j) f[j] = __hip_atomic_load(rtf + lane + 64 * j, __ATOMIC_RELAXED, __HIP_MEMORY_SCOPE_AGENT);
      }
      waitv<32>(); bar();
      if (chk) {
#pragma unroll
        for (int j = 0; j < 4; ++j) pin(f[j]);
#pragma unroll 1
        for (unsigned spins = 0; spins < SPIN_CAP && !__all(f[0] == tag && f[1] == tag && f[2] == tag && f[3] == tag); ++spins) {
#pragma unroll
          for (int j = 0; j < 4; ++j) f[j] = __hip_atomic_load(rtf + lane + 64 * j, __ATOMIC_RELAXED, __HIP_MEMORY_SCOPE_AGENT);
        }
      }
      waitv<0>(); bar();
      w3(3, 1); bar(); bar(); w3(0, 2); bar();
      w3(1, 3); bar(); w3(2, 0);
    } else {
      stamp(k, 4 + a4, sb + 3);
      const unsigned sba = uni(soab + a * 10240);
      dma_lin<SC1, 10>(rw, L + la0 + 20480, l16, sba + ktile(1) * 20480);
      unsigned so = uni(sba + ktile(2) * 20480), la = uni(la0 + 40960), t = 3;
      auto step = [&]() {
        bar();
        if (FINE == 2 && layer == FL) stamp(k, 4 + a4, 43 + t);
        dma_lin<SC1, 4>(rw, L + la, l16, so);
        sched();
        const unsigned sn = uni(sba + ktile(t) * 20480), ln = uni(la == la0 + 40960 ? la0 : la + 20480);
        t = uni(t + 1);
        sched();
        dma_lin<SC1, 6>(rw, L + la + 4096, l16, so + 4096);
        so = sn; la = ln;
      };
#pragma unroll 1
      do { waitv<10>(); step(); } while (t != 8);
      unsigned ja = L_JUNK + 256 + layer * 256 + a * 128;
      if (lane < 8) dma<SC1, 0>(rw, L + ja, l16, OFF_MB + (ev0 + EV_AB) * MB_EV + me * 128);
      ja += (lane & 15) * 8; pin(ja);
      waitv<11>(); step();
      waitv<10>();
      if (!__all(*(LAS u64*)(L + ja) == tag)) wait_mb(ev0 + EV_AB, me, 16);
      step();
#pragma unroll 1
      do { waitv<10>(); step(); } while (t != 17);
      waitv<10>(); bar(); if (FINE == 2 && layer == FL) stamp(k, 4 + a4, 60);
      waitv<0>(); bar(); if (FINE == 2 && layer == FL) stamp(k, 4 + a4, 61);

    }
    auto k4load = [&](int n) {
#pragma unroll
      for (int j = 0; j < 8; ++j) k4[n][j] = ld16(rw, lane * 16 + a4 * 1024, so4 + (5 + n) * 30720 + j * 4096);
    };
    push(ev0 + EV_EX, k.P * 16 + k.np, 4, 4, k.kq);
    if (layer == 0) k4load(0);
    poll(ev0 + EV_EX, me, 4);
    push(ev0 + EV_X1, k.P * 16, 1, 16, n3);

    if (layer == 0) {
      u32x2 cv[5];
      if (isW) {
#pragma unroll
        for (int j = 0; j < 5; ++j) cv[j] = __builtin_amdgcn_raw_buffer_load_b64(rw, lane * 8, OFF_CS + (j * 3072 + a * 1536 + 96 * n3) * 4, SC1);
      }
      k4load(1);
      if (isW) {
        sched();
        f32x2 s = {0.f, 0.f};
#pragma unroll
        for (int j = 0; j < 5; ++j) s += __builtin_bit_cast(f32x2, cv[j]);
        if (lane < 48) *(LAS f32x2*)(L + L_CS3 + (96 * a + 2 * lane) * 4) = s;
      }
    }
    stamp(k, 4 + a4, sb + 11);
    l16 = lane * 16; so0 = OFF_XT + 2 * layer * XT_BUF + (8 * k.P + 4 * a) * 20480; la0 = L_D + a * 8192;
    so0 = __builtin_amdgcn_readfirstlane(so0); la0 = __builtin_amdgcn_readfirstlane(la0);
    pin(l16); pins(so0); pins(la0);
    poll(ev0 + EV_X1, me, 16);
    if (!isW) {
#pragma unroll
      for (int j = 0; j < 4; ++j) dma_lin<SC1, 2>(rw, L + la0 + j * 2048, l16, so0 + j * 20480);
    }
    if (isW) {
      bar();
      __builtin_amdgcn_s_sleep(3);
      dma_lin<SC1, 4>(rw, L + L_D + 49152 + a * 4096, lane * 16, OFF_ST + 2 * layer * ST_BUF + (8 * k.P + 4 * a) * 1024);
      const unsigned lb3 = uni(L_WR + a * 12288), se = uni(so3 + 10 * 24576);
      unsigned so = uni(so3 + 4 * 24576), la = uni(lb3 + 49152);
#pragma unroll 1
      do {
        waitv<24>(); bar();
        dma_lin<SC1, 4>(rw, L + la, l16, so);
        sched();
        const unsigned sn = uni(so + 24576), ln = uni(la == lb3 + 73728 ? lb3 : la + 24576);
        sched();
        dma_lin<SC1, 8>(rw, L + la + 4096, l16, so + 4096);
        so = sn; la = ln;
      } while (so != se);
      waitv<24>(); bar();
      waitv<12>(); bar(); waitv<0>(); bar();
      bar();
      w4(2); w4(3); w4(4);
      stamp(k, 4 + a4, sb + 15);
    } else {
      stamp(k, 4 + a4, sb + 10);
#pragma unroll
      for (int j = 0; j < 4; ++j) dma_lin<SC1, 2>(rw, L + la0 + 16384 + j * 2048, l16, so0 + 2048 + j * 20480);
      unsigned so = uni(so0 + 4096), la = uni(la0 + 32768);
#pragma unroll 1
      do {
        waitv<8>(); bar();
        if (FINE == 3 && layer == FL) stamp(k, 4 + a4, 44 + ((so - so0) >> 11));
        dma_lin<SC1, 2>(rw, L + la, l16, so);
        sched();
        const unsigned sn = uni(so + 2048), ln = uni(la == la0 + 32768 ? la0 : la + 16384);
        sched();
#pragma unroll
        for (int j = 1; j < 4; ++j) dma_lin<SC1, 2>(rw, L + la + j * 2048, l16, so + j * 20480);
        so = sn; la = ln;
      } while (so != so0 + 20480);
      waitv<8>(); bar();
      if (FINE == 3 && layer == FL) stamp(k, 4 + a4, 54);
      w4(0);
      waitv<15>(); bar();
      if (FINE == 3 && layer == FL) stamp(k, 4 + a4, 55);
      w4(1);
      bar();
      if (FINE == 3 && layer == FL) stamp(k, 4 + a4, 56);
      waitv<0>();
    }
    bar();
    push(ev0 + EV_H, k.P * 16, 1, 16, n3);
    if (layer == 0) k4load(2);
    auto k4put = [&](int n, bool paced) {
      unsigned kb = L_W4 + w4slot(5 + n) * 30720 + a4 * 1024 + lane * 16; pin(kb);
#pragma unroll
      for (int j = 0; j < 8; ++j) if (j < 7 || isW) { *(LAS bf16x8*)(L + kb + j * 4096) = k4[n][j]; if (paced) __builtin_amdgcn_s_sleep(2); }
    };
    k4put(0, false); k4put(1, false);
    poll(ev0 + EV_H, me, 16);
    bar();
    k4put(2, true);
    bar(); bar();
    rot = 0;
    if (isW && layer == 0) w2n(0, 2);
    bar();
    if (layer == 0) {
      if (isW) w2n(2, 3);
      unsigned kb[2] = {L_D + lane * 16 + ((a4 >> 1) * 10 + a) * 1024, 0}; kb[1] = kb[0] + 61440; pin(kb[0]); pin(kb[1]);
      bar();
#pragma unroll
      for (int j = 0; j < 7; ++j) *(LAS bf16x8*)(L + kb[j / 15] + ((j / 5) % 3) * 20480 + (j % 5) * 2048) = k1[j];
      push(EV_PER_LAYER + EV_X, k.P * 16, 1, 16, n3);
#pragma unroll
      for (int j = 7; j < 25; ++j) *(LAS bf16x8*)(L + kb[j / 15] + ((j / 5) % 3) * 20480 + (j % 5) * 2048) = k1[j];
    }
  }
#undef lane
  STAMP_OUT(k, 4 + a4);
#define lane k.lane
  if (k.b == 0 && a4 == 2 && lane == 0) *(GAS u64*)k.ws = tag;
#undef lane
#undef a
#undef me
#undef n3
#undef sw
#undef vo2
#undef so2
#undef so3
#undef so4
}

__global__ void __launch_bounds__(512, 2) fnet_glu_kernel(const char* x0, const char* wsr, const char* wsi, const char* wdr, const char* wdi, const char* wg, const char* wu, const char* wdn, char* out, char* ws) {
  __shared__ __attribute__((aligned(16))) char smem[163840];
  Ctx k;
  k.lds = (LAS char*)smem; k.lane = threadIdx.x & 63; k.b = blockIdx.x;
  const int w = __builtin_amdgcn_readfirstlane(threadIdx.x >> 6);
  if (w < 4 || w == 7) {
    const char* pc; asm volatile("s_getpc_b64 %0" : "=s"(pc));
    touch<0>(mk(pc, CODE_BYTES), k.lds + L_JUNK, (w < 4 ? threadIdx.x : threadIdx.x - 192) * 128, 0);
  }
  asm volatile("" : "+s"(x0), "+s"(wsr), "+s"(wsi), "+s"(wdr), "+s"(wdi), "+s"(wg), "+s"(wu), "+s"(wdn), "+s"(out), "+s"(ws));
  k.ws = ws;
  const int x = k.b & 7, i = k.b >> 3;
  k.kq = x >> 1; k.P = 8 * (x & 1) + 4 * (i >> 4) + 2 * ((i >> 2) & 1) + ((i >> 3) & 1); k.np = i & 3;
  k.P1 = 4 * (x >> 1) + (i >> 3); k.mat = x & 1; k.ct1 = i & 7;
  if (w < 4) consumer(k, x0, wg, wu, wdn, out, w);
  else loader(k, x0, (x >> 2) ? wsi : wsr, (x & 1) ? wdi : wdr, w - 4);
}
}

extern "C" void kernel_launch(void* const* d_in, const int* in_sizes, int n_in, void* d_out, int out_size, void* d_ws, size_t ws_size, hipStream_t stream) {
  fnet_glu_kernel<<<256, 512, 0, stream>>>((const char*)d_in[0], (const char*)d_in[1], (const char*)d_in[2], (const char*)d_in[3], (const char*)d_in[4],
                                           (const char*)d_in[6], (const char*)d_in[7], (const char*)d_in[8], (char*)d_out, (char*)d_ws);
}
```

```cpp
#include <hip/hip_runtime.h>

typedef unsigned long long u64;
typedef short bf16x8 __attribute__((ext_vector_type(8)));
typedef short s16x4 __attribute__((ext_vector_type(4)));
typedef float f32x4 __attribute__((ext_vector_type(4)));
typedef float f32x2 __attribute__((ext_vector_type(2)));
typedef unsigned u32x4 __attribute__((ext_vector_type(4)));
typedef unsigned u32x2 __attribute__((ext_vector_type(2)));
typedef __bf16 bf2 __attribute__((ext_vector_type(2)));
#define LAS __attribute__((address_space(3)))
#define GAS __attribute__((address_space(1)))
#define DI __device__ __forceinline__
typedef __amdgpu_buffer_rsrc_t rsrc_t;

namespace {
constexpr int AHEAD = 10;
constexpr int LAG = 12, LAG2 = 48;
constexpr unsigned CODE_BYTES = 34944;
constexpr unsigned OFF_MB = 4096, MB_EV = 256 * 128;
constexpr int EV_X = 0, EV_AB = 1, EV_EX = 2, EV_X1 = 3, EV_H = 4, EV_PER_LAYER = 5, EV_RT = 0;
constexpr unsigned OFF_CS = 0x60000;
constexpr unsigned OFF_ST = 0x80000, ST_BUF = 8 * 2048 * 8;
constexpr unsigned OFF_XT = 0x100000, XT_BUF = 2048 * 640 * 2;
constexpr unsigned OFF_AB = OFF_XT + 3 * XT_BUF, AB_BUF = 2048 * 1280 * 2;
constexpr unsigned OFF_HT = OFF_AB + 2 * AB_BUF, HT_BUF = 2048 * 1536 * 2;
constexpr unsigned OFF_EX = OFF_HT + 2 * HT_BUF, EX_BUF = 256 * 12 * 2560;
constexpr unsigned OFF_W3 = OFF_EX + 2 * EX_BUF;
constexpr unsigned OFF_W4 = OFF_W3 + 16 * 20 * 12288;
constexpr unsigned WS_END = OFF_W4 + 8 * 48 * 5120;
static_assert(OFF_MB + 10 * MB_EV <= OFF_CS && OFF_CS + 5 * 3072 * 4 <= OFF_ST && OFF_ST + 3 * ST_BUF <= OFF_XT && WS_END <= 0x4000000, "ws map");
constexpr unsigned L_MEAN = 0, L_RSTD = 512, L_CS1 = 1024, L_STD = 1536, L_CS3 = 2304, L_JUNK = 3072;
constexpr unsigned L_D = 4096;
constexpr unsigned L_WR = L_D + 61440, L_W4 = L_D + 6144, L_CP = L_D + 102400, L_XR = L_D + 110592, L_RED = L_W4 + 30720;
constexpr int w4slot(int ch) { return ch == 4 ? 4 : ((ch < 5 ? ch : ch - 5) + 2) & 3; }
constexpr int w4off(int s) { return w4slot(s / 3) * 30720 + (s % 3) * 10240; }
constexpr int SC1 = 16;
constexpr unsigned SPIN_CAP = 1u << 21;

DI rsrc_t mk(const void* p, unsigned bytes) { return __builtin_amdgcn_make_buffer_rsrc((void*)p, 0, bytes, 0x00020000); }
template <int N> DI void waitv() { asm volatile("s_waitcnt vmcnt(%0)" ::"n"(N) : "memory"); }
DI void bar() { asm volatile("s_waitcnt lgkmcnt(0)\n\ts_barrier" ::: "memory"); }
DI void lgk0() { __builtin_amdgcn_sched_barrier(0); __builtin_amdgcn_s_waitcnt(0xC07F); __builtin_amdgcn_sched_barrier(0); }
DI void barv() { lgk0(); asm volatile("s_barrier" ::: "memory"); __builtin_amdgcn_sched_barrier(0); }
DI void drain_bar() {
  asm volatile("s_waitcnt vmcnt(0) lgkmcnt(0)\n\ts_barrier" ::: "memory");
  __builtin_amdgcn_s_waitcnt(0);
  asm volatile("" ::: "memory");
}
DI void sched() { __builtin_amdgcn_sched_barrier(0); }
template <class T> DI void pin(T& x) { asm volatile("" : "+v"(x)); }
template <class T> DI void pins(T& x) { asm volatile("" : "+s"(x)); }
template <int AUX, int IMM> DI void dma(rsrc_t r, LAS char* l, unsigned vo, unsigned so) { __builtin_amdgcn_raw_ptr_buffer_load_lds(r, (LAS void*)l, 16, vo, so, IMM, AUX); }
template <int AUX> DI void touch(rsrc_t r, LAS char* l, unsigned vo, unsigned so) { __builtin_amdgcn_raw_ptr_buffer_load_lds(r, (LAS void*)l, 4, vo, so, 0, AUX); }
template <int AUX, int N> DI void dma_lin(rsrc_t r, LAS char* l, unsigned vo, unsigned so) {
#pragma unroll
  for (int j = 0; j < N; j += 4) {
    dma<AUX, 0>(r, l + j * 1024, vo, so + j * 1024);
    if (j + 1 < N) dma<AUX, 1024>(r, l + j * 1024, vo, so + j * 1024);
    if (j + 2 < N) dma<AUX, 2048>(r, l + j * 1024, vo, so + j * 1024);
    if (j + 3 < N) dma<AUX, 3072>(r, l + j * 1024, vo, so + j * 1024);
  }
}
DI unsigned uni(unsigned x) { x = __builtin_amdgcn_readfirstlane(x); asm volatile("" : "+s"(x)); return x; }
DI bf16x8 ld16(rsrc_t r, unsigned vo, unsigned so) { return __builtin_bit_cast(bf16x8, __builtin_amdgcn_raw_buffer_load_b128(r, vo, so, SC1)); }
DI void st16(rsrc_t r, u32x4 v, unsigned vo) { __builtin_amdgcn_raw_buffer_store_b128(v, r, vo, 0, SC1); }
DI bf16x8 lds16(LAS char* l, unsigned a) { return *(LAS bf16x8*)(l + a); }
DI unsigned pk(float lo, float hi) { f32x2 v = {lo, hi}; return __builtin_bit_cast(unsigned, __builtin_convertvector(v, bf2)); }
DI float blo(unsigned u) { return __uint_as_float(u << 16); }
DI float bhi(unsigned u) { return __uint_as_float(u & 0xffff0000u); }
DI f32x4 unpk(unsigned a, unsigned b) { return (f32x4){blo(a), bhi(a), blo(b), bhi(b)}; }
DI f32x4 mfma(bf16x8 a, bf16x8 b, f32x4 c) { return __builtin_amdgcn_mfma_f32_16x16x32_bf16(a, b, c, 0, 0, 0); }
DI void mma(f32x4& c, const bf16x8& a, const bf16x8& b) { asm volatile("v_mfma_f32_16x16x32_bf16 %0, %1, %2, %0" : "+v"(c) : "v"(a), "v"(b)); __builtin_amdgcn_sched_barrier(0); }
DI void mma0(f32x4& c, const bf16x8& a, const bf16x8& b) { asm volatile("v_mfma_f32_16x16x32_bf16 %0, %1, %2, 0" : "=&v"(c) : "v"(a), "v"(b)); __builtin_amdgcn_sched_barrier(0); }
DI void settle() { asm volatile("s_nop 7"); __builtin_amdgcn_sched_barrier(0); }
DI float dot2(unsigned a, unsigned b, float s) { return __builtin_amdgcn_fdot2_f32_bf16(__builtin_bit_cast(bf2, a), __builtin_bit_cast(bf2, b), s, false); }
DI float one(float v) { asm("" : "+v"(v)); return v; }
DI u32x4 swap_pair(unsigned a0, unsigned a1, unsigned b0, unsigned b1) {
  const auto r0 = __builtin_amdgcn_permlane16_swap(a0, b0, false, false);
  const auto r1 = __builtin_amdgcn_permlane16_swap(a1, b1, false, false);
  return (u32x4){r0[0], r1[0], r0[1], r1[1]};
}

struct Ctx {
  char* ws; LAS char* lds; int lane, b;
  int kq, P, np;
  int P1, mat, ct1;
};

DI u32x4 x_pack(int j, const f32x4 (&v)[5], unsigned (&res)[10]) {
  const int i1 = j < 2 ? 2 * j + 1 : 4;
#pragma unroll
  for (int i = 2 * j; i <= i1; ++i) { res[2 * i] = pk(v[i][0], v[i][1]); res[2 * i + 1] = pk(v[i][2], v[i][3]); }
  return swap_pair(res[4 * j], res[4 * j + 1], res[2 * i1], res[2 * i1 + 1]);
}
DI void x_part(rsrc_t rw, unsigned vx, unsigned vx2, int j, const f32x4 (&v)[5], unsigned (&res)[10], float& s, float& ss) {
  const u32x4 o = x_pack(j, v, res);
  st16(rw, o, j < 2 ? vx + j * 1024 : vx2);
#pragma unroll
  for (int e = 4 * j; e < (j < 2 ? 4 * j + 4 : 10); ++e) { s = dot2(res[e], 0x3f803f80u, s); ss = dot2(res[e], res[e], ss); }
  pin(s); pin(ss);
}
DI void x_stats(rsrc_t rw, unsigned vs, float s, float ss) {
  const auto r = __builtin_amdgcn_permlane16_swap(__float_as_uint(s), __float_as_uint(ss), false, false);
  const unsigned t = __float_as_uint(__uint_as_float(r[0]) + __uint_as_float(r[1]));
  const auto r2 = __builtin_amdgcn_permlane32_swap(t, t, false, false);
  const float u = __uint_as_float(r2[0]) + __uint_as_float(r2[1]);
  __builtin_amdgcn_raw_buffer_store_b32(__float_as_uint(u), rw, vs, 0, SC1);
}

DI int rot1(const Ctx& k) { const int r = ((k.ct1 * 5) >> 2) + (((k.P1 & 3) * 5) >> 1); return r >= 10 ? r - 10 : r; }

#define PHASE_LANE() Ctx k = k0; int w = w0; asm volatile("" : "+v"(k.lane), "+s"(w), "+s"(k.P), "+s"(k.kq), "+s"(k.np), "+s"(k.P1), "+s"(k.ct1), "+s"(k.mat)); \
  const int tfo = 8 * k.P + 4 * (k.kq >> 1) + w, cto = 2 * k.np + (k.kq & 1); (void)tfo; (void)cto; \
  const int lane = k.lane, q = lane >> 4, c = lane & 15, qs = 2 * (q & 1) + (q >> 1); (void)qs; \
  const unsigned rowrd = (c >> 3) * 1024 + (c & 7) * 128 + ((q ^ (c >> 1)) * 16); (void)rowrd
#define X_ADDR(xb) unsigned vx = OFF_XT + (xb) * XT_BUF + tfo * 20480 + (10 * cto + qs) * 256 + c * 16, vx2 = vx + 2048 + ((lane & 16) << 26), \
  vs = OFF_ST + (xb) * ST_BUF + tfo * 1024 + cto * 128 + lane * 4 + ((lane & 32) << 25); pin(vx); pin(vx2); pin(vs)

DI void consumer(const Ctx& k0, const char* x0, const char* wg, const char* wu, const char* wdn, char* out, const int w0) {
  LAS char* const L = k0.lds;
  const rsrc_t rw = mk(k0.ws, WS_END);
  unsigned res[10];
  __builtin_amdgcn_s_setprio(2);
  auto retile = [&](const Ctx& k0, const int w) {
    int e = ((k0.b >> 7) * 64 + (k0.b & 63)) * 4 + w;
    pins(e);
    const int lane = k0.lane;
    const int g3 = ((e >> 2) * 2731) >> 16, ks3 = 4 * g3 + w, st3 = (e >> 2) - g3 * 24, ks4 = (e * 6554) >> 16, st4 = e - ks4 * 10;
    LAS char* t[3];
#pragma unroll
    for (int m = 0; m < 3; ++m) { const int u = rot1(k0) + (m < 2 ? 4 + w : 8 + (w >> 1)); t[m] = L + L_D + (u >= 10 ? u - 10 : u) * 10240 + (m < 2 ? m : w & 1) * 4096; }
    __builtin_amdgcn_s_sleep(LAG);
#pragma unroll
    for (int m = 0; m < 3; ++m) {
      const unsigned stride = m < 2 ? 3072 : 1280;
      const rsrc_t rr = mk(m == 0 ? wg : (m == 1 ? wu : wdn), e < 480 ? 640 * 1536 * 2 : 0);
#pragma unroll
      for (int j = 0; j < 4; ++j) dma<0, 0>(rr, t[m] + j * 1024, (lane >> 3) * stride + (lane & 7) * 16, (32 * (m < 2 ? ks3 : ks4) + 8 * j) * stride + 128 * (m < 2 ? st3 : st4));
    }
    const rsrc_t ro = mk(k0.ws, e < 480 ? WS_END : 0);
    const bf16x8 ones = {0x3f80, 0x3f80, 0x3f80, 0x3f80, 0x3f80, 0x3f80, 0x3f80, 0x3f80};
    const unsigned tr = (lane >> 4) * 1024 + ((lane >> 2) & 3) * 128 + (lane & 3) * 8;
    __builtin_amdgcn_s_waitcnt(0);
    asm volatile("" ::: "memory");
    u32x4 f[3][4];
#pragma unroll
    for (int m = 0; m < 3; ++m)
#pragma unroll
      for (int n = 0; n < 4; ++n) {
        const u32x2 lo = __builtin_bit_cast(u32x2, __builtin_amdgcn_ds_read_tr16_b64_v4i16((LAS s16x4*)(t[m] + tr + 32 * n)));
        const u32x2 hi = __builtin_bit_cast(u32x2, __builtin_amdgcn_ds_read_tr16_b64_v4i16((LAS s16x4*)(t[m] + tr + 32 * n + 512)));
        f[m][n] = (u32x4){lo[0], lo[1], hi[0], hi[1]};
      }
#pragma unroll
    for (int m = 0; m < 3; ++m) {
      f32x4 cs[4];
#pragma unroll
      for (int n = 0; n < 4; ++n) {
        const int nf = 4 * (m < 2 ? st3 : st4) + n;
        st16(ro, f[m][n], lane * 16 + (m < 2 ? OFF_W3 + m * 1024 + ks3 * 12288 + ((nf * 171) >> 10) * 19 * 12288 + nf * 2048 : OFF_W4 + ks4 * 5120 + ((nf * 205) >> 10) * 47 * 5120 + nf * 1024));
        if (m < 2) cs[n] = mfma(ones, __builtin_bit_cast(bf16x8, f[m][n]), (f32x4){0.f, 0.f, 0.f, 0.f});
      }
      if (m < 2) *(LAS float*)(L + L_CP + (m * 64 + lane) * 16 + w * 4) = (lane & 32) ? ((lane & 16) ? cs[3][0] : cs[2][0]) : ((lane & 16) ? cs[1][0] : cs[0][0]);
    }
  };
#pragma unroll 1
  for (int layer = 0; layer < 2; ++layer) {
    {
      PHASE_LANE();
      unsigned vo = OFF_AB + layer * AB_BUF + ((k.mat * 4 + (k.ct1 >> 1)) * 64 + 4 * k.P1 + w) * 10240 + 5 * (k.ct1 & 1) * 1024 + (qs * 16 + c) * 16;
      unsigned ta = (32 * w + 4 * q) * 4, tc = c * 4;
      pin(vo); pin(ta); pin(tc);
      f32x4 acc[2][5], mean[2], rstd[2]; float cs1[5];
      bf16x8 wf[4][5];
      auto tables = [&]() {
#pragma unroll
        for (int i = 0; i < 5; ++i) cs1[i] = *(LAS float*)(L + L_CS1 + tc + i * 64);
#pragma unroll
        for (int t = 0; t < 2; ++t) { mean[t] = *(LAS f32x4*)(L + ta + L_MEAN + 64 * t); rstd[t] = *(LAS f32x4*)(L + ta + L_RSTD + 64 * t); }
      };
#pragma unroll
      for (int i = 0; i < 5; ++i) { acc[0][i] = (f32x4){0.f, 0.f, 0.f, 0.f}; acc[1][i] = (f32x4){0.f, 0.f, 0.f, 0.f}; }
      if (layer == 0) {
        bf16x8 xf[4][2], cf[4], ones = {0x3f80, 0x3f80, 0x3f80, 0x3f80, 0x3f80, 0x3f80, 0x3f80, 0x3f80};
        f32x4 as[2], aq[2], ac[2];
        unsigned wt = rot1(k) * 10240, wa0 = L_D + rowrd;
        pin(wa0);
        unsigned wa = wa0 + wt, xr = L_XR + w * 4096 + rowrd, x_slot = 32768;
#pragma unroll
        for (int i = 0; i < 5; ++i) { pin(acc[0][i]); pin(acc[1][i]); }
#pragma unroll
        for (int t = 0; t < 2; ++t) { as[t] = (f32x4){0.f, 0.f, 0.f, 0.f}; aq[t] = as[t]; ac[t] = as[t]; pin(as[t]); pin(aq[t]); pin(ac[t]); }
        pin(wa); pin(xr); pin(ones);
        if (k.P & 1) retile(k, w);
        bar();
        unsigned xn[2] = {xr + 32768, (xr + 32768) ^ 64}, wn[2] = {wa, wa ^ 64};
        const unsigned wo = w * 2048;
        auto rd = [&](int p, int n) {
          const int e = n & 1, m = n >> 1;
          if (m < 2) xf[2 * p + e][m] = lds16(L, xn[e] + m * 2048);
          else if (m < 7) wf[2 * p + e][m - 2] = lds16(L, wn[e] + (m - 2) * 2048);
          else cf[2 * p + e] = lds16(L, wn[e] + wo);
        };
#pragma unroll
        for (int n = 0; n < 16; ++n) rd(0, n);
        lgk0();
        int left = 5;
#pragma unroll 1
        do {
#pragma unroll
          for (int p = 0; p < 2; ++p) {
#pragma unroll
            for (int n = 0; n < 32; ++n) {
              const int m = n >> 1, h = 2 * p + ((n ^ m) & 1);
              if (n == 10) barv();
              if (m < 10) { const int t = m / 5, i = t ? 9 - m : m; mma(acc[t][i], xf[h][t], wf[h][i]); }
              else if (m == 10) mma(aq[1], xf[h][1], xf[h][1]);
              else if (m == 11) mma(as[1], xf[h][1], ones);
              else if (m == 12) mma(as[0], xf[h][0], ones);
              else if (m == 13) mma(aq[0], xf[h][0], xf[h][0]);
              else if (m == 14) mma(ac[0], ones, cf[h]);
              else mma(ac[1], ones, wf[h][4]);
              if (n == 0) { x_slot = x_slot == 32768 ? 0 : x_slot + 16384; pins(x_slot); }
              if (n == 1) { wt = wt == 92160 ? 0 : wt + 10240; pins(wt); }
              if (n == 2) { xn[0] = xr + x_slot; wn[0] = wa0 + wt; pin(xn[0]); pin(wn[0]); }
              if (n == 4) { xn[1] = xn[0] ^ 64; wn[1] = wn[0] ^ 64; pin(xn[1]); pin(wn[1]); }
              if (n == 6 && p) { --left; pins(left); }
              if (n >= 10 && n < 26) rd(p ^ 1, n - 10);
              sched();
            }
            lgk0();
          }
        } while (left);
        settle();
        if (q == 0) { *(LAS float*)(L + L_CS1 + (16 * w + c) * 4) = ac[0][0]; *(LAS float*)(L + L_CS1 + (64 + c) * 4) = ac[1][0]; }
#pragma unroll
        for (int t = 0; t < 2; ++t) {
          const float s = ((c & 2) ? ((c & 1) ? as[t][3] : as[t][2]) : ((c & 1) ? as[t][1] : as[t][0])) * (1.f / 640.f);
          const float ss = ((c & 2) ? ((c & 1) ? aq[t][3] : aq[t][2]) : ((c & 1) ? aq[t][1] : aq[t][0])) * (1.f / 640.f);
          const float r = __builtin_amdgcn_rsqf(fmaxf(ss - s * s, 0.f) + 1e-5f);
          if ((c >> 2) == q) { *(LAS float*)(L + L_MEAN + (32 * w + 16 * t + c) * 4) = -s; *(LAS float*)(L + L_RSTD + (32 * w + 16 * t + c) * 4) = k.mat ? -r : r; }
        }
        bar();
        tables();
      } else {
#define KS(s) (((s) + 12) % 20)
        unsigned wb[2][2] = {{L_D + rowrd, L_D + rowrd + 51200}, {L_D + (rowrd ^ 64), L_D + (rowrd ^ 64) + 51200}};
        unsigned xv[2] = {OFF_XT + XT_BUF + (8 * k.P1 + 2 * w) * 20480 + lane * 16, 0}; xv[1] = xv[0] + 20480;
        pin(wb[0][0]); pin(wb[0][1]); pin(wb[1][0]); pin(wb[1][1]); pin(xv[0]); pin(xv[1]);
        constexpr int R = AHEAD + 2;
        bf16x8 xa[R];
        auto req = [&](int l) { const int ks = KS(2 * (l >> 2) + (l & 1)); if (l < 40) xa[l % R] = ld16(rw, xv[(l >> 1) & 1] + (ks & 3) * 1024, (ks >> 2) * 4096); };
        auto rd = [&](int d, int n) { const int s = 2 * d + (n & 1); wf[s & 3][n >> 1] = lds16(L, wb[s & 1][KS(s) / 10] + ((KS(s) >> 1) % 5) * 10240 + (n >> 1) * 2048); };
        bar();
#pragma unroll
        for (int l = 0; l < AHEAD; ++l) { req(l); sched(); }
#pragma unroll
        for (int n = 0; n < 10; ++n) rd(0, n);
        lgk0();
#pragma unroll
        for (int d = 0; d < 10; ++d) {
          if (d == 8) barv();
          if (d == 9) { tables(); sched(); }
#pragma unroll
          for (int n = 0; n < 20; ++n) {
            const int m = n >> 1, t = m / 5, i = t ? 9 - m : m, e = (n ^ m) & 1;
            if (d == 0 && !(n & 1)) mma0(acc[t][i], xa[(2 * t + e) % R], wf[e][i]); else mma(acc[t][i], xa[(4 * d + 2 * t + e) % R], wf[(2 * d + e) & 3][i]);
            if (n % 5 == 4) req(4 * d + n / 5 + AHEAD);
            else if (d < 9 && n < 13) { const int j = n - n / 5; if (j < 10) rd(d + 1, j); }
            if (n == 18 && d < 9) lgk0();
            sched();
          }
        }
        settle();
      }
#pragma unroll
      for (int i = 0; i < 5; ++i) {
        const float cs = cs1[i];
        const f32x4 a = (mean[0] * cs + acc[0][i]) * rstd[0], bb = (mean[1] * cs + acc[1][i]) * rstd[1];
        st16(rw, swap_pair(pk(a[0], a[1]), pk(a[2], a[3]), pk(bb[0], bb[1]), pk(bb[2], bb[3])), vo + i * 1024);
      }
      if (layer == 0 && (k.P & 1) && w < 2) {
        const int j = (k.b >> 7) * 64 + (k.b & 63), g3 = (j * 2731) >> 16;
        const f32x4 t = *(LAS f32x4*)(L + L_CP + (w * 64 + lane) * 16);
        if (j < 120) __builtin_amdgcn_raw_buffer_store_b32(__float_as_uint((t[0] + t[1]) + (t[2] + t[3])), rw, lane * 4, OFF_CS + (g3 * 3072 + w * 1536 + 64 * (j - g3 * 24)) * 4, SC1);
      }
      drain_bar();
    }
    {
      PHASE_LANE();
      const int wm = w >> 1, wn = w & 1;
      f32x4 acc[4][5];
      bf16x8 fa[2][4], fl[2][2], fb[2][2][4];
#pragma unroll
      for (int t = 0; t < 4; ++t)
#pragma unroll
        for (int i = 0; i < 5; ++i) { acc[t][i] = (f32x4){0.f, 0.f, 0.f, 0.f}; pin(acc[t][i]); }
      unsigned wb = L_WR + wm * 8192 + rowrd, ab = L_D + wn * 5120 + lane * 16;
      unsigned ws_slot = 65536, ab_slot = 20480;
      unsigned ev[2], ev8[2], en = w == k.kq ? 0 : WS_END;
      ev[0] = OFF_EX + layer * EX_BUF + ((k.P * 16 + w * 4 + k.np) * 3 + ((k.kq - w) & 3) - 1) * 10240 + lane * 16; ev[1] = ev[0] + 5120;
      ev8[0] = ev[0] - lane * 8 + 2048; ev8[1] = ev8[0] + 5120;
      unsigned lo = L_D + 20480 + w * 10240 + lane * 16, lo8 = lo - lane * 8 + 2048;
      pin(lo); pin(lo8);
      pin(wb); pin(ab); pin(ev[0]); pin(ev[1]); pin(ev8[0]); pin(ev8[1]); pins(en);
      const rsrc_t re = mk(k.ws, en);
      bar();
#pragma unroll
      for (int n = 0; n < 8; ++n) fb[0][n & 1][n >> 1] = lds16(L, (wb ^ ((n & 1) * 64)) + 49152 + (n >> 1) * 2048);
      unsigned an = ab + 20480, w0 = wb + 65536, w1 = w0 ^ 64;
      pin(an); pin(w0); pin(w1);
      bar();
#pragma unroll
      for (int n = 0; n < 10; ++n) { if (n < 8) fa[n & 1][n >> 1] = lds16(L, ab + (n & 1) * 10240 + (n >> 1) * 1024); else fl[0][n & 1] = lds16(L, ab + (n & 1) * 10240 + 4096); }
      lgk0();
      int left = 7;
      auto tile = [&](int p, bool last) {
#pragma unroll
        for (int n = 0; n < 40; ++n) {
          const int m = n >> 1, i = m >> 2, t = (i & 1) ? 3 - (m & 3) : m & 3, kk = (n ^ m) & 1, e = n & 1;
          if (n == 16) barv();
          mma(acc[t][i], i < 4 ? fa[kk][i & 3] : fl[p][kk], fb[p][kk][t]);
          if (n < 8) fb[p ^ 1][e][m] = lds16(L, (e ? w1 : w0) + m * 2048);
          else if (n >= 16 && n < 20) fa[e][m - 8] = lds16(L, an + e * 10240 + (m - 8) * 1024);
          else if (m == 10) fl[p ^ 1][e] = lds16(L, an + e * 10240 + 4096);
          else if (m == 12 || m == 16) fa[e][m / 4 - 1] = lds16(L, an + e * 10240 + (m / 4 - 1) * 1024);
          else if (!last) {
            if (n == 8) { ws_slot = ws_slot == 81920 ? 0 : ws_slot + 16384; pins(ws_slot); }
            if (n == 9) { ab_slot = ab_slot == 40960 ? 0 : ab_slot + 20480; pins(ab_slot); }
            if (n == 10) { w0 = wb + ws_slot; pin(w0); }
            if (n == 11) { w1 = w0 ^ 64; pin(w1); }
            if (n == 34) { an = ab + ab_slot; pin(an); if (p) { --left; pins(left); } }
          }
          sched();
        }
        lgk0();
      };
#pragma unroll 1
      do { tile(0, false); tile(1, false); } while (left);
      tile(0, true);
      {
        unsigned o[10];
        auto send = [&](int t, int j) {
          if (j < 5) { o[2 * j] = pk(acc[t][j][0], acc[t][j][1]); o[2 * j + 1] = pk(acc[t][j][2], acc[t][j][3]); }
          if (j == 5) st16(re, (u32x4){o[0], o[1], o[2], o[3]}, ev[t >> 1] + (t & 1) * 2560);
          if (j == 6) { *(LAS u32x4*)(L + lo + t * 2560) = (u32x4){o[0], o[1], o[2], o[3]}; *(LAS u32x2*)(L + lo8 + t * 2560) = (u32x2){o[8], o[9]}; }
          if (j == 7) st16(re, (u32x4){o[4], o[5], o[6], o[7]}, ev[t >> 1] + (t & 1) * 2560 + 1024);
          if (j == 8) { __builtin_amdgcn_raw_buffer_store_b64((u32x2){o[8], o[9]}, re, ev8[t >> 1] + (t & 1) * 2560, 0, SC1); *(LAS u32x4*)(L + lo + t * 2560 + 1024) = (u32x4){o[4], o[5], o[6], o[7]}; }
        };
#pragma unroll
        for (int n = 0; n < 40; ++n) {
          const int t = n / 10, i = (n % 10) >> 1, kk = (n ^ i) & 1;
          mma(acc[t][i], i < 4 ? fa[kk][i & 3] : fl[1][kk], fb[1][kk][t]);
          if (t) send(t - 1, n % 10);
          sched();
        }
#pragma unroll
        for (int j = 0; j < 9; ++j) { send(3, j); sched(); }
      }
      drain_bar();
      if (layer == 0) {
        const rsrc_t rx = mk(x0, 2048 * 640 * 2);
#pragma unroll
        for (int j = 0; j < 3; ++j) {
          const int u = 64 * j + lane, r = (u * 205) >> 11;
          dma<0, 0>(rx, L + L_D + w * 3072 + j * 1024, r * 1120 + u * 16 + (j == 2 ? (lane & 32) << 25 : 0), 16 * tfo * 1280 + cto * 160);
        }
        __builtin_amdgcn_s_waitcnt(0);
        asm volatile("" ::: "memory");
#pragma unroll
        for (int i = 0; i < 5; ++i) { const u32x2 t = *(LAS u32x2*)(L + L_D + w * 3072 + c * 160 + 8 * q + 32 * i); res[2 * i] = t[0]; res[2 * i + 1] = t[1]; }
      }
      f32x4 v[5];
      {
        LAS char* t = L + L_D + 20480 + k.kq * 10240 + w * 2560;
        const u32x4 a = *(LAS u32x4*)(t + lane * 16), bb = *(LAS u32x4*)(t + 1024 + lane * 16); const u32x2 d = *(LAS u32x2*)(t + 2048 + lane * 8);
        v[0] = unpk(a[0], a[1]); v[1] = unpk(a[2], a[3]); v[2] = unpk(bb[0], bb[1]); v[3] = unpk(bb[2], bb[3]); v[4] = unpk(d[0], d[1]);
#pragma unroll
        for (int i = 0; i < 5; ++i) { v[i] += unpk(res[2 * i], res[2 * i + 1]); pin(v[i]); }
      }
      unsigned rv = OFF_EX + layer * EX_BUF + (k.P * 16 + k.kq * 4 + k.np) * 30720 + w * 2560 + lane * 16, rv8 = rv - lane * 8;
      pin(rv); pin(rv8);
      X_ADDR(2 * layer);
      bar();
      u32x4 e[3][2]; u32x2 e8[3];
#pragma unroll
      for (int j = 0; j < 3; ++j)
#pragma unroll
        for (int n = 0; n < 3; ++n) {
          if (j < 2) e[n][j] = __builtin_amdgcn_raw_buffer_load_b128(rw, rv + j * 1024, n * 10240, SC1);
          else e8[n] = __builtin_amdgcn_raw_buffer_load_b64(rw, rv8 + 2048, n * 10240, SC1);
          sched();
        }
      float s = 0.f, ss = 0.f;
#pragma unroll
      for (int j = 0; j < 3; ++j) {
#pragma unroll
        for (int n = 0; n < 3; ++n) {
          if (j < 2) { v[2 * j] += unpk(e[n][j][0], e[n][j][1]); v[2 * j + 1] += unpk(e[n][j][2], e[n][j][3]); }
          else v[4] += unpk(e8[n][0], e8[n][1]);
        }
        x_part(rw, vx, vx2, j, v, res, s, ss);
        sched();
      }
      x_stats(rw, vs, s, ss);
      drain_bar();
    }
    {
      PHASE_LANE();
      const int wm = w >> 1, wn = w & 1;
      f32x4 acc[4][6];
      bf16x8 fa[2][5], fl[2][2], fb[2][2][4];
#pragma unroll
      for (int t = 0; t < 4; ++t)
#pragma unroll
        for (int i = 0; i < 6; ++i) { acc[t][i] = (f32x4){0.f, 0.f, 0.f, 0.f}; pin(acc[t][i]); }
      unsigned wb = L_WR + wn * 6144 + lane * 16, xb = L_D + wm * 8192 + lane * 16;
      unsigned w_slot = 73728, x_slot = 16384;
      unsigned vo = OFF_HT + layer * HT_BUF + ((8 * k.P + 4 * wm + (q & 1)) * 48 + 3 * (4 * k.kq + k.np)) * 1024 + (6 * wn + (q >> 1)) * 256 + c * 16, vo2 = vo + 98304;
      unsigned ta = (64 * wm + c) * 4, tc = L_CS3 + (48 * wn + 4 * q) * 4;
      unsigned sa = L_D + 49152 + (2 * w + (lane >> 5)) * 1024 + (lane & 31) * 4, sw = (32 * w + 16 * (lane >> 5) + c) * 4;
      pin(wb); pin(xb); pin(vo); pin(vo2); pin(ta); pin(tc); pin(sa); pin(sw);
      bar();
#pragma unroll
      for (int n = 0; n < 12; ++n) { if (n < 10) fa[n & 1][n >> 1] = lds16(L, wb + 49152 + (n & 1) * 12288 + (n >> 1) * 1024); else fl[0][n & 1] = lds16(L, wb + 49152 + (n & 1) * 12288 + 5120); }
      unsigned wn3 = wb + 73728, xn = xb + 16384;
      pin(wn3); pin(xn);
      bar();
#pragma unroll
      for (int n = 0; n < 8; ++n) fb[0][n & 1][n >> 1] = lds16(L, xb + n * 1024);
      lgk0();
      int left = 4;
      auto mm = [&](int p, int n) { const int m = n >> 1, i = m >> 2, t = (i & 1) ? 3 - (m & 3) : m & 3, kk = (n ^ m) & 1; mma(acc[t][i], i < 5 ? fa[kk][i < 5 ? i : 0] : fl[p][kk], fb[p][kk][t]); };
      auto rd = [&](int p, int n) {
        const int m = n >> 1, e = n & 1;
        if (m == 8 || m == 9) fa[e][m - 8] = lds16(L, wn3 + e * 12288 + (m - 8) * 1024);
        else if (m == 10) fl[p ^ 1][e] = lds16(L, wn3 + e * 12288 + 5120);
        else if (m == 12 || m == 16 || m == 20) fa[e][m / 4 - 1] = lds16(L, wn3 + e * 12288 + (m / 4 - 1) * 1024);
        else if (m == 11) fb[p ^ 1][e][0] = lds16(L, xn + e * 1024);
        else if (m >= 13 && m <= 15) fb[p ^ 1][e][m - 12] = lds16(L, xn + e * 1024 + (m - 12) * 2048);
      };
#pragma unroll 1
      do {
#pragma unroll
        for (int p = 0; p < 2; ++p) {
#pragma unroll
          for (int n = 0; n < 48; ++n) {
            if (n == 16) barv();
            mm(p, n); rd(p, n);
            if (n == 34) { x_slot = x_slot == 32768 ? 0 : x_slot + 16384; pins(x_slot); }
            if (n == 35) { w_slot = w_slot == 73728 ? 0 : w_slot + 24576; pins(w_slot); }
            if (n == 42) { wn3 = wb + w_slot; xn = xb + x_slot; pin(wn3); pin(xn); }
            if (n == 43 && p) { --left; pins(left); }
            sched();
          }
          lgk0();
        }
      } while (left);
      float mean[4], rl2[4], sd[4];
      f32x4 cs[6];
      unsigned h[4][2];
      f32x4 o[4];
      auto corr1 = [&](int f, int n) { const int t = n >> 3, i = 2 * f + ((n >> 2) & 1), e = n & 3; if (n < 32) acc[t][i][e] = one(fmaf(cs[i][e], mean[t], acc[t][i][e])); };
      auto glu1 = [&](int n) {
        const int t = n / 15, j = n % 15;
        if (j < 2) { o[t][2 * j] = one(acc[t][0][2 * j] * rl2[t]); o[t][2 * j + 1] = one(acc[t][0][2 * j + 1] * rl2[t]); }
        else if (j < 6) o[t][j - 2] = __builtin_amdgcn_exp2f(o[t][j - 2]);
        else if (j < 8) { o[t][2 * j - 12] = one(fmaf(o[t][2 * j - 12], sd[t], sd[t])); o[t][2 * j - 11] = one(fmaf(o[t][2 * j - 11], sd[t], sd[t])); }
        else if (j < 12) o[t][j - 8] = __builtin_amdgcn_rcpf(o[t][j - 8]);
        else if (j < 14) { o[t][2 * j - 24] = one(o[t][2 * j - 24] * acc[t][1][2 * j - 24]); o[t][2 * j - 23] = one(o[t][2 * j - 23] * acc[t][1][2 * j - 23]); }
        else { h[t][0] = pk(o[t][0], o[t][1]); h[t][1] = pk(o[t][2], o[t][3]); }
      };
      auto glu = [&](int f, int t, int from) {
        if (from < 1) { o[t] = acc[t][2 * f] * rl2[t]; asm("" : "+v"(o[t])); }
        if (from < 2) {
#pragma unroll
          for (int r = 0; r < 4; ++r) o[t][r] = __builtin_amdgcn_exp2f(o[t][r]);
        }
        o[t] = o[t] * sd[t] + sd[t]; asm("" : "+v"(o[t]));
#pragma unroll
        for (int r = 0; r < 4; ++r) o[t][r] = __builtin_amdgcn_rcpf(o[t][r]);
        o[t] *= acc[t][2 * f + 1];
        h[t][0] = pk(o[t][0], o[t][1]); h[t][1] = pk(o[t][2], o[t][3]);
      };
      auto tab2 = [&](float (&d)[4], unsigned off, int j) { d[2 * j] = *(LAS float*)(L + ta + off + 128 * j); d[2 * j + 1] = *(LAS float*)(L + ta + off + 128 * j + 64); };
      float pv[8], s1 = 0.f, s2 = 0.f;
#pragma unroll
      for (int n = 0; n < 26; ++n) {
        if (n == 16) barv();
        mm(0, n); rd(0, n);
        if (n < 2) {
#pragma unroll
          for (int j = 0; j < 4; ++j) pv[4 * n + j] = *(LAS float*)(L + sa + 128 * (4 * n + j));
        }
        if (n == 2) { cs[0] = *(LAS f32x4*)(L + tc); cs[1] = *(LAS f32x4*)(L + tc + 384); }
        if (n == 6) { s1 = one(pv[0] + pv[1]); s2 = one(pv[2] + pv[3]); }
        if (n == 7 || n == 8) { s1 = one(s1 + pv[2 * n - 10]); s2 = one(s2 + pv[2 * n - 9]); }
        if (n == 9) s1 = one(s1 + s2);
        if (n == 10) { s2 = one(s1 * (1.f / 640.f)); s1 = one(s1 * (1.f / 640.f)); }
        if (n == 11) { const auto r = __builtin_amdgcn_permlane16_swap(__float_as_uint(s1), __float_as_uint(s2), false, false); s1 = one(__uint_as_float(r[0])); s2 = one(__uint_as_float(r[1])); }
        if (n == 12) { s2 = one(fmaf(-s1, s1, s2)); s1 = one(-s1); }
        if (n == 13) { *(LAS float*)(L + sw + L_MEAN) = s1; s2 = one(fmaxf(s2, 0.f)); }
        if (n == 14) s2 = one(s2 + 1e-5f);
        if (n == 15) s1 = __builtin_amdgcn_rsqf(s2);
        if (n == 16 || n == 17) tab2(mean, L_MEAN, n & 1);
        if (n == 18) s2 = one(s2 * s1);
        if (n == 19) s1 = one(s1 * -1.44269504089f);
        if (n == 20) *(LAS float*)(L + sw + L_STD) = s2;
        if (n == 21) *(LAS float*)(L + sw + L_RSTD) = s1;
        sched();
      }
#pragma unroll
      for (int n = 26; n < 48; ++n) {
        mm(0, n); rd(0, n);
        if (n < 34) corr1(0, n - 26);
        if (n >= 34 && n < 40) { corr1(0, 2 * n - 60); corr1(0, 2 * n - 59); }
        if (n == 40 || n == 41) corr1(0, n - 20);
        if (n == 42) { cs[2] = *(LAS f32x4*)(L + tc + 64); cs[3] = *(LAS f32x4*)(L + tc + 384 + 64); }
        if (n > 42) { corr1(0, 2 * n - 64); corr1(0, 2 * n - 63); }
        sched();
      }
      barv();
#pragma unroll
      for (int n = 0; n < 48; ++n) {
        mm(1, n);
        if (n < 16) { corr1(1, 2 * n); corr1(1, 2 * n + 1); }
        if (n == 12 || n == 13) tab2(rl2, L_RSTD, n & 1);
        if (n == 14 || n == 15) tab2(sd, L_STD, n & 1);
        if (n == 16 || n == 17) cs[n - 12] = *(LAS f32x4*)(L + tc + 128 + 384 * (n & 1));
        if (n >= 18) glu1(n - 18);
        sched();
      }
      st16(rw, swap_pair(h[0][0], h[0][1], h[1][0], h[1][1]), vo);
      sched();
      glu(0, 2, 0); glu(0, 3, 0);
      st16(rw, swap_pair(h[2][0], h[2][1], h[3][0], h[3][1]), vo2);
      sched();
#pragma unroll
      for (int f = 1; f < 3; ++f)
#pragma unroll
        for (int t = 0; t < 4; t += 2) {
          if (f == 2) { acc[t][4] += cs[4] * mean[t]; acc[t][5] += cs[5] * mean[t]; acc[t + 1][4] += cs[4] * mean[t + 1]; acc[t + 1][5] += cs[5] * mean[t + 1]; }
          glu(f, t, 0); glu(f, t + 1, 0);
          st16(rw, swap_pair(h[t][0], h[t][1], h[t + 1][0], h[t + 1][1]), (t ? vo2 : vo) + f * 512);
          sched();
        }
    }
    {
      PHASE_LANE();
      const int kh = w & 1;
      unsigned wb[3] = {L_W4 + kh * 5120 + lane * 16, 0, 0}; wb[1] = wb[0] + 51200; wb[2] = wb[0] + 102400;
      pin(wb[0]); pin(wb[1]); pin(wb[2]);
      bar();
      constexpr int R = AHEAD + 2;
      bf16x8 ha[R], wf[6][5];
      f32x4 acc[2][5];
      auto rdw = [&](int s, int i) { wf[s % 6][i] = lds16(L, wb[w4off(s) / 51200] + w4off(s) % 51200 + i * 1024); };
#pragma unroll
      for (int n = 0; n < 30; ++n) rdw(n / 5, n % 5);
      lgk0();
      drain_bar();
      sched();
      unsigned hv[2] = {OFF_HT + layer * HT_BUF + tfo * 49152 + kh * 1024 + lane * 16, OFF_HT + layer * HT_BUF + (tfo ^ 1) * 49152 + kh * 1024 + lane * 16};
      unsigned rd = L_RED + w * 5120 + lane * 16, wr = L_RED + (w ^ 1) * 5120 + lane * 16;
      pin(hv[0]); pin(hv[1]); pin(rd); pin(wr);
      X_ADDR(1);
      unsigned ow = L_W4 + 61440 + w * 3072 + c * 160 + 8 * q, orr = L_W4 + 61440 + w * 3072 + lane * 16, vo3[3];
#pragma unroll
      for (int j = 0; j < 3; ++j) { const int u = 64 * j + lane; vo3[j] = 16 * tfo * 1280 + cto * 160 + ((u * 205) >> 11) * 1120 + u * 16 + (j == 2 ? (lane & 32) << 25 : 0); pin(vo3[j]); }
      pin(ow); pin(orr);
#pragma unroll
      for (int i = 0; i < 5; ++i) { acc[0][i] = unpk(res[2 * i], res[2 * i + 1]); pin(acc[0][i]); }
      bar();
      auto req = [&](int l) { if (l < 48) ha[l % R] = ld16(rw, hv[1 - ((l >> 1) & 1)] + (l & 1) * 2048, (l >> 2) * 4096); };
#pragma unroll
      for (int l = 0; l < AHEAD; ++l) { req(l); sched(); }
      f32x4 pt[5];
#pragma unroll
      for (int d = 0; d < 12; ++d) {
        if (d == 4 || d == 9 || d == 10) barv();
#pragma unroll
        for (int n = 0; n < 20; ++n) {
          const int m = n >> 1, v = m / 5, i = v && d < 11 ? 9 - m : m % 5, e = (n ^ m) & 1;
          if (d == 11 && n == 17) barv();
          if (d == 0 && !v && !(n & 1)) mma0(acc[1][i], wf[e][i], ha[e]); else mma(acc[1 - v][i], wf[(2 * d + e) % 6][i], ha[(4 * d + 2 * v + e) % R]);
          if (n % 5 == 4) req(4 * d + n / 5 + AHEAD);
          else if (d >= 2 && d < 11 && n < 13) { const int j = n - n / 5; if (j < 10) rdw(2 * d + 2 + (j & 1), j >> 1); }
          if (n == 18 && d >= 2 && d < 11) lgk0();
          if (d == 11) {
            if (n >= 10 && n < 15) *(LAS f32x4*)(L + wr + (n - 10) * 1024) = acc[1][n - 10];
            if (n >= 17) { pt[2 * n - 34] = *(LAS f32x4*)(L + rd + (2 * n - 34) * 1024); if (n < 19) pt[2 * n - 33] = *(LAS f32x4*)(L + rd + (2 * n - 33) * 1024); }
          }
          sched();
        }
      }
#pragma unroll
      for (int i = 0; i < 5; ++i) { acc[0][i] += pt[i]; pin(acc[0][i]); sched(); }
      if (layer == 1) {
        const rsrc_t ro = mk(out, 2048 * 640 * 2);
#pragma unroll
        for (int i = 0; i < 5; ++i) *(LAS u32x2*)(L + ow + 32 * i) = (u32x2){pk(acc[0][i][0], acc[0][i][1]), pk(acc[0][i][2], acc[0][i][3])};
        asm volatile("" ::: "memory");
#pragma unroll
        for (int j = 0; j < 3; ++j) st16(ro, *(LAS u32x4*)(L + orr + j * 1024), vo3[j]);
      } else {
        float s = 0.f, ss = 0.f;
#pragma unroll
        for (int j = 0; j < 3; ++j) x_part(rw, vx, vx2, j, acc[0], res, s, ss);
        x_stats(rw, vs, s, ss);
        bar();
        drain_bar();
      }
    }
  }
}

DI void stats_load(const Ctx& k, rsrc_t rw, int xb, int tf0, u32x2 (&sv)[8]) {
#pragma unroll
  for (int j = 0; j < 8; ++j) sv[j] = __builtin_amdgcn_raw_buffer_load_b64(rw, (k.lane >> 4) * 1024 + (k.lane & 15) * 8 + j * 128, OFF_ST + xb * ST_BUF + tf0 * 1024, SC1);
}
DI void stats_sum(u32x2 (&sv)[8], float (&s)[2]) {
#pragma unroll
  for (int j = 0; j < 8; ++j) pin(sv[j]);
#pragma unroll
  for (int e = 0; e < 2; ++e) {
    s[e] = __uint_as_float(sv[0][e]);
#pragma unroll
    for (int j = 1; j < 8; ++j) s[e] = one(s[e] + __uint_as_float(sv[j][e]));
    s[e] *= 1.f / 640.f; pin(s[e]);
  }
}
DI void stats_table(const Ctx& k, int a, float (&s)[2], float scale) {
  pin(s[0]); pin(s[1]);
  float r[2];
#pragma unroll
  for (int e = 0; e < 2; ++e) {
    const float ss = __builtin_bit_cast(float, __builtin_amdgcn_update_dpp(0, __builtin_bit_cast(int, s[e]), 0x128, 0xf, 0xf, true));
    const float v = one(fmaxf(one(fmaf(-s[e], s[e], ss)), 0.f) + 1e-5f), q = __builtin_amdgcn_rsqf(v);
    r[e] = one(q * scale);
  }
  if (!(k.lane & 8)) {
    const unsigned t = (64 * a + (k.lane & 48) + 2 * (k.lane & 7)) * 4;
    *(LAS f32x2*)(k.lds + L_MEAN + t) = (f32x2){one(-s[0]), one(-s[1])};
    *(LAS f32x2*)(k.lds + L_RSTD + t) = (f32x2){r[0], r[1]};
  }
}

DI void loader(const Ctx& k0, const char* x0, const char* wsp, const char* wdp, int a4) {
  Ctx k = k0;
  const bool isW = a4 < 2;
  LAS char* const L = k.lds;
  const rsrc_t rw = mk(k.ws, WS_END);
  const rsrc_t rs = mk(wsp, 2048 * 2048 * 2);
  auto fresh = [&]() { asm volatile("" : "+v"(k.lane), "+s"(k.P), "+s"(k.kq), "+s"(k.np), "+s"(k.P1), "+s"(k.ct1), "+s"(k.mat), "+s"(a4)); };
#define lane k.lane
#define a (a4 & 1)
#define me (k.P * 16 + k.kq * 4 + k.np)
#define n3 (4 * k.kq + k.np)
#define sw (((lane & 7) ^ (4 * a + (lane >> 4))) * 16)
#define vo2 ((lane >> 3) * 4096 + sw)
#define so2 ((128 * k.P + 8 * a) * 4096 + (k.kq & 1) * 2048)
#define so3 (OFF_W3 + n3 * 20 * 12288 + a * 12288)
#define so4 (OFF_W4 + (2 * k.np + (k.kq & 1)) * 48 * 5120)
  bf16x8 k1[25], k4[3][8];
  u64 tag = 0;
  int rot = 2 * k.np;
  auto ktile = [&](int t) { const int r = (t + rot) & 7; return 4 * (r >> 1) + 2 * (t >> 3) + (r & 1); };
  auto w2 = [&](int t, int slot) {
#pragma unroll
    for (int j = 0; j < 8; ++j) dma<0, 0>(rs, L + L_WR + slot * 16384 + (2 * j + a) * 1024, vo2, so2 + j * 16 * 4096 + ktile(t) * 128);
  };
  auto w2n = [&](int t0, int t1) {
#pragma unroll 1
    for (int t = t0; t < t1; ++t) w2(t, t < 3 ? t + 3 : t - 3);
  };
  auto w3 = [&](int t, int slot) { dma_lin<SC1, 12>(rw, L + L_WR + slot * 24576 + a * 12288, lane * 16, so3 + t * 24576); };
  auto w4 = [&](int ch) { dma_lin<SC1, 15>(rw, L + L_W4 + w4slot(ch) * 30720 + a * 15360, lane * 16, so4 + ch * 30720 + a * 15360); };
  auto push = [&](int ev, int wg0, int wgstep, int n, int slot) {
    if (a4 == 2) {
      unsigned dst = lane < n ? OFF_MB + ev * MB_EV + (wg0 + lane * wgstep) * 128 + slot * 8 : 0x7fffffff;
      pin(dst); pin(tag);
      bar();
      __builtin_amdgcn_raw_buffer_store_b64(__builtin_bit_cast(u32x2, tag), rw, dst, 0, SC1);
    } else bar();
    fresh();
  };
  unsigned budget = SPIN_CAP;
  auto wait_mb = [&](int ev, int wg, int n) {
    GAS u64* src = (GAS u64*)(k.ws + OFF_MB + ev * MB_EV + wg * 128) + (lane & (n - 1));
#pragma unroll 1
    for (; budget; budget = uni(budget - 1)) {
      const u64 v = __hip_atomic_load(src, __ATOMIC_RELAXED, __HIP_MEMORY_SCOPE_AGENT);
      if (__all(v == tag)) break;
      __builtin_amdgcn_s_sleep(1);
    }
  };
  auto poll = [&](int ev, int wg, int n) {
    if (a4 == 2) wait_mb(ev, wg, n);
    drain_bar();
  };
  auto get_tag = [&]() { const char* e = k.ws; pins(e); tag = *(const GAS u64*)e; };
  auto mix_tag = [&]() { pin(tag); tag = tag * 6364136223846793005ull + 1442695040888963407ull; };
  if (!isW) __builtin_amdgcn_s_setprio(1);
  {
    const rsrc_t r1 = isW ? mk(wdp, 640 * 640 * 2) : mk(x0, 2048 * 640 * 2);
    const unsigned v1 = (lane >> 3) * 1280 + sw, s1 = ((isW ? 80 * k.ct1 : 128 * k.P1) + 8 * a) * 1280;
    int slot = 2, wt = rot1(k);
    if ((k.P & 1) && !isW) __builtin_amdgcn_s_sleep(LAG);
#pragma unroll 1
    for (int t = 0; t < 10; ++t) {
      if (isW) {
        if (t >= 3) { waitv<10>(); bar(); }
#pragma unroll
        for (int j = 0; j < 5; ++j) dma<0, 0>(r1, L + L_D + wt * 10240 + (2 * j + a) * 1024, v1, s1 + 16 * j * 1280 + wt * 128);
      } else {
        if (t >= 2) { waitv<8>(); bar(); }
#pragma unroll
        for (int j = 0; j < 8; ++j) dma<0, 0>(r1, L + L_XR + slot * 16384 + (2 * j + a) * 1024, v1, s1 + 16 * j * 1280 + wt * 128);
      }
      slot = slot == 2 ? 0 : slot + 1; wt = wt == 9 ? 0 : wt + 1;
    }
    if (isW) {
      waitv<10>(); bar();
      waitv<5>(); bar(); w2(0, 3);
      waitv<8>(); bar(); w2(1, 4);
      bar();
    } else {
      get_tag();
      waitv<8>(); bar(); waitv<0>();
      mix_tag(); pin(tag);
      bar();
      if (!(k.P & 1)) {
#pragma unroll
        for (int j = 0; j < 2; ++j) touch<0>(rs, L + L_JUNK, (lane >> 1) * 4096 + (lane & 1) * 128, (128 * k.P + 64 * a + 32 * j) * 4096 + (k.kq & 1) * 2048 + ktile(8) * 128);
      }
      bar();
    }
  }
  {
    unsigned kb[2] = {L_D + lane * 16 + ((a4 >> 1) * 10 + a) * 1024, 0}; kb[1] = kb[0] + 61440; pin(kb[0]); pin(kb[1]);
    bar();
#pragma unroll
    for (int j = 0; j < 25; ++j) k1[j] = lds16(L, kb[j / 15] + ((j / 5) % 3) * 20480 + (j % 5) * 2048);
    if (isW) w2(2, 5);
  }
#pragma unroll 1
  for (int layer = 0; layer < 2; ++layer) {
    const int ev0 = layer * EV_PER_LAYER;
    if (layer) {
      poll(ev0 + EV_X, k.P1 * 16 + k.mat * 8 + k.ct1, 16);
      if (!isW) { u32x2 sv[8]; float sm[2]; stats_load(k, rw, 1, 8 * k.P1 + 4 * a, sv); stats_sum(sv, sm); stats_table(k, a, sm, k.mat ? -1.f : 1.f); }
      bar();
      if (isW) w2n(3, 6);
    }
    push(ev0 + EV_AB, (2 * k.mat + (k.P1 >> 3)) * 4 + (k.ct1 >> 1), 16, 16, 8 * (k.P1 & 1) + (k.P1 & 6) + (k.ct1 & 1));
    GAS u64* rtf = (GAS u64*)(k.ws + OFF_MB + EV_RT * MB_EV);
    if (layer == 0 && a4 == 3 && lane == 0) __hip_atomic_store(rtf + k.b, tag, __ATOMIC_RELAXED, __HIP_MEMORY_SCOPE_AGENT);
    if (isW && layer == 0) { get_tag(); w2n(3, 6); mix_tag(); }
    const unsigned soab = OFF_AB + layer * AB_BUF + (((k.kq >> 1) * 4 + k.np) * 64 + 32 * (k.kq & 1)) * 10240;
    unsigned l16 = lane * 16, so0 = soab + a * 10240 + ktile(0) * 20480, la0 = L_D + a * 10240;
    so0 = __builtin_amdgcn_readfirstlane(so0); la0 = __builtin_amdgcn_readfirstlane(la0);
    pin(l16); pins(so0); pins(la0);
    if (a4 == 3 && (k.P & 1) && layer == 0) __builtin_amdgcn_s_sleep(LAG2);
    poll(ev0 + EV_AB, me, 8);
    if (!isW) dma_lin<SC1, 10>(rw, L + la0, l16, so0);
    if (isW) {
      const unsigned sb2 = uni(so2), lb2 = uni(L_WR + a * 1024);
      unsigned v2 = vo2, so = uni(sb2 + ktile(6) * 128), la = uni(lb2 + 49152), t = 7;
      pin(v2);
#pragma unroll 1
      do {
        waitv<32>(); bar();
#pragma unroll
        for (int j = 0; j < 2; ++j) dma<0, 0>(rs, L + la + j * 2048, v2, so + j * 16 * 4096);
        sched();
        const unsigned sn = uni(sb2 + ktile(t) * 128), ln = uni(la == lb2 + 81920 ? lb2 : la + 16384);
        t = uni(t + 1);
        sched();
#pragma unroll
        for (int j = 2; j < 8; ++j) dma<0, 0>(rs, L + la + j * 2048, v2, so + j * 16 * 4096);
        so = sn; la = ln;
      } while (t != 17);
      const bool chk = layer == 0 && a == 0;
      u64 f[4];
      if (chk) {
#pragma unroll
        for (int j = 0; j < 4; ++j) f[j] = __hip_atomic_load(rtf + lane + 64 * j, __ATOMIC_RELAXED, __HIP_MEMORY_SCOPE_AGENT);
      }
      waitv<32>(); bar();
      if (chk) {
#pragma unroll
        for (int j = 0; j < 4; ++j) pin(f[j]);
#pragma unroll 1
        for (unsigned spins = 0; spins < SPIN_CAP && !__all(f[0] == tag && f[1] == tag && f[2] == tag && f[3] == tag); ++spins) {
#pragma unroll
          for (int j = 0; j < 4; ++j) f[j] = __hip_atomic_load(rtf + lane + 64 * j, __ATOMIC_RELAXED, __HIP_MEMORY_SCOPE_AGENT);
        }
      }
      waitv<0>(); bar();
      w3(3, 1); bar(); bar(); w3(0, 2); bar();
      w3(1, 3); bar(); w3(2, 0);
    } else {
      const unsigned sba = uni(soab + a * 10240);
      dma_lin<SC1, 10>(rw, L + la0 + 20480, l16, sba + ktile(1) * 20480);
      unsigned so = uni(sba + ktile(2) * 20480), la = uni(la0 + 40960), t = 3;
      auto step = [&]() {
        bar();
        dma_lin<SC1, 4>(rw, L + la, l16, so);
        sched();
        const unsigned sn = uni(sba + ktile(t) * 20480), ln = uni(la == la0 + 40960 ? la0 : la + 20480);
        t = uni(t + 1);
        sched();
        dma_lin<SC1, 6>(rw, L + la + 4096, l16, so + 4096);
        so = sn; la = ln;
      };
#pragma unroll 1
      do { waitv<10>(); step(); } while (t != 8);
      unsigned ja = L_JUNK + 256 + layer * 256 + a * 128;
      if (lane < 8) dma<SC1, 0>(rw, L + ja, l16, OFF_MB + (ev0 + EV_AB) * MB_EV + me * 128);
      ja += (lane & 15) * 8; pin(ja);
      waitv<11>(); step();
      waitv<10>();
      if (!__all(*(LAS u64*)(L + ja) == tag)) wait_mb(ev0 + EV_AB, me, 16);
      step();
#pragma unroll 1
      do { waitv<10>(); step(); } while (t != 17);
      waitv<10>(); bar();
      waitv<0>(); bar();
    }
    auto k4load = [&](int n) {
#pragma unroll
      for (int j = 0; j < 8; ++j) k4[n][j] = ld16(rw, lane * 16 + a4 * 1024, so4 + (5 + n) * 30720 + j * 4096);
    };
    push(ev0 + EV_EX, k.P * 16 + k.np, 4, 4, k.kq);
    if (layer == 0) k4load(0);
    poll(ev0 + EV_EX, me, 4);
    push(ev0 + EV_X1, k.P * 16, 1, 16, n3);
    if (layer == 0) {
      u32x2 cv[5];
      if (isW) {
#pragma unroll
        for (int j = 0; j < 5; ++j) cv[j] = __builtin_amdgcn_raw_buffer_load_b64(rw, lane * 8, OFF_CS + (j * 3072 + a * 1536 + 96 * n3) * 4, SC1);
      }
      k4load(1);
      if (isW) {
        sched();
        f32x2 s = {0.f, 0.f};
#pragma unroll
        for (int j = 0; j < 5; ++j) s += __builtin_bit_cast(f32x2, cv[j]);
        if (lane < 48) *(LAS f32x2*)(L + L_CS3 + (96 * a + 2 * lane) * 4) = s;
      }
    }
    l16 = lane * 16; so0 = OFF_XT + 2 * layer * XT_BUF + (8 * k.P + 4 * a) * 20480; la0 = L_D + a * 8192;
    so0 = __builtin_amdgcn_readfirstlane(so0); la0 = __builtin_amdgcn_readfirstlane(la0);
    pin(l16); pins(so0); pins(la0);
    poll(ev0 + EV_X1, me, 16);
    if (!isW) {
#pragma unroll
      for (int j = 0; j < 4; ++j) dma_lin<SC1, 2>(rw, L + la0 + j * 2048, l16, so0 + j * 20480);
    }
    if (isW) {
      bar();
      __builtin_amdgcn_s_sleep(3);
      dma_lin<SC1, 4>(rw, L + L_D + 49152 + a * 4096, lane * 16, OFF_ST + 2 * layer * ST_BUF + (8 * k.P + 4 * a) * 1024);
      const unsigned lb3 = uni(L_WR + a * 12288), se = uni(so3 + 10 * 24576);
      unsigned so = uni(so3 + 4 * 24576), la = uni(lb3 + 49152);
#pragma unroll 1
      do {
        waitv<24>(); bar();
        dma_lin<SC1, 4>(rw, L + la, l16, so);
        sched();
        const unsigned sn = uni(so + 24576), ln = uni(la == lb3 + 73728 ? lb3 : la + 24576);
        sched();
        dma_lin<SC1, 8>(rw, L + la + 4096, l16, so + 4096);
        so = sn; la = ln;
      } while (so != se);
      waitv<24>(); bar();
      waitv<12>(); bar(); waitv<0>(); bar();
      bar();
      w4(2); w4(3); w4(4);
    } else {
#pragma unroll
      for (int j = 0; j < 4; ++j) dma_lin<SC1, 2>(rw, L + la0 + 16384 + j * 2048, l16, so0 + 2048 + j * 20480);
      unsigned so = uni(so0 + 4096), la = uni(la0 + 32768);
#pragma unroll 1
      do {
        waitv<8>(); bar();
        dma_lin<SC1, 2>(rw, L + la, l16, so);
        sched();
        const unsigned sn = uni(so + 2048), ln = uni(la == la0 + 32768 ? la0 : la + 16384);
        sched();
#pragma unroll
        for (int j = 1; j < 4; ++j) dma_lin<SC1, 2>(rw, L + la + j * 2048, l16, so + j * 20480);
        so = sn; la = ln;
      } while (so != so0 + 20480);
      waitv<8>(); bar();

      w4(0);
      waitv<15>(); bar();

      w4(1);
      bar();

      waitv<0>();
    }
    bar();
    push(ev0 + EV_H, k.P * 16, 1, 16, n3);
    if (layer == 0) k4load(2);
    auto k4put = [&](int n, bool paced) {
      unsigned kb = L_W4 + w4slot(5 + n) * 30720 + a4 * 1024 + lane * 16; pin(kb);
#pragma unroll
      for (int j = 0; j < 8; ++j) if (j < 7 || isW) { *(LAS bf16x8*)(L + kb + j * 4096) = k4[n][j]; if (paced) __builtin_amdgcn_s_sleep(2); }
    };
    k4put(0, false); k4put(1, false);
    poll(ev0 + EV_H, me, 16);
    bar();
    k4put(2, true);
    bar(); bar();
    rot = 0;
    if (isW && layer == 0) w2n(0, 2);
    bar();
    if (layer == 0) {
      if (isW) w2n(2, 3);
      unsigned kb[2] = {L_D + lane * 16 + ((a4 >> 1) * 10 + a) * 1024, 0}; kb[1] = kb[0] + 61440; pin(kb[0]); pin(kb[1]);
      bar();
#pragma unroll
      for (int j = 0; j < 7; ++j) *(LAS bf16x8*)(L + kb[j / 15] + ((j / 5) % 3) * 20480 + (j % 5) * 2048) = k1[j];
      push(EV_PER_LAYER + EV_X, k.P * 16, 1, 16, n3);
#pragma unroll
      for (int j = 7; j < 25; ++j) *(LAS bf16x8*)(L + kb[j / 15] + ((j / 5) % 3) * 20480 + (j % 5) * 2048) = k1[j];
    }
  }
  if (k.b == 0 && a4 == 2 && lane == 0) *(GAS u64*)k.ws = tag;
#undef lane
#undef a
#undef me
#undef n3
#undef sw
#undef vo2
#undef so2
#undef so3
#undef so4
}

__global__ void __launch_bounds__(512, 2) fnet_glu_kernel(const char* x0, const char* wsr, const char* wsi, const char* wdr, const char* wdi, const char* wg, const char* wu, const char* wdn, char* out, char* ws) {
  __shared__ __attribute__((aligned(16))) char smem[163840];
  Ctx k;
  k.lds = (LAS char*)smem; k.lane = threadIdx.x & 63; k.b = blockIdx.x;
  const int w = __builtin_amdgcn_readfirstlane(threadIdx.x >> 6);
  if (w < 4 || w == 7) {
    const char* pc; asm volatile("s_getpc_b64 %0" : "=s"(pc));
    touch<0>(mk(pc, CODE_BYTES), k.lds + L_JUNK, (w < 4 ? threadIdx.x : threadIdx.x - 192) * 128, 0);
  }
  asm volatile("" : "+s"(x0), "+s"(wsr), "+s"(wsi), "+s"(wdr), "+s"(wdi), "+s"(wg), "+s"(wu), "+s"(wdn), "+s"(out), "+s"(ws));
  k.ws = ws;
  const int x = k.b & 7, i = k.b >> 3;
  k.kq = x >> 1; k.P = 8 * (x & 1) + 4 * (i >> 4) + 2 * ((i >> 2) & 1) + ((i >> 3) & 1); k.np = i & 3;
  k.P1 = 4 * (x >> 1) + (i >> 3); k.mat = x & 1; k.ct1 = i & 7;
  if (w < 4) consumer(k, x0, wg, wu, wdn, out, w);
  else loader(k, x0, (x >> 2) ? wsi : wsr, (x & 1) ? wdi : wdr, w - 4);
}
}

extern "C" void kernel_launch(void* const* d_in, const int* in_sizes, int n_in, void* d_out, int out_size, void* d_ws, size_t ws_size, hipStream_t stream) {
  fnet_glu_kernel<<<256, 512, 0, stream>>>((const char*)d_in[0], (const char*)d_in[1], (const char*)d_in[2], (const char*)d_in[3], (const char*)d_in[4],
                                           (const char*)d_in[6], (const char*)d_in[7], (const char*)d_in[8], (char*)d_out, (char*)d_ws);
}
```
